```python
import torch

def _softmax(x, axis=-1):
    return torch.softmax(x, dim=axis)
def _gelu(x):
    return torch.nn.functional.gelu(x, approximate="tanh")
def _silu(x):
    return torch.nn.functional.silu(x)
def _sigmoid(x):
    return torch.sigmoid(x)
def _layernorm(x, axis=-1, eps=1e-5):
    m = x.mean(dim=axis, keepdim=True)
    v = x.var(dim=axis, keepdim=True, unbiased=False)
    return (x - m) / torch.sqrt(v + eps)
def _scatter_add(tgt, idx, src, axis=0):
    return tgt.clone().index_add_(axis, idx, src)
def _segment_sum(src, seg, n):
    out = torch.zeros((n,) + src.shape[1:], dtype=src.dtype)
    return out.index_add_(0, seg, src)
def _pad(x, pw):
    flat = tuple(v for pair in reversed(pw) for v in pair)
    return torch.nn.functional.pad(x, flat)

kernel_name = 'sequential_preLN__spectral_gfnet__geglu_ffn__prompt'

def setup_inputs():

    _w = (lambda a: (lambda t: t.to(torch.bfloat16) if t.is_floating_point() else t)(torch.as_tensor(a)))
    def _A_setup():
        B, S, D = 1, 2048, 576
        Df = D // 2 + 1
        return {
            "x": _w((lambda *s: torch.randn(*s, dtype=torch.bfloat16) * 0.05)(B, S, D)),
            "w_r": _w((lambda *s: torch.randn(*s, dtype=torch.bfloat16) * 0.4)(S, Df) * 0.02),
            "w_i": _w((lambda *s: torch.randn(*s, dtype=torch.bfloat16) * 0.4)(S, Df) * 0.02),
        }
    _A_in = _A_setup()

    def _B_setup():
        B, S, D, F = 1, 2048, 576, 4608
        return {
            "x": _w((lambda *s: torch.randn(*s, dtype=torch.bfloat16) * 0.05)(B, S, D)),
            "w_gate": _w((lambda *s: torch.randn(*s, dtype=torch.bfloat16) * 0.05)(D, F)),
            "w_up": _w((lambda *s: torch.randn(*s, dtype=torch.bfloat16) * 0.05)(D, F)),
            "w_down": _w((lambda *s: torch.randn(*s, dtype=torch.bfloat16) * 0.05)(F, D)),
        }
    _B_in = _B_setup()

    return {
        "A_x": _A_in["x"],
        "A_w_r": _A_in["w_r"],
        "A_w_i": _A_in["w_i"],
        "B_x": _B_in["x"],
        "B_w_gate": _B_in["w_gate"],
        "B_w_up": _B_in["w_up"],
        "B_w_down": _B_in["w_down"],
    }

def reference(A_x, A_w_r, A_w_i, B_x, B_w_gate, B_w_up, B_w_down):
    def _A_ref(x, w_r, w_i):
        B, S, D = x.shape
        X = (lambda x, axes=(-2,-1): torch.fft.rfft2(x.float(), dim=axes))(x, axes=(-2, -1))
        W = w_r + 1j * w_i
        X = X * W
        y = (lambda x, s, axes=(-2,-1): torch.fft.irfft2(x, s=s, dim=axes).to(torch.bfloat16))(X, (S, D), axes=(-2, -1))
        return y

    def _B_ref(x, w_gate, w_up, w_down):
        return (_gelu(x @ w_gate) * (x @ w_up)) @ w_down

    x = A_x
    h = x + _A_ref(_layernorm(x, axis=-1), A_w_r, A_w_i)
    h2 = h + _B_ref(_layernorm(h, axis=-1), B_w_gate, B_w_up, B_w_down)
    return h2
```

```cpp
#include <hip/hip_runtime.h>
typedef float f32x4 __attribute__((ext_vector_type(4)));
typedef float f32x2 __attribute__((ext_vector_type(2)));
typedef __bf16 bf16x8 __attribute__((ext_vector_type(8)));
typedef __bf16 bf16x4 __attribute__((ext_vector_type(4)));
typedef __bf16 bf16x2 __attribute__((ext_vector_type(2)));
typedef short s16x4 __attribute__((ext_vector_type(4)));
typedef unsigned u32x4 __attribute__((ext_vector_type(4)));
typedef unsigned u32x2 __attribute__((ext_vector_type(2)));
#define DI __device__ __forceinline__
#define GAS __attribute__((address_space(1)))
#define LAS __attribute__((address_space(3)))

constexpr int S_ = 2048, D_ = 576, F_ = 4608, DF_ = 289;
constexpr unsigned Z1_OFF = 0;
constexpr unsigned WT_OFF = Z1_OFF + 144 * 16384;
constexpr unsigned Z2_OFF = WT_OFF + 144 * 16384;
constexpr unsigned LN_OFF = Z2_OFF + 256 * 9216;
constexpr unsigned H_OFF  = LN_OFF + 128 * 18432;
constexpr unsigned ACT_OFF = H_OFF + 128 * 18432;
constexpr unsigned WS_END = ACT_OFF + 128 * 147456;

DI unsigned pk2(float lo, float hi) { f32x2 v = {lo, hi}; return __builtin_bit_cast(unsigned, __builtin_convertvector(v, bf16x2)); }
DI float bflo(unsigned u) { return __builtin_bit_cast(float, u << 16); }
DI float bfhi(unsigned u) { return __builtin_bit_cast(float, u & 0xffff0000u); }
DI float asf(unsigned u) { return __builtin_bit_cast(float, u); }
DI bf16x2 asb2(unsigned u) { return __builtin_bit_cast(bf16x2, u); }
DI float rbf(float x) { return bflo(pk2(x, x)); }
DI float cosr(float rev) { return __builtin_amdgcn_cosf(rev); }
DI float sinr(float rev) { return __builtin_amdgcn_sinf(rev); }
DI bf16x8 asfrag(u32x4 v) { return __builtin_bit_cast(bf16x8, v); }
DI f32x4 mfma16(u32x4 a, u32x4 b, f32x4 c) { return __builtin_amdgcn_mfma_f32_16x16x32_bf16(asfrag(a), asfrag(b), c, 0, 0, 0); }
DI u32x2 tr_read(const char* lds_base, unsigned off) {
  s16x4 r = __builtin_amdgcn_ds_read_tr16_b64_v4i16((LAS s16x4*)(lds_base + off));
  return __builtin_bit_cast(u32x2, r);
}
DI void glds16(const char* g, char* l) { __builtin_amdgcn_global_load_lds((const GAS void*)g, (LAS void*)l, 16, 0, 0); }
DI __amdgpu_buffer_rsrc_t mk_rsrc(const void* p, unsigned bytes) { return __builtin_amdgcn_make_buffer_rsrc((void*)p, 0, bytes, 0x00020000); }
DI void st16_wt(u32x4 v, __amdgpu_buffer_rsrc_t r, unsigned off) { __builtin_amdgcn_raw_buffer_store_b128(v, r, off, 0, 16); }
template <int CTRL> DI float dpp_add(float v) {
  return v + __builtin_bit_cast(float, __builtin_amdgcn_update_dpp(0, __builtin_bit_cast(int, v), CTRL, 0xf, 0xf, true));
}
DI float swap16_add(float v) { unsigned u = __builtin_bit_cast(unsigned, v); auto r = __builtin_amdgcn_permlane16_swap(u, u, false, false); return asf(r[0]) + asf(r[1]); }
DI float swap32_add(float v) { unsigned u = __builtin_bit_cast(unsigned, v); auto r = __builtin_amdgcn_permlane32_swap(u, u, false, false); return asf(r[0]) + asf(r[1]); }
DI float wave_sum(float v) {
  v = dpp_add<0x128>(v);
  v = dpp_add<0x124>(v);
  v = dpp_add<0x122>(v);
  v = dpp_add<0x121>(v);
  v = swap16_add(v);
  return swap32_add(v);
}
DI float inv_std(float var) { return 1.0f / rbf(__builtin_sqrtf(rbf(rbf(var) + 1e-5f))); }


template <int NV> DI void side_copy(const char* src, char* dst, int blk, int nthr, int ldonly) {
  size_t b = ((size_t)blk * NV * nthr + threadIdx.x) * 16;
  u32x4 v[NV];
  #pragma unroll
  for (int i = 0; i < NV; ++i) v[i] = *(const u32x4*)(src + b + (size_t)i * nthr * 16);
  const __amdgpu_buffer_rsrc_t r = mk_rsrc(dst, 8u << 20);
  #pragma unroll
  for (int i = 0; i < NV; ++i) { if (ldonly) asm volatile("" :: "v"(v[i])); else st16_wt(v[i], r, b + i * nthr * 16); }
}
__global__ __launch_bounds__(576) void k_fft_rows(const unsigned short* __restrict__ x, const unsigned short* __restrict__ wr,
                                                   const unsigned short* __restrict__ wi, char* __restrict__ ws, const char* sw_) {
  if (blockIdx.x >= 256) { side_copy<4>(sw_, ws + (64u << 20), blockIdx.x - 256, 576, 1); return; }
  __shared__ __attribute__((aligned(16))) char lds[4 * 3072 + 9216];
  const int tid = threadIdx.x, l = tid & 63, wv = __builtin_amdgcn_readfirstlane(tid >> 6);
  const int a = blockIdx.x;
  const __amdgpu_buffer_rsrc_t rws = mk_rsrc(ws, WS_END);
  char* const outb = lds + 4 * 3072;
  if (wv < 4) {
    const int s2 = a & 31, o = a >> 5;
    const int sa = 128 * o + 32 * wv + s2;
    char* xb = lds + wv * 3072;
    {
      const char* xa = (const char*)x + sa * 1152;
      glds16(xa + l * 16, xb);
      { int sl = 64 + l; glds16(xa + (sl >= 72 ? 1024 * 1152 + (sl - 72) * 16 : sl * 16), xb + 1024); }
      { int sl = 56 + (l & 15); glds16(xa + 1024 * 1152 + sl * 16, xb + 2048); }
    }
    const int r = l & 15, q = l >> 4;
    u32x4 t18c, t18s;
    {
      float c[6], s[6];
      #pragma unroll
      for (int j = 0; j < 6; ++j) { int d1 = j < 4 ? 4 * q + j : 12 + j; float rev = (float)(d1 * r) * (1.0f / 18.0f); c[j] = cosr(rev); s[j] = -sinr(rev); }
      t18c = (u32x4){pk2(c[0], c[1]), pk2(c[2], c[3]), q == 0 ? pk2(c[4], c[5]) : 0u, 0u};
      t18s = (u32x4){pk2(s[0], s[1]), pk2(s[2], s[3]), q == 0 ? pk2(s[4], s[5]) : 0u, 0u};
    }
    u32x4 c32[2], s32[2], n32[2];
    {
      float c[4], s[4];
      #pragma unroll
      for (int j = 0; j < 4; ++j) { float rev = (float)((4 * q + j) * r) * (1.0f / 32.0f); c[j] = cosr(rev); s[j] = sinr(rev); }
      const unsigned sg = (r & 1) ? 0x80008000u : 0u;
      unsigned c0 = pk2(c[0], c[1]), c1 = pk2(c[2], c[3]), s0 = pk2(s[0], s[1]), s1 = pk2(s[2], s[3]);
      c32[0] = (u32x4){c0, c1, c0 ^ sg, c1 ^ sg}; s32[0] = (u32x4){s0, s1, s0 ^ sg, s1 ^ sg};
      c32[1] = c32[0] ^ 0x80000000u; s32[1] = s32[0] ^ 0x80000000u;
      n32[0] = s32[0] ^ 0x80008000u; n32[1] = s32[1] ^ 0x80008000u;
    }
    float twc[2][4], tws[2][4];
    #pragma unroll
    for (int t = 0; t < 2; ++t)
      #pragma unroll
      for (int j = 0; j < 4; ++j) { float rev = (float)((16 * t + 4 * q + j) * r) * (1.0f / 576.0f); twc[t][j] = cosr(rev); tws[t][j] = sinr(rev); }
    const unsigned ra0 = (4 * q + (r >> 2)) * 64 + (r & 3) * 8, ra1 = (16 + ((r >> 2) & 1)) * 64 + (r & 3) * 8;
    asm volatile("s_waitcnt vmcnt(0)" ::: "memory");
    f32x4 zr[2][2], zi[2][2]; float rs[2];
    #pragma unroll
    for (int k = 0; k < 2; ++k) {
      u32x4 xf[2]; f32x4 yr[2], yi[2];
      float s1 = 0.f, sq = 0.f;
      #pragma unroll
      for (int t = 0; t < 2; ++t) {
        u32x2 lo = tr_read(xb, k * 1152 + t * 32 + ra0), hi = tr_read(xb, k * 1152 + t * 32 + ra1);
        xf[t] = (u32x4){lo[0], lo[1], hi[0], hi[1]};
        yr[t] = mfma16(xf[t], t18c, (f32x4){0, 0, 0, 0});
        yi[t] = mfma16(xf[t], t18s, (f32x4){0, 0, 0, 0});
        const unsigned e = q == 0 ? hi[0] : 0u;
        const bf16x2 one = __builtin_bit_cast(bf16x2, 0x3f803f80u);
        bf16x2 v0 = asb2(lo[0]), v1 = asb2(lo[1]), v2 = asb2(e);
        s1 = __builtin_amdgcn_fdot2_f32_bf16(v0, one, s1, false); sq = __builtin_amdgcn_fdot2_f32_bf16(v0, v0, sq, false);
        s1 = __builtin_amdgcn_fdot2_f32_bf16(v1, one, s1, false); sq = __builtin_amdgcn_fdot2_f32_bf16(v1, v1, sq, false);
        s1 = __builtin_amdgcn_fdot2_f32_bf16(v2, one, s1, false); sq = __builtin_amdgcn_fdot2_f32_bf16(v2, v2, sq, false);
      }
      s1 = wave_sum(s1) * (1.0f / 576.0f); sq = wave_sum(sq) * (1.0f / 576.0f);
      rs[k] = inv_std(fmaxf(sq - s1 * s1, 0.f));
      u32x4 fr, fi;
      #pragma unroll
      for (int t = 0; t < 2; ++t) {
        float pr[4], pi[4];
        #pragma unroll
        for (int j = 0; j < 4; ++j) { pr[j] = yr[t][j] * twc[t][j] + yi[t][j] * tws[t][j]; pi[j] = yi[t][j] * twc[t][j] - yr[t][j] * tws[t][j]; }
        fr[2 * t] = pk2(pr[0], pr[1]); fr[2 * t + 1] = pk2(pr[2], pr[3]); fi[2 * t] = pk2(pi[0], pi[1]); fi[2 * t + 1] = pk2(pi[2], pi[3]);
      }
      #pragma unroll
      for (int t = 0; t < 2; ++t) {
        zr[k][t] = mfma16(s32[t], fi, mfma16(c32[t], fr, (f32x4){0, 0, 0, 0}));
        zi[k][t] = mfma16(n32[t], fr, mfma16(c32[t], fi, (f32x4){0, 0, 0, 0}));
      }
    }
    const int gam = r == 9 ? 0 : r;
    const unsigned swz = (gam + 4 * (q & 1)) & 7;
    #pragma unroll
    for (int t = 0; t < 2; ++t) {
      const bool ok = (r >= 1 && r <= 8) || (r == (t ? 0 : 9));
      if (ok) {
        #pragma unroll
        for (int j = 0; j < 4; ++j) {
          const float ar = zr[0][t][j] * rs[0], ai = zi[0][t][j] * rs[0];
          const unsigned up = pk2(ar + zr[1][t][j] * rs[1], ai + zi[1][t][j] * rs[1]), um = pk2(ar - zr[1][t][j] * rs[1], ai - zi[1][t][j] * rs[1]);
          const unsigned ub = (gam * 32 + 16 * t + 4 * q) * 2;
          *(unsigned*)(outb + (ub + ((2 * j) ^ swz)) * 16 + wv * 4) = up;
          *(unsigned*)(outb + (ub + ((2 * j + 1) ^ swz)) * 16 + wv * 4) = um;
        }
      }
    }
  } else {
    const int idx = a >> 2, q = a & 3, kap = idx & 31, k1 = 2 * kap + (idx >> 5);
    const unsigned obase = WT_OFF + idx * 256 + (q ^ (2 * ((kap >> 2) & 1))) * 64;
    #pragma unroll
    for (int rd = 0; rd < 2; ++rd) {
      const int n = (tid - 256) + 320 * rd;
      if (n < 576) {
        const int u = n & 3, pe = n >> 2, kp = pe / 9, gam = pe - 9 * kp, cc = u >> 1, t = u & 1;
        const int k2c = 2 * kp + cc;
        const int kf = (gam == 0 ? (k2c < 16 ? 9 : 0) : gam) + 18 * k2c;
        const int kd = k2c < 16 ? kf : 576 - kf;
        const unsigned sg = k2c < 16 ? 0u : 0x80000000u;
        u32x4 v;
        #pragma unroll
        for (int j = 0; j < 4; ++j) {
          const int ks = k1 + 64 * (16 * t + 4 * q + j);
          unsigned w = ((unsigned)wr[ks * DF_ + kd] | ((unsigned)wi[ks * DF_ + kd] << 16)) ^ sg;
          if (kd == 288) w = pk2(0.5f * bflo(w), 0.5f * bfhi(w));
          v[j] = w;
        }
        st16_wt(v, rws, obase + (gam * 16 + kp) * 16384 + (u ^ (kap & 3)) * 16);
      }
    }
  }
  __syncthreads();
  {
    const int s2 = a & 31, o = a >> 5;
    const int U = (tid & ~3) | ((tid & 3) ^ (s2 & 3));
    const unsigned swz = ((U >> 6) + 4 * ((U >> 3) & 1)) & 7;
    const u32x4 v = *(const u32x4*)(outb + (U ^ swz) * 16);
    st16_wt(v, rws, Z1_OFF + (tid >> 2) * 16384 + s2 * 512 + (o ^ (2 * ((s2 >> 2) & 1))) * 64 + (tid & 3) * 16);
  }
}

__global__ __launch_bounds__(512) void k_fft_cols(char* __restrict__ ws, const char* sw_) {
  if (blockIdx.x >= 144) { side_copy<6>(sw_, ws + (72u << 20), blockIdx.x - 144, 512, 1); return; }
  __shared__ __attribute__((aligned(16))) char lds[3 * 16384];
  const int tid = threadIdx.x, l = tid & 63, wv = __builtin_amdgcn_readfirstlane(tid >> 6);
  const int p = blockIdx.x;
  char* const zb = lds; char* const wb = lds + 16384; char* const rb = lds + 32768;
  glds16(ws + Z1_OFF + p * 16384 + tid * 16, zb + wv * 1024);
  glds16(ws + Z1_OFF + p * 16384 + 8192 + tid * 16, zb + 8192 + wv * 1024);
  glds16(ws + WT_OFF + p * 16384 + tid * 16, wb + wv * 1024);
  glds16(ws + WT_OFF + p * 16384 + 8192 + tid * 16, wb + 8192 + wv * 1024);
  const int r = l & 15, q = l >> 4;
  const int cc = wv >> 2, par = (wv >> 1) & 1, h = wv & 1;
  const float sg = (p & 8) ? -1.0f : 1.0f;
  const int k1 = 2 * (16 * h + r) + par;
  u32x4 t1r[2], t1i[2];
  #pragma unroll
  for (int ks = 0; ks < 2; ++ks)
    #pragma unroll
    for (int j = 0; j < 4; ++j) {
      const float rev = (float)((16 * ks + 4 * q + j) * k1) * (sg / 64.0f); const float c = cosr(rev), s = sinr(rev);
      t1r[ks][j] = pk2(c, s); t1i[ks][j] = pk2(-s, c);
    }
  u32x4 c32[2], s32[2], n32[2];
  {
    float c[4], s[4];
    #pragma unroll
    for (int j = 0; j < 4; ++j) { float rev = (float)((4 * q + j) * r) * (sg / 32.0f); c[j] = cosr(rev); s[j] = sinr(rev); }
    const unsigned m = (r & 1) ? 0x80008000u : 0u;
    unsigned c0 = pk2(c[0], c[1]), c1 = pk2(c[2], c[3]), s0 = pk2(s[0], s[1]), s1 = pk2(s[2], s[3]);
    c32[0] = (u32x4){c0, c1, c0 ^ m, c1 ^ m}; s32[0] = (u32x4){s0, s1, s0 ^ m, s1 ^ m};
    c32[1] = c32[0] ^ 0x80000000u; s32[1] = s32[0] ^ 0x80000000u;
    n32[0] = s32[0] ^ 0x80008000u; n32[1] = s32[1] ^ 0x80008000u;
  }
  float fc[2][4], fs[2][4], ic[2][4], is[2][4];
  #pragma unroll
  for (int t = 0; t < 2; ++t)
    #pragma unroll
    for (int j = 0; j < 4; ++j) {
      float rev = (float)((16 * t + 4 * q + j) * k1) * (sg / 2048.0f); fc[t][j] = cosr(rev); fs[t][j] = sinr(rev);
      rev = (float)((16 * t + r) * (2 * (16 * h + 4 * q + j) + par)) * (sg / 2048.0f); ic[t][j] = cosr(rev); is[t][j] = sinr(rev);
    }
  u32x4 t4r[2][2], t4i[2][2];
  {
    const int s1 = 16 * (wv & 1) + r; const unsigned m = (r & 1) ? 0x80008000u : 0u;
    #pragma unroll
    for (int pa = 0; pa < 2; ++pa) {
      #pragma unroll
      for (int j = 0; j < 4; ++j) {
        const float rev = (float)((2 * (4 * q + j) + pa) * s1) * (sg / 64.0f); const float c = cosr(rev), s = sinr(rev);
        t4r[pa][0][j] = pk2(c, -s); t4i[pa][0][j] = pk2(s, c);
      }
      t4r[pa][1] = t4r[pa][0] ^ m; t4i[pa][1] = t4i[pa][0] ^ m;
    }
  }
  const unsigned sw = ((q ^ (2 * ((r >> 2) & 1))) * 4) * 16;
  asm volatile("s_waitcnt vmcnt(0)" ::: "memory");
  __syncthreads();
  u32x4 fr, fi;
  #pragma unroll
  for (int t = 0; t < 2; ++t) {
    const char* zp = zb + (16 * t + r) * 512 + sw + (((cc * 2 + par) ^ (r & 3)) * 16);
    const u32x4 x0 = *(const u32x4*)zp, x1 = *(const u32x4*)(zp + 256);
    const f32x4 yr = mfma16(x1, t1r[1], mfma16(x0, t1r[0], (f32x4){0, 0, 0, 0}));
    const f32x4 yi = mfma16(x1, t1i[1], mfma16(x0, t1i[0], (f32x4){0, 0, 0, 0}));
    float pr[4], pi[4];
    #pragma unroll
    for (int j = 0; j < 4; ++j) { pr[j] = yr[j] * fc[t][j] + yi[j] * fs[t][j]; pi[j] = yi[j] * fc[t][j] - yr[j] * fs[t][j]; }
    fr[2 * t] = pk2(pr[0], pr[1]); fr[2 * t + 1] = pk2(pr[2], pr[3]); fi[2 * t] = pk2(pi[0], pi[1]); fi[2 * t + 1] = pk2(pi[2], pi[3]);
  }
  u32x4 vr, vi;
  #pragma unroll
  for (int t = 0; t < 2; ++t) {
    const u32x4 w = *(const u32x4*)(wb + (par * 32 + 16 * h + r) * 256 + sw + (((cc * 2 + t) ^ (r & 3)) * 16));
    const f32x4 zr = mfma16(s32[t], fi, mfma16(c32[t], fr, (f32x4){0, 0, 0, 0}));
    const f32x4 zi = mfma16(n32[t], fr, mfma16(c32[t], fi, (f32x4){0, 0, 0, 0}));
    float pr[4], pi[4];
    #pragma unroll
    for (int j = 0; j < 4; ++j) { const float a = bflo(w[j]), b = bfhi(w[j]); pr[j] = zr[j] * a - zi[j] * b; pi[j] = zr[j] * b + zi[j] * a; }
    vr[2 * t] = pk2(pr[0], pr[1]); vr[2 * t + 1] = pk2(pr[2], pr[3]); vi[2 * t] = pk2(pi[0], pi[1]); vi[2 * t + 1] = pk2(pi[2], pi[3]);
  }
  #pragma unroll
  for (int t = 0; t < 2; ++t) {
    const f32x4 rr = mfma16(vi, n32[t], mfma16(vr, c32[t], (f32x4){0, 0, 0, 0}));
    const f32x4 ri = mfma16(vi, c32[t], mfma16(vr, s32[t], (f32x4){0, 0, 0, 0}));
    u32x4 o;
    #pragma unroll
    for (int j = 0; j < 4; ++j) o[j] = pk2(rr[j] * ic[t][j] - ri[j] * is[t][j], rr[j] * is[t][j] + ri[j] * ic[t][j]);
    *(u32x4*)(rb + cc * 8192 + par * 4096 + (16 * t + r) * 128 + (((4 * h + q) ^ (r & 7)) * 16)) = o;
  }
  __syncthreads();
  {
    const int e = wv >> 1, g = wv & 1;
    const char* rp = rb + (r >> 3) * 8192 + (8 * e + (r & 7)) * 128;
    f32x4 er[2], ei[2];
    #pragma unroll
    for (int pa = 0; pa < 2; ++pa) {
      const u32x4 x0 = *(const u32x4*)(rp + pa * 4096 + ((q ^ (r & 7)) * 16)), x1 = *(const u32x4*)(rp + pa * 4096 + (((4 + q) ^ (r & 7)) * 16));
      er[pa] = mfma16(x1, t4r[pa][1], mfma16(x0, t4r[pa][0], (f32x4){0, 0, 0, 0}));
      ei[pa] = mfma16(x1, t4i[pa][1], mfma16(x0, t4i[pa][0], (f32x4){0, 0, 0, 0}));
    }
    const __amdgpu_buffer_rsrc_t rws = mk_rsrc(ws, WS_END);
    const unsigned ob = Z2_OFF + (4 * (16 * g + r) + e) * 9216 + p * 64 + q * 16;
    const f32x4 ar = er[0] + er[1], ai = ei[0] + ei[1], br = er[0] - er[1], bi = ei[0] - ei[1];
    st16_wt((u32x4){pk2(ar[0], ar[1]), pk2(ar[2], ar[3]), pk2(ai[0], ai[1]), pk2(ai[2], ai[3])}, rws, ob);
    st16_wt((u32x4){pk2(br[0], br[1]), pk2(br[2], br[3]), pk2(bi[0], bi[1]), pk2(bi[2], bi[3])}, rws, ob + 128 * 9216);
  }
}

__global__ __launch_bounds__(576) void k_ifft_rows(const unsigned short* __restrict__ x, char* __restrict__ ws, const char* sw_) {
  if (blockIdx.x >= 256) { side_copy<4>(sw_, ws + (80u << 20), blockIdx.x - 256, 576, 1); return; }
  constexpr int ZB = 9 * 1032 + 8, YB = 9312, YROW = 1168, SB = YB + 8 * YROW;
  __shared__ __attribute__((aligned(16))) char lds[SB + 9 * 64];
  static_assert(ZB <= YB, "");
  const int tid = threadIdx.x, l = tid & 63, wv = __builtin_amdgcn_readfirstlane(tid >> 6);
  const int c = blockIdx.x;
  glds16(ws + Z2_OFF + c * 9216 + tid * 16, lds + wv * 1032);
  const int ti = l & 7, n = tid >> 3;
  u32x4 xv;
  asm volatile("global_load_dwordx4 %0, %1, %2" : "=v"(xv) : "v"((8 * c + ti) * 1152 + n * 16), "s"(x) : "memory");
  if (tid == 0) *(u32x2*)(lds + 9288) = (u32x2){0u, 0u};
  const int r = l & 15, q = l >> 4, tau = wv >> 1, del = wv & 1;
  u32x4 c32, s32, n32, tlo, thi; float twc[3], tws[3]; unsigned ad[3][2];
  if (wv < 4) {
    const int d2 = 16 * del + r;
    float cv[4], sv[4];
    #pragma unroll
    for (int j = 0; j < 4; ++j) { float rev = (float)((4 * q + j) * d2) * (1.0f / 32.0f); cv[j] = cosr(rev); sv[j] = sinr(rev); }
    const unsigned m = (r & 1) ? 0x80008000u : 0u;
    unsigned c0 = pk2(cv[0], cv[1]), c1 = pk2(cv[2], cv[3]), s0 = pk2(sv[0], sv[1]), s1 = pk2(sv[2], sv[3]);
    c32 = (u32x4){c0, c1, c0 ^ m, c1 ^ m}; s32 = (u32x4){s0, s1, s0 ^ m, s1 ^ m}; n32 = s32 ^ 0x80008000u;
    #pragma unroll
    for (int mm = 0; mm < 3; ++mm) {
      const int k1 = 4 * mm + q;
      float rev = (float)(k1 * d2) * (1.0f / 576.0f); twc[mm] = cosr(rev) * (2.0f / (2048.0f * 576.0f)); tws[mm] = sinr(rev) * (2.0f / (2048.0f * 576.0f));
      rev = (float)(k1 * r) * (1.0f / 18.0f); tlo[mm] = pk2(cosr(rev), -sinr(rev));
      rev = (float)(k1 * (16 + (r & 1))) * (1.0f / 18.0f); thi[mm] = pk2(cosr(rev), -sinr(rev));
      const int kr = 4 * mm + (r & 3);
      const unsigned base = (4 * q + (r >> 2)) * 32 + tau * 16;
      ad[mm][0] = (kr == 0 || kr > 9) ? 9288u : (kr == 9 ? 0 : kr) * 1032 + base;
      ad[mm][1] = (kr >= 9) ? 9288u : kr * 1032 + base + 512;
    }
    tlo[3] = 0; thi[3] = 0;
  }
  asm volatile("s_waitcnt vmcnt(0)" : "+v"(xv) :: "memory");
  __syncthreads();
  if (wv < 4) {
    u32x4 fa[4];
    #pragma unroll
    for (int mm = 0; mm < 3; ++mm) {
      const unsigned i0 = ad[mm][0] == 9288u ? 0u : 8u, i1 = ad[mm][1] == 9288u ? 0u : 8u;
      const u32x2 r0 = tr_read(lds, ad[mm][0]), r1 = tr_read(lds, ad[mm][1]), m0 = tr_read(lds, ad[mm][0] + i0), m1 = tr_read(lds, ad[mm][1] + i1);
      const u32x4 zr = {r0[0], r0[1], r1[0], r1[1]}, zi = {m0[0], m0[1], m1[0], m1[1]};
      const f32x4 rr = mfma16(zi, n32, mfma16(zr, c32, (f32x4){0, 0, 0, 0}));
      const f32x4 ri = mfma16(zi, c32, mfma16(zr, s32, (f32x4){0, 0, 0, 0}));
      #pragma unroll
      for (int j = 0; j < 4; ++j) fa[j][mm] = pk2(rr[j] * twc[mm] - ri[j] * tws[mm], rr[j] * tws[mm] + ri[j] * twc[mm]);
    }
    f32x4 yh = {0, 0, 0, 0};
    #pragma unroll
    for (int j = 0; j < 4; ++j) {
      fa[j][3] = 0;
      const f32x4 y = mfma16(fa[j], tlo, (f32x4){0, 0, 0, 0});
      const u32x4 th = (r >> 1) == j ? thi : (u32x4){0, 0, 0, 0};
      yh = mfma16(fa[j], th, yh);
      *(u32x2*)(lds + YB + (4 * tau + j) * YROW + r * 64 + del * 32 + q * 8) = (u32x2){pk2(y[0], y[1]), pk2(y[2], y[3])};
    }
    if (r < 8) *(u32x2*)(lds + YB + (4 * tau + (r >> 1)) * YROW + (16 + (r & 1)) * 64 + del * 32 + q * 8) = (u32x2){pk2(yh[0], yh[1]), pk2(yh[2], yh[3])};
  }
  __syncthreads();
  const u32x4 yv = *(const u32x4*)(lds + YB + ti * YROW + n * 16);
  u32x4 hv; float hf[8]; float s1 = 0.f, sq = 0.f;
  #pragma unroll
  for (int j = 0; j < 4; ++j) {
    hv[j] = pk2(bflo(xv[j]) + bflo(yv[j]), bfhi(xv[j]) + bfhi(yv[j]));
    hf[2 * j] = bflo(hv[j]); hf[2 * j + 1] = bfhi(hv[j]);
    const bf16x2 v = asb2(hv[j]);
    s1 = __builtin_amdgcn_fdot2_f32_bf16(v, __builtin_bit_cast(bf16x2, 0x3f803f80u), s1, false);
    sq = __builtin_amdgcn_fdot2_f32_bf16(v, v, sq, false);
  }
  s1 = swap32_add(swap16_add(dpp_add<0x128>(s1)));
  sq = swap32_add(swap16_add(dpp_add<0x128>(sq)));
  if (l < 8) *(f32x2*)(lds + SB + wv * 64 + l * 8) = (f32x2){s1, sq};
  const __amdgpu_buffer_rsrc_t rws = mk_rsrc(ws, WS_END);
  const unsigned ob = (c >> 1) * 18432 + n * 256 + (8 * (c & 1) + ti) * 16;
  st16_wt(hv, rws, H_OFF + ob);
  asm volatile("s_waitcnt lgkmcnt(0)" ::: "memory");
  __builtin_amdgcn_s_barrier();
  asm volatile("" ::: "memory");
  f32x2 st = {0.f, 0.f};
  #pragma unroll
  for (int w = 0; w < 9; ++w) st += *(const f32x2*)(lds + SB + w * 64 + ti * 8);
  const float mu = st[0] * (1.0f / 576.0f);
  const float rs = inv_std(fmaxf(st[1] * (1.0f / 576.0f) - mu * mu, 0.f)), mb = rbf(mu);
  u32x4 o;
  #pragma unroll
  for (int j = 0; j < 4; ++j) { const unsigned d = pk2(hf[2 * j] - mb, hf[2 * j + 1] - mb); o[j] = pk2(bflo(d) * rs, bfhi(d) * rs); }
  st16_wt(o, rws, LN_OFF + ob);
}
__global__ __launch_bounds__(256) void k_g1_simple(const unsigned short* __restrict__ wg, const unsigned short* __restrict__ wu, char* __restrict__ ws) {
  __shared__ __attribute__((aligned(16))) char lds[2][8192];
  const int tid = threadIdx.x, l = tid & 63, wv = __builtin_amdgcn_readfirstlane(tid >> 6);
  const int ft = blockIdx.x % 72, mt = blockIdx.x / 72;
  const int r = l & 15, q = l >> 4, th = wv >> 1, fh = wv & 1;
  f32x4 acc[4][4] = {};
  auto stage = [&](int ks, int b) {
    const int row = ks * 32 + (tid >> 3);
    glds16((const char*)wg + row * 9216 + ft * 128 + (tid & 7) * 16, lds[b] + wv * 1024);
    glds16((const char*)wu + row * 9216 + ft * 128 + (tid & 7) * 16, lds[b] + 4096 + wv * 1024);
  };
  stage(0, 0);
  for (int ks = 0; ks < 18; ++ks) {
    u32x4 bfr[4];
    #pragma unroll
    for (int t = 0; t < 4; ++t) bfr[t] = *(const u32x4*)(ws + LN_OFF + (mt * 8 + th * 4 + t) * 18432 + ks * 1024 + l * 16);
    asm volatile("s_waitcnt vmcnt(0)" ::: "memory");
    __syncthreads();
    if (ks + 1 < 18) stage(ks + 1, (ks + 1) & 1);
    #pragma unroll
    for (int i = 0; i < 4; ++i) {
      const unsigned ad = (i >> 1) * 4096 + (8 * q + (r >> 2)) * 128 + fh * 64 + (r & 3) * 16 + (i & 1) * 8;
      const u32x2 lo = tr_read(lds[ks & 1], ad), hi = tr_read(lds[ks & 1], ad + 512);
      const u32x4 afr = {lo[0], lo[1], hi[0], hi[1]};
      #pragma unroll
      for (int t = 0; t < 4; ++t) acc[i][t] = mfma16(afr, bfr[t], acc[i][t]);
    }
  }
  #pragma unroll
  for (int t = 0; t < 4; ++t) {
    float a[8];
    #pragma unroll
    for (int j = 0; j < 8; ++j) {
      const float g = acc[j >> 2][t][j & 3], u = acc[2 + (j >> 2)][t][j & 3];
      const float z = g * (-2.302208198f - 0.1029432397f * g * g);
      a[j] = g * u * __builtin_amdgcn_rcpf(1.0f + __builtin_amdgcn_exp2f(z));
    }
    *(u32x4*)(ws + ACT_OFF + (mt * 8 + th * 4 + t) * 147456 + (ft * 2 + fh) * 1024 + l * 16) = (u32x4){pk2(a[0], a[1]), pk2(a[2], a[3]), pk2(a[4], a[5]), pk2(a[6], a[7])};
  }
}
__global__ __launch_bounds__(256) void k_g2_simple(const unsigned short* __restrict__ wd, char* __restrict__ ws, unsigned short* __restrict__ out) {
  __shared__ __attribute__((aligned(16))) char lds[4][8192];
  const int tid = threadIdx.x, l = tid & 63, wv = __builtin_amdgcn_readfirstlane(tid >> 6);
  const int nt = blockIdx.x % 9, mt = blockIdx.x / 9;
  const int r = l & 15, q = l >> 4;
  f32x4 acc[4][2] = {};
  auto stage = [&](int ks, int b) {
    #pragma unroll
    for (int i = 0; i < 4; ++i) glds16((const char*)wd + (ks * 32 + i * 8 + (l >> 3)) * 1152 + nt * 128 + (l & 7) * 16, lds[wv] + b * 4096 + i * 1024);
  };
  stage(wv, 0);
  for (int it = 0; it < 36; ++it) {
    const int ks = it * 4 + wv;
    u32x4 bfr[2];
    #pragma unroll
    for (int t = 0; t < 2; ++t) bfr[t] = *(const u32x4*)(ws + ACT_OFF + (mt * 2 + t) * 147456 + ks * 1024 + l * 16);
    asm volatile("s_waitcnt vmcnt(0)" ::: "memory");
    if (it + 1 < 36) stage(ks + 4, (it + 1) & 1);
    #pragma unroll
    for (int i = 0; i < 4; ++i) {
      const unsigned ad = (it & 1) * 4096 + (8 * q + (r >> 2)) * 128 + (i >> 1) * 64 + (r & 3) * 16 + (i & 1) * 8;
      const u32x2 lo = tr_read(lds[wv], ad), hi = tr_read(lds[wv], ad + 512);
      const u32x4 afr = {lo[0], lo[1], hi[0], hi[1]};
      #pragma unroll
      for (int t = 0; t < 2; ++t) acc[i][t] = mfma16(afr, bfr[t], acc[i][t]);
    }
    asm volatile("s_waitcnt lgkmcnt(0)" ::: "memory");
  }
  __syncthreads();
  #pragma unroll
  for (int i = 0; i < 4; ++i)
    #pragma unroll
    for (int t = 0; t < 2; ++t) *(f32x4*)(lds[wv] + (i * 2 + t) * 1024 + l * 16) = acc[i][t];
  __syncthreads();
  {
    const int hh = wv >> 1, t = wv & 1;
    f32x4 s0 = {0, 0, 0, 0}, s1 = {0, 0, 0, 0};
    #pragma unroll
    for (int w = 0; w < 4; ++w) { s0 += *(const f32x4*)(lds[w] + ((2 * hh) * 2 + t) * 1024 + l * 16); s1 += *(const f32x4*)(lds[w] + ((2 * hh + 1) * 2 + t) * 1024 + l * 16); }
    const int unit = nt * 8 + hh * 4 + q;
    const u32x4 hv = *(const u32x4*)(ws + H_OFF + (mt * 2 + t) * 18432 + unit * 256 + r * 16);
    const unsigned p0 = pk2(s0[0], s0[1]), p1 = pk2(s0[2], s0[3]), p2 = pk2(s1[0], s1[1]), p3 = pk2(s1[2], s1[3]);
    const u32x4 o = {pk2(bflo(hv[0]) + bflo(p0), bfhi(hv[0]) + bfhi(p0)), pk2(bflo(hv[1]) + bflo(p1), bfhi(hv[1]) + bfhi(p1)),
                     pk2(bflo(hv[2]) + bflo(p2), bfhi(hv[2]) + bfhi(p2)), pk2(bflo(hv[3]) + bflo(p3), bfhi(hv[3]) + bfhi(p3))};
    *(u32x4*)((char*)out + ((mt * 2 + t) * 16 + r) * 1152 + unit * 16) = o;
  }
}
extern "C" void kernel_launch(void* const* d_in, const int* in_sizes, int n_in, void* d_out, int out_size, void* d_ws, size_t ws_size, hipStream_t stream) {
  const unsigned short* x = (const unsigned short*)d_in[0];
  const unsigned short* wr = (const unsigned short*)d_in[1];
  const unsigned short* wi = (const unsigned short*)d_in[2];
  const unsigned short* wg = (const unsigned short*)d_in[4];
  const unsigned short* wu = (const unsigned short*)d_in[5];
  const unsigned short* wd = (const unsigned short*)d_in[6];
  char* ws = (char*)d_ws;
  k_fft_rows<<<256 + 144, 576, 0, stream>>>(x, wr, wi, ws, (const char*)wg);
  k_fft_cols<<<144 + 108, 512, 0, stream>>>(ws, (const char*)wu);
  k_ifft_rows<<<256 + 144, 576, 0, stream>>>(x, ws, (const char*)wd);
  k_g1_simple<<<16 * 72, 256, 0, stream>>>(wg, wu, ws);
  k_g2_simple<<<64 * 9, 256, 0, stream>>>(wd, ws, (unsigned short*)d_out);
}
```

```cpp
#include <hip/hip_runtime.h>

extern "C" __global__ __attribute__((aligned(4096))) __launch_bounds__(256, 1) void k_fused(const void* x, const void* wg, const void* wu, const void* wd, void* out, char* ws, const void* wr, const void* wi) {
  __shared__ __attribute__((aligned(16))) char lds[163840];
  asm volatile("" :: "s"((unsigned)(size_t)lds));
  unsigned tid = threadIdx.x;
  asm volatile(
  "s_memrealtime s[62:63]\n"
  "s_load_dwordx8 s[12:19], %1, 0x0\n"
  "s_load_dwordx4 s[20:23], %1, 0x20\n"
  "s_load_dwordx4 s[40:43], %1, 0x30\n"
  "s_getpc_b64 s[72:73]\n"
  "s_mov_b32 s74, 3968\n"
  "s_mov_b32 s75, 0x20000\n"
  "v_lshlrev_b32 v251, 7, v0\n"
  "buffer_load_dword v251, v251, s[72:75], 0 offen\n"
  "s_mov_b32 s26, 7159808\n"
  "s_mov_b32 s27, 0x20000\n"
  "s_mov_b32 s46, 2359296\n"
  "s_mov_b32 s47, 0x20000\n"
  "s_mov_b32 s38, 29917312\n"
  "s_mov_b32 s39, 0x20000\n"
  "v_lshrrev_b32 v1, 6, v0\n"
  "s_and_b32 s34, %2, 31\n"
  "s_lshr_b32 s35, %2, 5\n"
  "v_readfirstlane_b32 s60, v1\n"
  "s_lshl_b32 s61, s35, 2\n"
  "s_add_u32 s61, s61, s60\n"
  "s_lshl_b32 s61, s61, 5\n"
  "s_add_u32 s61, s61, s34\n"
  "s_and_b32 s84, %2, 3\n"
  "s_bfe_u32 s28, %2, 0x60002\n"
  "s_and_b32 s29, s28, 31\n"
  "s_lshl_b32 s30, s29, 1\n"
  "s_lshr_b32 s31, s28, 5\n"
  "s_add_u32 s30, s30, s31\n"
  "s_lshl_b32 s48, s30, 1\n"
  "s_and_b32 s48, s48, 15\n"
  "s_and_b32 s49, s60, 1\n"
  "s_lshl_b32 s49, s49, 2\n"
  "s_add_u32 s49, s49, s84\n"
  "s_lshl_b32 s49, s49, 8\n"
  "v_and_b32 v1, 15, v0\n"
  "v_bfe_u32 v2, v0, 4, 2\n"
  "s_add_u32 s49, s49, s30\n"
  "v_cvt_f32_ubyte0 v3, v1\n"
  "v_lshlrev_b32 v248, 2, v2\n"
  "s_mul_i32 s49, s49, 578\n"
  "v_cvt_f32_ubyte0 v249, v248\n"
  "v_mul_f32 v250, 0x3d638e39, v3\n"
  "s_sub_u32 s49, s49, s48\n"
  "v_and_b32 v4, 63, v0\n"
  "v_mul_f32 v5, v249, v250\n"
  "v_mul_f32 v6, 0x41800000, v250\n"
  "v_mul_f32 v7, 0x41880000, v250\n"
  "s_add_u32 s31, s49, 36352\n"
  "v_cmp_lt_u32 vcc, 7, v4\n"
  "v_add_f32 v8, v5, v250\n"
  "v_fma_f32 v9, v250, 2.0, v5\n"
  "v_fmamk_f32 v10, v250, 0x40400000, v5\n"
  "v_cos_f32 v11, v6\n"
  "v_sin_f32 v12, v6\n"
  "v_cos_f32 v13, v7\n"
  "v_sin_f32 v14, v7\n"
  "s_add_u32 s30, s49, 72704\n"
  "v_cos_f32 v15, v5\n"
  "v_sin_f32 v248, v5\n"
  "v_cos_f32 v16, v8\n"
  "v_sin_f32 v17, v8\n"
  "v_cos_f32 v18, v9\n"
  "v_sin_f32 v19, v9\n"
  "v_cos_f32 v20, v10\n"
  "v_sin_f32 v21, v10\n"
  "v_cmp_eq_u32 s[50:51], 0, v2\n"
  "s_add_u32 s52, s49, 109056\n"
  "s_mul_i32 s53, s60, 2560\n"
  "v_mov_b32 v22, 1178496\n"
  "s_add_u32 s53, s53, 29360\n"
  "v_lshlrev_b32 v6, 4, v4\n"
  "v_cndmask_b32 v7, 0, v22, vcc\n"
  "v_lshlrev_b32 v23, 4, v1\n"
  "v_cvt_pk_bf16_f32 v26, v11, v13\n"
  "v_cvt_pk_bf16_f32 v30, -v12, -v14\n"
  "v_lshrrev_b32 v5, 3, v0\n"
  "s_bfe_u32 s54, %2, 0x50003\n"
  "s_mov_b32 s58, 1183744\n"
  "s_mov_b32 s59, 0x20000\n"
  "s_mul_i32 s61, s61, 1152\n"
  "s_mul_i32 s55, s60, 3072\n"
  "v_add_u32 v250, v7, v6\n"
  "v_add_u32 v23, 1178496, v23\n"
  "v_cvt_pk_bf16_f32 v24, v15, v16\n"
  "v_cvt_pk_bf16_f32 v28, -v248, -v17\n"
  "v_cvt_pk_bf16_f32 v25, v18, v20\n"
  "v_cvt_pk_bf16_f32 v29, -v19, -v21\n"
  "v_cndmask_b32 v26, 0, v26, s[50:51]\n"
  "v_cndmask_b32 v30, 0, v30, s[50:51]\n"
  "v_mov_b32 v27, 0\n"
  "v_mov_b32 v31, 0\n"
  "s_mov_b32 s66, 88264\n"
  "s_mov_b32 s67, 0x20000\n"
  "v_lshlrev_b32 v22, 7, v5\n"
  "s_lshl_b32 s54, s54, 12\n"
  "s_mov_b32 s68, 29917184\n"
  "s_getpc_b64 s[64:65]\n"
  "s_and_b32 s64, s64, 0xfffff000\n"
  "s_mov_b32 m0, s55\n"
  "s_waitcnt lgkmcnt(0)\n"
  "s_mov_b64 s[44:45], s[12:13]\n"
  "s_mov_b64 s[36:37], s[22:23]\n"
  "s_add_u32 s24, s22, 33554432\n"
  "s_addc_u32 s25, s23, 0\n"
  "buffer_load_dwordx4 v6, s[44:47], s61 offen offset:0 lds\n"
  "buffer_load_dwordx4 v250, s[44:47], s61 offen offset:1024 lds\n"
  "buffer_load_dwordx4 v23, s[44:47], s61 offen offset:2048 lds\n"
  "s_mov_b32 m0, s53\n"
  "s_cmp_lt_u32 s60, 2\n"
  "s_cselect_b64 s[56:57], s[40:41], s[42:43]\n"
  "s_mov_b32 exec_hi, 0xff\n"
  "buffer_load_dwordx4 v6, s[56:59], s49 offen offset:0 lds\n"
  "buffer_load_dwordx4 v6, s[56:59], s31 offen offset:640 lds\n"
  "buffer_load_dwordx4 v6, s[56:59], s30 offen offset:1280 lds\n"
  "buffer_load_dwordx4 v6, s[56:59], s52 offen offset:1920 lds\n"
  "s_mov_b32 exec_hi, -1\n"
  "buffer_load_dword v213, off, s[36:39], s68 sc1\n"
  "buffer_load_dword v5, v22, s[64:67], s54 offen\n"
  "s_lshl_b32 s69, s60, 2\n"
  "s_add_u32 s69, s69, 12288\n"
  "s_and_b32 s70, s34, 3\n"
  "s_mul_i32 s71, s60, 1552\n"
  "s_add_u32 s71, s71, 12288\n"
  "s_lshr_b32 s53, s34, 1\n"
  "s_and_b32 s53, s53, 2\n"
  "s_xor_b32 s53, s53, s35\n"
  "s_lshl_b32 s53, s53, 6\n"
  "s_lshl_b32 s49, s34, 9\n"
  "s_add_u32 s53, s53, s49\n"
  "s_add_u32 s30, s53, 1048576\n"
  "s_add_u32 s31, s53, 2097152\n"
  "s_lshr_b32 s52, s29, 1\n"
  "s_and_b32 s52, s52, 2\n"
  "s_xor_b32 s52, s52, s84\n"
  "s_lshl_b32 s52, s52, 6\n"
  "s_lshl_b32 s54, s28, 8\n"
  "s_add_u32 s52, s52, s54\n"
  "s_add_u32 s52, s52, 2359296\n"
  "v_lshrrev_b32 v6, 2, v0\n"
  "s_lshl2_add_u32 s61, %2, s60\n"
  "v_add_u32 v7, 64, v6\n"
  "s_lshl_b32 s61, s61, 6\n"
  "v_mul_u32_u24 v248, 7282, v7\n"
  "s_add_u32 s61, s61, 7077888\n"
  "s_mul_i32 s34, %2, 9216\n"
  "v_mul_f32 v250, 0x3d000000, v3\n"
  "v_mul_u32_u24 v8, 7282, v6\n"
  "v_lshrrev_b32 v9, 16, v248\n"
  "v_lshrrev_b32 v10, 2, v4\n"
  "s_add_u32 s34, s34, 4718592\n"
  "v_mul_f32 v11, v249, v250\n"
  "v_lshrrev_b32 v12, 16, v8\n"
  "v_mad_i32_i24 v13, v9, -9, v7\n"
  "v_add_u32 v14, 128, v10\n"
  "s_add_u32 s28, s34, 4096\n"
  "v_add_f32 v15, v11, v250\n"
  "v_fma_f32 v16, v250, 2.0, v11\n"
  "v_fmamk_f32 v17, v250, 0x40400000, v11\n"
  "v_bfe_u32 v18, v4, 1, 1\n"
  "v_mad_i32_i24 v19, v12, -9, v6\n"
  "v_cmp_eq_u32 s[56:57], 0, v13\n"
  "v_mul_u32_u24 v20, 7282, v14\n"
  "s_add_u32 s35, s34, 8192\n"
  "v_cos_f32 v21, v11\n"
  "v_sin_f32 v22, v11\n"
  "v_cos_f32 v23, v15\n"
  "v_sin_f32 v248, v15\n"
  "v_cos_f32 v8, v16\n"
  "v_sin_f32 v7, v16\n"
  "v_cos_f32 v10, v17\n"
  "v_sin_f32 v32, v17\n"
  "v_cmp_eq_u32 s[58:59], 0, v19\n"
  "v_lshl_add_u32 v33, v9, 1, v18\n"
  "v_lshrrev_b32 v34, 1, v0\n"
  "v_lshrrev_b32 v35, 16, v20\n"
  "s_lshl_b32 s49, %2, 6\n"
  "v_and_b32 v6, 1, v1\n"
  "v_mul_f32 v250, 0x3ae38e39, v3\n"
  "v_and_b32 v36, 7, v4\n"
  "v_lshrrev_b32 v37, 3, v4\n"
  "v_cmp_gt_u32 vcc, 32, v0\n"
  "v_and_b32 v11, 1, v4\n"
  "v_mul_u32_u24 v15, 18, v33\n"
  "v_cmp_gt_u32 s[64:65], 32, v0\n"
  "v_cmp_eq_u32 s[66:67], 16, v34\n"
  "v_lshl_add_u32 v16, v35, 1, v18\n"
  "s_add_u32 s49, s49, 7143424\n"
  "v_mul_u32_u24 v17, 0x8000, v6\n"
  "v_mul_f32 v38, v249, v250\n"
  "v_xor_b32 v39, s70, v36\n"
  "v_mul_u32_u24 v20, 192, v37\n"
  "v_xor_b32 v40, s29, v4\n"
  "v_mul_u32_u24 v41, 2560, v11\n"
  "v_lshl_add_u32 v3, v12, 1, v18\n"
  "v_add_u32 v42, v13, v15\n"
  "v_cndmask_b32 v43, v13, 9, s[56:57]\n"
  "v_mad_i32_i24 v44, v35, -9, v14\n"
  "v_mul_u32_u24 v45, 18, v16\n"
  "s_cmp_eq_u32 s60, 3\n"
  "v_lshl_or_b32 v33, v17, 16, v17\n"
  "v_cvt_pk_bf16_f32 v48, v21, v23\n"
  "v_cvt_pk_bf16_f32 v52, v22, v248\n"
  "v_cvt_pk_bf16_f32 v56, -v22, -v248\n"
  "v_cvt_pk_bf16_f32 v49, v8, v10\n"
  "v_cvt_pk_bf16_f32 v53, v7, v32\n"
  "v_cvt_pk_bf16_f32 v57, -v7, -v32\n"
  "v_add_f32 v6, v38, v250\n"
  "v_fma_f32 v34, v250, 2.0, v38\n"
  "v_fmamk_f32 v11, v250, 0x40400000, v38\n"
  "v_fmamk_f32 v18, v250, 0x41800000, v38\n"
  "v_fmamk_f32 v36, v250, 0x41880000, v38\n"
  "v_fmamk_f32 v37, v250, 0x41900000, v38\n"
  "v_fmamk_f32 v14, v250, 0x41980000, v38\n"
  "v_and_b32 v16, 3, v4\n"
  "v_min_u32 v46, 10, v1\n"
  "v_lshl_add_u32 v17, v39, 4, v20\n"
  "v_mov_b32 v47, 13968\n"
  "v_lshrrev_b32 v249, 2, v0\n"
  "v_and_b32 v248, 3, v40\n"
  "v_add_u32 v8, s48, v41\n"
  "v_mul_u32_u24 v10, 18, v3\n"
  "v_cndmask_b32 v7, v19, 9, s[58:59]\n"
  "v_sub_u32 v32, 576, v42\n"
  "v_add_u32 v21, v43, v15\n"
  "v_mov_b32 v22, 0x80000000\n"
  "v_add_u32 v23, v44, v45\n"
  "s_cselect_b32 s61, s49, s61\n"
  "v_xor_b32 v50, v33, v48\n"
  "v_xor_b32 v54, v33, v52\n"
  "v_xor_b32 v58, v33, v56\n"
  "v_xor_b32 v51, v33, v49\n"
  "v_xor_b32 v55, v33, v53\n"
  "v_xor_b32 v59, v33, v57\n"
  "v_cos_f32 v60, v38\n"
  "v_sin_f32 v64, v38\n"
  "v_cos_f32 v61, v6\n"
  "v_sin_f32 v65, v6\n"
  "v_cos_f32 v62, v34\n"
  "v_sin_f32 v66, v34\n"
  "v_cos_f32 v63, v11\n"
  "v_sin_f32 v67, v11\n"
  "v_cos_f32 v40, v18\n"
  "v_sin_f32 v68, v18\n"
  "v_cos_f32 v41, v36\n"
  "v_sin_f32 v69, v36\n"
  "v_cos_f32 v42, v37\n"
  "v_sin_f32 v70, v37\n"
  "v_cos_f32 v43, v14\n"
  "v_sin_f32 v71, v14\n"
  "v_lshl_add_u32 v3, v16, 3, s55\n"
  "v_lshrrev_b32 v15, 2, v4\n"
  "v_bfe_u32 v20, v4, 2, 1\n"
  "v_mul_u32_u24 v6, 1552, v46\n"
  "v_mul_u32_u24 v33, 192, v2\n"
  "v_add_u32 v34, s71, v17\n"
  "v_cndmask_b32 v45, 0, v47, vcc\n"
  "v_lshlrev_b32 v11, 14, v249\n"
  "v_and_b32 v250, 3, v4\n"
  "v_lshlrev_b32 v18, 4, v248\n"
  "v_add_u32 v38, 29360, v8\n"
  "v_lshl_add_u32 v36, v19, 4, v12\n"
  "v_add_u32 v39, v19, v10\n"
  "v_add_u32 v37, v7, v10\n"
  "v_lshl_add_u32 v72, v13, 4, v9\n"
  "v_cndmask_b32 v14, v32, v21, s[64:65]\n"
  "v_cndmask_b32 v16, v22, 0, s[64:65]\n"
  "v_cndmask_b32 v73, 1.0, 0.5, s[66:67]\n"
  "v_lshl_add_u32 v74, v44, 4, v35\n"
  "v_sub_u32 v46, 576, v23\n"
  "s_lshl_b32 s29, %2, 6\n"
  "v_xor_b32 v76, 0x80000000, v48\n"
  "v_xor_b32 v77, 0x80000000, v49\n"
  "v_xor_b32 v78, 0x80000000, v50\n"
  "v_xor_b32 v79, 0x80000000, v51\n"
  "v_xor_b32 v80, 0x80000000, v52\n"
  "v_xor_b32 v81, 0x80000000, v53\n"
  "v_xor_b32 v82, 0x80000000, v54\n"
  "v_xor_b32 v83, 0x80000000, v55\n"
  "v_xor_b32 v84, 0x80000000, v56\n"
  "v_xor_b32 v85, 0x80000000, v57\n"
  "v_xor_b32 v86, 0x80000000, v58\n"
  "v_xor_b32 v87, 0x80000000, v59\n"
  "v_lshl_add_u32 v2, v15, 6, v3\n"
  "v_lshl_add_u32 v7, v20, 6, v3\n"
  "v_add3_u32 v8, v6, v33, s69\n"
  "v_add_u32 v9, v45, v34\n"
  "v_lshl_add_u32 v10, v250, 4, v11\n"
  "v_mov_b32 v12, 0\n"
  "v_mov_b32 v13, 0\n"
  "v_mov_b32 v17, 0\n"
  "v_mov_b32 v19, 0\n"
  "v_mov_b32 v32, 0x3f803f80\n"
  "v_lshl_add_u32 v35, v36, 14, v18\n"
  "v_sub_u32 v44, 576, v39\n"
  "v_lshl_add_u32 v47, v37, 1, v38\n"
  "v_lshl_add_u32 v75, v72, 14, v18\n"
  "v_xor_b32 v248, v16, v73\n"
  "v_lshl_add_u32 v249, v14, 1, v38\n"
  "v_lshl_add_u32 v15, v74, 14, v18\n"
  "v_lshl_add_u32 v3, v46, 1, v38\n"
  "v_mov_b32 v6, 0\n"
  "v_lshlrev_b32 v33, 2, v1\n"
  "v_lshlrev_b32 v11, 4, v0\n"
  "v_lshlrev_b32 v45, 2, v0\n"
  "v_mov_b32 v20, -1\n"
  "v_mov_b32 v21, -1\n"
  "v_mov_b32 v22, -1\n"
  "v_mov_b32 v23, -1\n"
  "s_add_u32 s29, s29, 29884416\n"
  "v_lshlrev_b32 v44, 2, v4\n"
  "s_waitcnt vmcnt(6)\n"
  "ds_read_b64_tr_b16 v[88:89], v2 offset:0\n"
  "ds_read_b64_tr_b16 v[90:91], v7 offset:1024\n"
  "ds_read_b64_tr_b16 v[92:93], v2 offset:32\n"
  "ds_read_b64_tr_b16 v[94:95], v7 offset:1056\n"
  "ds_read_b64_tr_b16 v[36:37], v2 offset:1152\n"
  "ds_read_b64_tr_b16 v[38:39], v7 offset:2176\n"
  "ds_read_b64_tr_b16 v[96:97], v2 offset:1184\n"
  "ds_read_b64_tr_b16 v[98:99], v7 offset:2208\n"
  "s_waitcnt lgkmcnt(7)\n"
  "v_dot2c_f32_bf16 v12, v32, v88\n"
  "v_dot2c_f32_bf16 v17, v88, v88\n"
  "v_dot2c_f32_bf16 v17, v89, v89\n"
  "v_dot2c_f32_bf16 v12, v32, v89\n"
  "s_waitcnt lgkmcnt(6)\n"
  "v_cndmask_b32 v1, 0, v90, s[50:51]\n"
  "v_dot2c_f32_bf16 v12, v32, v1\n"
  "v_dot2c_f32_bf16 v17, v1, v1\n"
  "v_mfma_f32_16x16x32_bf16 v[100:103], v[88:91], v[28:31], 0\n"
  "s_waitcnt lgkmcnt(4)\n"
  "v_cndmask_b32 v4, 0, v94, s[50:51]\n"
  "v_mfma_f32_16x16x32_bf16 v[104:107], v[92:95], v[24:27], 0\n"
  "s_waitcnt lgkmcnt(2)\n"
  "v_cndmask_b32 v14, 0, v38, s[50:51]\n"
  "v_mfma_f32_16x16x32_bf16 v[108:111], v[36:39], v[24:27], 0\n"
  "s_waitcnt lgkmcnt(0)\n"
  "v_cndmask_b32 v16, 0, v98, s[50:51]\n"
  "v_mfma_f32_16x16x32_bf16 v[112:115], v[36:39], v[28:31], 0\n"
  "v_mfma_f32_16x16x32_bf16 v[116:119], v[96:99], v[24:27], 0\n"
  "v_mfma_f32_16x16x32_bf16 v[120:123], v[96:99], v[28:31], 0\n"
  "v_mfma_f32_16x16x32_bf16 v[124:127], v[92:95], v[28:31], 0\n"
  "v_mfma_f32_16x16x32_bf16 v[128:131], v[88:91], v[24:27], 0\n"
  "s_nop 0\n"
  "v_pk_mul_f32 v[134:135], v[110:111], v[62:63]\n"
  "v_pk_mul_f32 v[138:139], v[106:107], v[42:43]\n"
  "v_dot2c_f32_bf16 v12, v32, v92\n"
  "v_pk_mul_f32 v[142:143], v[114:115], v[62:63]\n"
  "v_pk_fma_f32 v[134:135], v[114:115], v[66:67], v[134:135]\n"
  "v_pk_fma_f32 v[142:143], v[110:111], v[66:67], v[142:143] neg_lo:[1,0,0] neg_hi:[1,0,0]\n"
  "v_pk_fma_f32 v[138:139], v[126:127], v[70:71], v[138:139]\n"
  "v_pk_mul_f32 v[146:147], v[126:127], v[42:43]\n"
  "v_pk_fma_f32 v[146:147], v[106:107], v[70:71], v[146:147] neg_lo:[1,0,0] neg_hi:[1,0,0]\n"
  "v_dot2c_f32_bf16 v12, v32, v93\n"
  "v_pk_mul_f32 v[132:133], v[108:109], v[60:61]\n"
  "v_pk_fma_f32 v[132:133], v[112:113], v[64:65], v[132:133]\n"
  "v_pk_mul_f32 v[144:145], v[124:125], v[40:41]\n"
  "v_pk_fma_f32 v[144:145], v[104:105], v[68:69], v[144:145] neg_lo:[1,0,0] neg_hi:[1,0,0]\n"
  "v_dot2c_f32_bf16 v19, v36, v36\n"
  "v_pk_mul_f32 v[140:141], v[112:113], v[60:61]\n"
  "v_pk_fma_f32 v[140:141], v[108:109], v[64:65], v[140:141] neg_lo:[1,0,0] neg_hi:[1,0,0]\n"
  "v_dot2c_f32_bf16 v19, v37, v37\n"
  "v_dot2c_f32_bf16 v19, v14, v14\n"
  "v_dot2c_f32_bf16 v19, v96, v96\n"
  "v_dot2c_f32_bf16 v17, v92, v92\n"
  "v_dot2c_f32_bf16 v13, v32, v36\n"
  "v_dot2c_f32_bf16 v13, v32, v37\n"
  "v_dot2c_f32_bf16 v13, v32, v14\n"
  "v_pk_mul_f32 v[24:25], v[116:117], v[40:41]\n"
  "v_pk_fma_f32 v[24:25], v[120:121], v[68:69], v[24:25]\n"
  "v_pk_mul_f32 v[28:29], v[120:121], v[40:41]\n"
  "v_pk_fma_f32 v[28:29], v[116:117], v[68:69], v[28:29] neg_lo:[1,0,0] neg_hi:[1,0,0]\n"
  "v_dot2c_f32_bf16 v12, v32, v4\n"
  "v_pk_mul_f32 v[26:27], v[118:119], v[42:43]\n"
  "v_pk_fma_f32 v[26:27], v[122:123], v[70:71], v[26:27]\n"
  "v_pk_mul_f32 v[30:31], v[122:123], v[42:43]\n"
  "v_cvt_pk_bf16_f32 v88, v140, v141\n"
  "v_dot2c_f32_bf16 v17, v93, v93\n"
  "v_dot2c_f32_bf16 v17, v4, v4\n"
  "v_pk_mul_f32 v[136:137], v[104:105], v[40:41]\n"
  "v_pk_fma_f32 v[136:137], v[124:125], v[68:69], v[136:137]\n"
  "v_pk_fma_f32 v[30:31], v[118:119], v[70:71], v[30:31] neg_lo:[1,0,0] neg_hi:[1,0,0]\n"
  "v_cvt_pk_bf16_f32 v36, v132, v133\n"
  "v_dot2c_f32_bf16 v13, v32, v96\n"
  "v_dot2c_f32_bf16 v19, v97, v97\n"
  "v_dot2c_f32_bf16 v19, v16, v16\n"
  "v_pk_mul_f32 v[110:111], v[130:131], v[62:63]\n"
  "v_dot2c_f32_bf16 v13, v32, v97\n"
  "v_pk_fma_f32 v[110:111], v[102:103], v[66:67], v[110:111]\n"
  "v_permlane32_swap_b32 v17, v19\n"
  "v_add_f32 v1, v17, v19\n"
  "v_cvt_pk_bf16_f32 v113, v110, v111\n"
  "v_pk_mul_f32 v[122:123], v[102:103], v[62:63]\n"
  "v_pk_fma_f32 v[122:123], v[130:131], v[66:67], v[122:123] neg_lo:[1,0,0] neg_hi:[1,0,0]\n"
  "v_cvt_pk_bf16_f32 v93, v122, v123\n"
  "v_dot2c_f32_bf16 v13, v32, v16\n"
  "v_cvt_pk_bf16_f32 v94, v144, v145\n"
  "v_cvt_pk_bf16_f32 v95, v146, v147\n"
  "v_pk_mul_f32 v[120:121], v[100:101], v[60:61]\n"
  "v_pk_fma_f32 v[120:121], v[128:129], v[64:65], v[120:121] neg_lo:[1,0,0] neg_hi:[1,0,0]\n"
  "v_cvt_pk_bf16_f32 v92, v120, v121\n"
  "v_pk_mul_f32 v[108:109], v[128:129], v[60:61]\n"
  "v_pk_fma_f32 v[108:109], v[100:101], v[64:65], v[108:109]\n"
  "v_cvt_pk_bf16_f32 v112, v108, v109\n"
  "v_mfma_f32_16x16x32_bf16 v[40:43], v[48:51], v[92:95], 0\n"
  "v_cvt_pk_bf16_f32 v114, v136, v137\n"
  "v_cvt_pk_bf16_f32 v115, v138, v139\n"
  "v_mfma_f32_16x16x32_bf16 v[68:71], v[76:79], v[92:95], 0\n"
  "v_permlane32_swap_b32 v12, v13\n"
  "v_add_f32 v2, v12, v13\n"
  "v_mfma_f32_16x16x32_bf16 v[40:43], v[56:59], v[112:115], v[40:43]\n"
  "v_cvt_pk_bf16_f32 v89, v142, v143\n"
  "v_permlane16_swap_b32 v2, v1\n"
  "v_mfma_f32_16x16x32_bf16 v[16:19], v[76:79], v[112:115], 0\n"
  "v_add_f32 v4, v2, v1\n"
  "v_cvt_pk_bf16_f32 v37, v134, v135\n"
  "v_mfma_f32_16x16x32_bf16 v[16:19], v[80:83], v[92:95], v[16:19]\n"
  "v_add_f32_dpp v7, v4, v4 row_ror:8 row_mask:0xf bank_mask:0xf\n"
  "v_cvt_pk_bf16_f32 v91, v30, v31\n"
  "v_mfma_f32_16x16x32_bf16 v[60:63], v[48:51], v[112:115], 0\n"
  "v_add_f32_dpp v14, v7, v7 row_ror:4 row_mask:0xf bank_mask:0xf\n"
  "v_cvt_pk_bf16_f32 v90, v28, v29\n"
  "v_mfma_f32_16x16x32_bf16 v[60:63], v[52:55], v[92:95], v[60:63]\n"
  "v_add_f32_dpp v32, v14, v14 row_ror:2 row_mask:0xf bank_mask:0xf\n"
  "v_cvt_pk_bf16_f32 v38, v24, v25\n"
  "v_mfma_f32_16x16x32_bf16 v[68:71], v[84:87], v[112:115], v[68:71]\n"
  "v_add_f32_dpp v12, v32, v32 row_ror:1 row_mask:0xf bank_mask:0xf\n"
  "v_mul_f32 v13, 0x3ae38e39, v12\n"
  "v_mfma_f32_16x16x32_bf16 v[64:67], v[48:51], v[88:91], 0\n"
  "v_mul_f32 v46, 0x3ae38e39, v12\n"
  "v_cvt_pk_bf16_f32 v39, v26, v27\n"
  "v_mfma_f32_16x16x32_bf16 v[96:99], v[76:79], v[88:91], 0\n"
  "v_permlane16_swap_b32 v46, v13\n"
  "v_mfma_f32_16x16x32_bf16 v[64:67], v[56:59], v[36:39], v[64:67]\n"
  "v_fma_f32 v1, -v46, v46, v13\n"
  "v_max_f32 v2, 0, v1\n"
  "v_mfma_f32_16x16x32_bf16 v[100:103], v[76:79], v[36:39], 0\n"
  "v_cvt_pk_bf16_f32 v4, v2, v2\n"
  "v_lshlrev_b32 v7, 16, v4\n"
  "v_mfma_f32_16x16x32_bf16 v[28:31], v[48:51], v[36:39], 0\n"
  "v_add_f32 v14, 0x3727c5ac, v7\n"
  "v_cvt_pk_bf16_f32 v72, v14, v14\n"
  "v_mfma_f32_16x16x32_bf16 v[100:103], v[80:83], v[88:91], v[100:103]\n"
  "v_lshlrev_b32 v32, 16, v72\n"
  "v_sqrt_f32 v74, v32\n"
  "v_mfma_f32_16x16x32_bf16 v[28:31], v[52:55], v[88:91], v[28:31]\n"
  "v_cvt_pk_bf16_f32 v12, v74, v74\n"
  "v_lshlrev_b32 v250, 16, v12\n"
  "v_mfma_f32_16x16x32_bf16 v[96:99], v[84:87], v[36:39], v[96:99]\n"
  "v_rcp_f32 v24, v250\n"
  "s_nop 0\n"
  "v_readlane_b32 s55, v24, 32\n"
  "v_readlane_b32 s54, v24, 0\n"
  "s_nop 1\n"
  "v_pk_mul_f32 v[56:57], v[16:17], s[54:55] op_sel_hi:[1,0]\n"
  "v_pk_mul_f32 v[76:77], v[60:61], s[54:55] op_sel_hi:[1,0]\n"
  "v_pk_fma_f32 v[92:93], v[100:101], s[54:55], v[56:57] op_sel:[0,1,0] op_sel_hi:[1,1,1]\n"
  "v_pk_mul_f32 v[48:49], v[40:41], s[54:55] op_sel_hi:[1,0]\n"
  "v_pk_fma_f32 v[104:105], v[64:65], s[54:55], v[48:49] op_sel:[0,1,0] op_sel_hi:[1,1,1]\n"
  "v_pk_fma_f32 v[48:49], v[64:65], s[54:55], v[48:49] op_sel:[0,1,0] op_sel_hi:[1,1,1] neg_lo:[1,0,0] neg_hi:[1,0,0]\n"
  "v_pk_mul_f32 v[50:51], v[42:43], s[54:55] op_sel_hi:[1,0]\n"
  "v_pk_fma_f32 v[106:107], v[66:67], s[54:55], v[50:51] op_sel:[0,1,0] op_sel_hi:[1,1,1]\n"
  "v_pk_fma_f32 v[50:51], v[66:67], s[54:55], v[50:51] op_sel:[0,1,0] op_sel_hi:[1,1,1] neg_lo:[1,0,0] neg_hi:[1,0,0]\n"
  "v_pk_mul_f32 v[78:79], v[62:63], s[54:55] op_sel_hi:[1,0]\n"
  "v_pk_fma_f32 v[52:53], v[28:29], s[54:55], v[76:77] op_sel:[0,1,0] op_sel_hi:[1,1,1]\n"
  "v_cvt_pk_bf16_f32 v1, v52, v104\n"
  "v_cvt_pk_bf16_f32 v2, v53, v105\n"
  "v_pk_fma_f32 v[76:77], v[28:29], s[54:55], v[76:77] op_sel:[0,1,0] op_sel_hi:[1,1,1] neg_lo:[1,0,0] neg_hi:[1,0,0]\n"
  "v_cvt_pk_bf16_f32 v4, v76, v48\n"
  "v_cvt_pk_bf16_f32 v7, v77, v49\n"
  "v_pk_fma_f32 v[54:55], v[30:31], s[54:55], v[78:79] op_sel:[0,1,0] op_sel_hi:[1,1,1]\n"
  "v_pk_fma_f32 v[78:79], v[30:31], s[54:55], v[78:79] op_sel:[0,1,0] op_sel_hi:[1,1,1] neg_lo:[1,0,0] neg_hi:[1,0,0]\n"
  "v_pk_mul_f32 v[24:25], v[68:69], s[54:55] op_sel_hi:[1,0]\n"
  "v_pk_fma_f32 v[56:57], v[100:101], s[54:55], v[56:57] op_sel:[0,1,0] op_sel_hi:[1,1,1] neg_lo:[1,0,0] neg_hi:[1,0,0]\n"
  "v_cvt_pk_bf16_f32 v12, v54, v106\n"
  "v_pk_mul_f32 v[26:27], v[70:71], s[54:55] op_sel_hi:[1,0]\n"
  "v_cvt_pk_bf16_f32 v13, v78, v50\n"
  "v_pk_fma_f32 v[38:39], v[98:99], s[54:55], v[26:27] op_sel:[0,1,0] op_sel_hi:[1,1,1]\n"
  "v_pk_fma_f32 v[26:27], v[98:99], s[54:55], v[26:27] op_sel:[0,1,0] op_sel_hi:[1,1,1] neg_lo:[1,0,0] neg_hi:[1,0,0]\n"
  "v_pk_mul_f32 v[58:59], v[18:19], s[54:55] op_sel_hi:[1,0]\n"
  "v_pk_fma_f32 v[94:95], v[102:103], s[54:55], v[58:59] op_sel:[0,1,0] op_sel_hi:[1,1,1]\n"
  "v_cvt_pk_bf16_f32 v14, v95, v39\n"
  "v_pk_fma_f32 v[58:59], v[102:103], s[54:55], v[58:59] op_sel:[0,1,0] op_sel_hi:[1,1,1] neg_lo:[1,0,0] neg_hi:[1,0,0]\n"
  "v_cvt_pk_bf16_f32 v32, v59, v27\n"
  "v_cvt_pk_bf16_f32 v46, v58, v26\n"
  "v_cvt_pk_bf16_f32 v72, v79, v51\n"
  "v_cvt_pk_bf16_f32 v74, v55, v107\n"
  "v_pk_fma_f32 v[36:37], v[96:97], s[54:55], v[24:25] op_sel:[0,1,0] op_sel_hi:[1,1,1]\n"
  "v_pk_fma_f32 v[24:25], v[96:97], s[54:55], v[24:25] op_sel:[0,1,0] op_sel_hi:[1,1,1] neg_lo:[1,0,0] neg_hi:[1,0,0]\n"
  "v_cvt_pk_bf16_f32 v250, v92, v36\n"
  "v_cvt_pk_bf16_f32 v28, v94, v38\n"
  "v_cvt_pk_bf16_f32 v29, v56, v24\n"
  "v_cvt_pk_bf16_f32 v30, v93, v37\n"
  "v_cvt_pk_bf16_f32 v31, v57, v25\n"
  "ds_write2_b32 v8, v1, v4 offset0:0 offset1:4\n"
  "ds_write2_b32 v8, v2, v7 offset0:8 offset1:12\n"
  "ds_write2_b32 v8, v12, v13 offset0:16 offset1:20\n"
  "ds_write2_b32 v8, v74, v72 offset0:24 offset1:28\n"
  "ds_write2_b32 v8, v250, v29 offset0:192 offset1:196\n"
  "ds_write2_b32 v8, v30, v31 offset0:200 offset1:204\n"
  "ds_write2_b32 v8, v28, v46 offset0:208 offset1:212\n"
  "ds_write2_b32 v8, v14, v32 offset0:216 offset1:220\n"
  "s_waitcnt vmcnt(2) lgkmcnt(0)\n"
  "buffer_store_dwordx4 v[20:23], v11, s[24:27], s34 offen sc1\n"
  "buffer_store_dwordx4 v[20:23], v11, s[24:27], s28 offen sc1\n"
  "buffer_store_dword v20, v45, s[24:27], s35 offen sc1\n"
  "buffer_store_dword v6, v33, s[24:27], s61 offen sc1\n"
  "s_barrier\n"
  "ds_read_b128 v[16:19], v9\n"
  "ds_read_b128 v[24:27], v34 offset:6208\n"
  "ds_read_b128 v[36:39], v34 offset:12416\n"
  "ds_read_u16 v40, v47 offset:0\n"
  "ds_read_u16 v48, v47 offset:5120\n"
  "ds_read_u16 v41, v47 offset:640\n"
  "ds_read_u16 v49, v47 offset:5760\n"
  "ds_read_u16 v42, v47 offset:1280\n"
  "ds_read_u16 v50, v47 offset:6400\n"
  "ds_read_u16 v43, v47 offset:1920\n"
  "ds_read_u16 v51, v47 offset:7040\n"
  "s_waitcnt lgkmcnt(10)\n"
  "buffer_store_dwordx4 v[16:19], v10, s[24:27], s53 offen sc1\n"
  "s_waitcnt lgkmcnt(9)\n"
  "buffer_store_dwordx4 v[24:27], v10, s[24:27], s30 offen sc1\n"
  "ds_read_u16 v20, v249 offset:0\n"
  "ds_read_u16 v28, v249 offset:5120\n"
  "ds_read_u16 v21, v249 offset:640\n"
  "ds_read_u16 v29, v249 offset:5760\n"
  "ds_read_u16 v22, v249 offset:1280\n"
  "ds_read_u16 v30, v249 offset:6400\n"
  "ds_read_u16 v23, v249 offset:1920\n"
  "ds_read_u16 v31, v249 offset:7040\n"
  "s_cmp_lg_u32 s60, 0\n"
  "s_waitcnt lgkmcnt(8)\n"
  "s_cbranch_scc1 La_two%=\n"
  "buffer_store_dwordx4 v[36:39], v10, s[24:27], s31 offen sc1\n"
  "La_two%=:\n"
  "v_lshl_or_b32 v52, v48, 16, v40\n"
  "v_lshl_or_b32 v53, v49, 16, v41\n"
  "v_lshl_or_b32 v54, v50, 16, v42\n"
  "v_lshl_or_b32 v55, v51, 16, v43\n"
  "s_waitcnt lgkmcnt(7)\n"
  "v_lshlrev_b32 v1, 16, v20\n"
  "buffer_store_dwordx4 v[52:55], v35, s[24:27], s52 offen sc1\n"
  "s_waitcnt lgkmcnt(6)\n"
  "v_lshlrev_b32 v2, 16, v28\n"
  "v_mul_f32 v4, v73, v1\n"
  "v_mul_f32 v6, v248, v2\n"
  "s_waitcnt lgkmcnt(5)\n"
  "v_lshlrev_b32 v7, 16, v21\n"
  "v_cvt_pk_bf16_f32 v16, v4, v6\n"
  "s_waitcnt lgkmcnt(4)\n"
  "v_lshlrev_b32 v12, 16, v29\n"
  "v_mul_f32 v13, v73, v7\n"
  "v_mul_f32 v14, v248, v12\n"
  "s_waitcnt lgkmcnt(3)\n"
  "v_lshlrev_b32 v45, 16, v22\n"
  "v_cvt_pk_bf16_f32 v17, v13, v14\n"
  "s_waitcnt lgkmcnt(2)\n"
  "v_lshlrev_b32 v46, 16, v30\n"
  "v_mul_f32 v47, v73, v45\n"
  "v_mul_f32 v72, v248, v46\n"
  "s_waitcnt lgkmcnt(1)\n"
  "v_lshlrev_b32 v74, 16, v23\n"
  "v_cvt_pk_bf16_f32 v18, v47, v72\n"
  "s_waitcnt lgkmcnt(0)\n"
  "v_lshlrev_b32 v249, 16, v31\n"
  "v_mul_f32 v250, v73, v74\n"
  "v_mul_f32 v1, v248, v249\n"
  "v_cvt_pk_bf16_f32 v19, v250, v1\n"
  "buffer_store_dwordx4 v[16:19], v75, s[24:27], s52 offen sc1\n"
  "s_cmp_lg_u32 s60, 3\n"
  "s_cbranch_scc1 Lw_end%=\n"
  "ds_read_u16 v8, v3 offset:0\n"
  "ds_read_u16 v24, v3 offset:5120\n"
  "ds_read_u16 v9, v3 offset:640\n"
  "ds_read_u16 v25, v3 offset:5760\n"
  "ds_read_u16 v10, v3 offset:1280\n"
  "ds_read_u16 v26, v3 offset:6400\n"
  "ds_read_u16 v11, v3 offset:1920\n"
  "ds_read_u16 v27, v3 offset:7040\n"
  "s_waitcnt lgkmcnt(0)\n"
  "v_lshl_or_b32 v20, v24, 16, v8\n"
  "v_lshl_or_b32 v21, v25, 16, v9\n"
  "v_lshl_or_b32 v22, v26, 16, v10\n"
  "v_lshl_or_b32 v23, v27, 16, v11\n"
  "v_xor_b32 v20, 0x80000000, v20\n"
  "v_xor_b32 v21, 0x80000000, v21\n"
  "v_xor_b32 v22, 0x80000000, v22\n"
  "v_xor_b32 v23, 0x80000000, v23\n"
  "buffer_store_dwordx4 v[20:23], v15, s[24:27], s52 offen sc1\n"
  "Lw_end%=:\n"
  "v_lshrrev_b32 v251, 6, v0\n"
  "s_nop 0\n"
  "v_readfirstlane_b32 s10, v251\n"
  "s_getpc_b64 s[8:9]\n"
  "Lpc0%=:\n"
  "s_sub_u32 s8, s8, Lpc0%=+4096-Lstub%=\n"
  "s_subb_u32 s9, s9, 0\n"
  "s_add_u32 s11, s10, 1\n"
  "s_min_u32 s11, s11, 3\n"
  "s_lshl_b32 s11, s11, 12\n"
  "s_add_u32 s6, s8, s11\n"
  "s_addc_u32 s7, s9, 0\n"
  "s_mov_b64 exec, 0\n"
  "s_swappc_b64 s[8:9], s[6:7]\n"
  "s_mov_b64 exec, -1\n"
  "s_waitcnt vmcnt(0)\n"
  "v_add_u32 v213, 1, v213\n"
  "s_barrier\n"
  "s_cmp_lg_u32 s60, 0\n"
  "s_cbranch_scc1 La_pub%=\n"
  "s_mov_b32 exec_lo, 65535\n"
  "s_mov_b32 exec_hi, 0\n"
  "buffer_store_dword v213, v44, s[36:39], s29 offen sc1\n"
  "s_mov_b64 exec, -1\n"
  "La_pub%=:\n"
  "s_cmp_lt_u32 %2, 144\n"
  "s_cbranch_scc1 Lcol%=\n"
  "s_and_b32 s34, %2, 7\n"
  "s_lshr_b32 s35, s34, 1\n"
  "s_and_b32 s49, s34, 1\n"
  "s_mul_i32 s35, s35, 9\n"
  "s_lshl_b32 s49, s49, 2\n"
  "s_add_u32 s35, s35, s49\n"
  "s_lshl_b32 s35, s35, 8\n"
  "s_add_u32 s35, s35, 0\n"
  "s_lshr_b32 s50, %2, 3\n"
  "s_sub_u32 s50, s50, 18\n"
  "v_and_b32 v1, 63, v0\n"
  "v_bfe_u32 v2, v0, 6, 1\n"
  "s_mul_i32 s50, s50, 122\n"
  "v_min_u32 v3, 60, v1\n"
  "v_mul_u32_u24 v248, 61, v2\n"
  "v_add3_u32 v249, v3, v248, s50\n"
  "v_add_u32 v250, 1708, v249\n"
  "v_add_u32 v4, 3416, v249\n"
  "v_mul_u32_u24 v5, 52429, v249\n"
  "v_mul_u32_u24 v6, 52429, v250\n"
  "v_mul_u32_u24 v7, 52429, v4\n"
  "v_lshrrev_b32 v8, 7, v0\n"
  "v_lshrrev_b32 v9, 19, v5\n"
  "v_lshrrev_b32 v10, 19, v6\n"
  "v_lshrrev_b32 v11, 19, v7\n"
  "v_mad_i32_i24 v12, v9, -10, v249\n"
  "v_mul_u32_u24 v13, 9216, v9\n"
  "v_mad_i32_i24 v14, v10, -10, v250\n"
  "v_mul_u32_u24 v15, 9216, v10\n"
  "v_mad_i32_i24 v1, v11, -10, v4\n"
  "v_mul_u32_u24 v2, 9216, v11\n"
  "s_mov_b32 s42, 4718592\n"
  "s_mov_b32 s43, 0x20000\n"
  "v_readfirstlane_b32 s34, v8\n"
  "v_lshl_add_u32 v252, v12, 7, v13\n"
  "v_lshl_add_u32 v253, v14, 7, v15\n"
  "v_lshl_add_u32 v254, v1, 7, v2\n"
  "s_mov_b32 s61, s35\n"
  "s_waitcnt lgkmcnt(0)\n"
  "s_cmp_eq_u32 s34, 0\n"
  "s_cselect_b64 s[40:41], s[14:15], s[16:17]\n"
  "Lpfh0%=:\n"
  "Lstub%=:\n"
  "s_cbranch_execz Ltramp%=\n"
  "s_memrealtime s[6:7]\n"
  "s_waitcnt lgkmcnt(0)\n"
  "s_sub_u32 s6, s6, s62\n"
  "s_cmp_lt_u32 s6, 250\n"
  "s_cbranch_scc1 Lpfh0%=\n"
  "buffer_load_dword v252, v252, s[40:43], s61 offen\n"
  "s_bfe_u32 s66, %2, 0x20001\n"
  "s_and_b32 s49, %2, 1\n"
  "s_lshl_b32 s49, s49, 5\n"
  "s_lshr_b32 s50, %2, 3\n"
  "s_add_u32 s49, s49, s50\n"
  "s_mul_i32 s67, s49, 37\n"
  "s_lshr_b32 s67, s67, 8\n"
  "v_lshrrev_b32 v1, 6, v0\n"
  "s_mul_i32 s68, s67, 7\n"
  "s_sub_u32 s68, s49, s68\n"
  "s_mul_i32 s69, s66, 9\n"
  "v_readfirstlane_b32 s10, v1\n"
  "s_add_u32 s69, s69, s67\n"
  "s_and_b32 s34, s10, 1\n"
  "s_lshl1_add_u32 s35, s69, s34\n"
  "s_lshl_b32 s35, s35, 7\n"
  "s_sub_u32 s30, 5308416, s35\n"
  "s_cmp_lt_u32 s67, 9\n"
  "s_cselect_b32 s30, s30, 0\n"
  "s_lshl_b32 s48, s10, 12\n"
  "s_lshr_b32 s70, %2, 4\n"
  "v_and_b32 v2, 7, v0\n"
  "v_and_b32 v3, 16, v0\n"
  "s_lshl_b32 s70, s70, 6\n"
  "v_bfe_u32 v248, v0, 3, 3\n"
  "v_lshlrev_b32 v249, 4, v2\n"
  "v_lshlrev_b32 v250, 1, v3\n"
  "s_and_b32 s49, %2, 15\n"
  "v_mul_u32_u24 v1, 9216, v248\n"
  "v_xor_b32 v4, v249, v250\n"
  "v_and_b32 v5, 3, v2\n"
  "s_add_u32 s70, s70, s49\n"
  "v_add_u32 v214, v1, v4\n"
  "v_lshlrev_b32 v6, 5, v5\n"
  "v_lshrrev_b32 v7, 2, v2\n"
  "s_mul_i32 s70, s70, 2304\n"
  "s_lshl_b32 s71, s10, 5\n"
  "v_xor_b32 v8, 64, v214\n"
  "v_add_u32 v9, v6, v7\n"
  "s_add_u32 s71, s71, s70\n"
  "v_add_u32 v215, 72704, v8\n"
  "v_mul_u32_u24 v10, 1152, v9\n"
  "s_lshl_b32 s34, s10, 8\n"
  "s_mov_b32 s31, 0x20000\n"
  "s_mov_b32 s46, 2359296\n"
  "s_mov_b32 s47, 0x20000\n"
  "v_add_u32 v216, 145408, v214\n"
  "v_add_u32 v217, 145408, v215\n"
  "v_lshl_add_u32 v218, v248, 4, v10\n"
  "v_lshl_add_u32 v219, v248, 2, v10\n"
  "s_add_u32 s70, s70, s34\n"
  "s_waitcnt lgkmcnt(0)\n"
  "s_mov_b64 s[44:45], s[12:13]\n"
  "s_cmp_lt_u32 s10, 2\n"
  "s_cselect_b64 s[28:29], s[14:15], s[16:17]\n"
  "s_add_u32 s28, s28, s35\n"
  "s_addc_u32 s29, s29, 0\n"
  "buffer_load_dwordx4 v[204:207], v218, s[44:47], s70 offen\n"
  "buffer_load_dwordx4 v[208:211], v218, s[44:47], s70 offen offset:128\n"
  "buffer_load_dword v212, v219, s[44:47], s71 offen offset:1024\n"
  "s_branch Ljoin%=\n"
  "Ltramp%=:\n"
  "s_setpc_b64 s[8:9]\n"
  "Lcol%=:\n"
  "v_lshrrev_b32 v1, 6, v0\n"
  "v_and_b32 v2, 63, v0\n"
  "v_readfirstlane_b32 s34, v1\n"
  "s_lshl_b32 s35, s34, 12\n"
  "s_lshl_b32 s49, %2, 14\n"
  "s_add_u32 s49, s49, s35\n"
  "v_lshlrev_b32 v3, 4, v2\n"
  "s_add_u32 s50, s49, 2359296\n"
  "s_lshr_b32 s60, s34, 1\n"
  "s_and_b32 s61, s34, 1\n"
  "s_lshl_b32 s84, %2, 28\n"
  "s_and_b32 s84, s84, 0x80000000\n"
  "s_lshl_b32 s40, s61, 4\n"
  "s_xor_b32 s41, s84, 0x3c800000\n"
  "s_xor_b32 s42, s84, 0x3d000000\n"
  "s_lshl_b32 s43, s60, 13\n"
  "s_add_u32 s43, s43, 131072\n"
  "s_lshl_b32 s52, s40, 1\n"
  "s_add_u32 s52, s52, s60\n"
  "s_xor_b32 s53, s84, 0x3a800000\n"
  "v_and_b32 v1, 15, v0\n"
  "v_bfe_u32 v248, v0, 4, 2\n"
  "s_lshl_b32 s54, s61, 2\n"
  "v_lshlrev_b32 v2, 2, v248\n"
  "v_add_u32 v249, s40, v1\n"
  "v_cvt_f32_ubyte0 v250, v1\n"
  "s_lshl_b32 s34, s60, 12\n"
  "v_cvt_f32_ubyte0 v4, v2\n"
  "v_lshl_add_u32 v5, v249, 1, s60\n"
  "v_cvt_f32_ubyte0 v6, v249\n"
  "v_mul_f32 v7, s42, v250\n"
  "v_lshrrev_b32 v8, 1, v1\n"
  "s_add_u32 s34, s34, 114688\n"
  "v_lshrrev_b32 v9, 2, v1\n"
  "v_and_b32 v10, 1, v1\n"
  "v_cvt_f32_ubyte0 v11, v5\n"
  "v_mul_f32 v12, v4, v7\n"
  "v_and_b32 v13, 2, v8\n"
  "v_mul_f32 v14, s42, v6\n"
  "v_lshl_add_u32 v15, v9, 1, v10\n"
  "s_lshl_b32 s40, s60, 11\n"
  "v_mul_f32 v16, s41, v11\n"
  "v_add_f32 v17, v12, v7\n"
  "v_fma_f32 v2, v7, 2.0, v12\n"
  "v_fmamk_f32 v18, v7, 0x40400000, v12\n"
  "v_xor_b32 v19, v13, v248\n"
  "v_and_b32 v20, 3, v1\n"
  "v_mul_f32 v21, v4, v14\n"
  "v_bfe_u32 v22, v1, 1, 1\n"
  "v_xor_b32 v23, v248, v15\n"
  "s_add_u32 s40, s40, 114688\n"
  "v_mul_f32 v5, v4, v16\n"
  "v_cos_f32 v24, v12\n"
  "v_sin_f32 v8, v12\n"
  "v_cos_f32 v6, v17\n"
  "v_sin_f32 v10, v17\n"
  "v_cos_f32 v25, v2\n"
  "v_sin_f32 v11, v2\n"
  "v_cos_f32 v26, v18\n"
  "v_sin_f32 v27, v18\n"
  "v_lshl_add_u32 v7, v19, 2, v20\n"
  "v_cvt_f32_ubyte0 v13, s52\n"
  "v_add_f32 v28, 0x41800000, v250\n"
  "v_add_f32 v29, v21, v14\n"
  "v_fma_f32 v30, v14, 2.0, v21\n"
  "v_fmamk_f32 v31, v14, 0x40400000, v21\n"
  "v_fma_f32 v32, v14, 0.5, v21\n"
  "v_fmamk_f32 v33, v14, 0x3fc00000, v21\n"
  "v_fmamk_f32 v34, v14, 0x40200000, v21\n"
  "v_fmamk_f32 v12, v14, 0x40600000, v21\n"
  "v_add_u32 v35, s54, v248\n"
  "v_and_b32 v17, 7, v1\n"
  "v_xor_b32 v36, v23, v22\n"
  "s_mul_i32 s41, s60, 73728\n"
  "s_lshl_b32 s42, %2, 6\n"
  "v_add_f32 v2, v5, v16\n"
  "v_fma_f32 v18, v16, 2.0, v5\n"
  "v_fmamk_f32 v19, v16, 0x40400000, v5\n"
  "v_fmamk_f32 v37, v16, 0x41800000, v5\n"
  "v_fmamk_f32 v38, v16, 0x41880000, v5\n"
  "v_fmamk_f32 v39, v16, 0x41900000, v5\n"
  "v_fmamk_f32 v40, v16, 0x41980000, v5\n"
  "v_and_b32 v41, 1, v1\n"
  "v_xor_b32 v42, s60, v7\n"
  "v_fma_f32 v43, v13, 0.5, v4\n"
  "v_mul_f32 v14, s53, v250\n"
  "v_mul_f32 v248, s53, v28\n"
  "v_cos_f32 v44, v21\n"
  "v_sin_f32 v23, v21\n"
  "v_cos_f32 v45, v29\n"
  "v_sin_f32 v46, v29\n"
  "v_cos_f32 v47, v30\n"
  "v_sin_f32 v48, v30\n"
  "v_cos_f32 v49, v31\n"
  "v_sin_f32 v50, v31\n"
  "v_cos_f32 v51, v32\n"
  "v_sin_f32 v52, v32\n"
  "v_cos_f32 v16, v33\n"
  "v_sin_f32 v53, v33\n"
  "v_cos_f32 v54, v34\n"
  "v_sin_f32 v4, v34\n"
  "v_cos_f32 v13, v12\n"
  "v_sin_f32 v55, v12\n"
  "v_xor_b32 v250, v35, v17\n"
  "v_lshlrev_b32 v21, 4, v36\n"
  "s_add_u32 s41, s41, s42\n"
  "v_cos_f32 v28, v5\n"
  "v_sin_f32 v29, v5\n"
  "v_cos_f32 v30, v2\n"
  "v_sin_f32 v56, v2\n"
  "v_cos_f32 v31, v18\n"
  "v_sin_f32 v57, v18\n"
  "v_cos_f32 v32, v19\n"
  "v_sin_f32 v58, v19\n"
  "v_cos_f32 v33, v37\n"
  "v_sin_f32 v59, v37\n"
  "v_cos_f32 v34, v38\n"
  "v_sin_f32 v60, v38\n"
  "v_cos_f32 v12, v39\n"
  "v_sin_f32 v17, v39\n"
  "v_cos_f32 v36, v40\n"
  "v_sin_f32 v61, v40\n"
  "v_mul_u32_u24 v62, 0x8000, v41\n"
  "v_lshlrev_b32 v63, 4, v42\n"
  "v_lshlrev_b32 v64, 4, v7\n"
  "v_mul_f32 v65, v43, v14\n"
  "v_mul_f32 v66, v43, v248\n"
  "v_lshlrev_b32 v67, 4, v250\n"
  "v_lshl_add_u32 v68, v15, 7, v21\n"
  "s_add_u32 s41, s41, 4718592\n"
  "v_mul_f32 v69, 0x3d000000, v5\n"
  "v_mul_f32 v70, 0x3d000000, v2\n"
  "v_mul_f32 v71, 0x3d000000, v18\n"
  "v_mul_f32 v72, 0x3d000000, v19\n"
  "v_mul_f32 v73, 0x3d000000, v37\n"
  "v_mul_f32 v74, 0x3d000000, v38\n"
  "v_mul_f32 v75, 0x3d000000, v39\n"
  "v_mul_f32 v76, 0x3d000000, v40\n"
  "v_lshl_or_b32 v77, v62, 16, v62\n"
  "v_cvt_pk_bf16_f32 v80, v24, v6\n"
  "v_cvt_pk_bf16_f32 v84, v8, v10\n"
  "v_cvt_pk_bf16_f32 v88, -v8, -v10\n"
  "v_cvt_pk_bf16_f32 v81, v25, v26\n"
  "v_cvt_pk_bf16_f32 v85, v11, v27\n"
  "v_cvt_pk_bf16_f32 v89, -v11, -v27\n"
  "v_lshl_add_u32 v7, v1, 9, v63\n"
  "v_lshl_add_u32 v5, v249, 8, v64\n"
  "v_add_f32 v2, v65, v14\n"
  "v_fma_f32 v15, v14, 2.0, v65\n"
  "v_fmamk_f32 v18, v14, 0x40400000, v65\n"
  "v_add_f32 v19, v66, v248\n"
  "v_fma_f32 v21, v248, 2.0, v66\n"
  "v_fmamk_f32 v37, v248, 0x40400000, v66\n"
  "v_lshl_add_u32 v38, v1, 7, v67\n"
  "v_lshl_add_u32 v39, v22, 13, v68\n"
  "v_lshl_add_u32 v6, v35, 4, v9\n"
  "s_add_u32 s42, s41, 1179648\n"
  "v_lshrrev_b32 v62, 6, v0\n"
  "v_cos_f32 v40, v69\n"
  "v_sin_f32 v92, v69\n"
  "v_cos_f32 v41, v70\n"
  "v_sin_f32 v93, v70\n"
  "v_cos_f32 v42, v71\n"
  "v_sin_f32 v94, v71\n"
  "v_cos_f32 v43, v72\n"
  "v_sin_f32 v95, v72\n"
  "v_cos_f32 v24, v73\n"
  "v_sin_f32 v96, v73\n"
  "v_cos_f32 v25, v74\n"
  "v_sin_f32 v97, v74\n"
  "v_cos_f32 v26, v75\n"
  "v_sin_f32 v98, v75\n"
  "v_cos_f32 v27, v76\n"
  "v_sin_f32 v99, v76\n"
  "v_xor_b32 v82, v77, v80\n"
  "v_xor_b32 v86, v77, v84\n"
  "v_xor_b32 v90, v77, v88\n"
  "v_xor_b32 v83, v77, v81\n"
  "v_xor_b32 v87, v77, v85\n"
  "v_xor_b32 v91, v77, v89\n"
  "v_add_u32 v1, 147456, v7\n"
  "v_add_u32 v14, s43, v5\n"
  "v_cos_f32 v8, v65\n"
  "v_sin_f32 v68, v65\n"
  "v_cos_f32 v9, v2\n"
  "v_sin_f32 v69, v2\n"
  "v_cos_f32 v10, v15\n"
  "v_sin_f32 v70, v15\n"
  "v_cos_f32 v11, v18\n"
  "v_sin_f32 v71, v18\n"
  "v_cos_f32 v72, v66\n"
  "v_sin_f32 v100, v66\n"
  "v_cos_f32 v73, v19\n"
  "v_sin_f32 v101, v19\n"
  "v_cos_f32 v74, v21\n"
  "v_sin_f32 v102, v21\n"
  "v_cos_f32 v75, v37\n"
  "v_sin_f32 v103, v37\n"
  "v_cvt_pk_bf16_f32 v104, v44, -v23\n"
  "v_cvt_pk_bf16_f32 v108, v23, v44\n"
  "v_cvt_pk_bf16_f32 v105, v45, -v46\n"
  "v_cvt_pk_bf16_f32 v109, v46, v45\n"
  "v_cvt_pk_bf16_f32 v106, v47, -v48\n"
  "v_cvt_pk_bf16_f32 v110, v48, v47\n"
  "v_cvt_pk_bf16_f32 v107, v49, -v50\n"
  "v_cvt_pk_bf16_f32 v111, v50, v49\n"
  "v_cvt_pk_bf16_f32 v112, v51, -v52\n"
  "v_cvt_pk_bf16_f32 v64, v52, v51\n"
  "v_cvt_pk_bf16_f32 v113, v16, -v53\n"
  "v_cvt_pk_bf16_f32 v65, v53, v16\n"
  "v_cvt_pk_bf16_f32 v114, v54, -v4\n"
  "v_cvt_pk_bf16_f32 v66, v4, v54\n"
  "v_cvt_pk_bf16_f32 v115, v13, -v55\n"
  "v_cvt_pk_bf16_f32 v67, v55, v13\n"
  "v_add_u32 v2, s34, v38\n"
  "v_add_u32 v5, s40, v39\n"
  "v_mul_u32_u24 v7, 9216, v6\n"
  "s_add_u32 s43, s41, 36864\n"
  "v_cvt_pk_bf16_f32 v116, v28, v29\n"
  "v_cvt_pk_bf16_f32 v44, -v29, v28\n"
  "v_cvt_pk_bf16_f32 v117, v30, v56\n"
  "v_cvt_pk_bf16_f32 v45, -v56, v30\n"
  "v_cvt_pk_bf16_f32 v118, v31, v57\n"
  "v_cvt_pk_bf16_f32 v46, -v57, v31\n"
  "v_cvt_pk_bf16_f32 v119, v32, v58\n"
  "v_cvt_pk_bf16_f32 v47, -v58, v32\n"
  "v_cvt_pk_bf16_f32 v48, v33, v59\n"
  "v_cvt_pk_bf16_f32 v120, -v59, v33\n"
  "v_cvt_pk_bf16_f32 v49, v34, v60\n"
  "v_cvt_pk_bf16_f32 v121, -v60, v34\n"
  "v_cvt_pk_bf16_f32 v50, v12, v17\n"
  "v_cvt_pk_bf16_f32 v122, -v17, v12\n"
  "v_cvt_pk_bf16_f32 v51, v36, v61\n"
  "v_cvt_pk_bf16_f32 v123, -v61, v36\n"
  "v_xor_b32 v52, 0x80000000, v80\n"
  "v_xor_b32 v53, 0x80000000, v81\n"
  "v_xor_b32 v54, 0x80000000, v82\n"
  "v_xor_b32 v55, 0x80000000, v83\n"
  "v_xor_b32 v124, 0x80000000, v84\n"
  "v_xor_b32 v125, 0x80000000, v85\n"
  "v_xor_b32 v126, 0x80000000, v86\n"
  "v_xor_b32 v127, 0x80000000, v87\n"
  "v_xor_b32 v28, 0x80000000, v88\n"
  "v_xor_b32 v29, 0x80000000, v89\n"
  "v_xor_b32 v30, 0x80000000, v90\n"
  "v_xor_b32 v31, 0x80000000, v91\n"
  "v_xor_b32 v4, 32, v1\n"
  "v_xor_b32 v6, 16, v14\n"
  "v_xor_b32 v12, 32, v14\n"
  "v_xor_b32 v13, 48, v14\n"
  "v_xor_b32 v16, v77, v104\n"
  "v_xor_b32 v32, v77, v108\n"
  "v_xor_b32 v17, v77, v105\n"
  "v_xor_b32 v33, v77, v109\n"
  "v_xor_b32 v18, v77, v106\n"
  "v_xor_b32 v34, v77, v110\n"
  "v_xor_b32 v19, v77, v107\n"
  "v_xor_b32 v35, v77, v111\n"
  "v_xor_b32 v36, v77, v112\n"
  "v_xor_b32 v56, v77, v64\n"
  "v_xor_b32 v37, v77, v113\n"
  "v_xor_b32 v57, v77, v65\n"
  "v_xor_b32 v38, v77, v114\n"
  "v_xor_b32 v58, v77, v66\n"
  "v_xor_b32 v39, v77, v115\n"
  "v_xor_b32 v59, v77, v67\n"
  "v_xor_b32 v15, 16, v2\n"
  "v_xor_b32 v21, 64, v5\n"
  "v_lshl_add_u32 v22, v20, 4, v7\n"
  "s_add_u32 s34, s41, 1216512\n"
  "v_lshlrev_b32 v23, 6, v0\n"
  "s_mov_b32 s40, 29884416\n"
  "v_readfirstlane_b32 s60, v62\n"
  "Lrowh%=:\n"
  "s_memrealtime s[6:7]\n"
  "s_waitcnt lgkmcnt(0)\n"
  "s_sub_u32 s64, s6, s62\n"
  "s_cmp_lt_u32 s64, 295\n"
  "s_cbranch_scc1 Lrowh%=\n"
  "s_mov_b32 s64, 262144\n"
  "Lrowp%=:\n"
  "buffer_load_dword v248, v23, s[36:39], s40 offen sc1\n"
  "s_waitcnt vmcnt(0)\n"
  "v_cmp_ne_u32 vcc, v213, v248\n"
  "s_cmp_eq_u64 vcc, 0\n"
  "s_cbranch_scc1 Lrowok%=\n"
  "s_sub_u32 s64, s64, 1\n"
  "s_cbranch_scc0 Lrowp%=\n"
  "Lrowok%=:\n"
  "s_barrier\n"
  "s_or_b32 s60, s60, %2\n"
  "s_cmp_lg_u32 s60, 0\n"
  "s_cbranch_scc1 Lrows%=\n"
  "s_add_u32 s40, s40, 32768\n"
  "s_mov_b64 exec, 1\n"
  "buffer_store_dword v213, off, s[36:39], s40 sc1\n"
  "s_mov_b64 exec, -1\n"
  "Lrows%=:\n"
  "s_add_u32 m0, s35, 147456\n"
  "s_nop 0\n"
  "buffer_load_dwordx4 v3, s[24:27], s49 offen offset:0 sc1 lds\n"
  "buffer_load_dwordx4 v3, s[24:27], s49 offen offset:1024 sc1 lds\n"
  "buffer_load_dwordx4 v3, s[24:27], s49 offen offset:2048 sc1 lds\n"
  "buffer_load_dwordx4 v3, s[24:27], s49 offen offset:3072 sc1 lds\n"
  "s_add_u32 m0, s35, 131072\n"
  "s_nop 0\n"
  "buffer_load_dwordx4 v3, s[24:27], s50 offen offset:0 sc1 lds\n"
  "buffer_load_dwordx4 v3, s[24:27], s50 offen offset:1024 sc1 lds\n"
  "buffer_load_dwordx4 v3, s[24:27], s50 offen offset:2048 sc1 lds\n"
  "buffer_load_dwordx4 v3, s[24:27], s50 offen offset:3072 sc1 lds\n"
  "s_bfe_u32 s66, %2, 0x20001\n"
  "s_and_b32 s40, %2, 1\n"
  "s_lshl_b32 s40, s40, 5\n"
  "s_lshr_b32 s60, %2, 3\n"
  "s_add_u32 s40, s40, s60\n"
  "s_mul_i32 s67, s40, 37\n"
  "s_lshr_b32 s67, s67, 8\n"
  "v_lshrrev_b32 v7, 6, v0\n"
  "s_mul_i32 s68, s67, 7\n"
  "s_sub_u32 s68, s40, s68\n"
  "s_mul_i32 s69, s66, 9\n"
  "v_readfirstlane_b32 s10, v7\n"
  "s_add_u32 s69, s69, s67\n"
  "s_and_b32 s35, s10, 1\n"
  "s_lshl1_add_u32 s49, s69, s35\n"
  "s_lshl_b32 s49, s49, 7\n"
  "s_sub_u32 s30, 5308416, s49\n"
  "s_cmp_lt_u32 s67, 9\n"
  "s_cselect_b32 s30, s30, 0\n"
  "s_lshl_b32 s48, s10, 12\n"
  "s_lshr_b32 s70, %2, 4\n"
  "v_and_b32 v3, 7, v0\n"
  "v_and_b32 v20, 16, v0\n"
  "s_lshl_b32 s70, s70, 6\n"
  "v_bfe_u32 v23, v0, 3, 3\n"
  "v_lshlrev_b32 v248, 4, v3\n"
  "v_lshlrev_b32 v249, 1, v20\n"
  "s_and_b32 s40, %2, 15\n"
  "v_mul_u32_u24 v7, 9216, v23\n"
  "v_xor_b32 v250, v248, v249\n"
  "v_and_b32 v60, 3, v3\n"
  "s_add_u32 s70, s70, s40\n"
  "v_add_u32 v214, v7, v250\n"
  "v_lshlrev_b32 v61, 5, v60\n"
  "v_lshrrev_b32 v62, 2, v3\n"
  "s_mul_i32 s70, s70, 2304\n"
  "s_lshl_b32 s71, s10, 5\n"
  "v_xor_b32 v63, 64, v214\n"
  "v_add_u32 v76, v61, v62\n"
  "s_add_u32 s71, s71, s70\n"
  "v_add_u32 v215, 72704, v63\n"
  "v_mul_u32_u24 v77, 1152, v76\n"
  "s_lshl_b32 s35, s10, 8\n"
  "s_mov_b32 s31, 0x20000\n"
  "s_mov_b32 s46, 2359296\n"
  "s_mov_b32 s47, 0x20000\n"
  "v_add_u32 v216, 145408, v214\n"
  "v_add_u32 v217, 145408, v215\n"
  "v_lshl_add_u32 v218, v23, 4, v77\n"
  "v_lshl_add_u32 v219, v23, 2, v77\n"
  "s_add_u32 s70, s70, s35\n"
  "s_waitcnt lgkmcnt(0)\n"
  "s_mov_b64 s[44:45], s[12:13]\n"
  "s_cmp_lt_u32 s10, 2\n"
  "s_cselect_b64 s[28:29], s[14:15], s[16:17]\n"
  "s_add_u32 s28, s28, s49\n"
  "s_addc_u32 s29, s29, 0\n"
  "s_waitcnt vmcnt(4) lgkmcnt(0)\n"
  "s_barrier\n"
  "ds_read_b128 v[60:63], v1 offset:0\n"
  "ds_read_b128 v[128:131], v1 offset:256\n"
  "ds_read_b128 v[132:135], v1 offset:8192\n"
  "ds_read_b128 v[136:139], v1 offset:8448\n"
  "ds_read_b128 v[140:143], v4 offset:0\n"
  "ds_read_b128 v[144:147], v4 offset:256\n"
  "ds_read_b128 v[76:79], v4 offset:8192\n"
  "s_waitcnt lgkmcnt(6)\n"
  "v_mfma_f32_16x16x32_bf16 v[148:151], v[60:63], v[44:47], 0\n"
  "ds_read_b128 v[152:155], v4 offset:8448\n"
  "s_waitcnt lgkmcnt(6)\n"
  "v_mfma_f32_16x16x32_bf16 v[148:151], v[128:131], v[120:123], v[148:151]\n"
  "s_waitcnt lgkmcnt(5)\n"
  "v_mfma_f32_16x16x32_bf16 v[156:159], v[132:135], v[116:119], 0\n"
  "v_mfma_f32_16x16x32_bf16 v[160:163], v[132:135], v[44:47], 0\n"
  "s_waitcnt lgkmcnt(4)\n"
  "v_mfma_f32_16x16x32_bf16 v[160:163], v[136:139], v[120:123], v[160:163]\n"
  "v_mfma_f32_16x16x32_bf16 v[156:159], v[136:139], v[48:51], v[156:159]\n"
  "s_waitcnt lgkmcnt(1)\n"
  "v_mfma_f32_16x16x32_bf16 v[164:167], v[76:79], v[116:119], 0\n"
  "s_waitcnt lgkmcnt(0)\n"
  "v_mfma_f32_16x16x32_bf16 v[164:167], v[152:155], v[48:51], v[164:167]\n"
  "v_mfma_f32_16x16x32_bf16 v[168:171], v[76:79], v[44:47], 0\n"
  "v_mfma_f32_16x16x32_bf16 v[168:171], v[152:155], v[120:123], v[168:171]\n"
  "v_mfma_f32_16x16x32_bf16 v[172:175], v[140:143], v[116:119], 0\n"
  "buffer_load_dwordx4 v[204:207], v218, s[44:47], s70 offen\n"
  "v_mfma_f32_16x16x32_bf16 v[172:175], v[144:147], v[48:51], v[172:175]\n"
  "v_mfma_f32_16x16x32_bf16 v[176:179], v[140:143], v[44:47], 0\n"
  "v_mfma_f32_16x16x32_bf16 v[176:179], v[144:147], v[120:123], v[176:179]\n"
  "v_mfma_f32_16x16x32_bf16 v[180:183], v[60:63], v[116:119], 0\n"
  "v_mfma_f32_16x16x32_bf16 v[180:183], v[128:131], v[48:51], v[180:183]\n"
  "v_pk_mul_f32 v[186:187], v[150:151], v[42:43]\n"
  "v_pk_mul_f32 v[188:189], v[156:157], v[24:25]\n"
  "v_pk_mul_f32 v[134:135], v[166:167], v[26:27]\n"
  "v_pk_mul_f32 v[192:193], v[168:169], v[24:25]\n"
  "v_pk_fma_f32 v[192:193], v[164:165], v[96:97], v[192:193] neg_lo:[1,0,0] neg_hi:[1,0,0]\n"
  "v_pk_mul_f32 v[138:139], v[174:175], v[42:43]\n"
  "v_pk_mul_f32 v[198:199], v[178:179], v[42:43]\n"
  "v_pk_fma_f32 v[138:139], v[178:179], v[94:95], v[138:139]\n"
  "v_pk_fma_f32 v[186:187], v[182:183], v[94:95], v[186:187] neg_lo:[1,0,0] neg_hi:[1,0,0]\n"
  "v_cvt_pk_bf16_f32 v77, v186, v187\n"
  "v_pk_mul_f32 v[154:155], v[182:183], v[42:43]\n"
  "v_pk_fma_f32 v[154:155], v[150:151], v[94:95], v[154:155]\n"
  "v_cvt_pk_bf16_f32 v45, v154, v155\n"
  "v_pk_fma_f32 v[198:199], v[174:175], v[94:95], v[198:199] neg_lo:[1,0,0] neg_hi:[1,0,0]\n"
  "v_pk_fma_f32 v[134:135], v[170:171], v[98:99], v[134:135]\n"
  "v_pk_mul_f32 v[132:133], v[164:165], v[24:25]\n"
  "v_pk_fma_f32 v[132:133], v[168:169], v[96:97], v[132:133]\n"
  "v_pk_mul_f32 v[184:185], v[148:149], v[40:41]\n"
  "v_pk_fma_f32 v[184:185], v[180:181], v[92:93], v[184:185] neg_lo:[1,0,0] neg_hi:[1,0,0]\n"
  "v_cvt_pk_bf16_f32 v76, v184, v185\n"
  "v_pk_mul_f32 v[48:49], v[160:161], v[24:25]\n"
  "v_pk_fma_f32 v[48:49], v[156:157], v[96:97], v[48:49] neg_lo:[1,0,0] neg_hi:[1,0,0]\n"
  "v_pk_mul_f32 v[190:191], v[158:159], v[26:27]\n"
  "v_pk_fma_f32 v[188:189], v[160:161], v[96:97], v[188:189]\n"
  "v_pk_mul_f32 v[194:195], v[170:171], v[26:27]\n"
  "v_pk_mul_f32 v[136:137], v[172:173], v[40:41]\n"
  "v_pk_fma_f32 v[136:137], v[176:177], v[92:93], v[136:137]\n"
  "v_pk_mul_f32 v[50:51], v[162:163], v[26:27]\n"
  "v_pk_fma_f32 v[50:51], v[158:159], v[98:99], v[50:51] neg_lo:[1,0,0] neg_hi:[1,0,0]\n"
  "v_cvt_pk_bf16_f32 v79, v50, v51\n"
  "v_pk_fma_f32 v[194:195], v[166:167], v[98:99], v[194:195] neg_lo:[1,0,0] neg_hi:[1,0,0]\n"
  "v_pk_mul_f32 v[152:153], v[180:181], v[40:41]\n"
  "v_pk_fma_f32 v[152:153], v[148:149], v[92:93], v[152:153]\n"
  "v_cvt_pk_bf16_f32 v44, v152, v153\n"
  "v_pk_fma_f32 v[190:191], v[162:163], v[98:99], v[190:191]\n"
  "v_pk_mul_f32 v[196:197], v[176:177], v[40:41]\n"
  "v_pk_fma_f32 v[196:197], v[172:173], v[92:93], v[196:197] neg_lo:[1,0,0] neg_hi:[1,0,0]\n"
  "v_cvt_pk_bf16_f32 v78, v48, v49\n"
  "v_cvt_pk_bf16_f32 v47, v190, v191\n"
  "v_cvt_pk_bf16_f32 v46, v188, v189\n"
  "v_mfma_f32_16x16x32_bf16 v[60:63], v[80:83], v[76:79], 0\n"
  "s_waitcnt vmcnt(1)\n"
  "ds_read_b128 v[116:119], v14\n"
  "ds_read_b128 v[24:27], v6\n"
  "v_mfma_f32_16x16x32_bf16 v[120:123], v[80:83], v[44:47], 0\n"
  "ds_read_b128 v[128:131], v12\n"
  "v_cvt_pk_bf16_f32 v142, v132, v133\n"
  "v_mfma_f32_16x16x32_bf16 v[120:123], v[84:87], v[76:79], v[120:123]\n"
  "v_cvt_pk_bf16_f32 v146, v192, v193\n"
  "v_cvt_pk_bf16_f32 v143, v134, v135\n"
  "v_mfma_f32_16x16x32_bf16 v[60:63], v[88:91], v[44:47], v[60:63]\n"
  "ds_read_b128 v[40:43], v13\n"
  "v_cvt_pk_bf16_f32 v141, v138, v139\n"
  "v_mfma_f32_16x16x32_bf16 v[48:51], v[52:55], v[76:79], 0\n"
  "buffer_load_dwordx4 v[208:211], v218, s[44:47], s70 offen offset:128\n"
  "v_cvt_pk_bf16_f32 v145, v198, v199\n"
  "v_cvt_pk_bf16_f32 v147, v194, v195\n"
  "v_mfma_f32_16x16x32_bf16 v[48:51], v[28:31], v[44:47], v[48:51]\n"
  "v_cvt_pk_bf16_f32 v140, v136, v137\n"
  "v_cvt_pk_bf16_f32 v144, v196, v197\n"
  "v_mfma_f32_16x16x32_bf16 v[92:95], v[52:55], v[44:47], 0\n"
  "s_waitcnt lgkmcnt(2)\n"
  "v_and_b32 v99, 0xffff0000, v27\n"
  "v_mfma_f32_16x16x32_bf16 v[148:151], v[52:55], v[140:143], 0\n"
  "v_lshlrev_b32 v152, 16, v116\n"
  "v_and_b32 v156, 0xffff0000, v116\n"
  "v_mfma_f32_16x16x32_bf16 v[148:151], v[124:127], v[144:147], v[148:151]\n"
  "v_and_b32 v98, 0xffff0000, v26\n"
  "v_lshlrev_b32 v155, 16, v119\n"
  "v_mfma_f32_16x16x32_bf16 v[132:135], v[52:55], v[144:147], 0\n"
  "v_lshlrev_b32 v161, 16, v25\n"
  "v_and_b32 v97, 0xffff0000, v25\n"
  "v_mfma_f32_16x16x32_bf16 v[164:167], v[80:83], v[144:147], 0\n"
  "v_lshlrev_b32 v163, 16, v27\n"
  "v_lshlrev_b32 v153, 16, v117\n"
  "v_mfma_f32_16x16x32_bf16 v[132:135], v[28:31], v[140:143], v[132:135]\n"
  "v_lshlrev_b32 v162, 16, v26\n"
  "v_and_b32 v159, 0xffff0000, v119\n"
  "v_mfma_f32_16x16x32_bf16 v[92:95], v[124:127], v[76:79], v[92:95]\n"
  "v_and_b32 v96, 0xffff0000, v24\n"
  "v_lshlrev_b32 v160, 16, v24\n"
  "v_mfma_f32_16x16x32_bf16 v[44:47], v[80:83], v[140:143], 0\n"
  "v_and_b32 v158, 0xffff0000, v118\n"
  "v_lshlrev_b32 v154, 16, v118\n"
  "v_mfma_f32_16x16x32_bf16 v[44:47], v[84:87], v[144:147], v[44:47]\n"
  "v_and_b32 v157, 0xffff0000, v117\n"
  "s_waitcnt lgkmcnt(0)\n"
  "v_lshlrev_b32 v136, 16, v40\n"
  "v_lshlrev_b32 v168, 16, v128\n"
  "v_and_b32 v174, 0xffff0000, v130\n"
  "v_pk_mul_f32 v[176:177], v[60:61], v[152:153]\n"
  "v_pk_mul_f32 v[180:181], v[48:49], v[160:161]\n"
  "v_pk_fma_f32 v[180:181], v[92:93], v[96:97], v[180:181]\n"
  "v_cvt_pk_bf16_f32 v186, v180, v181\n"
  "v_pk_mul_f32 v[188:189], v[120:121], v[152:153]\n"
  "v_pk_mul_f32 v[78:79], v[94:95], v[162:163]\n"
  "v_pk_fma_f32 v[78:79], v[50:51], v[98:99], v[78:79] neg_lo:[1,0,0] neg_hi:[1,0,0]\n"
  "v_cvt_pk_bf16_f32 v27, v78, v79\n"
  "v_pk_fma_f32 v[188:189], v[60:61], v[156:157], v[188:189] neg_lo:[1,0,0] neg_hi:[1,0,0]\n"
  "v_pk_mul_f32 v[76:77], v[92:93], v[160:161]\n"
  "v_pk_fma_f32 v[76:77], v[48:49], v[96:97], v[76:77] neg_lo:[1,0,0] neg_hi:[1,0,0]\n"
  "v_cvt_pk_bf16_f32 v26, v76, v77\n"
  "v_pk_mul_f32 v[190:191], v[122:123], v[154:155]\n"
  "v_pk_fma_f32 v[190:191], v[62:63], v[158:159], v[190:191] neg_lo:[1,0,0] neg_hi:[1,0,0]\n"
  "v_cvt_pk_bf16_f32 v25, v190, v191\n"
  "v_pk_fma_f32 v[176:177], v[120:121], v[156:157], v[176:177]\n"
  "v_pk_mul_f32 v[182:183], v[50:51], v[162:163]\n"
  "v_pk_fma_f32 v[182:183], v[94:95], v[98:99], v[182:183]\n"
  "v_cvt_pk_bf16_f32 v187, v182, v183\n"
  "v_cvt_pk_bf16_f32 v24, v188, v189\n"
  "v_pk_mul_f32 v[178:179], v[62:63], v[154:155]\n"
  "v_pk_fma_f32 v[178:179], v[122:123], v[158:159], v[178:179]\n"
  "v_mfma_f32_16x16x32_bf16 v[116:119], v[24:27], v[84:87], 0\n"
  "v_cvt_pk_bf16_f32 v185, v178, v179\n"
  "v_and_b32 v144, 0xffff0000, v40\n"
  "v_mfma_f32_16x16x32_bf16 v[192:195], v[24:27], v[124:127], 0\n"
  "v_lshlrev_b32 v137, 16, v41\n"
  "v_lshlrev_b32 v171, 16, v131\n"
  "v_mfma_f32_16x16x32_bf16 v[76:79], v[24:27], v[80:83], 0\n"
  "v_and_b32 v175, 0xffff0000, v131\n"
  "v_cvt_pk_bf16_f32 v184, v176, v177\n"
  "v_mfma_f32_16x16x32_bf16 v[164:167], v[88:91], v[140:143], v[164:167]\n"
  "v_and_b32 v145, 0xffff0000, v41\n"
  "buffer_load_dword v212, v219, s[44:47], s71 offen offset:1024\n"
  "v_lshlrev_b32 v138, 16, v42\n"
  "v_mfma_f32_16x16x32_bf16 v[116:119], v[184:187], v[80:83], v[116:119]\n"
  "v_lshlrev_b32 v170, 16, v130\n"
  "v_and_b32 v172, 0xffff0000, v128\n"
  "v_mfma_f32_16x16x32_bf16 v[76:79], v[184:187], v[88:91], v[76:79]\n"
  "v_and_b32 v146, 0xffff0000, v42\n"
  "v_and_b32 v147, 0xffff0000, v43\n"
  "v_mfma_f32_16x16x32_bf16 v[192:195], v[184:187], v[52:55], v[192:195]\n"
  "v_lshlrev_b32 v169, 16, v129\n"
  "v_and_b32 v173, 0xffff0000, v129\n"
  "v_mfma_f32_16x16x32_bf16 v[48:51], v[24:27], v[52:55], 0\n"
  "v_lshlrev_b32 v139, 16, v43\n"
  "v_mfma_f32_16x16x32_bf16 v[48:51], v[184:187], v[28:31], v[48:51]\n"
  "v_pk_mul_f32 v[60:61], v[164:165], v[168:169]\n"
  "v_pk_mul_f32 v[92:93], v[44:45], v[168:169]\n"
  "v_pk_fma_f32 v[92:93], v[164:165], v[172:173], v[92:93] neg_lo:[1,0,0] neg_hi:[1,0,0]\n"
  "v_pk_mul_f32 v[96:97], v[192:193], v[72:73]\n"
  "v_pk_fma_f32 v[60:61], v[44:45], v[172:173], v[60:61]\n"
  "v_cvt_pk_bf16_f32 v120, v92, v93\n"
  "v_pk_mul_f32 v[140:141], v[76:77], v[8:9]\n"
  "v_pk_mul_f32 v[94:95], v[46:47], v[170:171]\n"
  "v_pk_mul_f32 v[152:153], v[48:49], v[72:73]\n"
  "v_pk_fma_f32 v[152:153], v[192:193], v[100:101], v[152:153] neg_lo:[1,0,0] neg_hi:[1,0,0]\n"
  "v_pk_fma_f32 v[94:95], v[166:167], v[174:175], v[94:95] neg_lo:[1,0,0] neg_hi:[1,0,0]\n"
  "v_pk_mul_f32 v[156:157], v[116:117], v[8:9]\n"
  "v_pk_mul_f32 v[62:63], v[166:167], v[170:171]\n"
  "v_pk_fma_f32 v[62:63], v[46:47], v[174:175], v[62:63]\n"
  "v_pk_mul_f32 v[98:99], v[194:195], v[74:75]\n"
  "v_pk_fma_f32 v[98:99], v[50:51], v[102:103], v[98:99]\n"
  "v_pk_mul_f32 v[154:155], v[50:51], v[74:75]\n"
  "v_pk_fma_f32 v[154:155], v[194:195], v[102:103], v[154:155] neg_lo:[1,0,0] neg_hi:[1,0,0]\n"
  "v_pk_fma_f32 v[96:97], v[48:49], v[100:101], v[96:97]\n"
  "v_cvt_pk_bf16_f32 v24, v60, v61\n"
  "v_pk_mul_f32 v[42:43], v[150:151], v[138:139]\n"
  "v_pk_mul_f32 v[142:143], v[78:79], v[10:11]\n"
  "v_pk_fma_f32 v[142:143], v[118:119], v[70:71], v[142:143] neg_lo:[1,0,0] neg_hi:[1,0,0]\n"
  "v_pk_mul_f32 v[158:159], v[118:119], v[10:11]\n"
  "v_pk_fma_f32 v[158:159], v[78:79], v[70:71], v[158:159]\n"
  "v_pk_fma_f32 v[42:43], v[134:135], v[146:147], v[42:43] neg_lo:[1,0,0] neg_hi:[1,0,0]\n"
  "v_pk_mul_f32 v[40:41], v[148:149], v[136:137]\n"
  "v_pk_fma_f32 v[40:41], v[132:133], v[144:145], v[40:41] neg_lo:[1,0,0] neg_hi:[1,0,0]\n"
  "v_cvt_pk_bf16_f32 v122, v40, v41\n"
  "v_pk_mul_f32 v[46:47], v[134:135], v[138:139]\n"
  "v_pk_fma_f32 v[46:47], v[150:151], v[146:147], v[46:47]\n"
  "v_cvt_pk_bf16_f32 v27, v46, v47\n"
  "v_cvt_pk_bf16_f32 v123, v42, v43\n"
  "v_pk_fma_f32 v[140:141], v[116:117], v[68:69], v[140:141] neg_lo:[1,0,0] neg_hi:[1,0,0]\n"
  "v_pk_fma_f32 v[156:157], v[76:77], v[68:69], v[156:157]\n"
  "v_cvt_pk_bf16_f32 v121, v94, v95\n"
  "v_pk_mul_f32 v[44:45], v[132:133], v[136:137]\n"
  "v_pk_fma_f32 v[44:45], v[148:149], v[144:145], v[44:45]\n"
  "v_mfma_f32_16x16x32_bf16 v[48:51], v[120:123], v[80:83], 0\n"
  "v_cvt_pk_bf16_f32 v26, v44, v45\n"
  "v_cvt_pk_bf16_f32 v25, v62, v63\n"
  "v_mfma_f32_16x16x32_bf16 v[128:131], v[120:123], v[84:87], 0\n"
  "v_cvt_pk_bf16_f32 v162, v142, v158\n"
  "v_cvt_pk_bf16_f32 v166, v154, v98\n"
  "v_mfma_f32_16x16x32_bf16 v[48:51], v[24:27], v[88:91], v[48:51]\n"
  "v_cvt_pk_bf16_f32 v165, v153, v97\n"
  "v_cvt_pk_bf16_f32 v160, v140, v156\n"
  "v_mfma_f32_16x16x32_bf16 v[128:131], v[24:27], v[80:83], v[128:131]\n"
  "v_cvt_pk_bf16_f32 v161, v141, v157\n"
  "v_cvt_pk_bf16_f32 v163, v143, v159\n"
  "v_mfma_f32_16x16x32_bf16 v[40:43], v[120:123], v[124:127], 0\n"
  "ds_write_b128 v2, v[160:163] offset:0\n"
  "v_mfma_f32_16x16x32_bf16 v[40:43], v[24:27], v[52:55], v[40:43]\n"
  "v_cvt_pk_bf16_f32 v164, v152, v96\n"
  "v_cvt_pk_bf16_f32 v167, v155, v99\n"
  "v_mfma_f32_16x16x32_bf16 v[44:47], v[120:123], v[52:55], 0\n"
  "ds_write_b128 v2, v[164:167] offset:2048\n"
  "v_pk_mul_f32 v[62:63], v[50:51], v[10:11]\n"
  "v_pk_mul_f32 v[76:77], v[128:129], v[8:9]\n"
  "v_pk_fma_f32 v[76:77], v[48:49], v[68:69], v[76:77]\n"
  "s_nop 0\n"
  "s_cbranch_execz Ltramp%=\n"
  "v_pk_mul_f32 v[78:79], v[130:131], v[10:11]\n"
  "v_pk_mul_f32 v[84:85], v[40:41], v[72:73]\n"
  "v_pk_mul_f32 v[86:87], v[42:43], v[74:75]\n"
  "v_pk_fma_f32 v[62:63], v[130:131], v[70:71], v[62:63] neg_lo:[1,0,0] neg_hi:[1,0,0]\n"
  "v_pk_fma_f32 v[78:79], v[50:51], v[70:71], v[78:79]\n"
  "v_pk_mul_f32 v[60:61], v[48:49], v[8:9]\n"
  "v_pk_fma_f32 v[60:61], v[128:129], v[68:69], v[60:61] neg_lo:[1,0,0] neg_hi:[1,0,0]\n"
  "v_mfma_f32_16x16x32_bf16 v[44:47], v[24:27], v[28:31], v[44:47]\n"
  "v_cvt_pk_bf16_f32 v82, v62, v78\n"
  "v_cvt_pk_bf16_f32 v80, v60, v76\n"
  "v_cvt_pk_bf16_f32 v81, v61, v77\n"
  "v_cvt_pk_bf16_f32 v83, v63, v79\n"
  "ds_write_b128 v15, v[80:83] offset:8192\n"
  "s_nop 2\n"
  "v_pk_fma_f32 v[86:87], v[46:47], v[102:103], v[86:87]\n"
  "v_pk_mul_f32 v[54:55], v[46:47], v[74:75]\n"
  "v_pk_fma_f32 v[54:55], v[42:43], v[102:103], v[54:55] neg_lo:[1,0,0] neg_hi:[1,0,0]\n"
  "v_cvt_pk_bf16_f32 v91, v55, v87\n"
  "v_cvt_pk_bf16_f32 v90, v54, v86\n"
  "v_pk_mul_f32 v[52:53], v[44:45], v[72:73]\n"
  "v_pk_fma_f32 v[52:53], v[40:41], v[100:101], v[52:53] neg_lo:[1,0,0] neg_hi:[1,0,0]\n"
  "v_pk_fma_f32 v[84:85], v[44:45], v[100:101], v[84:85]\n"
  "v_cvt_pk_bf16_f32 v88, v52, v84\n"
  "v_cvt_pk_bf16_f32 v89, v53, v85\n"
  "ds_write_b128 v15, v[88:91] offset:10240\n"
  "s_waitcnt lgkmcnt(0)\n"
  "s_barrier\n"
  "ds_read_b128 v[8:11], v5 offset:0\n"
  "ds_read_b128 v[24:27], v21 offset:0\n"
  "ds_read_b128 v[28:31], v5 offset:4096\n"
  "ds_read_b128 v[48:51], v21 offset:4096\n"
  "ds_read_b128 v[60:63], v5 offset:1024\n"
  "ds_read_b128 v[68:71], v21 offset:1024\n"
  "ds_read_b128 v[76:79], v5 offset:5120\n"
  "s_waitcnt lgkmcnt(6)\n"
  "v_mfma_f32_16x16x32_bf16 v[80:83], v[108:111], v[8:11], 0\n"
  "ds_read_b128 v[72:75], v21 offset:5120\n"
  "s_waitcnt lgkmcnt(6)\n"
  "v_mfma_f32_16x16x32_bf16 v[80:83], v[32:35], v[24:27], v[80:83]\n"
  "s_waitcnt lgkmcnt(5)\n"
  "v_mfma_f32_16x16x32_bf16 v[40:43], v[64:67], v[28:31], 0\n"
  "s_waitcnt lgkmcnt(4)\n"
  "v_mfma_f32_16x16x32_bf16 v[40:43], v[56:59], v[48:51], v[40:43]\n"
  "v_mfma_f32_16x16x32_bf16 v[44:47], v[112:115], v[28:31], 0\n"
  "v_mfma_f32_16x16x32_bf16 v[44:47], v[36:39], v[48:51], v[44:47]\n"
  "s_waitcnt lgkmcnt(1)\n"
  "v_mfma_f32_16x16x32_bf16 v[12:15], v[64:67], v[76:79], 0\n"
  "s_waitcnt lgkmcnt(0)\n"
  "v_mfma_f32_16x16x32_bf16 v[12:15], v[56:59], v[72:75], v[12:15]\n"
  "s_nop 1\n"
  "v_add_f32 v1, v80, v40\n"
  "v_mfma_f32_16x16x32_bf16 v[52:55], v[104:107], v[8:11], 0\n"
  "v_add_f32 v2, v82, v42\n"
  "v_add_f32 v3, v83, v43\n"
  "v_mfma_f32_16x16x32_bf16 v[52:55], v[16:19], v[24:27], v[52:55]\n"
  "v_cvt_pk_bf16_f32 v87, v2, v3\n"
  "v_add_f32 v20, v81, v41\n"
  "v_cvt_pk_bf16_f32 v86, v1, v20\n"
  "v_mfma_f32_16x16x32_bf16 v[4:7], v[112:115], v[76:79], 0\n"
  "v_sub_f32 v21, v82, v42\n"
  "v_sub_f32 v23, v81, v41\n"
  "v_mfma_f32_16x16x32_bf16 v[4:7], v[36:39], v[72:75], v[4:7]\n"
  "v_sub_f32 v248, v80, v40\n"
  "v_add_f32 v249, v52, v44\n"
  "v_add_f32 v250, v53, v45\n"
  "v_cvt_pk_bf16_f32 v84, v249, v250\n"
  "v_add_f32 v28, v54, v46\n"
  "v_add_f32 v29, v55, v47\n"
  "v_cvt_pk_bf16_f32 v85, v28, v29\n"
  "v_sub_f32 v8, v52, v44\n"
  "v_cvt_pk_bf16_f32 v26, v248, v23\n"
  "v_mfma_f32_16x16x32_bf16 v[48:51], v[108:111], v[60:63], 0\n"
  "buffer_store_dwordx4 v[84:87], v22, s[24:27], s41 offen sc1\n"
  "v_sub_f32 v1, v53, v45\n"
  "v_mfma_f32_16x16x32_bf16 v[48:51], v[32:35], v[68:71], v[48:51]\n"
  "v_sub_f32 v2, v55, v47\n"
  "v_cvt_pk_bf16_f32 v24, v8, v1\n"
  "v_mfma_f32_16x16x32_bf16 v[36:39], v[104:107], v[60:63], 0\n"
  "v_sub_f32 v3, v83, v43\n"
  "v_cvt_pk_bf16_f32 v27, v21, v3\n"
  "v_mfma_f32_16x16x32_bf16 v[36:39], v[16:19], v[68:71], v[36:39]\n"
  "v_sub_f32 v20, v54, v46\n"
  "v_cvt_pk_bf16_f32 v25, v20, v2\n"
  "buffer_store_dwordx4 v[24:27], v22, s[24:27], s42 offen sc1\n"
  "v_pk_add_f32 v[10:11], v[48:49], v[12:13] neg_lo:[0,1] neg_hi:[0,1]\n"
  "v_pk_add_f32 v[28:29], v[48:49], v[12:13]\n"
  "v_cvt_pk_bf16_f32 v58, v28, v29\n"
  "v_pk_add_f32 v[248:249], v[50:51], v[14:15] neg_lo:[0,1] neg_hi:[0,1]\n"
  "v_cvt_pk_bf16_f32 v66, v10, v11\n"
  "v_pk_add_f32 v[30:31], v[36:37], v[4:5]\n"
  "v_cvt_pk_bf16_f32 v56, v30, v31\n"
  "v_pk_add_f32 v[32:33], v[38:39], v[6:7] neg_lo:[0,1] neg_hi:[0,1]\n"
  "v_cvt_pk_bf16_f32 v65, v32, v33\n"
  "v_pk_add_f32 v[8:9], v[38:39], v[6:7]\n"
  "v_cvt_pk_bf16_f32 v57, v8, v9\n"
  "v_pk_add_f32 v[34:35], v[36:37], v[4:5] neg_lo:[0,1] neg_hi:[0,1]\n"
  "v_cvt_pk_bf16_f32 v64, v34, v35\n"
  "v_cvt_pk_bf16_f32 v67, v248, v249\n"
  "v_pk_add_f32 v[2:3], v[50:51], v[14:15]\n"
  "v_cvt_pk_bf16_f32 v59, v2, v3\n"
  "buffer_store_dwordx4 v[56:59], v22, s[24:27], s43 offen sc1\n"
  "buffer_store_dwordx4 v[64:67], v22, s[24:27], s34 offen sc1\n"
  "Ljoin%=:\n"
  "s_add_u32 m0, s48, 0\n"
  "s_mov_b32 s51, 0\n"
  "buffer_load_dwordx4 v214, s[28:31], s51 offen offset:0 lds\n"
  "v_and_b32 v1, 15, v0\n"
  "v_and_b32 v2, 1, v1\n"
  "v_bfe_u32 v3, v0, 4, 2\n"
  "v_add_u32 v200, 16, v2\n"
  "v_cvt_f32_ubyte0 v201, v3\n"
  "buffer_load_dwordx4 v215, s[28:31], s51 offen offset:1024 lds\n"
  "v_cvt_f32_ubyte0 v202, v200\n"
  "v_mul_f32 v4, 0x3d638e39, v202\n"
  "v_add_f32 v5, 0x40800000, v201\n"
  "v_add_f32 v6, 0x41000000, v201\n"
  "v_mul_f32 v7, v201, v4\n"
  "v_mul_f32 v8, v5, v4\n"
  "v_mul_f32 v9, v6, v4\n"
  "v_cos_f32 v10, v7\n"
  "v_sin_f32 v11, v7\n"
  "v_cos_f32 v12, v8\n"
  "buffer_load_dwordx4 v216, s[28:31], s51 offen offset:2048 lds\n"
  "v_sin_f32 v13, v8\n"
  "v_cos_f32 v14, v9\n"
  "v_sin_f32 v15, v9\n"
  "v_lshrrev_b32 v16, 1, v1\n"
  "v_cmp_eq_u32 vcc, 0, v16\n"
  "v_cvt_pk_bf16_f32 v18, v10, -v11\n"
  "v_cvt_pk_bf16_f32 v19, v12, -v13\n"
  "buffer_load_dwordx4 v217, s[28:31], s51 offen offset:3072 lds\n"
  "v_cvt_pk_bf16_f32 v20, v14, -v15\n"
  "v_cndmask_b32 v24, 0, v18, vcc\n"
  "v_cndmask_b32 v25, 0, v19, vcc\n"
  "v_cndmask_b32 v26, 0, v20, vcc\n"
  "v_cmp_eq_u32 vcc, 1, v16\n"
  "v_and_b32 v4, 63, v0\n"
  "v_lshrrev_b32 v7, 2, v4\n"
  "v_cndmask_b32 v28, 0, v18, vcc\n"
  "v_cndmask_b32 v29, 0, v19, vcc\n"
  "v_cndmask_b32 v30, 0, v20, vcc\n"
  "v_cmp_eq_u32 vcc, 2, v16\n"
  "v_lshrrev_b32 v17, 7, v0\n"
  "s_add_u32 m0, s48, 16384\n"
  "s_mov_b32 s51, 294912\n"
  "buffer_load_dwordx4 v214, s[28:31], s51 offen offset:0 lds\n"
  "v_and_b32 v21, 3, v4\n"
  "v_cndmask_b32 v8, 0, v18, vcc\n"
  "v_cndmask_b32 v9, 0, v19, vcc\n"
  "v_cndmask_b32 v10, 0, v20, vcc\n"
  "v_cmp_eq_u32 vcc, 3, v16\n"
  "v_lshlrev_b32 v22, 5, v7\n"
  "s_mul_i32 s34, %2, 9216\n"
  "v_cndmask_b32 v12, 0, v18, vcc\n"
  "v_cndmask_b32 v13, 0, v19, vcc\n"
  "v_cndmask_b32 v14, 0, v20, vcc\n"
  "v_bfe_u32 v23, v0, 6, 1\n"
  "v_lshl_add_u32 v200, v17, 4, v22\n"
  "buffer_load_dwordx4 v215, s[28:31], s51 offen offset:1024 lds\n"
  "v_cmp_eq_u32 vcc, 0, v21\n"
  "s_add_u32 s34, s34, 4718592\n"
  "v_lshl_add_u32 v202, v23, 4, v1\n"
  "v_add_u32 v32, 131072, v200\n"
  "v_mul_u32_u24 v33, 1032, v21\n"
  "s_add_u32 s35, s34, 4096\n"
  "v_cvt_f32_ubyte0 v34, v202\n"
  "v_mov_b32 v35, 140360\n"
  "v_add_u32 v7, v33, v32\n"
  "v_add_u32 v36, 8256, v32\n"
  "v_mov_b32 v37, 139848\n"
  "v_lshrrev_b32 v38, 6, v0\n"
  "buffer_load_dwordx4 v216, s[28:31], s51 offen offset:2048 lds\n"
  "s_add_u32 s70, s34, 8192\n"
  "v_mul_f32 v18, 0x3d000000, v34\n"
  "v_cndmask_b32 v19, v7, v35, vcc\n"
  "v_cndmask_b32 v20, v37, v36, vcc\n"
  "v_cndmask_b32 v22, v35, v36, vcc\n"
  "v_mul_f32 v39, v201, v18\n"
  "v_cmp_eq_u32 vcc, 1, v21\n"
  "v_lshlrev_b32 v200, 2, v38\n"
  "v_lshrrev_b32 v40, 5, v4\n"
  "s_lshr_b32 s71, %2, 1\n"
  "v_cvt_f32_ubyte0 v41, v1\n"
  "buffer_load_dwordx4 v217, s[28:31], s51 offen offset:3072 lds\n"
  "v_mul_f32 v33, 4.0, v39\n"
  "v_lshrrev_b32 v42, 2, v4\n"
  "v_add3_u32 v43, v200, v40, 0\n"
  "v_add3_u32 v202, v200, v40, 2\n"
  "s_and_b32 s84, %2, 1\n"
  "v_lshlrev_b32 v44, 10, v3\n"
  "v_bfe_u32 v45, v4, 2, 2\n"
  "v_and_b32 v37, 7, v0\n"
  "v_add_f32 v36, v33, v18\n"
  "v_fma_f32 v46, v18, 2.0, v33\n"
  "v_fmamk_f32 v47, v18, 0x40400000, v33\n"
  "v_mul_f32 v48, 0x3ae38e39, v34\n"
  "v_mul_f32 v49, 0x3d638e39, v41\n"
  "v_lshl_add_u32 v50, v23, 2, v3\n"
  "v_and_b32 v51, 6, v42\n"
  "v_and_b32 v52, 7, v43\n"
  "v_and_b32 v39, 7, v202\n"
  "s_mul_i32 s71, s71, 18432\n"
  "s_lshl_b32 s84, s84, 7\n"
  "v_lshl_add_u32 v53, v45, 7, v44\n"
  "v_lshrrev_b32 v54, 3, v4\n"
  "v_cos_f32 v55, v33\n"
  "v_sin_f32 v200, v33\n"
  "v_cos_f32 v56, v36\n"
  "v_sin_f32 v57, v36\n"
  "v_cos_f32 v58, v46\n"
  "v_sin_f32 v18, v46\n"
  "v_cos_f32 v34, v47\n"
  "v_sin_f32 v41, v47\n"
  "v_mul_f32 v3, v201, v48\n"
  "v_mul_f32 v42, v201, v49\n"
  "v_mul_f32 v59, v5, v48\n"
  "v_mul_f32 v60, v5, v49\n"
  "v_mul_f32 v61, v6, v48\n"
  "v_mul_f32 v62, v6, v49\n"
  "v_cndmask_b32 v22, v22, v32, vcc\n"
  "v_xor_b32 v63, v50, v2\n"
  "v_xor_b32 v44, v52, v51\n"
  "v_xor_b32 v33, v39, v51\n"
  "v_lshlrev_b32 v45, 4, v37\n"
  "s_add_u32 s71, s71, s84\n"
  "v_lshl_add_u32 v36, v21, 3, v53\n"
  "v_bfe_u32 v46, v4, 3, 2\n"
  "v_lshlrev_b32 v64, 1, v23\n"
  "v_and_b32 v47, 1, v1\n"
  "v_cos_f32 v65, v3\n"
  "v_sin_f32 v66, v3\n"
  "v_cos_f32 v67, v42\n"
  "v_sin_f32 v5, v42\n"
  "v_cos_f32 v48, v59\n"
  "v_sin_f32 v6, v59\n"
  "v_cos_f32 v32, v60\n"
  "v_sin_f32 v49, v60\n"
  "v_cos_f32 v52, v61\n"
  "v_sin_f32 v39, v61\n"
  "v_cos_f32 v51, v62\n"
  "v_sin_f32 v201, v62\n"
  "v_mul_u32_u24 v21, 9280, v17\n"
  "v_xor_b32 v53, v50, v37\n"
  "v_lshlrev_b32 v23, 4, v63\n"
  "v_cmp_gt_u32 vcc, 8, v1\n"
  "v_lshlrev_b32 v68, 4, v44\n"
  "v_lshlrev_b32 v3, 4, v33\n"
  "v_xor_b32 v69, v50, v17\n"
  "v_lshl_add_u32 v42, v40, 8, v45\n"
  "v_and_b32 v70, 3, v54\n"
  "v_lshlrev_b32 v59, 3, v37\n"
  "s_add_u32 s84, s71, 2359296\n"
  "v_lshl_add_u32 v71, v17, 12, v36\n"
  "v_xor_b32 v60, v46, v64\n"
  "s_mul_i32 s72, s68, 10\n"
  "v_lshlrev_b32 v89, 4, v4\n"
  "v_lshlrev_b32 v61, 4, v0\n"
  "v_mul_u32_u24 v62, 0x8000, v47\n"
  "v_mov_b32 v72, 0\n"
  "v_mov_b32 v73, 0\n"
  "v_add_u32 v63, 140384, v21\n"
  "v_lshlrev_b32 v44, 4, v53\n"
  "v_lshl_add_u32 v33, v2, 7, v23\n"
  "v_mul_u32_u24 v50, 2320, v16\n"
  "v_mul_u32_u24 v74, 2320, v37\n"
  "v_lshl_add_u32 v75, v43, 7, v68\n"
  "v_lshl_add_u32 v76, v202, 7, v3\n"
  "v_lshlrev_b32 v77, 4, v69\n"
  "v_lshl_add_u32 v36, v70, 2, v42\n"
  "v_add_u32 v46, 158944, v59\n"
  "v_lshl_add_u32 v218, v60, 5, v71\n"
  "s_add_u32 s72, s72, 512\n"
  "v_min_u32 v64, 9, v4\n"
  "v_lshlrev_b32 v47, 2, v0\n"
  "v_lshl_or_b32 v78, v62, 16, v62\n"
  "v_cvt_pk_bf16_f32 v80, v55, v56\n"
  "v_cvt_pk_bf16_f32 v84, v200, v57\n"
  "v_cvt_pk_bf16_f32 v112, -v200, -v57\n"
  "v_cvt_pk_bf16_f32 v81, v58, v34\n"
  "v_cvt_pk_bf16_f32 v85, v18, v41\n"
  "v_cvt_pk_bf16_f32 v113, -v18, -v41\n"
  "v_lshl_add_u32 v16, v1, 7, v44\n"
  "v_add3_u32 v3, v33, v50, v63\n"
  "v_add_u32 v21, 157152, v89\n"
  "v_lshl_add_u32 v23, v38, 3, v61\n"
  "v_add_u32 v37, 140384, v74\n"
  "v_xor_b32 v42, 16, v75\n"
  "v_xor_b32 v43, 16, v76\n"
  "v_lshl_add_u32 v53, v17, 7, v77\n"
  "v_and_b32 v60, 8, v4\n"
  "v_lshl_add_u32 v62, v38, 9, v36\n"
  "v_lshl_add_u32 v55, v54, 8, v45\n"
  "v_lshl_add_u32 v79, v38, 6, v46\n"
  "v_xor_b32 v219, 32, v218\n"
  "v_add_u32 v34, s72, v64\n"
  "v_xor_b32 v82, v78, v80\n"
  "v_xor_b32 v86, v78, v84\n"
  "v_xor_b32 v114, v78, v112\n"
  "v_xor_b32 v83, v78, v81\n"
  "v_xor_b32 v87, v78, v85\n"
  "v_xor_b32 v115, v78, v113\n"
  "v_mul_f32 v56, 0x35e38e39, v65\n"
  "v_mul_f32 v57, 0x35e38e39, v66\n"
  "v_cvt_pk_bf16_f32 v68, v67, -v5\n"
  "v_mul_f32 v58, 0x35e38e39, v48\n"
  "v_mul_f32 v59, 0x35e38e39, v6\n"
  "v_cvt_pk_bf16_f32 v69, v32, -v49\n"
  "v_mul_f32 v44, 0x35e38e39, v52\n"
  "v_mul_f32 v45, 0x35e38e39, v39\n"
  "v_cvt_pk_bf16_f32 v70, v51, -v201\n"
  "v_mov_b32 v71, 0\n"
  "v_mov_b32 v27, 0\n"
  "v_mov_b32 v119, 0\n"
  "v_mov_b32 v31, 0\n"
  "v_mov_b32 v123, 0\n"
  "v_mov_b32 v11, 0\n"
  "v_mov_b32 v127, 0\n"
  "v_mov_b32 v15, 0\n"
  "v_mov_b32 v131, 0\n"
  "v_add_u32 v1, v16, v63\n"
  "v_cndmask_b32 v5, v21, v3, vcc\n"
  "v_add_u32 v6, 131072, v23\n"
  "v_add_u32 v17, 139328, v47\n"
  "v_add_u32 v18, v75, v37\n"
  "v_add_u32 v32, v42, v37\n"
  "v_add_u32 v33, v76, v37\n"
  "v_add_u32 v36, v43, v37\n"
  "v_add3_u32 v39, v53, v60, v37\n"
  "v_add_u32 v41, 16384, v62\n"
  "v_lshl_add_u32 v52, v38, 12, v55\n"
  "v_lshl_add_u32 v54, v40, 2, v79\n"
  "v_add_u32 v220, 65536, v218\n"
  "v_add_u32 v221, 65536, v219\n"
  "v_add_u32 v222, 131072, v218\n"
  "v_add_u32 v223, 131072, v219\n"
  "v_mul_u32_u24 v102, 9216, v34\n"
  "ds_write_b64 v35, v[72:73]\n"
  "ds_write_b64 v35, v[72:73] offset:8\n"
  "s_add_u32 m0, s48, 32768\n"
  "s_mov_b32 s51, 589824\n"
  "buffer_load_dwordx4 v214, s[28:31], s51 offen offset:0 lds\n"
  "buffer_load_dwordx4 v215, s[28:31], s51 offen offset:1024 lds\n"
  "buffer_load_dwordx4 v216, s[28:31], s51 offen offset:2048 lds\n"
  "buffer_load_dwordx4 v217, s[28:31], s51 offen offset:3072 lds\n"
  "s_cmp_lt_u32 %2, 144\n"
  "s_cbranch_scc1 Lgo%=\n"
  "s_cmp_lg_u32 s10, 0\n"
  "s_cbranch_scc1 Lseen%=\n"
  "s_mov_b32 s64, 262144\n"
  "s_mov_b32 s65, 29917184\n"
  "Lsee%=:\n"
  "buffer_load_dword v251, off, s[36:39], s65 sc1\n"
  "s_waitcnt vmcnt(0)\n"
  "v_cmp_ne_u32 vcc, v213, v251\n"
  "s_cmp_eq_u64 vcc, 0\n"
  "s_cbranch_scc1 Lseen%=\n"
  "s_sub_u32 s64, s64, 1\n"
  "s_cbranch_scc0 Lsee%=\n"
  "Lseen%=:\n"
  "s_barrier\n"
  "s_memrealtime s[62:63]\n"
  "s_waitcnt lgkmcnt(0)\n"
  "buffer_load_dword v253, v253, s[40:43], s61 offen\n"
  "buffer_load_dword v254, v254, s[40:43], s61 offen\n"
  "Lgate%=:\n"
  "s_memrealtime s[6:7]\n"
  "s_waitcnt lgkmcnt(0)\n"
  "s_sub_u32 s64, s6, s62\n"
  "s_cmp_lt_u32 s64, 175\n"
  "s_cbranch_scc1 Lgate%=\n"
  "Lgo%=:\n"
  "buffer_load_dwordx4 v[48:51], v61, s[24:27], s34 offen sc1\n"
  "buffer_load_dwordx4 v[64:67], v61, s[24:27], s35 offen sc1\n"
  "buffer_load_dword v3, v47, s[24:27], s70 offen sc1\n"
  "s_add_u32 m0, s48, 49152\n"
  "s_mov_b32 s51, 884736\n"
  "buffer_load_dwordx4 v214, s[28:31], s51 offen offset:0 lds\n"
  "buffer_load_dwordx4 v215, s[28:31], s51 offen offset:1024 lds\n"
  "buffer_load_dwordx4 v216, s[28:31], s51 offen offset:2048 lds\n"
  "buffer_load_dwordx4 v217, s[28:31], s51 offen offset:3072 lds\n"
  "s_add_u32 m0, s48, 65536\n"
  "s_mov_b32 s51, 1179648\n"
  "buffer_load_dwordx4 v214, s[28:31], s51 offen offset:0 lds\n"
  "buffer_load_dwordx4 v215, s[28:31], s51 offen offset:1024 lds\n"
  "buffer_load_dwordx4 v216, s[28:31], s51 offen offset:2048 lds\n"
  "buffer_load_dwordx4 v217, s[28:31], s51 offen offset:3072 lds\n"
  "s_mov_b32 s64, 262144\n"
  "s_waitcnt vmcnt(10)\n"
  "v_max3_u32 v16, v48, v49, v50\n"
  "s_waitcnt vmcnt(9)\n"
  "v_max3_u32 v16, v51, v64, v16\n"
  "v_max3_u32 v16, v65, v66, v16\n"
  "s_waitcnt vmcnt(8)\n"
  "v_max3_u32 v16, v67, v3, v16\n"
  "v_cmp_eq_u32 vcc, -1, v16\n"
  "s_cmp_eq_u64 vcc, 0\n"
  "s_cbranch_scc1 Lz2ok%=\n"
  "Lz2%=:\n"
  "buffer_load_dwordx4 v[48:51], v61, s[24:27], s34 offen sc1\n"
  "buffer_load_dwordx4 v[64:67], v61, s[24:27], s35 offen sc1\n"
  "buffer_load_dword v3, v47, s[24:27], s70 offen sc1\n"
  "s_waitcnt vmcnt(0)\n"
  "v_max3_u32 v16, v48, v49, v50\n"
  "v_max3_u32 v16, v51, v64, v16\n"
  "v_max3_u32 v16, v65, v66, v16\n"
  "v_max3_u32 v16, v67, v3, v16\n"
  "v_cmp_eq_u32 vcc, -1, v16\n"
  "s_cmp_eq_u64 vcc, 0\n"
  "s_cbranch_scc1 Lz2ok%=\n"
  "s_sub_u32 s64, s64, 1\n"
  "s_cbranch_scc0 Lz2%=\n"
  "Lz2ok%=:\n"
  "ds_write_b64 v6, v[48:49] offset:0\n"
  "ds_write_b64 v6, v[50:51] offset:8\n"
  "ds_write_b64 v6, v[64:65] offset:4128\n"
  "ds_write_b64 v6, v[66:67] offset:4136\n"
  "ds_write_b32 v17, v3\n"
  "buffer_load_dword v103, v102, s[28:31], 0 offen\n"
  "s_waitcnt lgkmcnt(0)\n"
  "s_barrier\n"
  "ds_read_b64_tr_b16 v[60:61], v19 offset:0\n"
  "ds_read_b64_tr_b16 v[62:63], v7 offset:512\n"
  "ds_read_b64_tr_b16 v[72:73], v19 offset:8\n"
  "ds_read_b64_tr_b16 v[74:75], v7 offset:520\n"
  "ds_read_b64_tr_b16 v[76:77], v7 offset:4128\n"
  "ds_read_b64_tr_b16 v[78:79], v7 offset:4640\n"
  "ds_read_b64_tr_b16 v[132:133], v7 offset:4136\n"
  "ds_read_b64_tr_b16 v[134:135], v7 offset:4648\n"
  "ds_read_b64_tr_b16 v[136:137], v22 offset:0\n"
  "ds_read_b64_tr_b16 v[138:139], v20 offset:512\n"
  "ds_read_b64_tr_b16 v[48:49], v22 offset:8\n"
  "ds_read_b64_tr_b16 v[50:51], v20 offset:520\n"
  "s_waitcnt lgkmcnt(10)\n"
  "v_mfma_f32_16x16x32_bf16 v[64:67], v[60:63], v[84:87], 0\n"
  "v_lshlrev_b32 v146, 16, v207\n"
  "v_mfma_f32_16x16x32_bf16 v[148:151], v[60:63], v[80:83], 0\n"
  "v_and_b32 v147, 0xffff0000, v207\n"
  "s_waitcnt lgkmcnt(8)\n"
  "v_mfma_f32_16x16x32_bf16 v[148:151], v[72:75], v[112:115], v[148:151]\n"
  "v_lshlrev_b32 v156, 16, v210\n"
  "v_mfma_f32_16x16x32_bf16 v[64:67], v[72:75], v[80:83], v[64:67]\n"
  "v_and_b32 v17, 0xffff0000, v212\n"
  "s_waitcnt lgkmcnt(6)\n"
  "v_mfma_f32_16x16x32_bf16 v[160:163], v[76:79], v[80:83], 0\n"
  "v_and_b32 v141, 0xffff0000, v204\n"
  "s_waitcnt lgkmcnt(2)\n"
  "v_mfma_f32_16x16x32_bf16 v[164:167], v[136:139], v[80:83], 0\n"
  "v_and_b32 v153, 0xffff0000, v208\n"
  "v_mfma_f32_16x16x32_bf16 v[168:171], v[136:139], v[84:87], 0\n"
  "v_lshlrev_b32 v142, 16, v205\n"
  "s_waitcnt lgkmcnt(0)\n"
  "v_mfma_f32_16x16x32_bf16 v[168:171], v[48:51], v[80:83], v[168:171]\n"
  "v_lshlrev_b32 v152, 16, v208\n"
  "v_mfma_f32_16x16x32_bf16 v[164:167], v[48:51], v[112:115], v[164:167]\n"
  "v_lshlrev_b32 v140, 16, v204\n"
  "v_mfma_f32_16x16x32_bf16 v[160:163], v[132:135], v[112:115], v[160:163]\n"
  "v_and_b32 v145, 0xffff0000, v206\n"
  "v_mfma_f32_16x16x32_bf16 v[20:23], v[76:79], v[84:87], 0\n"
  "v_and_b32 v155, 0xffff0000, v209\n"
  "v_and_b32 v159, 0xffff0000, v211\n"
  "v_mfma_f32_16x16x32_bf16 v[20:23], v[132:135], v[80:83], v[20:23]\n"
  "v_lshlrev_b32 v16, 16, v212\n"
  "v_lshlrev_b32 v154, 16, v209\n"
  "v_and_b32 v157, 0xffff0000, v210\n"
  "v_and_b32 v143, 0xffff0000, v205\n"
  "v_pk_mul_f32 v[62:63], v[150:151], v[56:57] op_sel_hi:[1,0]\n"
  "v_pk_fma_f32 v[62:63], v[66:67], v[56:57], v[62:63] op_sel:[0,1,0] op_sel_hi:[1,1,1] neg_lo:[1,0,0] neg_hi:[1,0,0]\n"
  "v_pk_mul_f32 v[74:75], v[66:67], v[56:57] op_sel_hi:[1,0]\n"
  "v_pk_fma_f32 v[74:75], v[150:151], v[56:57], v[74:75] op_sel:[0,1,0] op_sel_hi:[1,1,1]\n"
  "v_pk_mul_f32 v[48:49], v[20:21], v[58:59] op_sel_hi:[1,0]\n"
  "v_pk_mul_f32 v[136:137], v[160:161], v[58:59] op_sel_hi:[1,0]\n"
  "v_pk_mul_f32 v[72:73], v[64:65], v[56:57] op_sel_hi:[1,0]\n"
  "v_pk_fma_f32 v[72:73], v[148:149], v[56:57], v[72:73] op_sel:[0,1,0] op_sel_hi:[1,1,1]\n"
  "v_pk_fma_f32 v[136:137], v[20:21], v[58:59], v[136:137] op_sel:[0,1,0] op_sel_hi:[1,1,1] neg_lo:[1,0,0] neg_hi:[1,0,0]\n"
  "v_pk_mul_f32 v[76:77], v[164:165], v[44:45] op_sel_hi:[1,0]\n"
  "v_pk_fma_f32 v[76:77], v[168:169], v[44:45], v[76:77] op_sel:[0,1,0] op_sel_hi:[1,1,1] neg_lo:[1,0,0] neg_hi:[1,0,0]\n"
  "v_pk_mul_f32 v[78:79], v[166:167], v[44:45] op_sel_hi:[1,0]\n"
  "v_pk_mul_f32 v[50:51], v[22:23], v[58:59] op_sel_hi:[1,0]\n"
  "v_pk_mul_f32 v[60:61], v[148:149], v[56:57] op_sel_hi:[1,0]\n"
  "v_pk_fma_f32 v[60:61], v[64:65], v[56:57], v[60:61] op_sel:[0,1,0] op_sel_hi:[1,1,1] neg_lo:[1,0,0] neg_hi:[1,0,0]\n"
  "v_cvt_pk_bf16_f32 v116, v60, v72\n"
  "v_pk_mul_f32 v[82:83], v[170:171], v[44:45] op_sel_hi:[1,0]\n"
  "v_pk_fma_f32 v[82:83], v[166:167], v[44:45], v[82:83] op_sel:[0,1,0] op_sel_hi:[1,1,1]\n"
  "v_pk_mul_f32 v[80:81], v[168:169], v[44:45] op_sel_hi:[1,0]\n"
  "v_pk_fma_f32 v[50:51], v[162:163], v[58:59], v[50:51] op_sel:[0,1,0] op_sel_hi:[1,1,1]\n"
  "v_pk_fma_f32 v[80:81], v[164:165], v[44:45], v[80:81] op_sel:[0,1,0] op_sel_hi:[1,1,1]\n"
  "v_cvt_pk_bf16_f32 v118, v76, v80\n"
  "v_pk_fma_f32 v[48:49], v[160:161], v[58:59], v[48:49] op_sel:[0,1,0] op_sel_hi:[1,1,1]\n"
  "v_cvt_pk_bf16_f32 v117, v136, v48\n"
  "v_cvt_pk_bf16_f32 v122, v77, v81\n"
  "s_nop 0\n"
  "v_mfma_f32_16x16x32_bf16 v[84:87], v[116:119], v[68:71], 0\n"
  "v_cvt_pk_bf16_f32 v121, v137, v49\n"
  "v_mfma_f32_16x16x32_bf16 v[112:115], v[116:119], v[24:27], 0\n"
  "v_cvt_pk_bf16_f32 v120, v61, v73\n"
  "v_cvt_pk_bf16_f32 v124, v62, v74\n"
  "s_nop 0\n"
  "v_mfma_f32_16x16x32_bf16 v[64:67], v[120:123], v[68:71], 0\n"
  "v_cvt_pk_bf16_f32 v128, v63, v75\n"
  "v_mfma_f32_16x16x32_bf16 v[112:115], v[120:123], v[28:31], v[112:115]\n"
  "ds_write_b128 v1, v[84:87] offset:0\n"
  "v_pk_fma_f32 v[78:79], v[170:171], v[44:45], v[78:79] op_sel:[0,1,0] op_sel_hi:[1,1,1] neg_lo:[1,0,0] neg_hi:[1,0,0]\n"
  "v_cvt_pk_bf16_f32 v126, v78, v82\n"
  "v_pk_mul_f32 v[138:139], v[162:163], v[58:59] op_sel_hi:[1,0]\n"
  "v_pk_fma_f32 v[138:139], v[22:23], v[58:59], v[138:139] op_sel:[0,1,0] op_sel_hi:[1,1,1] neg_lo:[1,0,0] neg_hi:[1,0,0]\n"
  "v_cvt_pk_bf16_f32 v129, v139, v51\n"
  "v_cvt_pk_bf16_f32 v125, v138, v50\n"
  "v_cvt_pk_bf16_f32 v130, v79, v83\n"
  "s_nop 0\n"
  "v_mfma_f32_16x16x32_bf16 v[132:135], v[124:127], v[68:71], 0\n"
  "ds_write_b128 v1, v[64:67] offset:2320\n"
  "v_mfma_f32_16x16x32_bf16 v[24:27], v[128:131], v[68:71], 0\n"
  "v_lshlrev_b32 v144, 16, v206\n"
  "v_mfma_f32_16x16x32_bf16 v[112:115], v[124:127], v[8:11], v[112:115]\n"
  "v_lshlrev_b32 v158, 16, v211\n"
  "v_mfma_f32_16x16x32_bf16 v[112:115], v[128:131], v[12:15], v[112:115]\n"
  "s_nop 1\n"
  "ds_write_b128 v1, v[132:135] offset:4640\n"
  "s_nop 0\n"
  "ds_write_b128 v1, v[24:27] offset:6960\n"
  "s_nop 2\n"
  "ds_write_b128 v5, v[112:115] offset:2048\n"
  "s_waitcnt lgkmcnt(0)\n"
  "s_barrier\n"
  "ds_read_b128 v[56:59], v18\n"
  "ds_read_b128 v[60:63], v32\n"
  "ds_read_b128 v[116:119], v33\n"
  "ds_read_b128 v[120:123], v36\n"
  "ds_read_b64 v[6:7], v39 offset:2048\n"
  "s_waitcnt lgkmcnt(4)\n"
  "v_pk_add_f32 v[56:57], v[56:57], v[140:141]\n"
  "v_pk_mul_f32 v[42:43], v[56:57], v[56:57]\n"
  "v_pk_add_f32 v[58:59], v[58:59], v[142:143]\n"
  "v_pk_fma_f32 v[42:43], v[58:59], v[58:59], v[42:43]\n"
  "v_pk_add_f32 v[44:45], v[58:59], v[56:57]\n"
  "s_waitcnt lgkmcnt(3)\n"
  "v_pk_add_f32 v[60:61], v[60:61], v[144:145]\n"
  "v_pk_fma_f32 v[42:43], v[60:61], v[60:61], v[42:43]\n"
  "v_pk_add_f32 v[44:45], v[60:61], v[44:45]\n"
  "s_waitcnt lgkmcnt(2)\n"
  "v_pk_add_f32 v[116:117], v[116:117], v[152:153]\n"
  "v_pk_add_f32 v[118:119], v[118:119], v[154:155]\n"
  "s_waitcnt lgkmcnt(1)\n"
  "v_pk_add_f32 v[122:123], v[122:123], v[158:159]\n"
  "v_pk_add_f32 v[62:63], v[62:63], v[146:147]\n"
  "v_pk_fma_f32 v[42:43], v[62:63], v[62:63], v[42:43]\n"
  "v_pk_add_f32 v[44:45], v[62:63], v[44:45]\n"
  "v_pk_add_f32 v[44:45], v[116:117], v[44:45]\n"
  "v_pk_add_f32 v[44:45], v[118:119], v[44:45]\n"
  "v_pk_fma_f32 v[42:43], v[116:117], v[116:117], v[42:43]\n"
  "v_pk_fma_f32 v[42:43], v[118:119], v[118:119], v[42:43]\n"
  "v_pk_add_f32 v[120:121], v[120:121], v[156:157]\n"
  "v_pk_add_f32 v[44:45], v[120:121], v[44:45]\n"
  "v_pk_fma_f32 v[42:43], v[120:121], v[120:121], v[42:43]\n"
  "v_pk_add_f32 v[44:45], v[122:123], v[44:45]\n"
  "v_pk_fma_f32 v[42:43], v[122:123], v[122:123], v[42:43]\n"
  "s_waitcnt lgkmcnt(0)\n"
  "v_pk_add_f32 v[6:7], v[6:7], v[16:17]\n"
  "v_pk_fma_f32 v[42:43], v[6:7], v[6:7], v[42:43]\n"
  "v_add_f32 v1, v42, v43\n"
  "v_pk_add_f32 v[44:45], v[6:7], v[44:45]\n"
  "v_add_f32 v3, v44, v45\n"
  "s_nop 1\n"
  "v_permlane32_swap_b32 v3, v1\n"
  "v_add_f32 v5, v3, v1\n"
  "v_cvt_pk_bf16_f32 v8, v56, v57\n"
  "v_cvt_pk_bf16_f32 v10, v60, v61\n"
  "v_add_f32_dpp v40, v5, v5 row_ror:8 row_mask:0xf bank_mask:0xf\n"
  "v_mov_b32 v47, v40\n"
  "v_cvt_pk_bf16_f32 v11, v62, v63\n"
  "s_nop 0\n"
  "v_permlane16_swap_b32 v40, v47\n"
  "v_add_f32 v53, v40, v47\n"
  "ds_write_b32 v54, v53\n"
  "v_cvt_pk_bf16_f32 v13, v118, v119\n"
  "v_cvt_pk_bf16_f32 v55, v6, v7\n"
  "v_cvt_pk_bf16_f32 v15, v122, v123\n"
  "v_cvt_pk_bf16_f32 v14, v120, v121\n"
  "v_cvt_pk_bf16_f32 v12, v116, v117\n"
  "v_cvt_pk_bf16_f32 v9, v58, v59\n"
  "buffer_store_dwordx4 v[8:11], v52, s[36:39], s84 offen sc1\n"
  "s_waitcnt vmcnt(2) lgkmcnt(0)\n"
  "s_barrier\n"
  "ds_read_b64 v[42:43], v46 offset:0\n"
  "ds_read_b64 v[44:45], v46 offset:64\n"
  "ds_read_b64 v[200:201], v46 offset:128\n"
  "ds_read_b64 v[16:17], v46 offset:192\n"
  "buffer_store_dwordx4 v[12:15], v52, s[36:39], s84 offen offset:2048 sc1\n"
  "buffer_store_dword v55, v41, s[36:39], s84 offen sc1\n"
  "s_waitcnt lgkmcnt(2)\n"
  "v_pk_add_f32 v[42:43], v[42:43], v[44:45]\n"
  "s_waitcnt lgkmcnt(0)\n"
  "v_pk_add_f32 v[200:201], v[200:201], v[16:17]\n"
  "v_pk_add_f32 v[42:43], v[42:43], v[200:201]\n"
  "v_mul_f32 v1, 0x3ae38e39, v42\n"
  "v_mul_f32 v3, 0x3ae38e39, v43\n"
  "v_fma_f32 v5, -v1, v1, v3\n"
  "v_max_f32 v40, 0, v5\n"
  "v_cvt_pk_bf16_f32 v53, v40, v40\n"
  "v_lshlrev_b32 v54, 16, v53\n"
  "v_add_f32 v202, 0x3727c5ac, v54\n"
  "v_cvt_pk_bf16_f32 v8, v202, v202\n"
  "v_cvt_pk_bf16_f32 v9, v1, v1\n"
  "v_lshlrev_b32 v10, 16, v8\n"
  "v_sqrt_f32 v11, v10\n"
  "v_lshlrev_b32 v12, 16, v9\n"
  "v_cvt_pk_bf16_f32 v13, v11, v11\n"
  "v_lshlrev_b32 v14, 16, v13\n"
  "v_rcp_f32 v15, v14\n"
  "s_nop 0\n"
  "v_mov_b32 v200, v15\n"
  "v_mul_f32_e64 v201, -v12, v15\n"
  "v_pk_fma_f32 v[56:57], v[56:57], v[200:201], v[200:201] op_sel:[0,0,1] op_sel_hi:[1,0,1]\n"
  "v_cvt_pk_bf16_f32 v16, v56, v57\n"
  "v_pk_fma_f32 v[58:59], v[58:59], v[200:201], v[200:201] op_sel:[0,0,1] op_sel_hi:[1,0,1]\n"
  "v_pk_fma_f32 v[60:61], v[60:61], v[200:201], v[200:201] op_sel:[0,0,1] op_sel_hi:[1,0,1]\n"
  "v_cvt_pk_bf16_f32 v17, v58, v59\n"
  "v_cvt_pk_bf16_f32 v18, v60, v61\n"
  "v_pk_fma_f32 v[62:63], v[62:63], v[200:201], v[200:201] op_sel:[0,0,1] op_sel_hi:[1,0,1]\n"
  "v_cvt_pk_bf16_f32 v19, v62, v63\n"
  "buffer_store_dwordx4 v[16:19], v52, s[36:39], s71 offen offset:0 sc1\n"
  "v_pk_fma_f32 v[116:117], v[116:117], v[200:201], v[200:201] op_sel:[0,0,1] op_sel_hi:[1,0,1]\n"
  "v_cvt_pk_bf16_f32 v20, v116, v117\n"
  "v_pk_fma_f32 v[118:119], v[118:119], v[200:201], v[200:201] op_sel:[0,0,1] op_sel_hi:[1,0,1]\n"
  "v_cvt_pk_bf16_f32 v21, v118, v119\n"
  "v_pk_fma_f32 v[120:121], v[120:121], v[200:201], v[200:201] op_sel:[0,0,1] op_sel_hi:[1,0,1]\n"
  "v_pk_fma_f32 v[122:123], v[122:123], v[200:201], v[200:201] op_sel:[0,0,1] op_sel_hi:[1,0,1]\n"
  "v_cvt_pk_bf16_f32 v22, v120, v121\n"
  "v_cvt_pk_bf16_f32 v23, v122, v123\n"
  "buffer_store_dwordx4 v[20:23], v52, s[36:39], s71 offen offset:2048 sc1\n"
  "v_pk_fma_f32 v[6:7], v[6:7], v[200:201], v[200:201] op_sel:[0,0,1] op_sel_hi:[1,0,1]\n"
  "v_cvt_pk_bf16_f32 v1, v6, v7\n"
  "buffer_store_dword v1, v41, s[36:39], s71 offen offset:0 sc1\n"
  "s_add_u32 m0, s48, 81920\n"
  "ds_read_b64_tr_b16 a[0:1], v218 offset:0\n"
  "ds_read_b64_tr_b16 a[2:3], v218 offset:512\n"
  "ds_read_b64_tr_b16 a[4:5], v219 offset:0\n"
  "s_mov_b32 s51, 1474560\n"
  "ds_read_b64_tr_b16 a[6:7], v219 offset:512\n"
  "ds_read_b64_tr_b16 a[8:9], v218 offset:8192\n"
  "s_nop 0\n"
  "s_cbranch_execz Ltramp%=\n"
  "buffer_load_dwordx4 v214, s[28:31], s51 offen offset:0 lds\n"
  "ds_read_b64_tr_b16 a[10:11], v218 offset:8704\n"
  "ds_read_b64_tr_b16 a[12:13], v219 offset:8192\n"
  "buffer_load_dwordx4 v215, s[28:31], s51 offen offset:1024 lds\n"
  "ds_read_b64_tr_b16 a[14:15], v219 offset:8704\n"
  "ds_read_b64_tr_b16 a[16:17], v218 offset:16384\n"
  "buffer_load_dwordx4 v216, s[28:31], s51 offen offset:2048 lds\n"
  "ds_read_b64_tr_b16 a[18:19], v218 offset:16896\n"
  "ds_read_b64_tr_b16 a[20:21], v219 offset:16384\n"
  "ds_read_b64_tr_b16 a[22:23], v219 offset:16896\n"
  "buffer_load_dwordx4 v217, s[28:31], s51 offen offset:3072 lds\n"
  "ds_read_b64_tr_b16 a[24:25], v218 offset:24576\n"
  "ds_read_b64_tr_b16 a[26:27], v218 offset:25088\n"
  "s_add_u32 m0, s48, 98304\n"
  "ds_read_b64_tr_b16 a[28:29], v219 offset:24576\n"
  "ds_read_b64_tr_b16 a[30:31], v219 offset:25088\n"
  "s_mov_b32 s51, 1769472\n"
  "ds_read_b64_tr_b16 a[32:33], v218 offset:32768\n"
  "ds_read_b64_tr_b16 a[34:35], v218 offset:33280\n"
  "buffer_load_dwordx4 v214, s[28:31], s51 offen offset:0 lds\n"
  "ds_read_b64_tr_b16 a[36:37], v219 offset:32768\n"
  "ds_read_b64_tr_b16 a[38:39], v219 offset:33280\n"
  "buffer_load_dwordx4 v215, s[28:31], s51 offen offset:1024 lds\n"
  "ds_read_b64_tr_b16 a[40:41], v218 offset:40960\n"
  "ds_read_b64_tr_b16 a[42:43], v218 offset:41472\n"
  "ds_read_b64_tr_b16 a[44:45], v219 offset:40960\n"
  "buffer_load_dwordx4 v216, s[28:31], s51 offen offset:2048 lds\n"
  "ds_read_b64_tr_b16 a[46:47], v219 offset:41472\n"
  "ds_read_b64_tr_b16 a[48:49], v218 offset:49152\n"
  "buffer_load_dwordx4 v217, s[28:31], s51 offen offset:3072 lds\n"
  "ds_read_b64_tr_b16 a[50:51], v218 offset:49664\n"
  "ds_read_b64_tr_b16 a[52:53], v219 offset:49152\n"
  "s_add_u32 m0, s48, 114688\n"
  "ds_read_b64_tr_b16 a[54:55], v219 offset:49664\n"
  "ds_read_b64_tr_b16 a[56:57], v218 offset:57344\n"
  "s_mov_b32 s51, 2064384\n"
  "ds_read_b64_tr_b16 a[58:59], v218 offset:57856\n"
  "ds_read_b64_tr_b16 a[60:61], v219 offset:57344\n"
  "ds_read_b64_tr_b16 a[62:63], v219 offset:57856\n"
  "buffer_load_dwordx4 v214, s[28:31], s51 offen offset:0 lds\n"
  "ds_read_b64_tr_b16 a[64:65], v220 offset:0\n"
  "ds_read_b64_tr_b16 a[66:67], v220 offset:512\n"
  "buffer_load_dwordx4 v215, s[28:31], s51 offen offset:1024 lds\n"
  "ds_read_b64_tr_b16 a[68:69], v221 offset:0\n"
  "ds_read_b64_tr_b16 a[70:71], v221 offset:512\n"
  "buffer_load_dwordx4 v216, s[28:31], s51 offen offset:2048 lds\n"
  "ds_read_b64_tr_b16 a[72:73], v220 offset:8192\n"
  "ds_read_b64_tr_b16 a[74:75], v220 offset:8704\n"
  "buffer_load_dwordx4 v217, s[28:31], s51 offen offset:3072 lds\n"
  "ds_read_b64_tr_b16 a[76:77], v221 offset:8192\n"
  "ds_read_b64_tr_b16 a[78:79], v221 offset:8704\n"
  "s_lshr_b32 s34, %2, 0\n"
  "s_lshl_b32 s34, s34, 6\n"
  "s_add_u32 s34, s34, 7143424\n"
  "s_sub_u32 s35, 4, s66\n"
  "s_and_b32 s35, s35, 3\n"
  "s_min_u32 s70, s68, 2\n"
  "s_mul_i32 s71, s68, 18\n"
  "s_add_u32 s70, s70, s71\n"
  "s_lshl_b32 s84, s68, 2\n"
  "s_lshr_b32 s84, 0xCA69521, s84\n"
  "s_bfm_b32 s72, s35, 0\n"
  "s_and_b32 s72, s72, s84\n"
  "s_bcnt1_i32_b32 s72, s72\n"
  "s_mul_i32 s73, s35, 5\n"
  "s_add_u32 s73, s73, s70\n"
  "s_sub_u32 s73, s73, s72\n"
  "s_cmp_lt_u32 s68, 2\n"
  "s_cselect_b32 s11, 1, 0\n"
  "s_add_u32 s74, s70, s11\n"
  "s_add_u32 s74, s74, 18\n"
  "s_lshl2_add_u32 s75, s69, s10\n"
  "s_lshl_b32 s75, s75, 10\n"
  "s_mul_i32 s52, s73, 18432\n"
  "s_add_u32 s75, s75, 4718592\n"
  "v_lshrrev_b32 v1, 1, v4\n"
  "s_mul_i32 s53, s70, 18432\n"
  "s_lshl3_add_u32 s55, s52, s75\n"
  "v_min_u32 v3, 18, v1\n"
  "s_mul_i32 s54, s74, 18432\n"
  "s_lshl3_add_u32 s56, s53, s75\n"
  "v_add_u32 v200, s73, v3\n"
  "s_lshl3_add_u32 s57, s54, s75\n"
  "v_cmp_le_u32 vcc, s74, v200\n"
  "s_add_u32 s35, s11, 18\n"
  "v_subrev_u32 v201, s35, v200\n"
  "s_and_b32 s49, s10, 1\n"
  "v_cndmask_b32 v202, v200, v201, vcc\n"
  "s_lshl_b32 s49, s49, 10\n"
  "v_lshl_add_u32 v5, v202, 1, v2\n"
  "s_add_u32 s49, s49, 16384\n"
  "v_lshlrev_b32 v4, 6, v5\n"
  "s_lshl_b32 s50, s10, 10\n"
  "v_mov_b32 v104, 1\n"
  "v_add_u32 v105, 7143424, v4\n"
  "v_add_u32 v92, s48, v89\n"
  "v_add_u32 v93, s49, v89\n"
  "v_mov_b32 v88, 0xbdd2d3e8\n"
  "v_add_u32 v90, 0x10000, v89\n"
  "v_add_u32 v91, 0x20000, v89\n"
  "s_lshl_b32 s70, s10, 28\n"
  "s_getpc_b64 s[22:23]\n"
  "Lpc1%=:\n"
  "s_sub_u32 s22, s22, Lpc1%=+4096-Lstub%=\n"
  "s_subb_u32 s23, s23, 0\n"
  "s_cmp_eq_u32 s11, 1\n"
  "s_cselect_b32 s35, 12, 4\n"
  "s_cselect_b32 s71, 21, 12\n"
  "s_add_u32 s35, s35, s10\n"
  "s_min_u32 s64, s35, s71\n"
  "s_lshl_b32 s64, s64, 12\n"
  "s_add_u32 s6, s22, s64\n"
  "s_addc_u32 s7, s23, 0\n"
  "s_mov_b64 exec, 0\n"
  "s_swappc_b64 s[8:9], s[6:7]\n"
  "s_mov_b64 exec, -1\n"
  "s_add_u32 s35, s35, 4\n"
  "s_waitcnt vmcnt(12) lgkmcnt(0)\n"
  "s_barrier\n"
  "s_cmp_lg_u32 s10, 0\n"
  "s_cbranch_scc1 Lnosig%=\n"
  "v_lshrrev_b32 v251, 2, v89\n"
  "s_mov_b64 exec, 0xffff\n"
  "buffer_store_dword v104, v251, s[24:27], s34 offen sc1\n"
  "s_mov_b64 exec, -1\n"
  "s_lshl_b32 s64, %2, 6\n"
  "s_add_u32 s64, s64, 29884416\n"
  "v_xor_b32 v213, 0x80000000, v213\n"
  "v_lshrrev_b32 v251, 2, v89\n"
  "s_mov_b64 exec, 0xffff\n"
  "buffer_store_dword v213, v251, s[36:39], s64 offen sc1\n"
  "s_mov_b64 exec, -1\n"
  "Lnosig%=:\n"
  "s_cmp_ge_u32 s67, 9\n"
  "s_cbranch_scc1 Lend%=\n"
  "s_add_u32 m0, s48, 131072\n"
  "s_mov_b32 s51, 2359296\n"
  "buffer_load_dwordx4 v214, s[28:31], s51 offen offset:0 lds\n"
  "buffer_load_dwordx4 v215, s[28:31], s51 offen offset:1024 lds\n"
  "buffer_load_dwordx4 v216, s[28:31], s51 offen offset:2048 lds\n"
  "buffer_load_dwordx4 v217, s[28:31], s51 offen offset:3072 lds\n"
  "s_add_u32 m0, s48, 147456\n"
  "s_mov_b32 s51, 2654208\n"
  "buffer_load_dwordx4 v214, s[28:31], s51 offen offset:0 lds\n"
  "buffer_load_dwordx4 v215, s[28:31], s51 offen offset:1024 lds\n"
  "buffer_load_dwordx4 v216, s[28:31], s51 offen offset:2048 lds\n"
  "buffer_load_dwordx4 v217, s[28:31], s51 offen offset:3072 lds\n"
  "s_add_u32 m0, s48, 32768\n"
  "s_mov_b32 s51, 2949120\n"
  "buffer_load_dwordx4 v214, s[28:31], s51 offen offset:0 lds\n"
  "buffer_load_dwordx4 v215, s[28:31], s51 offen offset:1024 lds\n"
  "buffer_load_dwordx4 v216, s[28:31], s51 offen offset:2048 lds\n"
  "buffer_load_dwordx4 v217, s[28:31], s51 offen offset:3072 lds\n"
  "s_min_u32 s64, s35, s71\n"
  "s_lshl_b32 s64, s64, 12\n"
  "s_add_u32 s6, s22, s64\n"
  "s_addc_u32 s7, s23, 0\n"
  "s_mov_b64 exec, 0\n"
  "s_swappc_b64 s[8:9], s[6:7]\n"
  "s_mov_b64 exec, -1\n"
  "s_add_u32 s35, s35, 4\n"
  "s_waitcnt vmcnt(4)\n"
  "s_barrier\n"
  "s_add_u32 m0, s48, 49152\n"
  "ds_read_b64_tr_b16 a[80:81], v220 offset:16384\n"
  "ds_read_b64_tr_b16 a[82:83], v220 offset:16896\n"
  "ds_read_b64_tr_b16 a[84:85], v221 offset:16384\n"
  "ds_read_b64_tr_b16 a[86:87], v221 offset:16896\n"
  "s_mov_b32 s51, 3244032\n"
  "ds_read_b64_tr_b16 a[88:89], v220 offset:24576\n"
  "ds_read_b64_tr_b16 a[90:91], v220 offset:25088\n"
  "ds_read_b64_tr_b16 a[92:93], v221 offset:24576\n"
  "buffer_load_dwordx4 v214, s[28:31], s51 offen offset:0 lds\n"
  "ds_read_b64_tr_b16 a[94:95], v221 offset:25088\n"
  "ds_read_b64_tr_b16 a[96:97], v220 offset:32768\n"
  "ds_read_b64_tr_b16 a[98:99], v220 offset:33280\n"
  "buffer_load_dwordx4 v215, s[28:31], s51 offen offset:1024 lds\n"
  "ds_read_b64_tr_b16 a[100:101], v221 offset:32768\n"
  "ds_read_b64_tr_b16 a[102:103], v221 offset:33280\n"
  "ds_read_b64_tr_b16 a[104:105], v220 offset:40960\n"
  "ds_read_b64_tr_b16 a[106:107], v220 offset:41472\n"
  "buffer_load_dwordx4 v216, s[28:31], s51 offen offset:2048 lds\n"
  "ds_read_b64_tr_b16 a[108:109], v221 offset:40960\n"
  "ds_read_b64_tr_b16 a[110:111], v221 offset:41472\n"
  "ds_read_b64_tr_b16 a[112:113], v220 offset:49152\n"
  "buffer_load_dwordx4 v217, s[28:31], s51 offen offset:3072 lds\n"
  "ds_read_b64_tr_b16 a[114:115], v220 offset:49664\n"
  "ds_read_b64_tr_b16 a[116:117], v221 offset:49152\n"
  "ds_read_b64_tr_b16 a[118:119], v221 offset:49664\n"
  "s_add_u32 m0, s48, 65536\n"
  "ds_read_b64_tr_b16 a[120:121], v220 offset:57344\n"
  "ds_read_b64_tr_b16 a[122:123], v220 offset:57856\n"
  "ds_read_b64_tr_b16 a[124:125], v221 offset:57344\n"
  "ds_read_b64_tr_b16 a[126:127], v221 offset:57856\n"
  "s_mov_b32 s51, 3538944\n"
  "ds_read_b64_tr_b16 a[128:129], v222 offset:0\n"
  "ds_read_b64_tr_b16 a[130:131], v222 offset:512\n"
  "ds_read_b64_tr_b16 a[132:133], v223 offset:0\n"
  "buffer_load_dwordx4 v214, s[28:31], s51 offen offset:0 lds\n"
  "ds_read_b64_tr_b16 a[134:135], v223 offset:512\n"
  "ds_read_b64_tr_b16 a[136:137], v222 offset:8192\n"
  "ds_read_b64_tr_b16 a[138:139], v222 offset:8704\n"
  "buffer_load_dwordx4 v215, s[28:31], s51 offen offset:1024 lds\n"
  "ds_read_b64_tr_b16 a[140:141], v223 offset:8192\n"
  "ds_read_b64_tr_b16 a[142:143], v223 offset:8704\n"
  "ds_read_b64_tr_b16 a[144:145], v222 offset:16384\n"
  "ds_read_b64_tr_b16 a[146:147], v222 offset:16896\n"
  "buffer_load_dwordx4 v216, s[28:31], s51 offen offset:2048 lds\n"
  "ds_read_b64_tr_b16 a[148:149], v223 offset:16384\n"
  "ds_read_b64_tr_b16 a[150:151], v223 offset:16896\n"
  "ds_read_b64_tr_b16 a[152:153], v222 offset:24576\n"
  "buffer_load_dwordx4 v217, s[28:31], s51 offen offset:3072 lds\n"
  "ds_read_b64_tr_b16 a[154:155], v222 offset:25088\n"
  "ds_read_b64_tr_b16 a[156:157], v223 offset:24576\n"
  "ds_read_b64_tr_b16 a[158:159], v223 offset:25088\n"
  "s_waitcnt vmcnt(4) lgkmcnt(0)\n"
  "s_barrier\n"
  "s_add_u32 m0, s48, 81920\n"
  "ds_read_b64_tr_b16 a[160:161], v218 offset:32768\n"
  "s_mov_b32 s51, 3833856\n"
  "ds_read_b64_tr_b16 a[162:163], v218 offset:33280\n"
  "buffer_load_dwordx4 v214, s[28:31], s51 offen offset:0 lds\n"
  "ds_read_b64_tr_b16 a[164:165], v219 offset:32768\n"
  "buffer_load_dwordx4 v215, s[28:31], s51 offen offset:1024 lds\n"
  "ds_read_b64_tr_b16 a[166:167], v219 offset:33280\n"
  "buffer_load_dwordx4 v216, s[28:31], s51 offen offset:2048 lds\n"
  "ds_read_b64_tr_b16 a[168:169], v218 offset:40960\n"
  "buffer_load_dwordx4 v217, s[28:31], s51 offen offset:3072 lds\n"
  "buffer_load_dword v106, v105, s[24:27], s70 offen sc1\n"
  "ds_read_b64_tr_b16 a[170:171], v218 offset:41472\n"
  "s_add_u32 m0, s48, 98304\n"
  "ds_read_b64_tr_b16 a[172:173], v219 offset:40960\n"
  "s_mov_b32 s51, 4128768\n"
  "ds_read_b64_tr_b16 a[174:175], v219 offset:41472\n"
  "buffer_load_dwordx4 v214, s[28:31], s51 offen offset:0 lds\n"
  "buffer_load_dwordx4 v215, s[28:31], s51 offen offset:1024 lds\n"
  "ds_read_b64_tr_b16 a[176:177], v218 offset:49152\n"
  "buffer_load_dwordx4 v216, s[28:31], s51 offen offset:2048 lds\n"
  "ds_read_b64_tr_b16 a[178:179], v218 offset:49664\n"
  "buffer_load_dwordx4 v217, s[28:31], s51 offen offset:3072 lds\n"
  "ds_read_b64_tr_b16 a[180:181], v219 offset:49152\n"
  "s_add_u32 m0, s48, 114688\n"
  "ds_read_b64_tr_b16 a[182:183], v219 offset:49664\n"
  "s_mov_b32 s51, 4423680\n"
  "ds_read_b64_tr_b16 a[184:185], v218 offset:57344\n"
  "buffer_load_dwordx4 v214, s[28:31], s51 offen offset:0 lds\n"
  "ds_read_b64_tr_b16 a[186:187], v218 offset:57856\n"
  "buffer_load_dwordx4 v215, s[28:31], s51 offen offset:1024 lds\n"
  "ds_read_b64_tr_b16 a[188:189], v219 offset:57344\n"
  "buffer_load_dwordx4 v216, s[28:31], s51 offen offset:2048 lds\n"
  "ds_read_b64_tr_b16 a[190:191], v219 offset:57856\n"
  "buffer_load_dwordx4 v217, s[28:31], s51 offen offset:3072 lds\n"
  "s_waitcnt vmcnt(8) lgkmcnt(0)\n"
  "s_cmp_lg_u32 s10, 0\n"
  "s_cbranch_scc1 Lpollrc%=\n"
  "v_cmp_gt_u32 vcc, 1, v106\n"
  "s_and_b64 vcc, vcc, -1\n"
  "s_cbranch_scc1 Lpollc%=\n"
  "Lpollrc%=:\n"
  "s_barrier\n"
  "s_add_u32 m0, s48, 0\n"
  "s_nop 0\n"
  "buffer_load_dwordx4 v92, s[36:39], s52 offen offset:0 sc1 lds\n"
  "buffer_load_dwordx4 v92, s[36:39], s52 offen offset:1024 sc1 lds\n"
  "buffer_load_dwordx4 v92, s[36:39], s52 offen offset:2048 sc1 lds\n"
  "buffer_load_dwordx4 v92, s[36:39], s52 offen offset:3072 sc1 lds\n"
  "s_add_u32 m0, s49, 0\n"
  "s_nop 0\n"
  "buffer_load_dwordx4 v93, s[36:39], s52 offen sc1 lds\n"
  "s_add_u32 s52, s52, 18432\n"
  "s_cmp_eq_u32 s52, s54\n"
  "s_cselect_b32 s52, s53, s52\n"
  "s_add_u32 m0, s48, 131072\n"
  "ds_read_b64_tr_b16 a[192:193], v220 offset:0\n"
  "ds_read_b64_tr_b16 a[194:195], v220 offset:512\n"
  "s_mov_b32 s51, 4718592\n"
  "ds_read_b64_tr_b16 a[196:197], v221 offset:0\n"
  "buffer_load_dwordx4 v214, s[28:31], s51 offen offset:0 lds\n"
  "ds_read_b64_tr_b16 a[198:199], v221 offset:512\n"
  "buffer_load_dwordx4 v215, s[28:31], s51 offen offset:1024 lds\n"
  "ds_read_b64_tr_b16 a[200:201], v220 offset:8192\n"
  "ds_read_b64_tr_b16 a[202:203], v220 offset:8704\n"
  "buffer_load_dwordx4 v216, s[28:31], s51 offen offset:2048 lds\n"
  "ds_read_b64_tr_b16 a[204:205], v221 offset:8192\n"
  "buffer_load_dwordx4 v217, s[28:31], s51 offen offset:3072 lds\n"
  "ds_read_b64_tr_b16 a[206:207], v221 offset:8704\n"
  "s_add_u32 m0, s48, 147456\n"
  "ds_read_b64_tr_b16 a[208:209], v220 offset:16384\n"
  "ds_read_b64_tr_b16 a[210:211], v220 offset:16896\n"
  "s_mov_b32 s51, 5013504\n"
  "ds_read_b64_tr_b16 a[212:213], v221 offset:16384\n"
  "buffer_load_dwordx4 v214, s[28:31], s51 offen offset:0 lds\n"
  "ds_read_b64_tr_b16 a[214:215], v221 offset:16896\n"
  "buffer_load_dwordx4 v215, s[28:31], s51 offen offset:1024 lds\n"
  "ds_read_b64_tr_b16 a[216:217], v220 offset:24576\n"
  "ds_read_b64_tr_b16 a[218:219], v220 offset:25088\n"
  "buffer_load_dwordx4 v216, s[28:31], s51 offen offset:2048 lds\n"
  "ds_read_b64_tr_b16 a[220:221], v221 offset:24576\n"
  "buffer_load_dwordx4 v217, s[28:31], s51 offen offset:3072 lds\n"
  "ds_read_b64_tr_b16 a[222:223], v221 offset:25088\n"
  "s_min_u32 s64, s35, s71\n"
  "s_lshl_b32 s64, s64, 12\n"
  "s_add_u32 s6, s22, s64\n"
  "s_addc_u32 s7, s23, 0\n"
  "s_mov_b64 exec, 0\n"
  "s_swappc_b64 s[8:9], s[6:7]\n"
  "s_mov_b64 exec, -1\n"
  "s_add_u32 s35, s35, 4\n"
  "s_waitcnt vmcnt(8) lgkmcnt(0)\n"
  "s_barrier\n"
  "s_cmp_eq_u32 s11, 1\n"
  "s_cbranch_scc1 Lp19%=\n"
  "Lp18%=:\n"
  "ds_read_b128 v[32:35], v89 offset:0\n"
  "ds_read_b128 v[36:39], v89 offset:1024\n"
  "ds_read_b128 v[40:43], v89 offset:2048\n"
  "ds_read_b128 v[44:47], v89 offset:3072\n"
  "ds_read_b128 v[48:51], v89 offset:4096\n"
  "ds_read_b128 v[52:55], v89 offset:5120\n"
  "s_waitcnt lgkmcnt(0)\n"
  "v_mfma_f32_16x16x32_bf16 v[0:3], a[0:3], v[32:35], 0\n"
  "s_add_u32 m0, s48, 32768\n"
  "ds_read_b128 v[56:59], v89 offset:6144\n"
  "ds_read_b128 v[60:63], v89 offset:7168\n"
  "v_mfma_f32_16x16x32_bf16 v[0:3], a[16:19], v[36:39], v[0:3]\n"
  "buffer_load_dwordx4 v92, s[36:39], s52 offen offset:0 sc1 lds\n"
  "ds_read_b128 v[64:67], v89 offset:8192\n"
  "v_mfma_f32_16x16x32_bf16 v[0:3], a[32:35], v[40:43], v[0:3]\n"
  "s_sub_u32 s6, 4, s66\n"
  "s_and_b32 s6, s6, 3\n"
  "ds_read_b128 v[68:71], v89 offset:9216\n"
  "v_mfma_f32_16x16x32_bf16 v[0:3], a[48:51], v[44:47], v[0:3]\n"
  "s_mul_i32 s7, s67, 11\n"
  "ds_read_b128 v[72:75], v89 offset:10240\n"
  "ds_read_b128 v[76:79], v89 offset:11264\n"
  "v_mfma_f32_16x16x32_bf16 v[0:3], a[64:67], v[48:51], v[0:3]\n"
  "s_lshr_b32 s7, s7, 5\n"
  "s_mul_i32 s8, s7, 3\n"
  "ds_read_b64_tr_b16 a[224:225], v220 offset:32768\n"
  "v_mfma_f32_16x16x32_bf16 v[0:3], a[80:83], v[52:55], v[0:3]\n"
  "s_sub_u32 s8, s67, s8\n"
  "ds_read_b64_tr_b16 a[226:227], v220 offset:33280\n"
  "v_mfma_f32_16x16x32_bf16 v[4:7], a[4:7], v[32:35], 0\n"
  "ds_read_b64_tr_b16 a[228:229], v221 offset:32768\n"
  "ds_read_b64_tr_b16 a[230:231], v221 offset:33280\n"
  "v_mfma_f32_16x16x32_bf16 v[4:7], a[20:23], v[36:39], v[4:7]\n"
  "buffer_load_dwordx4 v92, s[36:39], s52 offen offset:1024 sc1 lds\n"
  "ds_read_b64_tr_b16 a[232:233], v220 offset:40960\n"
  "v_mfma_f32_16x16x32_bf16 v[4:7], a[36:39], v[40:43], v[4:7]\n"
  "s_sub_u32 s40, s18, 2048\n"
  "s_subb_u32 s41, s19, 0\n"
  "ds_read_b64_tr_b16 a[234:235], v220 offset:41472\n"
  "v_mfma_f32_16x16x32_bf16 v[4:7], a[52:55], v[44:47], v[4:7]\n"
  "s_mul_i32 s9, s6, 3\n"
  "ds_read_b64_tr_b16 a[236:237], v221 offset:40960\n"
  "v_lshrrev_b32 v200, 4, v89\n"
  "v_mfma_f32_16x16x32_bf16 v[4:7], a[68:71], v[48:51], v[4:7]\n"
  "s_add_u32 s9, s9, s7\n"
  "ds_read_b64_tr_b16 a[238:239], v221 offset:41472\n"
  "ds_read_b64_tr_b16 a[240:241], v220 offset:49152\n"
  "v_mfma_f32_16x16x32_bf16 v[4:7], a[84:87], v[52:55], v[4:7]\n"
  "buffer_load_dwordx4 v92, s[36:39], s52 offen offset:2048 sc1 lds\n"
  "ds_read_b64_tr_b16 a[242:243], v220 offset:49664\n"
  "v_mfma_f32_16x16x32_bf16 v[8:11], a[8:11], v[32:35], 0\n"
  "s_lshl2_add_u32 s22, s68, s10\n"
  "ds_read_b64_tr_b16 a[244:245], v221 offset:49152\n"
  "v_lshrrev_b32 v201, 2, v200\n"
  "v_mfma_f32_16x16x32_bf16 v[8:11], a[24:27], v[36:39], v[8:11]\n"
  "s_mul_i32 s22, s22, 14\n"
  "ds_read_b64_tr_b16 a[246:247], v221 offset:49664\n"
  "ds_read_b64_tr_b16 a[248:249], v220 offset:57344\n"
  "v_mfma_f32_16x16x32_bf16 v[8:11], a[40:43], v[40:43], v[8:11]\n"
  "ds_read_b64_tr_b16 a[250:251], v220 offset:57856\n"
  "v_min_u32 v202, 6, v201\n"
  "v_mfma_f32_16x16x32_bf16 v[8:11], a[56:59], v[44:47], v[8:11]\n"
  "ds_read_b64_tr_b16 a[252:253], v221 offset:57344\n"
  "v_add_u32 v112, s22, v202\n"
  "v_mfma_f32_16x16x32_bf16 v[8:11], a[72:75], v[48:51], v[8:11]\n"
  "buffer_load_dwordx4 v92, s[36:39], s52 offen offset:3072 sc1 lds\n"
  "ds_read_b64_tr_b16 a[254:255], v221 offset:57856\n"
  "v_mfma_f32_16x16x32_bf16 v[8:11], a[88:91], v[52:55], v[8:11]\n"
  "v_and_b32 v113, 3, v200\n"
  "v_min_u32 v114, 383, v112\n"
  "v_mfma_f32_16x16x32_bf16 v[12:15], a[12:15], v[32:35], 0\n"
  "s_mul_i32 s23, s8, 384\n"
  "v_min_u32 v115, 2, v113\n"
  "v_mul_u32_u24 v116, 1152, v114\n"
  "v_mfma_f32_16x16x32_bf16 v[12:15], a[28:31], v[36:39], v[12:15]\n"
  "s_mul_i32 s60, s9, 442368\n"
  "v_lshl_add_u32 v117, v115, 7, v116\n"
  "v_mfma_f32_16x16x32_bf16 v[12:15], a[44:47], v[40:43], v[12:15]\n"
  "s_add_u32 m0, s49, 2048\n"
  "s_add_u32 s34, s23, 2048\n"
  "s_mov_b32 s42, 5310464\n"
  "v_mfma_f32_16x16x32_bf16 v[12:15], a[60:63], v[44:47], v[12:15]\n"
  "buffer_load_dwordx4 v93, s[36:39], s52 offen sc1 lds\n"
  "v_mfma_f32_16x16x32_bf16 v[12:15], a[76:79], v[48:51], v[12:15]\n"
  "s_add_u32 s52, s52, 18432\n"
  "s_mov_b32 s43, 0x20000\n"
  "s_mul_i32 s9, s9, 12288\n"
  "v_mfma_f32_16x16x32_bf16 v[12:15], a[92:95], v[52:55], v[12:15]\n"
  "s_waitcnt vmcnt(5) lgkmcnt(0)\n"
  "s_barrier\n"
  "v_mfma_f32_16x16x32_bf16 v[0:3], a[96:99], v[56:59], v[0:3]\n"
  "s_cmp_eq_u32 s52, s54\n"
  "s_cselect_b32 s52, s53, s52\n"
  "ds_read_b128 v[32:35], v89 offset:12288\n"
  "ds_read_b128 v[36:39], v89 offset:13312\n"
  "v_mfma_f32_16x16x32_bf16 v[0:3], a[112:115], v[60:63], v[0:3]\n"
  "s_add_u32 m0, s48, 49152\n"
  "ds_read_b128 v[40:43], v89 offset:14336\n"
  "v_min_u32 v118, 13, v200\n"
  "v_mfma_f32_16x16x32_bf16 v[0:3], a[128:131], v[64:67], v[0:3]\n"
  "buffer_load_dwordx4 v92, s[36:39], s52 offen offset:0 sc1 lds\n"
  "ds_read_b128 v[44:47], v89 offset:15360\n"
  "v_mfma_f32_16x16x32_bf16 v[0:3], a[144:147], v[68:71], v[0:3]\n"
  "ds_read_b128 v[48:51], v89 offset:16384\n"
  "ds_read_b128 v[52:55], v89 offset:17408\n"
  "v_mfma_f32_16x16x32_bf16 v[0:3], a[160:163], v[72:75], v[0:3]\n"
  "ds_read_b64_tr_b16 v[224:225], v222 offset:0\n"
  "v_add_u32 v102, s34, v117\n"
  "v_mfma_f32_16x16x32_bf16 v[0:3], a[176:179], v[76:79], v[0:3]\n"
  "s_lshr_b32 s35, s10, 1\n"
  "s_and_b32 s70, s10, 1\n"
  "ds_read_b64_tr_b16 v[226:227], v222 offset:512\n"
  "v_mfma_f32_16x16x32_bf16 v[4:7], a[100:103], v[56:59], v[4:7]\n"
  "s_lshl4_add_u32 s22, s68, s66\n"
  "ds_read_b64_tr_b16 v[228:229], v223 offset:0\n"
  "ds_read_b64_tr_b16 v[230:231], v223 offset:512\n"
  "v_mfma_f32_16x16x32_bf16 v[4:7], a[116:119], v[60:63], v[4:7]\n"
  "buffer_load_dwordx4 v92, s[36:39], s52 offen offset:1024 sc1 lds\n"
  "ds_read_b64_tr_b16 v[232:233], v222 offset:8192\n"
  "v_mfma_f32_16x16x32_bf16 v[4:7], a[132:135], v[64:67], v[4:7]\n"
  "s_lshl2_add_u32 s22, s22, s7\n"
  "s_lshl_b32 s22, s22, 7\n"
  "ds_read_b64_tr_b16 v[234:235], v222 offset:8704\n"
  "v_mfma_f32_16x16x32_bf16 v[4:7], a[148:151], v[68:71], v[4:7]\n"
  "s_add_u32 s22, s22, 7081984\n"
  "s_add_u32 s12, s6, 0\n"
  "ds_read_b64_tr_b16 v[236:237], v223 offset:8192\n"
  "v_mfma_f32_16x16x32_bf16 v[4:7], a[164:167], v[72:75], v[4:7]\n"
  "s_and_b32 s12, s12, 3\n"
  "ds_read_b64_tr_b16 v[238:239], v223 offset:8704\n"
  "ds_read_b64_tr_b16 v[240:241], v222 offset:16384\n"
  "v_mfma_f32_16x16x32_bf16 v[4:7], a[180:183], v[76:79], v[4:7]\n"
  "s_lshl_b32 s12, s12, 11\n"
  "s_add_u32 s12, s12, s22\n"
  "ds_read_b64_tr_b16 v[242:243], v222 offset:16896\n"
  "v_mfma_f32_16x16x32_bf16 v[8:11], a[104:107], v[56:59], v[8:11]\n"
  "buffer_load_dwordx4 v92, s[36:39], s52 offen offset:2048 sc1 lds\n"
  "ds_read_b64_tr_b16 v[244:245], v223 offset:16384\n"
  "v_mfma_f32_16x16x32_bf16 v[8:11], a[120:123], v[60:63], v[8:11]\n"
  "s_add_u32 s13, s6, 1\n"
  "ds_read_b64_tr_b16 v[246:247], v223 offset:16896\n"
  "ds_read_b64_tr_b16 v[248:249], v222 offset:24576\n"
  "v_mfma_f32_16x16x32_bf16 v[8:11], a[136:139], v[64:67], v[8:11]\n"
  "s_and_b32 s13, s13, 3\n"
  "s_lshl_b32 s13, s13, 11\n"
  "ds_read_b64_tr_b16 v[250:251], v222 offset:25088\n"
  "v_mfma_f32_16x16x32_bf16 v[8:11], a[152:155], v[68:71], v[8:11]\n"
  "s_add_u32 s13, s13, s22\n"
  "s_add_u32 s14, s6, 2\n"
  "ds_read_b64_tr_b16 v[252:253], v223 offset:24576\n"
  "v_mfma_f32_16x16x32_bf16 v[8:11], a[168:171], v[72:75], v[8:11]\n"
  "s_and_b32 s14, s14, 3\n"
  "s_lshl_b32 s14, s14, 11\n"
  "ds_read_b64_tr_b16 v[254:255], v223 offset:25088\n"
  "v_mfma_f32_16x16x32_bf16 v[8:11], a[184:187], v[76:79], v[8:11]\n"
  "buffer_load_dwordx4 v92, s[36:39], s52 offen offset:3072 sc1 lds\n"
  "v_mfma_f32_16x16x32_bf16 v[12:15], a[108:111], v[56:59], v[12:15]\n"
  "s_add_u32 s14, s14, s22\n"
  "s_add_u32 s15, s6, 3\n"
  "s_and_b32 s15, s15, 3\n"
  "v_mfma_f32_16x16x32_bf16 v[12:15], a[124:127], v[60:63], v[12:15]\n"
  "s_lshl_b32 s15, s15, 11\n"
  "s_add_u32 s15, s15, s22\n"
  "v_mfma_f32_16x16x32_bf16 v[12:15], a[140:143], v[64:67], v[12:15]\n"
  "s_cmp_eq_u32 s66, 0\n"
  "s_cselect_b32 s34, 106173216, 0\n"
  "v_mfma_f32_16x16x32_bf16 v[12:15], a[156:159], v[68:71], v[12:15]\n"
  "s_add_u32 m0, s49, 4096\n"
  "s_cmp_eq_u32 s66, 1\n"
  "s_cselect_b32 s34, 103941441, s34\n"
  "v_mfma_f32_16x16x32_bf16 v[12:15], a[172:175], v[72:75], v[12:15]\n"
  "buffer_load_dwordx4 v93, s[36:39], s52 offen sc1 lds\n"
  "v_mfma_f32_16x16x32_bf16 v[12:15], a[188:191], v[76:79], v[12:15]\n"
  "s_waitcnt vmcnt(5) lgkmcnt(0)\n"
  "s_barrier\n"
  "v_mfma_f32_16x16x32_bf16 v[0:3], a[192:195], v[32:35], v[0:3]\n"
  "s_add_u32 s52, s52, 18432\n"
  "ds_read_b128 v[56:59], v89 offset:32768\n"
  "v_mfma_f32_16x16x32_bf16 v[0:3], a[208:211], v[36:39], v[0:3]\n"
  "s_cmp_eq_u32 s52, s54\n"
  "s_cselect_b32 s52, s53, s52\n"
  "v_mfma_f32_16x16x32_bf16 v[0:3], a[224:227], v[40:43], v[0:3]\n"
  "s_add_u32 m0, s48, 65536\n"
  "ds_read_b128 v[60:63], v89 offset:33792\n"
  "v_mfma_f32_16x16x32_bf16 v[0:3], a[240:243], v[44:47], v[0:3]\n"
  "buffer_load_dwordx4 v92, s[36:39], s52 offen offset:0 sc1 lds\n"
  "v_mfma_f32_16x16x32_bf16 v[0:3], v[224:227], v[48:51], v[0:3]\n"
  "s_cmp_eq_u32 s66, 2\n"
  "s_cselect_b32 s34, 87098406, s34\n"
  "v_mfma_f32_16x16x32_bf16 v[0:3], v[240:243], v[52:55], v[0:3]\n"
  "s_cmp_eq_u32 s66, 3\n"
  "s_cselect_b32 s34, 69273171, s34\n"
  "ds_read_b128 v[64:67], v89 offset:34816\n"
  "v_mfma_f32_16x16x32_bf16 v[4:7], a[196:199], v[32:35], v[4:7]\n"
  "s_lshl_b32 s71, s68, 2\n"
  "s_lshr_b32 s34, s34, s71\n"
  "s_and_b32 s34, s34, 15\n"
  "v_mfma_f32_16x16x32_bf16 v[4:7], a[212:215], v[36:39], v[4:7]\n"
  "s_min_u32 s84, s34, 2\n"
  "s_mul_i32 s28, s34, 18\n"
  "s_add_u32 s84, s84, s28\n"
  "v_mfma_f32_16x16x32_bf16 v[4:7], a[228:231], v[40:43], v[4:7]\n"
  "buffer_load_dwordx4 v92, s[36:39], s52 offen offset:1024 sc1 lds\n"
  "ds_read_b128 v[68:71], v89 offset:35840\n"
  "v_mfma_f32_16x16x32_bf16 v[4:7], a[244:247], v[44:47], v[4:7]\n"
  "s_lshl_b32 s29, s34, 2\n"
  "s_lshr_b32 s29, 0xCA69521, s29\n"
  "s_bfm_b32 s22, s66, 0\n"
  "v_mfma_f32_16x16x32_bf16 v[4:7], v[228:231], v[48:51], v[4:7]\n"
  "s_and_b32 s22, s22, s29\n"
  "s_bcnt1_i32_b32 s22, s22\n"
  "s_mul_i32 s30, s66, 5\n"
  "v_mfma_f32_16x16x32_bf16 v[4:7], v[244:247], v[52:55], v[4:7]\n"
  "s_add_u32 s30, s30, s84\n"
  "s_sub_u32 s30, s30, s22\n"
  "ds_read_b128 v[72:75], v89 offset:36864\n"
  "v_mfma_f32_16x16x32_bf16 v[8:11], a[200:203], v[32:35], v[8:11]\n"
  "s_lshr_b32 s31, s29, s66\n"
  "s_lshl_b32 s31, s31, 31\n"
  "s_mul_i32 s59, s30, 147456\n"
  "v_mfma_f32_16x16x32_bf16 v[8:11], a[216:219], v[36:39], v[8:11]\n"
  "buffer_load_dwordx4 v92, s[36:39], s52 offen offset:2048 sc1 lds\n"
  "v_mfma_f32_16x16x32_bf16 v[8:11], a[232:235], v[40:43], v[8:11]\n"
  "s_add_u32 s59, s59, s9\n"
  "s_add_u32 s59, s59, 4715520\n"
  "ds_read_b128 v[76:79], v89 offset:37888\n"
  "v_mfma_f32_16x16x32_bf16 v[8:11], a[248:251], v[44:47], v[8:11]\n"
  "s_or_b32 s71, s31, s50\n"
  "s_add_u32 s71, s71, 592896\n"
  "s_mul_i32 s28, s10, 9216\n"
  "v_mfma_f32_16x16x32_bf16 v[8:11], v[232:235], v[48:51], v[8:11]\n"
  "s_add_u32 s28, s28, s23\n"
  "s_lshl_b32 s72, s7, 1\n"
  "s_lshr_b32 s72, 0x249, s72\n"
  "v_mfma_f32_16x16x32_bf16 v[8:11], v[248:251], v[52:55], v[8:11]\n"
  "s_mul_i32 s73, s66, 7\n"
  "s_add_u32 s73, s73, s68\n"
  "s_mul_i32 s73, s73, 3\n"
  "v_mfma_f32_16x16x32_bf16 v[12:15], a[204:207], v[32:35], v[12:15]\n"
  "buffer_load_dwordx4 v92, s[36:39], s52 offen offset:3072 sc1 lds\n"
  "v_mfma_f32_16x16x32_bf16 v[12:15], a[220:223], v[36:39], v[12:15]\n"
  "s_add_u32 s73, s73, s8\n"
  "s_mul_i32 s73, s73, 3\n"
  "s_bfe_u32 s22, s72, 131072\n"
  "v_mfma_f32_16x16x32_bf16 v[12:15], a[236:239], v[40:43], v[12:15]\n"
  "s_add_u32 s16, s22, s73\n"
  "s_mul_i32 s29, s10, 3072\n"
  "s_mul_i32 s16, s16, 24576\n"
  "v_mfma_f32_16x16x32_bf16 v[12:15], a[252:255], v[44:47], v[12:15]\n"
  "s_add_u32 s16, s16, s29\n"
  "s_add_u32 s16, s16, 23592960\n"
  "s_lshl_b32 s22, s22, 7\n"
  "v_mfma_f32_16x16x32_bf16 v[12:15], v[236:239], v[48:51], v[12:15]\n"
  "s_add_u32 m0, s49, 145408\n"
  "s_add_u32 s22, s22, 2048\n"
  "s_bfe_u32 s9, s72, 131074\n"
  "v_mfma_f32_16x16x32_bf16 v[12:15], v[252:255], v[52:55], v[12:15]\n"
  "s_waitcnt lgkmcnt(0)\n"
  "s_add_u32 s17, s9, s73\n"
  "s_mul_i32 s17, s17, 24576\n"
  "v_mfma_f32_16x16x32_bf16 v[16:19], a[0:3], v[56:59], 0\n"
  "buffer_load_dwordx4 v93, s[36:39], s52 offen sc1 lds\n"
  "ds_read_b128 v[32:35], v89 offset:38912\n"
  "v_mfma_f32_16x16x32_bf16 v[16:19], a[16:19], v[60:63], v[16:19]\n"
  "s_add_u32 s52, s52, 18432\n"
  "v_mul_f32 v80, v0, v0\n"
  "v_mul_f32 v81, v1, v1\n"
  "v_mfma_f32_16x16x32_bf16 v[16:19], a[32:35], v[64:67], v[16:19]\n"
  "s_cmp_eq_u32 s52, s54\n"
  "s_cselect_b32 s52, s53, s52\n"
  "ds_read_b128 v[36:39], v89 offset:39936\n"
  "v_mfma_f32_16x16x32_bf16 v[16:19], a[48:51], v[68:71], v[16:19]\n"
  "s_add_u32 m0, s48, 81920\n"
  "v_fmaak_f32 v80, v88, v80, 0xc0135761\n"
  "v_fmaak_f32 v81, v88, v81, 0xc0135761\n"
  "v_mfma_f32_16x16x32_bf16 v[16:19], a[64:67], v[72:75], v[16:19]\n"
  "buffer_load_dwordx4 v92, s[36:39], s52 offen offset:0 sc1 lds\n"
  "v_mul_f32 v80, v0, v80\n"
  "v_mfma_f32_16x16x32_bf16 v[16:19], a[80:83], v[76:79], v[16:19]\n"
  "s_add_u32 s17, s17, s29\n"
  "ds_read_b128 v[40:43], v89 offset:40960\n"
  "v_mul_f32 v81, v1, v81\n"
  "v_mfma_f32_16x16x32_bf16 v[20:23], a[4:7], v[56:59], 0\n"
  "s_add_u32 s17, s17, 23605248\n"
  "v_exp_f32 v80, v80\n"
  "v_mfma_f32_16x16x32_bf16 v[20:23], a[20:23], v[60:63], v[20:23]\n"
  "s_lshl_b32 s9, s9, 7\n"
  "v_exp_f32 v81, v81\n"
  "v_mfma_f32_16x16x32_bf16 v[20:23], a[36:39], v[64:67], v[20:23]\n"
  "s_add_u32 s9, s9, 1024\n"
  "ds_read_b128 v[44:47], v89 offset:41984\n"
  "v_mul_f32 v82, v0, v8\n"
  "v_mfma_f32_16x16x32_bf16 v[20:23], a[52:55], v[68:71], v[20:23]\n"
  "buffer_load_dwordx4 v92, s[36:39], s52 offen offset:1024 sc1 lds\n"
  "v_mul_f32 v83, v1, v9\n"
  "v_mfma_f32_16x16x32_bf16 v[20:23], a[68:71], v[72:75], v[20:23]\n"
  "s_bfe_u32 s23, s72, 131076\n"
  "v_add_f32 v80, 1.0, v80\n"
  "v_add_f32 v81, 1.0, v81\n"
  "v_mfma_f32_16x16x32_bf16 v[20:23], a[84:87], v[76:79], v[20:23]\n"
  "s_lshl_b32 s23, s23, 7\n"
  "s_cbranch_execz Ltramp%=\n"
  "s_add_u32 s23, s23, 0\n"
  "ds_read_b128 v[48:51], v89 offset:43008\n"
  "v_mfma_f32_16x16x32_bf16 v[24:27], a[8:11], v[56:59], 0\n"
  "s_add_u32 s61, s73, s7\n"
  "v_rcp_f32 v80, v80\n"
  "v_mfma_f32_16x16x32_bf16 v[24:27], a[24:27], v[60:63], v[24:27]\n"
  "s_mul_i32 s61, s61, 24576\n"
  "v_rcp_f32 v81, v81\n"
  "v_mfma_f32_16x16x32_bf16 v[24:27], a[40:43], v[64:67], v[24:27]\n"
  "buffer_load_dwordx4 v92, s[36:39], s52 offen offset:2048 sc1 lds\n"
  "ds_read_b128 v[52:55], v89 offset:44032\n"
  "v_mfma_f32_16x16x32_bf16 v[24:27], a[56:59], v[68:71], v[24:27]\n"
  "s_add_u32 s61, s61, 23592960\n"
  "v_mul_f32 v82, v82, v80\n"
  "v_mul_f32 v83, v83, v81\n"
  "v_mfma_f32_16x16x32_bf16 v[24:27], a[72:75], v[72:75], v[24:27]\n"
  "s_add_u32 s62, s61, 12288\n"
  "v_cvt_pk_bf16_f32 v84, v82, v83\n"
  "v_mul_f32 v80, v2, v2\n"
  "v_mfma_f32_16x16x32_bf16 v[24:27], a[88:91], v[76:79], v[24:27]\n"
  "s_lshl_b32 s84, s10, 30\n"
  "v_mul_f32 v81, v3, v3\n"
  "v_fmaak_f32 v80, v88, v80, 0xc0135761\n"
  "v_mfma_f32_16x16x32_bf16 v[28:31], a[12:15], v[56:59], 0\n"
  "s_or_b32 s31, s31, s84\n"
  "v_fmaak_f32 v81, v88, v81, 0xc0135761\n"
  "v_mul_f32 v80, v2, v80\n"
  "v_mfma_f32_16x16x32_bf16 v[28:31], a[28:31], v[60:63], v[28:31]\n"
  "buffer_load_dwordx4 v92, s[36:39], s52 offen offset:3072 sc1 lds\n"
  "v_mul_f32 v81, v3, v81\n"
  "v_mfma_f32_16x16x32_bf16 v[28:31], a[44:47], v[64:67], v[28:31]\n"
  "s_and_b32 s31, s31, 0x80000000\n"
  "v_exp_f32 v80, v80\n"
  "v_mfma_f32_16x16x32_bf16 v[28:31], a[60:63], v[68:71], v[28:31]\n"
  "s_mul_i32 s74, s8, 3\n"
  "v_exp_f32 v81, v81\n"
  "v_mfma_f32_16x16x32_bf16 v[28:31], a[76:79], v[72:75], v[28:31]\n"
  "s_add_u32 s74, s74, s7\n"
  "v_mul_f32 v82, v2, v10\n"
  "v_mul_f32 v83, v3, v11\n"
  "v_mfma_f32_16x16x32_bf16 v[28:31], a[92:95], v[76:79], v[28:31]\n"
  "s_waitcnt lgkmcnt(0)\n"
  "v_add_f32 v80, 1.0, v80\n"
  "v_add_f32 v81, 1.0, v81\n"
  "v_mfma_f32_16x16x32_bf16 v[16:19], a[96:99], v[32:35], v[16:19]\n"
  "s_add_u32 m0, s49, 143360\n"
  "s_lshl_b32 s75, s74, 7\n"
  "ds_read_b128 v[56:59], v89 offset:45056\n"
  "v_mfma_f32_16x16x32_bf16 v[16:19], a[112:115], v[36:39], v[16:19]\n"
  "buffer_load_dwordx4 v93, s[36:39], s52 offen sc1 lds\n"
  "v_mfma_f32_16x16x32_bf16 v[16:19], a[128:131], v[40:43], v[16:19]\n"
  "s_add_u32 s52, s52, 18432\n"
  "s_lshl1_add_u32 s76, s30, s10\n"
  "ds_read_b128 v[60:63], v89 offset:46080\n"
  "v_mfma_f32_16x16x32_bf16 v[16:19], a[144:147], v[44:47], v[16:19]\n"
  "s_cmp_eq_u32 s52, s54\n"
  "s_cselect_b32 s52, s53, s52\n"
  "s_lshr_b32 s77, s76, 4\n"
  "v_mfma_f32_16x16x32_bf16 v[16:19], a[160:163], v[48:51], v[16:19]\n"
  "s_add_u32 m0, s48, 0\n"
  "v_rcp_f32 v80, v80\n"
  "v_mfma_f32_16x16x32_bf16 v[16:19], a[176:179], v[52:55], v[16:19]\n"
  "buffer_load_dwordx4 v92, s[36:39], s52 offen offset:0 sc1 lds\n"
  "ds_read_b128 v[64:67], v89 offset:47104\n"
  "v_mfma_f32_16x16x32_bf16 v[20:23], a[100:103], v[32:35], v[20:23]\n"
  "s_lshl_b32 s77, s77, 6\n"
  "v_rcp_f32 v81, v81\n"
  "v_mfma_f32_16x16x32_bf16 v[20:23], a[116:119], v[36:39], v[20:23]\n"
  "s_and_b32 s72, s76, 15\n"
  "v_mul_f32 v82, v82, v80\n"
  "v_mul_f32 v83, v83, v81\n"
  "v_mfma_f32_16x16x32_bf16 v[20:23], a[132:135], v[40:43], v[20:23]\n"
  "s_add_u32 s77, s77, s72\n"
  "ds_read_b128 v[68:71], v89 offset:48128\n"
  "v_cvt_pk_bf16_f32 v85, v82, v83\n"
  "v_mfma_f32_16x16x32_bf16 v[20:23], a[148:151], v[44:47], v[20:23]\n"
  "s_mul_i32 s77, s77, 2304\n"
  "v_mul_f32 v80, v4, v4\n"
  "v_mul_f32 v81, v5, v5\n"
  "v_mfma_f32_16x16x32_bf16 v[20:23], a[164:167], v[48:51], v[20:23]\n"
  "buffer_load_dwordx4 v92, s[36:39], s52 offen offset:1024 sc1 lds\n"
  "v_fmaak_f32 v80, v88, v80, 0xc0135761\n"
  "v_mfma_f32_16x16x32_bf16 v[20:23], a[180:183], v[52:55], v[20:23]\n"
  "s_add_u32 s77, s77, s75\n"
  "ds_read_b128 v[72:75], v89 offset:18432\n"
  "v_fmaak_f32 v81, v88, v81, 0xc0135761\n"
  "v_mfma_f32_16x16x32_bf16 v[24:27], a[104:107], v[32:35], v[24:27]\n"
  "s_add_u32 s76, s76, 4\n"
  "v_mul_f32 v80, v4, v80\n"
  "v_mul_f32 v81, v5, v81\n"
  "v_mfma_f32_16x16x32_bf16 v[24:27], a[120:123], v[36:39], v[24:27]\n"
  "s_lshr_b32 s73, s76, 4\n"
  "v_exp_f32 v80, v80\n"
  "v_mfma_f32_16x16x32_bf16 v[24:27], a[136:139], v[40:43], v[24:27]\n"
  "s_lshl_b32 s73, s73, 6\n"
  "s_and_b32 s78, s76, 15\n"
  "ds_read_b128 v[76:79], v89 offset:19456\n"
  "v_mfma_f32_16x16x32_bf16 v[24:27], a[152:155], v[44:47], v[24:27]\n"
  "buffer_load_dwordx4 v92, s[36:39], s52 offen offset:2048 sc1 lds\n"
  "v_mfma_f32_16x16x32_bf16 v[24:27], a[168:171], v[48:51], v[24:27]\n"
  "s_add_u32 s73, s73, s78\n"
  "v_exp_f32 v81, v81\n"
  "v_mfma_f32_16x16x32_bf16 v[24:27], a[184:187], v[52:55], v[24:27]\n"
  "s_mul_i32 s73, s73, 2304\n"
  "v_mul_f32 v82, v4, v12\n"
  "v_mul_f32 v83, v5, v13\n"
  "v_mfma_f32_16x16x32_bf16 v[28:31], a[108:111], v[32:35], v[28:31]\n"
  "s_add_u32 s73, s73, s75\n"
  "v_add_f32 v80, 1.0, v80\n"
  "v_add_f32 v81, 1.0, v81\n"
  "v_mfma_f32_16x16x32_bf16 v[28:31], a[124:127], v[36:39], v[28:31]\n"
  "s_add_u32 s76, s76, 4\n"
  "v_rcp_f32 v80, v80\n"
  "v_mfma_f32_16x16x32_bf16 v[28:31], a[140:143], v[40:43], v[28:31]\n"
  "buffer_load_dwordx4 v92, s[36:39], s52 offen offset:3072 sc1 lds\n"
  "v_mfma_f32_16x16x32_bf16 v[28:31], a[156:159], v[44:47], v[28:31]\n"
  "s_lshr_b32 s74, s76, 4\n"
  "v_rcp_f32 v81, v81\n"
  "v_mfma_f32_16x16x32_bf16 v[28:31], a[172:175], v[48:51], v[28:31]\n"
  "s_lshl_b32 s74, s74, 6\n"
  "v_mul_f32 v82, v82, v80\n"
  "v_mul_f32 v83, v83, v81\n"
  "v_mfma_f32_16x16x32_bf16 v[28:31], a[188:191], v[52:55], v[28:31]\n"
  "s_waitcnt vmcnt(9) lgkmcnt(0)\n"
  "s_barrier\n"
  "v_mfma_f32_16x16x32_bf16 v[16:19], a[192:195], v[56:59], v[16:19]\n"
  "s_add_u32 m0, s49, 141312\n"
  "ds_read_b128 v[32:35], v89 offset:49152\n"
  "v_cvt_pk_bf16_f32 v86, v82, v83\n"
  "v_mfma_f32_16x16x32_bf16 v[16:19], a[208:211], v[60:63], v[16:19]\n"
  "buffer_load_dwordx4 v93, s[36:39], s52 offen sc1 lds\n"
  "v_mul_f32 v80, v6, v6\n"
  "v_mfma_f32_16x16x32_bf16 v[16:19], a[224:227], v[64:67], v[16:19]\n"
  "s_add_u32 s52, s52, 18432\n"
  "ds_read_b128 v[36:39], v89 offset:50176\n"
  "v_mul_f32 v81, v7, v7\n"
  "v_mfma_f32_16x16x32_bf16 v[16:19], a[240:243], v[68:71], v[16:19]\n"
  "s_cmp_eq_u32 s52, s54\n"
  "s_cselect_b32 s52, s53, s52\n"
  "v_fmaak_f32 v80, v88, v80, 0xc0135761\n"
  "v_mfma_f32_16x16x32_bf16 v[16:19], v[224:227], v[72:75], v[16:19]\n"
  "s_and_b32 s79, s76, 15\n"
  "v_fmaak_f32 v81, v88, v81, 0xc0135761\n"
  "v_mul_f32 v80, v6, v80\n"
  "v_mfma_f32_16x16x32_bf16 v[16:19], v[240:243], v[76:79], v[16:19]\n"
  "s_add_u32 m0, s48, 98304\n"
  "ds_read_b128 v[40:43], v89 offset:51200\n"
  "v_mul_f32 v81, v7, v81\n"
  "v_mfma_f32_16x16x32_bf16 v[20:23], a[196:199], v[56:59], v[20:23]\n"
  "buffer_load_dwordx4 v92, s[36:39], s52 offen offset:0 sc1 lds\n"
  "v_mfma_f32_16x16x32_bf16 v[20:23], a[212:215], v[60:63], v[20:23]\n"
  "s_add_u32 s74, s74, s79\n"
  "v_exp_f32 v80, v80\n"
  "v_mfma_f32_16x16x32_bf16 v[20:23], a[228:231], v[64:67], v[20:23]\n"
  "s_mul_i32 s74, s74, 2304\n"
  "s_add_u32 s74, s74, s75\n"
  "ds_read_b128 v[44:47], v89 offset:52224\n"
  "v_mfma_f32_16x16x32_bf16 v[20:23], a[244:247], v[68:71], v[20:23]\n"
  "s_or_b32 s74, s74, s31\n"
  "v_exp_f32 v81, v81\n"
  "v_mfma_f32_16x16x32_bf16 v[20:23], v[228:231], v[72:75], v[20:23]\n"
  "s_mul_i32 s63, s30, 18432\n"
  "v_mul_f32 v82, v6, v14\n"
  "v_mul_f32 v83, v7, v15\n"
  "v_mfma_f32_16x16x32_bf16 v[20:23], v[244:247], v[76:79], v[20:23]\n"
  "buffer_load_dwordx4 v92, s[36:39], s52 offen offset:1024 sc1 lds\n"
  "ds_read_b128 v[48:51], v89 offset:53248\n"
  "v_mfma_f32_16x16x32_bf16 v[24:27], a[200:203], v[56:59], v[24:27]\n"
  "s_lshl4_add_u32 s63, s75, s63\n"
  "v_add_f32 v80, 1.0, v80\n"
  "v_add_f32 v81, 1.0, v81\n"
  "v_mfma_f32_16x16x32_bf16 v[24:27], a[216:219], v[60:63], v[24:27]\n"
  "s_add_u32 s63, s63, 2359296\n"
  "v_rcp_f32 v80, v80\n"
  "v_mfma_f32_16x16x32_bf16 v[24:27], a[232:235], v[64:67], v[24:27]\n"
  "s_lshl2_add_u32 s72, s34, s66\n"
  "s_lshl4_add_u32 s72, s72, s7\n"
  "ds_read_b128 v[52:55], v89 offset:54272\n"
  "v_mfma_f32_16x16x32_bf16 v[24:27], a[248:251], v[68:71], v[24:27]\n"
  "s_lshl_b32 s72, s72, 7\n"
  "v_rcp_f32 v81, v81\n"
  "v_mfma_f32_16x16x32_bf16 v[24:27], v[232:235], v[72:75], v[24:27]\n"
  "buffer_load_dwordx4 v92, s[36:39], s52 offen offset:2048 sc1 lds\n"
  "v_mul_f32 v82, v82, v80\n"
  "v_mfma_f32_16x16x32_bf16 v[24:27], v[248:251], v[76:79], v[24:27]\n"
  "v_mul_f32 v83, v83, v81\n"
  "v_cvt_pk_bf16_f32 v87, v82, v83\n"
  "v_mfma_f32_16x16x32_bf16 v[28:31], a[204:207], v[56:59], v[28:31]\n"
  "v_and_b32 v201, 7, v200\n"
  "v_mfma_f32_16x16x32_bf16 v[28:31], a[220:223], v[60:63], v[28:31]\n"
  "s_add_u32 s72, s72, 7081984\n"
  "v_lshrrev_b32 v202, 5, v200\n"
  "v_mov_b32 v112, s35\n"
  "v_mfma_f32_16x16x32_bf16 v[28:31], a[236:239], v[64:67], v[28:31]\n"
  "v_lshrrev_b32 v113, 1, v201\n"
  "v_mfma_f32_16x16x32_bf16 v[28:31], a[252:255], v[68:71], v[28:31]\n"
  "buffer_load_dwordx4 v92, s[36:39], s52 offen offset:3072 sc1 lds\n"
  "v_mfma_f32_16x16x32_bf16 v[28:31], v[236:239], v[72:75], v[28:31]\n"
  "buffer_store_dwordx4 v[84:87], v89, s[36:39], s55 offen sc1\n"
  "v_mfma_f32_16x16x32_bf16 v[28:31], v[252:255], v[76:79], v[28:31]\n"
  "s_waitcnt lgkmcnt(0)\n"
  "s_lshl2_add_u32 s58, s68, s66\n"
  "v_lshrrev_b32 v114, 3, v200\n"
  "v_mfma_f32_16x16x32_bf16 v[0:3], a[0:3], v[32:35], 0\n"
  "s_add_u32 s55, s55, 147456\n"
  "ds_read_b128 v[56:59], v89 offset:55296\n"
  "v_and_b32 v115, 3, v200\n"
  "v_mfma_f32_16x16x32_bf16 v[0:3], a[16:19], v[36:39], v[0:3]\n"
  "s_add_u32 m0, s49, 139264\n"
  "v_mul_f32 v80, v16, v16\n"
  "v_mul_f32 v81, v17, v17\n"
  "v_mfma_f32_16x16x32_bf16 v[0:3], a[32:35], v[40:43], v[0:3]\n"
  "s_cmp_eq_u32 s55, s57\n"
  "s_cselect_b32 s55, s56, s55\n"
  "buffer_load_dwordx4 v93, s[36:39], s52 offen sc1 lds\n"
  "ds_read_b128 v[60:63], v89 offset:56320\n"
  "v_mfma_f32_16x16x32_bf16 v[0:3], a[48:51], v[44:47], v[0:3]\n"
  "s_add_u32 s52, s52, 18432\n"
  "v_fmaak_f32 v80, v88, v80, 0xc0135761\n"
  "v_fmaak_f32 v81, v88, v81, 0xc0135761\n"
  "v_mfma_f32_16x16x32_bf16 v[0:3], a[64:67], v[48:51], v[0:3]\n"
  "s_cmp_eq_u32 s52, s54\n"
  "s_cselect_b32 s52, s53, s52\n"
  "v_mul_f32 v80, v16, v80\n"
  "v_mfma_f32_16x16x32_bf16 v[0:3], a[80:83], v[52:55], v[0:3]\n"
  "ds_read_b128 v[64:67], v89 offset:57344\n"
  "v_mul_f32 v81, v17, v81\n"
  "v_mfma_f32_16x16x32_bf16 v[4:7], a[4:7], v[32:35], 0\n"
  "s_add_u32 m0, s48, 114688\n"
  "v_exp_f32 v80, v80\n"
  "v_mfma_f32_16x16x32_bf16 v[4:7], a[20:23], v[36:39], v[4:7]\n"
  "buffer_load_dwordx4 v92, s[36:39], s52 offen offset:0 sc1 lds\n"
  "v_lshrrev_b32 v116, 4, v200\n"
  "v_mfma_f32_16x16x32_bf16 v[4:7], a[36:39], v[40:43], v[4:7]\n"
  "ds_read_b128 v[68:71], v89 offset:58368\n"
  "v_and_b32 v117, 1, v200\n"
  "v_mfma_f32_16x16x32_bf16 v[4:7], a[52:55], v[44:47], v[4:7]\n"
  "v_exp_f32 v81, v81\n"
  "v_mfma_f32_16x16x32_bf16 v[4:7], a[68:71], v[48:51], v[4:7]\n"
  "v_mul_f32 v82, v16, v24\n"
  "v_mul_f32 v83, v17, v25\n"
  "v_mfma_f32_16x16x32_bf16 v[4:7], a[84:87], v[52:55], v[4:7]\n"
  "ds_read_b128 v[72:75], v89 offset:59392\n"
  "v_add_f32 v80, 1.0, v80\n"
  "v_mfma_f32_16x16x32_bf16 v[8:11], a[8:11], v[32:35], 0\n"
  "buffer_load_dwordx4 v92, s[36:39], s52 offen offset:1024 sc1 lds\n"
  "v_add_f32 v81, 1.0, v81\n"
  "v_mfma_f32_16x16x32_bf16 v[8:11], a[24:27], v[36:39], v[8:11]\n"
  "v_rcp_f32 v80, v80\n"
  "v_mfma_f32_16x16x32_bf16 v[8:11], a[40:43], v[40:43], v[8:11]\n"
  "ds_read_b128 v[76:79], v89 offset:60416\n"
  "v_mov_b32 v118, s70\n"
  "v_mfma_f32_16x16x32_bf16 v[8:11], a[56:59], v[44:47], v[8:11]\n"
  "v_rcp_f32 v81, v81\n"
  "v_mfma_f32_16x16x32_bf16 v[8:11], a[72:75], v[48:51], v[8:11]\n"
  "v_mul_f32 v82, v82, v80\n"
  "v_mul_f32 v83, v83, v81\n"
  "v_mfma_f32_16x16x32_bf16 v[8:11], a[88:91], v[52:55], v[8:11]\n"
  "buffer_load_dwordx4 v92, s[36:39], s52 offen offset:2048 sc1 lds\n"
  "v_cvt_pk_bf16_f32 v84, v82, v83\n"
  "v_mfma_f32_16x16x32_bf16 v[12:15], a[12:15], v[32:35], 0\n"
  "v_mul_f32 v80, v18, v18\n"
  "v_mul_f32 v81, v19, v19\n"
  "v_mfma_f32_16x16x32_bf16 v[12:15], a[28:31], v[36:39], v[12:15]\n"
  "v_fmaak_f32 v80, v88, v80, 0xc0135761\n"
  "v_fmaak_f32 v81, v88, v81, 0xc0135761\n"
  "v_mfma_f32_16x16x32_bf16 v[12:15], a[44:47], v[40:43], v[12:15]\n"
  "v_mul_f32 v80, v18, v80\n"
  "v_mul_f32 v81, v19, v81\n"
  "v_mfma_f32_16x16x32_bf16 v[12:15], a[60:63], v[44:47], v[12:15]\n"
  "v_exp_f32 v80, v80\n"
  "v_mfma_f32_16x16x32_bf16 v[12:15], a[76:79], v[48:51], v[12:15]\n"
  "buffer_load_dwordx4 v92, s[36:39], s52 offen offset:3072 sc1 lds\n"
  "v_lshlrev_b32 v119, 12, v202\n"
  "v_mfma_f32_16x16x32_bf16 v[12:15], a[92:95], v[52:55], v[12:15]\n"
  "s_waitcnt lgkmcnt(0)\n"
  "v_exp_f32 v81, v81\n"
  "v_mfma_f32_16x16x32_bf16 v[0:3], a[96:99], v[56:59], v[0:3]\n"
  "ds_read_b128 v[32:35], v89 offset:61440\n"
  "v_mul_f32 v82, v18, v26\n"
  "v_mfma_f32_16x16x32_bf16 v[0:3], a[112:115], v[60:63], v[0:3]\n"
  "v_mul_f32 v83, v19, v27\n"
  "v_add_f32 v80, 1.0, v80\n"
  "v_mfma_f32_16x16x32_bf16 v[0:3], a[128:131], v[64:67], v[0:3]\n"
  "s_add_u32 m0, s49, 137216\n"
  "ds_read_b128 v[36:39], v89 offset:62464\n"
  "v_add_f32 v81, 1.0, v81\n"
  "v_mfma_f32_16x16x32_bf16 v[0:3], a[144:147], v[68:71], v[0:3]\n"
  "buffer_load_dwordx4 v93, s[36:39], s52 offen sc1 lds\n"
  "v_bfe_u32 v120, v200, 2, 3\n"
  "v_mfma_f32_16x16x32_bf16 v[0:3], a[160:163], v[72:75], v[0:3]\n"
  "s_add_u32 s52, s52, 18432\n"
  "v_rcp_f32 v80, v80\n"
  "v_mfma_f32_16x16x32_bf16 v[0:3], a[176:179], v[76:79], v[0:3]\n"
  "s_cmp_eq_u32 s52, s54\n"
  "s_cselect_b32 s52, s53, s52\n"
  "ds_read_b128 v[40:43], v89 offset:63488\n"
  "v_mfma_f32_16x16x32_bf16 v[4:7], a[100:103], v[56:59], v[4:7]\n"
  "v_rcp_f32 v81, v81\n"
  "v_mfma_f32_16x16x32_bf16 v[4:7], a[116:119], v[60:63], v[4:7]\n"
  "s_add_u32 m0, s48, 131072\n"
  "v_mul_f32 v82, v82, v80\n"
  "v_mul_f32 v83, v83, v81\n"
  "v_mfma_f32_16x16x32_bf16 v[4:7], a[132:135], v[64:67], v[4:7]\n"
  "buffer_load_dwordx4 v92, s[36:39], s52 offen offset:0 sc1 lds\n"
  "ds_read_b128 v[44:47], v89 offset:64512\n"
  "v_mfma_f32_16x16x32_bf16 v[4:7], a[148:151], v[68:71], v[4:7]\n"
  "v_cvt_pk_bf16_f32 v85, v82, v83\n"
  "v_mul_f32 v80, v20, v20\n"
  "v_mfma_f32_16x16x32_bf16 v[4:7], a[164:167], v[72:75], v[4:7]\n"
  "v_mul_f32 v81, v21, v21\n"
  "v_fmaak_f32 v80, v88, v80, 0xc0135761\n"
  "v_mfma_f32_16x16x32_bf16 v[4:7], a[180:183], v[76:79], v[4:7]\n"
  "ds_read_b128 v[48:51], v89 offset:20480\n"
  "v_fmaak_f32 v81, v88, v81, 0xc0135761\n"
  "v_mfma_f32_16x16x32_bf16 v[8:11], a[104:107], v[56:59], v[8:11]\n"
  "v_mul_f32 v80, v20, v80\n"
  "v_mul_f32 v81, v21, v81\n"
  "v_mfma_f32_16x16x32_bf16 v[8:11], a[120:123], v[60:63], v[8:11]\n"
  "buffer_load_dwordx4 v92, s[36:39], s52 offen offset:1024 sc1 lds\n"
  "v_bfe_u32 v121, v200, 3, 2\n"
  "v_mfma_f32_16x16x32_bf16 v[8:11], a[136:139], v[64:67], v[8:11]\n"
  "ds_read_b128 v[52:55], v89 offset:21504\n"
  "v_mul_u32_u24 v122, 18432, v112\n"
  "v_mfma_f32_16x16x32_bf16 v[8:11], a[152:155], v[68:71], v[8:11]\n"
  "v_exp_f32 v80, v80\n"
  "v_mfma_f32_16x16x32_bf16 v[8:11], a[168:171], v[72:75], v[8:11]\n"
  "v_exp_f32 v81, v81\n"
  "v_mfma_f32_16x16x32_bf16 v[8:11], a[184:187], v[76:79], v[8:11]\n"
  "v_mul_f32 v82, v20, v28\n"
  "v_mul_f32 v83, v21, v29\n"
  "v_mfma_f32_16x16x32_bf16 v[12:15], a[108:111], v[56:59], v[12:15]\n"
  "buffer_load_dwordx4 v92, s[36:39], s52 offen offset:2048 sc1 lds\n"
  "v_add_f32 v80, 1.0, v80\n"
  "v_mfma_f32_16x16x32_bf16 v[12:15], a[124:127], v[60:63], v[12:15]\n"
  "v_add_f32 v81, 1.0, v81\n"
  "v_mul_u32_u24 v123, 3072, v113\n"
  "v_mfma_f32_16x16x32_bf16 v[12:15], a[140:143], v[64:67], v[12:15]\n"
  "s_lshl2_add_u32 s58, s58, s8\n"
  "v_rcp_f32 v80, v80\n"
  "v_mfma_f32_16x16x32_bf16 v[12:15], a[156:159], v[68:71], v[12:15]\n"
  "v_rcp_f32 v81, v81\n"
  "v_mfma_f32_16x16x32_bf16 v[12:15], a[172:175], v[72:75], v[12:15]\n"
  "v_mul_f32 v82, v82, v80\n"
  "v_mul_f32 v83, v83, v81\n"
  "v_mfma_f32_16x16x32_bf16 v[12:15], a[188:191], v[76:79], v[12:15]\n"
  "s_waitcnt lgkmcnt(0)\n"
  "v_cvt_pk_bf16_f32 v86, v82, v83\n"
  "v_mul_f32 v80, v22, v22\n"
  "v_mfma_f32_16x16x32_bf16 v[0:3], a[192:195], v[32:35], v[0:3]\n"
  "buffer_load_dwordx4 v92, s[36:39], s52 offen offset:3072 sc1 lds\n"
  "ds_read_b128 v[56:59], v90 offset:0\n"
  "v_mfma_f32_16x16x32_bf16 v[0:3], a[208:211], v[36:39], v[0:3]\n"
  "v_mul_f32 v81, v23, v23\n"
  "v_fmaak_f32 v80, v88, v80, 0xc0135761\n"
  "v_mfma_f32_16x16x32_bf16 v[0:3], a[224:227], v[40:43], v[0:3]\n"
  "ds_read_b128 v[60:63], v90 offset:1024\n"
  "v_fmaak_f32 v81, v88, v81, 0xc0135761\n"
  "v_mfma_f32_16x16x32_bf16 v[0:3], a[240:243], v[44:47], v[0:3]\n"
  "s_add_u32 m0, s49, 135168\n"
  "v_mul_f32 v80, v22, v80\n"
  "v_mul_f32 v81, v23, v81\n"
  "v_mfma_f32_16x16x32_bf16 v[0:3], v[224:227], v[48:51], v[0:3]\n"
  "buffer_load_dwordx4 v93, s[36:39], s52 offen sc1 lds\n"
  "v_and_b32 v124, 0x70, v89\n"
  "v_mfma_f32_16x16x32_bf16 v[0:3], v[240:243], v[52:55], v[0:3]\n"
  "s_add_u32 s52, s52, 18432\n"
  "ds_read_b128 v[64:67], v90 offset:2048\n"
  "v_lshlrev_b32 v125, 5, v116\n"
  "v_mfma_f32_16x16x32_bf16 v[4:7], a[196:199], v[32:35], v[4:7]\n"
  "s_cmp_eq_u32 s52, s54\n"
  "s_cselect_b32 s52, s53, s52\n"
  "v_lshl_add_u32 v126, v120, 7, v119\n"
  "v_mfma_f32_16x16x32_bf16 v[4:7], a[212:215], v[36:39], v[4:7]\n"
  "v_exp_f32 v80, v80\n"
  "v_mfma_f32_16x16x32_bf16 v[4:7], a[228:231], v[40:43], v[4:7]\n"
  "s_add_u32 m0, s48, 32768\n"
  "ds_read_b128 v[68:71], v90 offset:3072\n"
  "v_lshl_add_u32 v127, v121, 5, v202\n"
  "v_mfma_f32_16x16x32_bf16 v[4:7], a[244:247], v[44:47], v[4:7]\n"
  "buffer_load_dwordx4 v92, s[36:39], s52 offen offset:0 sc1 lds\n"
  "v_lshl_add_u32 v128, v118, 3, v114\n"
  "v_mfma_f32_16x16x32_bf16 v[4:7], v[228:231], v[48:51], v[4:7]\n"
  "v_exp_f32 v81, v81\n"
  "v_mfma_f32_16x16x32_bf16 v[4:7], v[244:247], v[52:55], v[4:7]\n"
  "ds_read_b128 v[72:75], v90 offset:4096\n"
  "v_mul_f32 v82, v22, v30\n"
  "v_mfma_f32_16x16x32_bf16 v[8:11], a[200:203], v[32:35], v[8:11]\n"
  "v_mul_f32 v83, v23, v31\n"
  "v_add_f32 v80, 1.0, v80\n"
  "v_mfma_f32_16x16x32_bf16 v[8:11], a[216:219], v[36:39], v[8:11]\n"
  "v_add_f32 v81, 1.0, v81\n"
  "v_lshl_add_u32 v129, v201, 8, v122\n"
  "v_mfma_f32_16x16x32_bf16 v[8:11], a[232:235], v[40:43], v[8:11]\n"
  "buffer_load_dwordx4 v92, s[36:39], s52 offen offset:1024 sc1 lds\n"
  "ds_read_b128 v[76:79], v90 offset:5120\n"
  "v_mfma_f32_16x16x32_bf16 v[8:11], a[248:251], v[44:47], v[8:11]\n"
  "v_rcp_f32 v80, v80\n"
  "v_mfma_f32_16x16x32_bf16 v[8:11], v[232:235], v[48:51], v[8:11]\n"
  "v_rcp_f32 v81, v81\n"
  "v_mfma_f32_16x16x32_bf16 v[8:11], v[248:251], v[52:55], v[8:11]\n"
  "v_mul_f32 v82, v82, v80\n"
  "v_mul_f32 v83, v83, v81\n"
  "v_mfma_f32_16x16x32_bf16 v[12:15], a[204:207], v[32:35], v[12:15]\n"
  "v_cvt_pk_bf16_f32 v87, v82, v83\n"
  "v_lshl_add_u32 v130, v118, 1, v202\n"
  "v_mfma_f32_16x16x32_bf16 v[12:15], a[220:223], v[36:39], v[12:15]\n"
  "buffer_load_dwordx4 v92, s[36:39], s52 offen offset:2048 sc1 lds\n"
  "v_lshl_add_u32 v131, v117, 9, v123\n"
  "v_mfma_f32_16x16x32_bf16 v[12:15], a[236:239], v[40:43], v[12:15]\n"
  "s_lshl_b32 s58, s58, 9\n"
  "v_add_u32 v132, s6, v115\n"
  "v_mul_u32_u24 v133, 1152, v114\n"
  "v_mfma_f32_16x16x32_bf16 v[12:15], a[252:255], v[44:47], v[12:15]\n"
  "buffer_store_dwordx4 v[84:87], v89, s[36:39], s55 offen sc1\n"
  "v_xor_b32 v134, v124, v125\n"
  "v_mfma_f32_16x16x32_bf16 v[12:15], v[236:239], v[48:51], v[12:15]\n"
  "s_add_u32 s55, s55, 147456\n"
  "v_lshl_add_u32 v135, v115, 3, v126\n"
  "v_xor_b32 v200, s10, v121\n"
  "v_mfma_f32_16x16x32_bf16 v[12:15], v[252:255], v[52:55], v[12:15]\n"
  "s_waitcnt lgkmcnt(0)\n"
  "v_mul_u32_u24 v113, 1152, v127\n"
  "v_lshl_add_u32 v207, v128, 4, v129\n"
  "v_mfma_f32_16x16x32_bf16 v[16:19], a[0:3], v[56:59], 0\n"
  "s_cmp_eq_u32 s55, s57\n"
  "s_cselect_b32 s55, s56, s55\n"
  "buffer_load_dwordx4 v92, s[36:39], s52 offen offset:3072 sc1 lds\n"
  "ds_read_b128 v[32:35], v90 offset:6144\n"
  "v_mfma_f32_16x16x32_bf16 v[16:19], a[16:19], v[60:63], v[16:19]\n"
  "v_mul_f32 v80, v0, v0\n"
  "v_mul_f32 v81, v1, v1\n"
  "v_mfma_f32_16x16x32_bf16 v[16:19], a[32:35], v[64:67], v[16:19]\n"
  "ds_read_b128 v[36:39], v90 offset:7168\n"
  "v_fmaak_f32 v80, v88, v80, 0xc0135761\n"
  "v_mfma_f32_16x16x32_bf16 v[16:19], a[48:51], v[68:71], v[16:19]\n"
  "v_fmaak_f32 v81, v88, v81, 0xc0135761\n"
  "v_mul_f32 v80, v0, v80\n"
  "v_mfma_f32_16x16x32_bf16 v[16:19], a[64:67], v[72:75], v[16:19]\n"
  "s_add_u32 m0, s49, 133120\n"
  "v_mul_f32 v81, v1, v81\n"
  "v_lshlrev_b32 v136, 6, v130\n"
  "v_mfma_f32_16x16x32_bf16 v[16:19], a[80:83], v[76:79], v[16:19]\n"
  "buffer_load_dwordx4 v93, s[36:39], s52 offen sc1 lds\n"
  "ds_read_b128 v[40:43], v90 offset:8192\n"
  "v_mfma_f32_16x16x32_bf16 v[20:23], a[4:7], v[56:59], 0\n"
  "s_add_u32 s52, s52, 18432\n"
  "v_exp_f32 v80, v80\n"
  "v_mfma_f32_16x16x32_bf16 v[20:23], a[20:23], v[60:63], v[20:23]\n"
  "s_cmp_eq_u32 s52, s54\n"
  "s_cselect_b32 s52, s53, s52\n"
  "v_lshlrev_b32 v137, 12, v112\n"
  "v_mfma_f32_16x16x32_bf16 v[20:23], a[36:39], v[64:67], v[20:23]\n"
  "ds_read_b128 v[44:47], v90 offset:9216\n"
  "v_lshlrev_b32 v138, 2, v115\n"
  "v_mfma_f32_16x16x32_bf16 v[20:23], a[52:55], v[68:71], v[20:23]\n"
  "v_exp_f32 v81, v81\n"
  "v_mfma_f32_16x16x32_bf16 v[20:23], a[68:71], v[72:75], v[20:23]\n"
  "v_mul_f32 v82, v0, v8\n"
  "v_mul_f32 v83, v1, v9\n"
  "v_mfma_f32_16x16x32_bf16 v[20:23], a[84:87], v[76:79], v[20:23]\n"
  "ds_read_b128 v[48:51], v90 offset:10240\n"
  "v_add_f32 v80, 1.0, v80\n"
  "v_mfma_f32_16x16x32_bf16 v[24:27], a[8:11], v[56:59], 0\n"
  "v_add_f32 v81, 1.0, v81\n"
  "v_lshl_add_u32 v139, v112, 8, v131\n"
  "v_mfma_f32_16x16x32_bf16 v[24:27], a[24:27], v[60:63], v[24:27]\n"
  "s_add_u32 s58, s58, 7082368\n"
  "v_rcp_f32 v80, v80\n"
  "v_mfma_f32_16x16x32_bf16 v[24:27], a[40:43], v[64:67], v[24:27]\n"
  "ds_read_b128 v[52:55], v90 offset:11264\n"
  "v_and_b32 v116, 3, v132\n"
  "v_mfma_f32_16x16x32_bf16 v[24:27], a[56:59], v[68:71], v[24:27]\n"
  "s_mul_i32 s7, s10, 147456\n"
  "v_rcp_f32 v81, v81\n"
  "v_mfma_f32_16x16x32_bf16 v[24:27], a[72:75], v[72:75], v[24:27]\n"
  "v_mul_f32 v82, v82, v80\n"
  "v_mul_f32 v83, v83, v81\n"
  "v_mfma_f32_16x16x32_bf16 v[24:27], a[88:91], v[76:79], v[24:27]\n"
  "v_cvt_pk_bf16_f32 v84, v82, v83\n"
  "v_mul_f32 v80, v2, v2\n"
  "v_mfma_f32_16x16x32_bf16 v[28:31], a[12:15], v[56:59], 0\n"
  "v_mul_f32 v81, v3, v3\n"
  "v_fmaak_f32 v80, v88, v80, 0xc0135761\n"
  "v_mfma_f32_16x16x32_bf16 v[28:31], a[28:31], v[60:63], v[28:31]\n"
  "v_fmaak_f32 v81, v88, v81, 0xc0135761\n"
  "v_mul_f32 v80, v2, v80\n"
  "v_mfma_f32_16x16x32_bf16 v[28:31], a[44:47], v[64:67], v[28:31]\n"
  "v_mul_f32 v81, v3, v81\n"
  "v_add3_u32 v117, v133, v134, s28\n"
  "v_mfma_f32_16x16x32_bf16 v[28:31], a[60:63], v[68:71], v[28:31]\n"
  "v_exp_f32 v80, v80\n"
  "v_mfma_f32_16x16x32_bf16 v[28:31], a[76:79], v[72:75], v[28:31]\n"
  "v_exp_f32 v81, v81\n"
  "v_mfma_f32_16x16x32_bf16 v[28:31], a[92:95], v[76:79], v[28:31]\n"
  "s_waitcnt lgkmcnt(0)\n"
  "v_mul_f32 v82, v2, v10\n"
  "v_mul_f32 v83, v3, v11\n"
  "v_mfma_f32_16x16x32_bf16 v[16:19], a[96:99], v[32:35], v[16:19]\n"
  "ds_read_b128 v[56:59], v90 offset:12288\n"
  "v_add_f32 v80, 1.0, v80\n"
  "v_mfma_f32_16x16x32_bf16 v[16:19], a[112:115], v[36:39], v[16:19]\n"
  "v_add_f32 v81, 1.0, v81\n"
  "v_lshl_add_u32 v99, v200, 5, v135\n"
  "v_mfma_f32_16x16x32_bf16 v[16:19], a[128:131], v[40:43], v[16:19]\n"
  "ds_read_b128 v[60:63], v90 offset:13312\n"
  "v_lshl_add_u32 v118, v201, 4, v113\n"
  "v_mfma_f32_16x16x32_bf16 v[16:19], a[144:147], v[44:47], v[16:19]\n"
  "v_rcp_f32 v80, v80\n"
  "v_mfma_f32_16x16x32_bf16 v[16:19], a[160:163], v[48:51], v[16:19]\n"
  "v_rcp_f32 v81, v81\n"
  "v_mfma_f32_16x16x32_bf16 v[16:19], a[176:179], v[52:55], v[16:19]\n"
  "ds_read_b128 v[64:67], v90 offset:14336\n"
  "v_mul_f32 v82, v82, v80\n"
  "v_mfma_f32_16x16x32_bf16 v[20:23], a[100:103], v[32:35], v[20:23]\n"
  "v_mul_f32 v83, v83, v81\n"
  "v_cvt_pk_bf16_f32 v85, v82, v83\n"
  "v_mfma_f32_16x16x32_bf16 v[20:23], a[116:119], v[36:39], v[20:23]\n"
  "v_mul_f32 v80, v4, v4\n"
  "v_mul_f32 v81, v5, v5\n"
  "v_mfma_f32_16x16x32_bf16 v[20:23], a[132:135], v[40:43], v[20:23]\n"
  "ds_read_b128 v[68:71], v90 offset:15360\n"
  "v_fmaak_f32 v80, v88, v80, 0xc0135761\n"
  "v_mfma_f32_16x16x32_bf16 v[20:23], a[148:151], v[44:47], v[20:23]\n"
  "v_fmaak_f32 v81, v88, v81, 0xc0135761\n"
  "v_mul_f32 v80, v4, v80\n"
  "v_mfma_f32_16x16x32_bf16 v[20:23], a[164:167], v[48:51], v[20:23]\n"
  "v_mul_f32 v81, v5, v81\n"
  "v_add_u32 v119, 73728, v207\n"
  "v_mfma_f32_16x16x32_bf16 v[20:23], a[180:183], v[52:55], v[20:23]\n"
  "ds_read_b128 v[72:75], v91 offset:30720\n"
  "v_xor_b32 v202, v136, v89\n"
  "v_mfma_f32_16x16x32_bf16 v[24:27], a[104:107], v[32:35], v[24:27]\n"
  "v_exp_f32 v80, v80\n"
  "v_mfma_f32_16x16x32_bf16 v[24:27], a[120:123], v[36:39], v[24:27]\n"
  "v_exp_f32 v81, v81\n"
  "v_mfma_f32_16x16x32_bf16 v[24:27], a[136:139], v[40:43], v[24:27]\n"
  "ds_read_b128 v[76:79], v91 offset:31744\n"
  "v_mul_f32 v82, v4, v12\n"
  "v_mfma_f32_16x16x32_bf16 v[24:27], a[152:155], v[44:47], v[24:27]\n"
  "v_mul_f32 v83, v5, v13\n"
  "v_add_f32 v80, 1.0, v80\n"
  "v_mfma_f32_16x16x32_bf16 v[24:27], a[168:171], v[48:51], v[24:27]\n"
  "v_add_f32 v81, 1.0, v81\n"
  "v_lshl_add_u32 v114, v201, 9, v137\n"
  "v_mfma_f32_16x16x32_bf16 v[24:27], a[184:187], v[52:55], v[24:27]\n"
  "v_rcp_f32 v80, v80\n"
  "v_mfma_f32_16x16x32_bf16 v[28:31], a[108:111], v[32:35], v[28:31]\n"
  "v_rcp_f32 v81, v81\n"
  "v_mfma_f32_16x16x32_bf16 v[28:31], a[124:127], v[36:39], v[28:31]\n"
  "v_mul_f32 v82, v82, v80\n"
  "v_mul_f32 v83, v83, v81\n"
  "v_mfma_f32_16x16x32_bf16 v[28:31], a[140:143], v[40:43], v[28:31]\n"
  "v_cvt_pk_bf16_f32 v86, v82, v83\n"
  "v_mul_f32 v80, v6, v6\n"
  "v_mfma_f32_16x16x32_bf16 v[28:31], a[156:159], v[44:47], v[28:31]\n"
  "v_mul_f32 v81, v7, v7\n"
  "v_fmaak_f32 v80, v88, v80, 0xc0135761\n"
  "v_mfma_f32_16x16x32_bf16 v[28:31], a[172:175], v[48:51], v[28:31]\n"
  "v_fmaak_f32 v81, v88, v81, 0xc0135761\n"
  "v_mul_f32 v80, v6, v80\n"
  "v_mfma_f32_16x16x32_bf16 v[28:31], a[188:191], v[52:55], v[28:31]\n"
  "s_waitcnt vmcnt(22) lgkmcnt(0)\n"
  "s_barrier\n"
  "v_mfma_f32_16x16x32_bf16 v[16:19], a[192:195], v[56:59], v[16:19]\n"
  "s_add_u32 m0, s48, 16384\n"
  "ds_read_b128 v[32:35], v90 offset:16384\n"
  "v_mul_f32 v81, v7, v81\n"
  "v_mfma_f32_16x16x32_bf16 v[16:19], a[208:211], v[60:63], v[16:19]\n"
  "buffer_load_dwordx4 v92, s[36:39], s52 offen offset:0 sc1 lds\n"
  "v_xor_b32 v120, v138, v128\n"
  "v_mfma_f32_16x16x32_bf16 v[16:19], a[224:227], v[64:67], v[16:19]\n"
  "ds_read_b128 v[36:39], v90 offset:17408\n"
  "v_lshl_add_u32 v212, v128, 4, v139\n"
  "v_mfma_f32_16x16x32_bf16 v[16:19], a[240:243], v[68:71], v[16:19]\n"
  "v_exp_f32 v80, v80\n"
  "v_mfma_f32_16x16x32_bf16 v[16:19], v[224:227], v[72:75], v[16:19]\n"
  "v_exp_f32 v81, v81\n"
  "v_mfma_f32_16x16x32_bf16 v[16:19], v[240:243], v[76:79], v[16:19]\n"
  "ds_read_b128 v[40:43], v90 offset:18432\n"
  "v_mul_f32 v82, v6, v14\n"
  "v_mfma_f32_16x16x32_bf16 v[20:23], a[196:199], v[56:59], v[20:23]\n"
  "buffer_load_dwordx4 v92, s[36:39], s52 offen offset:1024 sc1 lds\n"
  "v_mul_f32 v83, v7, v15\n"
  "v_mfma_f32_16x16x32_bf16 v[20:23], a[212:215], v[60:63], v[20:23]\n"
  "v_add_f32 v80, 1.0, v80\n"
  "v_add_f32 v81, 1.0, v81\n"
  "v_mfma_f32_16x16x32_bf16 v[20:23], a[228:231], v[64:67], v[20:23]\n"
  "ds_read_b128 v[44:47], v90 offset:19456\n"
  "v_lshl_add_u32 v121, v116, 9, s72\n"
  "v_mfma_f32_16x16x32_bf16 v[20:23], a[244:247], v[68:71], v[20:23]\n"
  "v_rcp_f32 v80, v80\n"
  "v_mfma_f32_16x16x32_bf16 v[20:23], v[228:231], v[72:75], v[20:23]\n"
  "v_rcp_f32 v81, v81\n"
  "v_mfma_f32_16x16x32_bf16 v[20:23], v[244:247], v[76:79], v[20:23]\n"
  "buffer_load_dwordx4 v92, s[36:39], s52 offen offset:2048 sc1 lds\n"
  "ds_read_b128 v[48:51], v90 offset:20480\n"
  "v_mfma_f32_16x16x32_bf16 v[24:27], a[200:203], v[56:59], v[24:27]\n"
  "v_mul_f32 v82, v82, v80\n"
  "v_mul_f32 v83, v83, v81\n"
  "v_mfma_f32_16x16x32_bf16 v[24:27], a[216:219], v[60:63], v[24:27]\n"
  "v_cvt_pk_bf16_f32 v87, v82, v83\n"
  "v_mov_b32 v122, s58\n"
  "v_mfma_f32_16x16x32_bf16 v[24:27], a[232:235], v[64:67], v[24:27]\n"
  "s_mov_b64 s[34:35], 16\n"
  "s_mov_b64 s[44:45], s[20:21]\n"
  "ds_read_b128 v[52:55], v90 offset:21504\n"
  "v_mfma_f32_16x16x32_bf16 v[24:27], a[248:251], v[68:71], v[24:27]\n"
  "v_add_u32 v97, s7, v89\n"
  "v_add_u32 v98, s71, v89\n"
  "v_mfma_f32_16x16x32_bf16 v[24:27], v[232:235], v[72:75], v[24:27]\n"
  "buffer_load_dwordx4 v92, s[36:39], s52 offen offset:3072 sc1 lds\n"
  "v_add_u32 v107, s29, v89\n"
  "v_mfma_f32_16x16x32_bf16 v[24:27], v[248:251], v[76:79], v[24:27]\n"
  "v_add_u32 v94, s22, v117\n"
  "v_add_u32 v95, s9, v117\n"
  "v_mfma_f32_16x16x32_bf16 v[28:31], a[204:207], v[56:59], v[28:31]\n"
  "v_add_u32 v96, s23, v117\n"
  "v_mov_b32 v108, -1\n"
  "v_mfma_f32_16x16x32_bf16 v[28:31], a[220:223], v[60:63], v[28:31]\n"
  "v_mov_b32 v109, -1\n"
  "v_mov_b32 v110, -1\n"
  "v_mfma_f32_16x16x32_bf16 v[28:31], a[236:239], v[64:67], v[28:31]\n"
  "s_add_u32 m0, s49, 131072\n"
  "v_mov_b32 v111, -1\n"
  "v_add_u32 v100, 0x10000, v99\n"
  "v_mfma_f32_16x16x32_bf16 v[28:31], a[252:255], v[68:71], v[28:31]\n"
  "buffer_load_dwordx4 v93, s[36:39], s52 offen sc1 lds\n"
  "v_add_u32 v101, 0x20000, v99\n"
  "v_mfma_f32_16x16x32_bf16 v[28:31], v[236:239], v[72:75], v[28:31]\n"
  "s_add_u32 s52, s52, 18432\n"
  "buffer_store_dwordx4 v[84:87], v89, s[36:39], s55 offen sc1\n"
  "v_add_u32 v204, s77, v118\n"
  "v_mfma_f32_16x16x32_bf16 v[28:31], v[252:255], v[76:79], v[28:31]\n"
  "s_waitcnt lgkmcnt(0)\n"
  "s_add_u32 s55, s55, 147456\n"
  "v_add_u32 v205, s73, v118\n"
  "v_mfma_f32_16x16x32_bf16 v[0:3], a[0:3], v[32:35], 0\n"
  "s_cmp_eq_u32 s52, s54\n"
  "s_cbranch_execz Ltramp%=\n"
  "s_cselect_b32 s52, s53, s52\n"
  "ds_read_b128 v[56:59], v90 offset:22528\n"
  "v_mfma_f32_16x16x32_bf16 v[0:3], a[16:19], v[36:39], v[0:3]\n"
  "s_add_u32 m0, s48, 49152\n"
  "s_cmp_eq_u32 s55, s57\n"
  "s_cselect_b32 s55, s56, s55\n"
  "v_mfma_f32_16x16x32_bf16 v[0:3], a[32:35], v[40:43], v[0:3]\n"
  "buffer_load_dwordx4 v92, s[36:39], s52 offen offset:0 sc1 lds\n"
  "ds_read_b128 v[60:63], v90 offset:23552\n"
  "v_mfma_f32_16x16x32_bf16 v[0:3], a[48:51], v[44:47], v[0:3]\n"
  "v_mul_f32 v80, v16, v16\n"
  "v_mul_f32 v81, v17, v17\n"
  "v_mfma_f32_16x16x32_bf16 v[0:3], a[64:67], v[48:51], v[0:3]\n"
  "v_fmaak_f32 v80, v88, v80, 0xc0135761\n"
  "v_fmaak_f32 v81, v88, v81, 0xc0135761\n"
  "v_mfma_f32_16x16x32_bf16 v[0:3], a[80:83], v[52:55], v[0:3]\n"
  "ds_read_b128 v[64:67], v90 offset:24576\n"
  "v_mul_f32 v80, v16, v80\n"
  "v_mfma_f32_16x16x32_bf16 v[4:7], a[4:7], v[32:35], 0\n"
  "v_mul_f32 v81, v17, v81\n"
  "v_add_u32 v206, s74, v118\n"
  "v_mfma_f32_16x16x32_bf16 v[4:7], a[20:23], v[36:39], v[4:7]\n"
  "buffer_load_dwordx4 v92, s[36:39], s52 offen offset:1024 sc1 lds\n"
  "v_add_u32 v208, 36864, v207\n"
  "v_mfma_f32_16x16x32_bf16 v[4:7], a[36:39], v[40:43], v[4:7]\n"
  "ds_read_b128 v[68:71], v90 offset:25600\n"
  "v_or_b32 v209, s31, v119\n"
  "v_mfma_f32_16x16x32_bf16 v[4:7], a[52:55], v[44:47], v[4:7]\n"
  "v_exp_f32 v80, v80\n"
  "v_mfma_f32_16x16x32_bf16 v[4:7], a[68:71], v[48:51], v[4:7]\n"
  "v_exp_f32 v81, v81\n"
  "v_mfma_f32_16x16x32_bf16 v[4:7], a[84:87], v[52:55], v[4:7]\n"
  "ds_read_b128 v[72:75], v90 offset:26624\n"
  "v_mul_f32 v82, v16, v24\n"
  "v_mfma_f32_16x16x32_bf16 v[8:11], a[8:11], v[32:35], 0\n"
  "buffer_load_dwordx4 v92, s[36:39], s52 offen offset:2048 sc1 lds\n"
  "v_mul_f32 v83, v17, v25\n"
  "v_mfma_f32_16x16x32_bf16 v[8:11], a[24:27], v[36:39], v[8:11]\n"
  "v_add_f32 v80, 1.0, v80\n"
  "v_add_f32 v81, 1.0, v81\n"
  "v_mfma_f32_16x16x32_bf16 v[8:11], a[40:43], v[40:43], v[8:11]\n"
  "ds_read_b128 v[76:79], v90 offset:27648\n"
  "v_add_u32 v210, s50, v202\n"
  "v_mfma_f32_16x16x32_bf16 v[8:11], a[56:59], v[44:47], v[8:11]\n"
  "v_rcp_f32 v80, v80\n"
  "v_mfma_f32_16x16x32_bf16 v[8:11], a[72:75], v[48:51], v[8:11]\n"
  "v_rcp_f32 v81, v81\n"
  "v_mfma_f32_16x16x32_bf16 v[8:11], a[88:91], v[52:55], v[8:11]\n"
  "buffer_load_dwordx4 v92, s[36:39], s52 offen offset:3072 sc1 lds\n"
  "v_mul_f32 v82, v82, v80\n"
  "v_mfma_f32_16x16x32_bf16 v[12:15], a[12:15], v[32:35], 0\n"
  "v_mul_f32 v83, v83, v81\n"
  "v_cvt_pk_bf16_f32 v84, v82, v83\n"
  "v_mfma_f32_16x16x32_bf16 v[12:15], a[28:31], v[36:39], v[12:15]\n"
  "v_mul_f32 v80, v18, v18\n"
  "v_mul_f32 v81, v19, v19\n"
  "v_mfma_f32_16x16x32_bf16 v[12:15], a[44:47], v[40:43], v[12:15]\n"
  "v_fmaak_f32 v80, v88, v80, 0xc0135761\n"
  "v_fmaak_f32 v81, v88, v81, 0xc0135761\n"
  "v_mfma_f32_16x16x32_bf16 v[12:15], a[60:63], v[44:47], v[12:15]\n"
  "s_add_u32 m0, s49, 145408\n"
  "v_mul_f32 v80, v18, v80\n"
  "v_mul_f32 v81, v19, v81\n"
  "v_mfma_f32_16x16x32_bf16 v[12:15], a[76:79], v[48:51], v[12:15]\n"
  "buffer_load_dwordx4 v93, s[36:39], s52 offen sc1 lds\n"
  "v_lshl_add_u32 v211, v120, 4, v114\n"
  "v_mfma_f32_16x16x32_bf16 v[12:15], a[92:95], v[52:55], v[12:15]\n"
  "s_waitcnt lgkmcnt(0)\n"
  "v_exp_f32 v80, v80\n"
  "v_mfma_f32_16x16x32_bf16 v[0:3], a[96:99], v[56:59], v[0:3]\n"
  "s_add_u32 s52, s52, 18432\n"
  "ds_read_b128 v[32:35], v90 offset:28672\n"
  "v_or_b32 v213, s31, v212\n"
  "v_mfma_f32_16x16x32_bf16 v[0:3], a[112:115], v[60:63], v[0:3]\n"
  "s_cmp_eq_u32 s52, s54\n"
  "s_cselect_b32 s52, s53, s52\n"
  "v_cndmask_b32 v203, v121, v122, s[34:35]\n"
  "v_mfma_f32_16x16x32_bf16 v[0:3], a[128:131], v[64:67], v[0:3]\n"
  "ds_read_b128 v[36:39], v90 offset:29696\n"
  "v_mov_b32 v105, v203\n"
  "v_mfma_f32_16x16x32_bf16 v[0:3], a[144:147], v[68:71], v[0:3]\n"
  "v_exp_f32 v81, v81\n"
  "v_mfma_f32_16x16x32_bf16 v[0:3], a[160:163], v[72:75], v[0:3]\n"
  "v_mul_f32 v82, v18, v26\n"
  "v_mul_f32 v83, v19, v27\n"
  "v_mfma_f32_16x16x32_bf16 v[0:3], a[176:179], v[76:79], v[0:3]\n"
  "ds_read_b128 v[40:43], v90 offset:30720\n"
  "v_add_f32 v80, 1.0, v80\n"
  "v_mfma_f32_16x16x32_bf16 v[4:7], a[100:103], v[56:59], v[4:7]\n"
  "v_add_f32 v81, 1.0, v81\n"
  "v_mfma_f32_16x16x32_bf16 v[4:7], a[116:119], v[60:63], v[4:7]\n"
  "v_rcp_f32 v80, v80\n"
  "v_mfma_f32_16x16x32_bf16 v[4:7], a[132:135], v[64:67], v[4:7]\n"
  "ds_read_b128 v[44:47], v90 offset:31744\n"
  "v_mfma_f32_16x16x32_bf16 v[4:7], a[148:151], v[68:71], v[4:7]\n"
  "v_rcp_f32 v81, v81\n"
  "v_mfma_f32_16x16x32_bf16 v[4:7], a[164:167], v[72:75], v[4:7]\n"
  "v_mul_f32 v82, v82, v80\n"
  "v_mul_f32 v83, v83, v81\n"
  "v_mfma_f32_16x16x32_bf16 v[4:7], a[180:183], v[76:79], v[4:7]\n"
  "ds_read_b128 v[48:51], v91 offset:28672\n"
  "v_cvt_pk_bf16_f32 v85, v82, v83\n"
  "v_mfma_f32_16x16x32_bf16 v[8:11], a[104:107], v[56:59], v[8:11]\n"
  "v_mul_f32 v80, v20, v20\n"
  "v_mul_f32 v81, v21, v21\n"
  "v_mfma_f32_16x16x32_bf16 v[8:11], a[120:123], v[60:63], v[8:11]\n"
  "buffer_load_dword v214, v102, s[40:43], s60 offen offset:0\n"
  "v_fmaak_f32 v80, v88, v80, 0xc0135761\n"
  "v_mfma_f32_16x16x32_bf16 v[8:11], a[136:139], v[64:67], v[8:11]\n"
  "ds_read_b128 v[52:55], v91 offset:29696\n"
  "v_add_u32 v102, 8064, v102\n"
  "v_mfma_f32_16x16x32_bf16 v[8:11], a[152:155], v[68:71], v[8:11]\n"
  "v_fmaak_f32 v81, v88, v81, 0xc0135761\n"
  "v_mul_f32 v80, v20, v80\n"
  "v_mfma_f32_16x16x32_bf16 v[8:11], a[168:171], v[72:75], v[8:11]\n"
  "v_mul_f32 v81, v21, v81\n"
  "v_mfma_f32_16x16x32_bf16 v[8:11], a[184:187], v[76:79], v[8:11]\n"
  "v_exp_f32 v80, v80\n"
  "v_mfma_f32_16x16x32_bf16 v[12:15], a[108:111], v[56:59], v[12:15]\n"
  "v_exp_f32 v81, v81\n"
  "v_mfma_f32_16x16x32_bf16 v[12:15], a[124:127], v[60:63], v[12:15]\n"
  "v_mul_f32 v82, v20, v28\n"
  "v_mul_f32 v83, v21, v29\n"
  "v_mfma_f32_16x16x32_bf16 v[12:15], a[140:143], v[64:67], v[12:15]\n"
  "v_add_f32 v80, 1.0, v80\n"
  "v_add_f32 v81, 1.0, v81\n"
  "v_mfma_f32_16x16x32_bf16 v[12:15], a[156:159], v[68:71], v[12:15]\n"
  "v_rcp_f32 v80, v80\n"
  "v_mfma_f32_16x16x32_bf16 v[12:15], a[172:175], v[72:75], v[12:15]\n"
  "v_rcp_f32 v81, v81\n"
  "v_mfma_f32_16x16x32_bf16 v[12:15], a[188:191], v[76:79], v[12:15]\n"
  "s_waitcnt lgkmcnt(0)\n"
  "v_mul_f32 v82, v82, v80\n"
  "v_mul_f32 v83, v83, v81\n"
  "v_mfma_f32_16x16x32_bf16 v[0:3], a[192:195], v[32:35], v[0:3]\n"
  "ds_read_b128 v[56:59], v89 offset:0\n"
  "v_cvt_pk_bf16_f32 v86, v82, v83\n"
  "v_mfma_f32_16x16x32_bf16 v[0:3], a[208:211], v[36:39], v[0:3]\n"
  "v_mul_f32 v80, v22, v22\n"
  "v_mul_f32 v81, v23, v23\n"
  "v_mfma_f32_16x16x32_bf16 v[0:3], a[224:227], v[40:43], v[0:3]\n"
  "ds_read_b128 v[60:63], v89 offset:1024\n"
  "v_fmaak_f32 v80, v88, v80, 0xc0135761\n"
  "v_mfma_f32_16x16x32_bf16 v[0:3], a[240:243], v[44:47], v[0:3]\n"
  "v_fmaak_f32 v81, v88, v81, 0xc0135761\n"
  "v_mul_f32 v80, v22, v80\n"
  "v_mfma_f32_16x16x32_bf16 v[0:3], v[224:227], v[48:51], v[0:3]\n"
  "v_mul_f32 v81, v23, v81\n"
  "v_mfma_f32_16x16x32_bf16 v[0:3], v[240:243], v[52:55], v[0:3]\n"
  "ds_read_b128 v[64:67], v89 offset:2048\n"
  "v_mfma_f32_16x16x32_bf16 v[4:7], a[196:199], v[32:35], v[4:7]\n"
  "v_exp_f32 v80, v80\n"
  "v_mfma_f32_16x16x32_bf16 v[4:7], a[212:215], v[36:39], v[4:7]\n"
  "v_exp_f32 v81, v81\n"
  "v_mfma_f32_16x16x32_bf16 v[4:7], a[228:231], v[40:43], v[4:7]\n"
  "ds_read_b128 v[68:71], v89 offset:3072\n"
  "v_mul_f32 v82, v22, v30\n"
  "v_mfma_f32_16x16x32_bf16 v[4:7], a[244:247], v[44:47], v[4:7]\n"
  "v_mul_f32 v83, v23, v31\n"
  "v_add_f32 v80, 1.0, v80\n"
  "v_mfma_f32_16x16x32_bf16 v[4:7], v[228:231], v[48:51], v[4:7]\n"
  "v_add_f32 v81, 1.0, v81\n"
  "v_mfma_f32_16x16x32_bf16 v[4:7], v[244:247], v[52:55], v[4:7]\n"
  "ds_read_b128 v[72:75], v89 offset:4096\n"
  "v_mfma_f32_16x16x32_bf16 v[8:11], a[200:203], v[32:35], v[8:11]\n"
  "v_rcp_f32 v80, v80\n"
  "v_mfma_f32_16x16x32_bf16 v[8:11], a[216:219], v[36:39], v[8:11]\n"
  "v_rcp_f32 v81, v81\n"
  "v_mfma_f32_16x16x32_bf16 v[8:11], a[232:235], v[40:43], v[8:11]\n"
  "ds_read_b128 v[76:79], v89 offset:5120\n"
  "v_mul_f32 v82, v82, v80\n"
  "v_mfma_f32_16x16x32_bf16 v[8:11], a[248:251], v[44:47], v[8:11]\n"
  "v_mul_f32 v83, v83, v81\n"
  "v_cvt_pk_bf16_f32 v87, v82, v83\n"
  "v_mfma_f32_16x16x32_bf16 v[8:11], v[232:235], v[48:51], v[8:11]\n"
  "v_mfma_f32_16x16x32_bf16 v[8:11], v[248:251], v[52:55], v[8:11]\n"
  "v_mfma_f32_16x16x32_bf16 v[12:15], a[204:207], v[32:35], v[12:15]\n"
  "v_mfma_f32_16x16x32_bf16 v[12:15], a[220:223], v[36:39], v[12:15]\n"
  "v_mfma_f32_16x16x32_bf16 v[12:15], a[236:239], v[40:43], v[12:15]\n"
  "v_mfma_f32_16x16x32_bf16 v[12:15], a[252:255], v[44:47], v[12:15]\n"
  "buffer_store_dwordx4 v[84:87], v89, s[36:39], s55 offen sc1\n"
  "v_mfma_f32_16x16x32_bf16 v[12:15], v[236:239], v[48:51], v[12:15]\n"
  "s_add_u32 s55, s55, 147456\n"
  "v_mfma_f32_16x16x32_bf16 v[12:15], v[252:255], v[52:55], v[12:15]\n"
  "s_waitcnt lgkmcnt(0)\n"
  "v_mfma_f32_16x16x32_bf16 v[16:19], a[0:3], v[56:59], 0\n"
  "s_cmp_eq_u32 s55, s57\n"
  "s_cselect_b32 s55, s56, s55\n"
  "ds_read_b128 v[32:35], v89 offset:6144\n"
  "v_mfma_f32_16x16x32_bf16 v[16:19], a[16:19], v[60:63], v[16:19]\n"
  "v_mul_f32 v80, v0, v0\n"
  "v_mul_f32 v81, v1, v1\n"
  "v_mfma_f32_16x16x32_bf16 v[16:19], a[32:35], v[64:67], v[16:19]\n"
  "ds_read_b128 v[36:39], v89 offset:7168\n"
  "v_fmaak_f32 v80, v88, v80, 0xc0135761\n"
  "v_mfma_f32_16x16x32_bf16 v[16:19], a[48:51], v[68:71], v[16:19]\n"
  "v_fmaak_f32 v81, v88, v81, 0xc0135761\n"
  "v_mul_f32 v80, v0, v80\n"
  "v_mfma_f32_16x16x32_bf16 v[16:19], a[64:67], v[72:75], v[16:19]\n"
  "v_mul_f32 v81, v1, v81\n"
  "v_mfma_f32_16x16x32_bf16 v[16:19], a[80:83], v[76:79], v[16:19]\n"
  "ds_read_b128 v[40:43], v89 offset:8192\n"
  "v_mfma_f32_16x16x32_bf16 v[20:23], a[4:7], v[56:59], 0\n"
  "v_exp_f32 v80, v80\n"
  "v_mfma_f32_16x16x32_bf16 v[20:23], a[20:23], v[60:63], v[20:23]\n"
  "v_exp_f32 v81, v81\n"
  "v_mfma_f32_16x16x32_bf16 v[20:23], a[36:39], v[64:67], v[20:23]\n"
  "ds_read_b128 v[44:47], v89 offset:9216\n"
  "v_mul_f32 v82, v0, v8\n"
  "v_mfma_f32_16x16x32_bf16 v[20:23], a[52:55], v[68:71], v[20:23]\n"
  "v_mul_f32 v83, v1, v9\n"
  "v_add_f32 v80, 1.0, v80\n"
  "v_mfma_f32_16x16x32_bf16 v[20:23], a[68:71], v[72:75], v[20:23]\n"
  "v_add_f32 v81, 1.0, v81\n"
  "v_mfma_f32_16x16x32_bf16 v[20:23], a[84:87], v[76:79], v[20:23]\n"
  "ds_read_b128 v[48:51], v89 offset:10240\n"
  "v_mfma_f32_16x16x32_bf16 v[24:27], a[8:11], v[56:59], 0\n"
  "v_rcp_f32 v80, v80\n"
  "v_mfma_f32_16x16x32_bf16 v[24:27], a[24:27], v[60:63], v[24:27]\n"
  "v_rcp_f32 v81, v81\n"
  "v_mfma_f32_16x16x32_bf16 v[24:27], a[40:43], v[64:67], v[24:27]\n"
  "ds_read_b128 v[52:55], v89 offset:11264\n"
  "v_mul_f32 v82, v82, v80\n"
  "v_mfma_f32_16x16x32_bf16 v[24:27], a[56:59], v[68:71], v[24:27]\n"
  "v_mul_f32 v83, v83, v81\n"
  "v_cvt_pk_bf16_f32 v84, v82, v83\n"
  "v_mfma_f32_16x16x32_bf16 v[24:27], a[72:75], v[72:75], v[24:27]\n"
  "v_mul_f32 v80, v2, v2\n"
  "v_mul_f32 v81, v3, v3\n"
  "v_mfma_f32_16x16x32_bf16 v[24:27], a[88:91], v[76:79], v[24:27]\n"
  "v_fmaak_f32 v80, v88, v80, 0xc0135761\n"
  "v_fmaak_f32 v81, v88, v81, 0xc0135761\n"
  "v_mfma_f32_16x16x32_bf16 v[28:31], a[12:15], v[56:59], 0\n"
  "v_mul_f32 v80, v2, v80\n"
  "v_mul_f32 v81, v3, v81\n"
  "v_mfma_f32_16x16x32_bf16 v[28:31], a[28:31], v[60:63], v[28:31]\n"
  "v_exp_f32 v80, v80\n"
  "v_mfma_f32_16x16x32_bf16 v[28:31], a[44:47], v[64:67], v[28:31]\n"
  "v_exp_f32 v81, v81\n"
  "v_mfma_f32_16x16x32_bf16 v[28:31], a[60:63], v[68:71], v[28:31]\n"
  "v_mul_f32 v82, v2, v10\n"
  "v_mul_f32 v83, v3, v11\n"
  "v_mfma_f32_16x16x32_bf16 v[28:31], a[76:79], v[72:75], v[28:31]\n"
  "v_add_f32 v80, 1.0, v80\n"
  "v_add_f32 v81, 1.0, v81\n"
  "v_mfma_f32_16x16x32_bf16 v[28:31], a[92:95], v[76:79], v[28:31]\n"
  "s_waitcnt lgkmcnt(0)\n"
  "v_rcp_f32 v80, v80\n"
  "v_mfma_f32_16x16x32_bf16 v[16:19], a[96:99], v[32:35], v[16:19]\n"
  "ds_read_b128 v[56:59], v89 offset:12288\n"
  "v_mfma_f32_16x16x32_bf16 v[16:19], a[112:115], v[36:39], v[16:19]\n"
  "v_rcp_f32 v81, v81\n"
  "v_mfma_f32_16x16x32_bf16 v[16:19], a[128:131], v[40:43], v[16:19]\n"
  "ds_read_b128 v[60:63], v89 offset:13312\n"
  "v_mul_f32 v82, v82, v80\n"
  "v_mfma_f32_16x16x32_bf16 v[16:19], a[144:147], v[44:47], v[16:19]\n"
  "v_mul_f32 v83, v83, v81\n"
  "v_cvt_pk_bf16_f32 v85, v82, v83\n"
  "v_mfma_f32_16x16x32_bf16 v[16:19], a[160:163], v[48:51], v[16:19]\n"
  "v_mul_f32 v80, v4, v4\n"
  "v_mul_f32 v81, v5, v5\n"
  "v_mfma_f32_16x16x32_bf16 v[16:19], a[176:179], v[52:55], v[16:19]\n"
  "ds_read_b128 v[64:67], v89 offset:14336\n"
  "v_fmaak_f32 v80, v88, v80, 0xc0135761\n"
  "v_mfma_f32_16x16x32_bf16 v[20:23], a[100:103], v[32:35], v[20:23]\n"
  "v_fmaak_f32 v81, v88, v81, 0xc0135761\n"
  "v_mul_f32 v80, v4, v80\n"
  "v_mfma_f32_16x16x32_bf16 v[20:23], a[116:119], v[36:39], v[20:23]\n"
  "v_mul_f32 v81, v5, v81\n"
  "v_mfma_f32_16x16x32_bf16 v[20:23], a[132:135], v[40:43], v[20:23]\n"
  "ds_read_b128 v[68:71], v89 offset:15360\n"
  "v_mfma_f32_16x16x32_bf16 v[20:23], a[148:151], v[44:47], v[20:23]\n"
  "v_exp_f32 v80, v80\n"
  "v_mfma_f32_16x16x32_bf16 v[20:23], a[164:167], v[48:51], v[20:23]\n"
  "buffer_store_dwordx4 v[108:111], v107, s[36:39], s61 offen offset:0 sc1\n"
  "v_mfma_f32_16x16x32_bf16 v[20:23], a[180:183], v[52:55], v[20:23]\n"
  "ds_read_b128 v[72:75], v91 offset:26624\n"
  "v_mfma_f32_16x16x32_bf16 v[24:27], a[104:107], v[32:35], v[24:27]\n"
  "v_exp_f32 v81, v81\n"
  "v_mfma_f32_16x16x32_bf16 v[24:27], a[120:123], v[36:39], v[24:27]\n"
  "v_mul_f32 v82, v4, v12\n"
  "v_mul_f32 v83, v5, v13\n"
  "v_mfma_f32_16x16x32_bf16 v[24:27], a[136:139], v[40:43], v[24:27]\n"
  "ds_read_b128 v[76:79], v91 offset:27648\n"
  "v_add_f32 v80, 1.0, v80\n"
  "v_mfma_f32_16x16x32_bf16 v[24:27], a[152:155], v[44:47], v[24:27]\n"
  "v_add_f32 v81, 1.0, v81\n"
  "v_mfma_f32_16x16x32_bf16 v[24:27], a[168:171], v[48:51], v[24:27]\n"
  "v_rcp_f32 v80, v80\n"
  "v_mfma_f32_16x16x32_bf16 v[24:27], a[184:187], v[52:55], v[24:27]\n"
  "v_rcp_f32 v81, v81\n"
  "v_mfma_f32_16x16x32_bf16 v[28:31], a[108:111], v[32:35], v[28:31]\n"
  "v_mul_f32 v82, v82, v80\n"
  "v_mul_f32 v83, v83, v81\n"
  "v_mfma_f32_16x16x32_bf16 v[28:31], a[124:127], v[36:39], v[28:31]\n"
  "v_cvt_pk_bf16_f32 v86, v82, v83\n"
  "v_mul_f32 v80, v6, v6\n"
  "v_mfma_f32_16x16x32_bf16 v[28:31], a[140:143], v[40:43], v[28:31]\n"
  "v_mul_f32 v81, v7, v7\n"
  "v_fmaak_f32 v80, v88, v80, 0xc0135761\n"
  "v_mfma_f32_16x16x32_bf16 v[28:31], a[156:159], v[44:47], v[28:31]\n"
  "v_fmaak_f32 v81, v88, v81, 0xc0135761\n"
  "v_mul_f32 v80, v6, v80\n"
  "v_mfma_f32_16x16x32_bf16 v[28:31], a[172:175], v[48:51], v[28:31]\n"
  "v_mul_f32 v81, v7, v81\n"
  "v_mfma_f32_16x16x32_bf16 v[28:31], a[188:191], v[52:55], v[28:31]\n"
  "s_waitcnt vmcnt(25) lgkmcnt(0)\n"
  "s_barrier\n"
  "v_mfma_f32_16x16x32_bf16 v[16:19], a[192:195], v[56:59], v[16:19]\n"
  "ds_read_b128 v[32:35], v90 offset:32768\n"
  "v_mfma_f32_16x16x32_bf16 v[16:19], a[208:211], v[60:63], v[16:19]\n"
  "v_exp_f32 v80, v80\n"
  "v_mfma_f32_16x16x32_bf16 v[16:19], a[224:227], v[64:67], v[16:19]\n"
  "ds_read_b128 v[36:39], v90 offset:33792\n"
  "v_mfma_f32_16x16x32_bf16 v[16:19], a[240:243], v[68:71], v[16:19]\n"
  "v_exp_f32 v81, v81\n"
  "v_mfma_f32_16x16x32_bf16 v[16:19], v[224:227], v[72:75], v[16:19]\n"
  "v_mul_f32 v82, v6, v14\n"
  "v_mul_f32 v83, v7, v15\n"
  "v_mfma_f32_16x16x32_bf16 v[16:19], v[240:243], v[76:79], v[16:19]\n"
  "ds_read_b128 v[40:43], v90 offset:34816\n"
  "v_add_f32 v80, 1.0, v80\n"
  "v_mfma_f32_16x16x32_bf16 v[20:23], a[196:199], v[56:59], v[20:23]\n"
  "v_add_f32 v81, 1.0, v81\n"
  "v_mfma_f32_16x16x32_bf16 v[20:23], a[212:215], v[60:63], v[20:23]\n"
  "v_rcp_f32 v80, v80\n"
  "v_mfma_f32_16x16x32_bf16 v[20:23], a[228:231], v[64:67], v[20:23]\n"
  "ds_read_b128 v[44:47], v90 offset:35840\n"
  "v_mfma_f32_16x16x32_bf16 v[20:23], a[244:247], v[68:71], v[20:23]\n"
  "v_rcp_f32 v81, v81\n"
  "v_mfma_f32_16x16x32_bf16 v[20:23], v[228:231], v[72:75], v[20:23]\n"
  "v_mul_f32 v82, v82, v80\n"
  "v_mul_f32 v83, v83, v81\n"
  "v_mfma_f32_16x16x32_bf16 v[20:23], v[244:247], v[76:79], v[20:23]\n"
  "ds_read_b128 v[48:51], v90 offset:36864\n"
  "v_cvt_pk_bf16_f32 v87, v82, v83\n"
  "v_mfma_f32_16x16x32_bf16 v[24:27], a[200:203], v[56:59], v[24:27]\n"
  "v_mfma_f32_16x16x32_bf16 v[24:27], a[216:219], v[60:63], v[24:27]\n"
  "v_mfma_f32_16x16x32_bf16 v[24:27], a[232:235], v[64:67], v[24:27]\n"
  "ds_read_b128 v[52:55], v90 offset:37888\n"
  "v_mfma_f32_16x16x32_bf16 v[24:27], a[248:251], v[68:71], v[24:27]\n"
  "v_mfma_f32_16x16x32_bf16 v[24:27], v[232:235], v[72:75], v[24:27]\n"
  "v_mfma_f32_16x16x32_bf16 v[24:27], v[248:251], v[76:79], v[24:27]\n"
  "v_mfma_f32_16x16x32_bf16 v[28:31], a[204:207], v[56:59], v[28:31]\n"
  "v_mfma_f32_16x16x32_bf16 v[28:31], a[220:223], v[60:63], v[28:31]\n"
  "v_mfma_f32_16x16x32_bf16 v[28:31], a[236:239], v[64:67], v[28:31]\n"
  "v_mfma_f32_16x16x32_bf16 v[28:31], a[252:255], v[68:71], v[28:31]\n"
  "buffer_store_dwordx4 v[84:87], v89, s[36:39], s55 offen sc1\n"
  "v_mfma_f32_16x16x32_bf16 v[28:31], v[236:239], v[72:75], v[28:31]\n"
  "s_add_u32 s55, s55, 147456\n"
  "v_mfma_f32_16x16x32_bf16 v[28:31], v[252:255], v[76:79], v[28:31]\n"
  "s_waitcnt lgkmcnt(0)\n"
  "v_mfma_f32_16x16x32_bf16 v[0:3], a[0:3], v[32:35], 0\n"
  "s_cmp_eq_u32 s55, s57\n"
  "s_cselect_b32 s55, s56, s55\n"
  "ds_read_b128 v[56:59], v90 offset:38912\n"
  "v_mfma_f32_16x16x32_bf16 v[0:3], a[16:19], v[36:39], v[0:3]\n"
  "v_mul_f32 v80, v16, v16\n"
  "v_mul_f32 v81, v17, v17\n"
  "v_mfma_f32_16x16x32_bf16 v[0:3], a[32:35], v[40:43], v[0:3]\n"
  "ds_read_b128 v[60:63], v90 offset:39936\n"
  "v_fmaak_f32 v80, v88, v80, 0xc0135761\n"
  "v_mfma_f32_16x16x32_bf16 v[0:3], a[48:51], v[44:47], v[0:3]\n"
  "v_fmaak_f32 v81, v88, v81, 0xc0135761\n"
  "v_mul_f32 v80, v16, v80\n"
  "v_mfma_f32_16x16x32_bf16 v[0:3], a[64:67], v[48:51], v[0:3]\n"
  "v_mul_f32 v81, v17, v81\n"
  "v_mfma_f32_16x16x32_bf16 v[0:3], a[80:83], v[52:55], v[0:3]\n"
  "ds_read_b128 v[64:67], v90 offset:40960\n"
  "v_mfma_f32_16x16x32_bf16 v[4:7], a[4:7], v[32:35], 0\n"
  "v_exp_f32 v80, v80\n"
  "v_mfma_f32_16x16x32_bf16 v[4:7], a[20:23], v[36:39], v[4:7]\n"
  "v_exp_f32 v81, v81\n"
  "v_mfma_f32_16x16x32_bf16 v[4:7], a[36:39], v[40:43], v[4:7]\n"
  "ds_read_b128 v[68:71], v90 offset:41984\n"
  "v_mul_f32 v82, v16, v24\n"
  "v_mfma_f32_16x16x32_bf16 v[4:7], a[52:55], v[44:47], v[4:7]\n"
  "v_mul_f32 v83, v17, v25\n"
  "v_add_f32 v80, 1.0, v80\n"
  "v_mfma_f32_16x16x32_bf16 v[4:7], a[68:71], v[48:51], v[4:7]\n"
  "v_add_f32 v81, 1.0, v81\n"
  "v_mfma_f32_16x16x32_bf16 v[4:7], a[84:87], v[52:55], v[4:7]\n"
  "ds_read_b128 v[72:75], v90 offset:43008\n"
  "v_mfma_f32_16x16x32_bf16 v[8:11], a[8:11], v[32:35], 0\n"
  "v_rcp_f32 v80, v80\n"
  "v_mfma_f32_16x16x32_bf16 v[8:11], a[24:27], v[36:39], v[8:11]\n"
  "v_rcp_f32 v81, v81\n"
  "v_mfma_f32_16x16x32_bf16 v[8:11], a[40:43], v[40:43], v[8:11]\n"
  "ds_read_b128 v[76:79], v90 offset:44032\n"
  "v_mul_f32 v82, v82, v80\n"
  "v_mfma_f32_16x16x32_bf16 v[8:11], a[56:59], v[44:47], v[8:11]\n"
  "v_mul_f32 v83, v83, v81\n"
  "v_cvt_pk_bf16_f32 v84, v82, v83\n"
  "v_mfma_f32_16x16x32_bf16 v[8:11], a[72:75], v[48:51], v[8:11]\n"
  "v_mul_f32 v80, v18, v18\n"
  "v_mul_f32 v81, v19, v19\n"
  "v_mfma_f32_16x16x32_bf16 v[8:11], a[88:91], v[52:55], v[8:11]\n"
  "v_fmaak_f32 v80, v88, v80, 0xc0135761\n"
  "v_fmaak_f32 v81, v88, v81, 0xc0135761\n"
  "v_mfma_f32_16x16x32_bf16 v[12:15], a[12:15], v[32:35], 0\n"
  "v_mul_f32 v80, v18, v80\n"
  "v_mul_f32 v81, v19, v81\n"
  "v_mfma_f32_16x16x32_bf16 v[12:15], a[28:31], v[36:39], v[12:15]\n"
  "v_exp_f32 v80, v80\n"
  "v_mfma_f32_16x16x32_bf16 v[12:15], a[44:47], v[40:43], v[12:15]\n"
  "v_exp_f32 v81, v81\n"
  "v_mfma_f32_16x16x32_bf16 v[12:15], a[60:63], v[44:47], v[12:15]\n"
  "v_mul_f32 v82, v18, v26\n"
  "v_mul_f32 v83, v19, v27\n"
  "v_mfma_f32_16x16x32_bf16 v[12:15], a[76:79], v[48:51], v[12:15]\n"
  "v_add_f32 v80, 1.0, v80\n"
  "v_add_f32 v81, 1.0, v81\n"
  "v_mfma_f32_16x16x32_bf16 v[12:15], a[92:95], v[52:55], v[12:15]\n"
  "s_waitcnt lgkmcnt(0)\n"
  "v_rcp_f32 v80, v80\n"
  "v_mfma_f32_16x16x32_bf16 v[0:3], a[96:99], v[56:59], v[0:3]\n"
  "ds_read_b128 v[32:35], v90 offset:45056\n"
  "v_mfma_f32_16x16x32_bf16 v[0:3], a[112:115], v[60:63], v[0:3]\n"
  "v_rcp_f32 v81, v81\n"
  "v_mfma_f32_16x16x32_bf16 v[0:3], a[128:131], v[64:67], v[0:3]\n"
  "ds_read_b128 v[36:39], v90 offset:46080\n"
  "v_mul_f32 v82, v82, v80\n"
  "v_mfma_f32_16x16x32_bf16 v[0:3], a[144:147], v[68:71], v[0:3]\n"
  "v_mul_f32 v83, v83, v81\n"
  "v_cvt_pk_bf16_f32 v85, v82, v83\n"
  "v_mfma_f32_16x16x32_bf16 v[0:3], a[160:163], v[72:75], v[0:3]\n"
  "v_mul_f32 v80, v20, v20\n"
  "v_mul_f32 v81, v21, v21\n"
  "v_mfma_f32_16x16x32_bf16 v[0:3], a[176:179], v[76:79], v[0:3]\n"
  "ds_read_b128 v[40:43], v90 offset:47104\n"
  "v_fmaak_f32 v80, v88, v80, 0xc0135761\n"
  "v_mfma_f32_16x16x32_bf16 v[4:7], a[100:103], v[56:59], v[4:7]\n"
  "v_fmaak_f32 v81, v88, v81, 0xc0135761\n"
  "v_mul_f32 v80, v20, v80\n"
  "v_mfma_f32_16x16x32_bf16 v[4:7], a[116:119], v[60:63], v[4:7]\n"
  "v_mul_f32 v81, v21, v81\n"
  "v_mfma_f32_16x16x32_bf16 v[4:7], a[132:135], v[64:67], v[4:7]\n"
  "ds_read_b128 v[44:47], v90 offset:48128\n"
  "v_mfma_f32_16x16x32_bf16 v[4:7], a[148:151], v[68:71], v[4:7]\n"
  "v_exp_f32 v80, v80\n"
  "v_mfma_f32_16x16x32_bf16 v[4:7], a[164:167], v[72:75], v[4:7]\n"
  "buffer_store_dwordx4 v[108:111], v107, s[36:39], s61 offen offset:1024 sc1\n"
  "v_mfma_f32_16x16x32_bf16 v[4:7], a[180:183], v[76:79], v[4:7]\n"
  "ds_read_b128 v[48:51], v91 offset:24576\n"
  "v_mfma_f32_16x16x32_bf16 v[8:11], a[104:107], v[56:59], v[8:11]\n"
  "v_exp_f32 v81, v81\n"
  "v_mfma_f32_16x16x32_bf16 v[8:11], a[120:123], v[60:63], v[8:11]\n"
  "buffer_load_dword v215, v102, s[40:43], s60 offen offset:0\n"
  "v_mul_f32 v82, v20, v28\n"
  "v_mfma_f32_16x16x32_bf16 v[8:11], a[136:139], v[64:67], v[8:11]\n"
  "ds_read_b128 v[52:55], v91 offset:25600\n"
  "v_add_u32 v102, 8064, v102\n"
  "v_mfma_f32_16x16x32_bf16 v[8:11], a[152:155], v[68:71], v[8:11]\n"
  "v_mul_f32 v83, v21, v29\n"
  "v_add_f32 v80, 1.0, v80\n"
  "v_mfma_f32_16x16x32_bf16 v[8:11], a[168:171], v[72:75], v[8:11]\n"
  "v_add_f32 v81, 1.0, v81\n"
  "v_mfma_f32_16x16x32_bf16 v[8:11], a[184:187], v[76:79], v[8:11]\n"
  "v_rcp_f32 v80, v80\n"
  "v_mfma_f32_16x16x32_bf16 v[12:15], a[108:111], v[56:59], v[12:15]\n"
  "v_rcp_f32 v81, v81\n"
  "v_mfma_f32_16x16x32_bf16 v[12:15], a[124:127], v[60:63], v[12:15]\n"
  "v_mul_f32 v82, v82, v80\n"
  "v_mul_f32 v83, v83, v81\n"
  "v_mfma_f32_16x16x32_bf16 v[12:15], a[140:143], v[64:67], v[12:15]\n"
  "v_cvt_pk_bf16_f32 v86, v82, v83\n"
  "v_mul_f32 v80, v22, v22\n"
  "v_mfma_f32_16x16x32_bf16 v[12:15], a[156:159], v[68:71], v[12:15]\n"
  "v_mul_f32 v81, v23, v23\n"
  "v_fmaak_f32 v80, v88, v80, 0xc0135761\n"
  "v_mfma_f32_16x16x32_bf16 v[12:15], a[172:175], v[72:75], v[12:15]\n"
  "v_fmaak_f32 v81, v88, v81, 0xc0135761\n"
  "v_mul_f32 v80, v22, v80\n"
  "v_mfma_f32_16x16x32_bf16 v[12:15], a[188:191], v[76:79], v[12:15]\n"
  "s_waitcnt lgkmcnt(0)\n"
  "v_mul_f32 v81, v23, v81\n"
  "v_mfma_f32_16x16x32_bf16 v[0:3], a[192:195], v[32:35], v[0:3]\n"
  "ds_read_b128 v[56:59], v90 offset:49152\n"
  "v_mfma_f32_16x16x32_bf16 v[0:3], a[208:211], v[36:39], v[0:3]\n"
  "v_exp_f32 v80, v80\n"
  "v_mfma_f32_16x16x32_bf16 v[0:3], a[224:227], v[40:43], v[0:3]\n"
  "ds_read_b128 v[60:63], v90 offset:50176\n"
  "v_mfma_f32_16x16x32_bf16 v[0:3], a[240:243], v[44:47], v[0:3]\n"
  "v_exp_f32 v81, v81\n"
  "v_mfma_f32_16x16x32_bf16 v[0:3], v[224:227], v[48:51], v[0:3]\n"
  "v_mul_f32 v82, v22, v30\n"
  "v_mul_f32 v83, v23, v31\n"
  "v_mfma_f32_16x16x32_bf16 v[0:3], v[240:243], v[52:55], v[0:3]\n"
  "ds_read_b128 v[64:67], v90 offset:51200\n"
  "v_add_f32 v80, 1.0, v80\n"
  "v_mfma_f32_16x16x32_bf16 v[4:7], a[196:199], v[32:35], v[4:7]\n"
  "v_add_f32 v81, 1.0, v81\n"
  "v_mfma_f32_16x16x32_bf16 v[4:7], a[212:215], v[36:39], v[4:7]\n"
  "v_rcp_f32 v80, v80\n"
  "v_mfma_f32_16x16x32_bf16 v[4:7], a[228:231], v[40:43], v[4:7]\n"
  "ds_read_b128 v[68:71], v90 offset:52224\n"
  "v_mfma_f32_16x16x32_bf16 v[4:7], a[244:247], v[44:47], v[4:7]\n"
  "v_rcp_f32 v81, v81\n"
  "v_mfma_f32_16x16x32_bf16 v[4:7], v[228:231], v[48:51], v[4:7]\n"
  "v_mul_f32 v82, v82, v80\n"
  "v_mul_f32 v83, v83, v81\n"
  "v_mfma_f32_16x16x32_bf16 v[4:7], v[244:247], v[52:55], v[4:7]\n"
  "ds_read_b128 v[72:75], v90 offset:53248\n"
  "v_cvt_pk_bf16_f32 v87, v82, v83\n"
  "v_mfma_f32_16x16x32_bf16 v[8:11], a[200:203], v[32:35], v[8:11]\n"
  "v_mfma_f32_16x16x32_bf16 v[8:11], a[216:219], v[36:39], v[8:11]\n"
  "v_mfma_f32_16x16x32_bf16 v[8:11], a[232:235], v[40:43], v[8:11]\n"
  "ds_read_b128 v[76:79], v90 offset:54272\n"
  "v_mfma_f32_16x16x32_bf16 v[8:11], a[248:251], v[44:47], v[8:11]\n"
  "v_mfma_f32_16x16x32_bf16 v[8:11], v[232:235], v[48:51], v[8:11]\n"
  "v_mfma_f32_16x16x32_bf16 v[8:11], v[248:251], v[52:55], v[8:11]\n"
  "v_mfma_f32_16x16x32_bf16 v[12:15], a[204:207], v[32:35], v[12:15]\n"
  "v_mfma_f32_16x16x32_bf16 v[12:15], a[220:223], v[36:39], v[12:15]\n"
  "v_mfma_f32_16x16x32_bf16 v[12:15], a[236:239], v[40:43], v[12:15]\n"
  "v_mfma_f32_16x16x32_bf16 v[12:15], a[252:255], v[44:47], v[12:15]\n"
  "buffer_store_dwordx4 v[84:87], v89, s[36:39], s55 offen sc1\n"
  "v_mfma_f32_16x16x32_bf16 v[12:15], v[236:239], v[48:51], v[12:15]\n"
  "s_add_u32 s55, s55, 147456\n"
  "v_mfma_f32_16x16x32_bf16 v[12:15], v[252:255], v[52:55], v[12:15]\n"
  "s_waitcnt lgkmcnt(0)\n"
  "v_mfma_f32_16x16x32_bf16 v[16:19], a[0:3], v[56:59], 0\n"
  "s_cmp_eq_u32 s55, s57\n"
  "s_cselect_b32 s55, s56, s55\n"
  "ds_read_b128 v[32:35], v90 offset:55296\n"
  "v_mfma_f32_16x16x32_bf16 v[16:19], a[16:19], v[60:63], v[16:19]\n"
  "v_mul_f32 v80, v0, v0\n"
  "v_mul_f32 v81, v1, v1\n"
  "v_mfma_f32_16x16x32_bf16 v[16:19], a[32:35], v[64:67], v[16:19]\n"
  "ds_read_b128 v[36:39], v90 offset:56320\n"
  "v_fmaak_f32 v80, v88, v80, 0xc0135761\n"
  "v_mfma_f32_16x16x32_bf16 v[16:19], a[48:51], v[68:71], v[16:19]\n"
  "s_waitcnt vmcnt(3)\n"
  "s_mov_b64 exec, 1\n"
  "buffer_atomic_add v104, off, s[24:27], s12 sc1\n"
  "s_mov_b64 exec, -1\n"
  "v_mfma_f32_16x16x32_bf16 v[16:19], a[64:67], v[72:75], v[16:19]\n"
  "v_fmaak_f32 v81, v88, v81, 0xc0135761\n"
  "v_mul_f32 v80, v0, v80\n"
  "v_mfma_f32_16x16x32_bf16 v[16:19], a[80:83], v[76:79], v[16:19]\n"
  "ds_read_b128 v[40:43], v90 offset:57344\n"
  "v_mul_f32 v81, v1, v81\n"
  "v_mfma_f32_16x16x32_bf16 v[20:23], a[4:7], v[56:59], 0\n"
  "v_exp_f32 v80, v80\n"
  "v_mfma_f32_16x16x32_bf16 v[20:23], a[20:23], v[60:63], v[20:23]\n"
  "v_exp_f32 v81, v81\n"
  "v_mfma_f32_16x16x32_bf16 v[20:23], a[36:39], v[64:67], v[20:23]\n"
  "ds_read_b128 v[44:47], v90 offset:58368\n"
  "v_mul_f32 v82, v0, v8\n"
  "v_mfma_f32_16x16x32_bf16 v[20:23], a[52:55], v[68:71], v[20:23]\n"
  "v_mul_f32 v83, v1, v9\n"
  "v_add_f32 v80, 1.0, v80\n"
  "v_mfma_f32_16x16x32_bf16 v[20:23], a[68:71], v[72:75], v[20:23]\n"
  "v_add_f32 v81, 1.0, v81\n"
  "v_mfma_f32_16x16x32_bf16 v[20:23], a[84:87], v[76:79], v[20:23]\n"
  "ds_read_b128 v[48:51], v90 offset:59392\n"
  "v_mfma_f32_16x16x32_bf16 v[24:27], a[8:11], v[56:59], 0\n"
  "v_rcp_f32 v80, v80\n"
  "v_mfma_f32_16x16x32_bf16 v[24:27], a[24:27], v[60:63], v[24:27]\n"
  "v_rcp_f32 v81, v81\n"
  "v_mfma_f32_16x16x32_bf16 v[24:27], a[40:43], v[64:67], v[24:27]\n"
  "ds_read_b128 v[52:55], v90 offset:60416\n"
  "v_mul_f32 v82, v82, v80\n"
  "v_mfma_f32_16x16x32_bf16 v[24:27], a[56:59], v[68:71], v[24:27]\n"
  "v_mul_f32 v83, v83, v81\n"
  "v_cvt_pk_bf16_f32 v84, v82, v83\n"
  "v_mfma_f32_16x16x32_bf16 v[24:27], a[72:75], v[72:75], v[24:27]\n"
  "v_mul_f32 v80, v2, v2\n"
  "v_mul_f32 v81, v3, v3\n"
  "v_mfma_f32_16x16x32_bf16 v[24:27], a[88:91], v[76:79], v[24:27]\n"
  "v_fmaak_f32 v80, v88, v80, 0xc0135761\n"
  "v_fmaak_f32 v81, v88, v81, 0xc0135761\n"
  "v_mfma_f32_16x16x32_bf16 v[28:31], a[12:15], v[56:59], 0\n"
  "v_mul_f32 v80, v2, v80\n"
  "v_mul_f32 v81, v3, v81\n"
  "v_mfma_f32_16x16x32_bf16 v[28:31], a[28:31], v[60:63], v[28:31]\n"
  "v_exp_f32 v80, v80\n"
  "v_mfma_f32_16x16x32_bf16 v[28:31], a[44:47], v[64:67], v[28:31]\n"
  "v_exp_f32 v81, v81\n"
  "v_mfma_f32_16x16x32_bf16 v[28:31], a[60:63], v[68:71], v[28:31]\n"
  "v_mul_f32 v82, v2, v10\n"
  "v_mul_f32 v83, v3, v11\n"
  "v_mfma_f32_16x16x32_bf16 v[28:31], a[76:79], v[72:75], v[28:31]\n"
  "v_add_f32 v80, 1.0, v80\n"
  "v_add_f32 v81, 1.0, v81\n"
  "v_mfma_f32_16x16x32_bf16 v[28:31], a[92:95], v[76:79], v[28:31]\n"
  "s_waitcnt lgkmcnt(0)\n"
  "v_rcp_f32 v80, v80\n"
  "v_mfma_f32_16x16x32_bf16 v[16:19], a[96:99], v[32:35], v[16:19]\n"
  "ds_read_b128 v[56:59], v90 offset:61440\n"
  "v_mfma_f32_16x16x32_bf16 v[16:19], a[112:115], v[36:39], v[16:19]\n"
  "v_rcp_f32 v81, v81\n"
  "v_mfma_f32_16x16x32_bf16 v[16:19], a[128:131], v[40:43], v[16:19]\n"
  "ds_read_b128 v[60:63], v90 offset:62464\n"
  "v_mul_f32 v82, v82, v80\n"
  "v_mfma_f32_16x16x32_bf16 v[16:19], a[144:147], v[44:47], v[16:19]\n"
  "v_mul_f32 v83, v83, v81\n"
  "v_cvt_pk_bf16_f32 v85, v82, v83\n"
  "v_mfma_f32_16x16x32_bf16 v[16:19], a[160:163], v[48:51], v[16:19]\n"
  "v_mul_f32 v80, v4, v4\n"
  "v_mul_f32 v81, v5, v5\n"
  "v_mfma_f32_16x16x32_bf16 v[16:19], a[176:179], v[52:55], v[16:19]\n"
  "ds_read_b128 v[64:67], v90 offset:63488\n"
  "v_fmaak_f32 v80, v88, v80, 0xc0135761\n"
  "v_mfma_f32_16x16x32_bf16 v[20:23], a[100:103], v[32:35], v[20:23]\n"
  "v_fmaak_f32 v81, v88, v81, 0xc0135761\n"
  "v_mul_f32 v80, v4, v80\n"
  "v_mfma_f32_16x16x32_bf16 v[20:23], a[116:119], v[36:39], v[20:23]\n"
  "v_mul_f32 v81, v5, v81\n"
  "v_mfma_f32_16x16x32_bf16 v[20:23], a[132:135], v[40:43], v[20:23]\n"
  "ds_read_b128 v[68:71], v90 offset:64512\n"
  "v_mfma_f32_16x16x32_bf16 v[20:23], a[148:151], v[44:47], v[20:23]\n"
  "v_exp_f32 v80, v80\n"
  "v_mfma_f32_16x16x32_bf16 v[20:23], a[164:167], v[48:51], v[20:23]\n"
  "buffer_store_dwordx4 v[108:111], v107, s[36:39], s61 offen offset:2048 sc1\n"
  "v_mfma_f32_16x16x32_bf16 v[20:23], a[180:183], v[52:55], v[20:23]\n"
  "ds_read_b128 v[72:75], v91 offset:22528\n"
  "v_mfma_f32_16x16x32_bf16 v[24:27], a[104:107], v[32:35], v[24:27]\n"
  "v_exp_f32 v81, v81\n"
  "v_mfma_f32_16x16x32_bf16 v[24:27], a[120:123], v[36:39], v[24:27]\n"
  "v_mul_f32 v82, v4, v12\n"
  "v_mul_f32 v83, v5, v13\n"
  "v_mfma_f32_16x16x32_bf16 v[24:27], a[136:139], v[40:43], v[24:27]\n"
  "ds_read_b128 v[76:79], v91 offset:23552\n"
  "v_add_f32 v80, 1.0, v80\n"
  "v_mfma_f32_16x16x32_bf16 v[24:27], a[152:155], v[44:47], v[24:27]\n"
  "v_add_f32 v81, 1.0, v81\n"
  "v_mfma_f32_16x16x32_bf16 v[24:27], a[168:171], v[48:51], v[24:27]\n"
  "v_rcp_f32 v80, v80\n"
  "v_mfma_f32_16x16x32_bf16 v[24:27], a[184:187], v[52:55], v[24:27]\n"
  "v_rcp_f32 v81, v81\n"
  "s_nop 0\n"
  "s_cbranch_execz Ltramp%=\n"
  "v_mfma_f32_16x16x32_bf16 v[28:31], a[108:111], v[32:35], v[28:31]\n"
  "v_mul_f32 v82, v82, v80\n"
  "v_mul_f32 v83, v83, v81\n"
  "v_mfma_f32_16x16x32_bf16 v[28:31], a[124:127], v[36:39], v[28:31]\n"
  "v_cvt_pk_bf16_f32 v86, v82, v83\n"
  "v_mul_f32 v80, v6, v6\n"
  "v_mfma_f32_16x16x32_bf16 v[28:31], a[140:143], v[40:43], v[28:31]\n"
  "v_mul_f32 v81, v7, v7\n"
  "v_fmaak_f32 v80, v88, v80, 0xc0135761\n"
  "v_mfma_f32_16x16x32_bf16 v[28:31], a[156:159], v[44:47], v[28:31]\n"
  "v_fmaak_f32 v81, v88, v81, 0xc0135761\n"
  "v_mul_f32 v80, v6, v80\n"
  "v_mfma_f32_16x16x32_bf16 v[28:31], a[172:175], v[48:51], v[28:31]\n"
  "v_mul_f32 v81, v7, v81\n"
  "v_mfma_f32_16x16x32_bf16 v[28:31], a[188:191], v[52:55], v[28:31]\n"
  "s_waitcnt vmcnt(20) lgkmcnt(0)\n"
  "s_barrier\n"
  "v_mfma_f32_16x16x32_bf16 v[16:19], a[192:195], v[56:59], v[16:19]\n"
  "ds_read_b128 v[32:35], v91 offset:0\n"
  "v_mfma_f32_16x16x32_bf16 v[16:19], a[208:211], v[60:63], v[16:19]\n"
  "v_exp_f32 v80, v80\n"
  "v_mfma_f32_16x16x32_bf16 v[16:19], a[224:227], v[64:67], v[16:19]\n"
  "ds_read_b128 v[36:39], v91 offset:1024\n"
  "v_mfma_f32_16x16x32_bf16 v[16:19], a[240:243], v[68:71], v[16:19]\n"
  "v_exp_f32 v81, v81\n"
  "v_mfma_f32_16x16x32_bf16 v[16:19], v[224:227], v[72:75], v[16:19]\n"
  "v_mul_f32 v82, v6, v14\n"
  "v_mul_f32 v83, v7, v15\n"
  "v_mfma_f32_16x16x32_bf16 v[16:19], v[240:243], v[76:79], v[16:19]\n"
  "ds_read_b128 v[40:43], v91 offset:2048\n"
  "v_add_f32 v80, 1.0, v80\n"
  "v_mfma_f32_16x16x32_bf16 v[20:23], a[196:199], v[56:59], v[20:23]\n"
  "v_add_f32 v81, 1.0, v81\n"
  "v_mfma_f32_16x16x32_bf16 v[20:23], a[212:215], v[60:63], v[20:23]\n"
  "v_rcp_f32 v80, v80\n"
  "v_mfma_f32_16x16x32_bf16 v[20:23], a[228:231], v[64:67], v[20:23]\n"
  "ds_read_b128 v[44:47], v91 offset:3072\n"
  "v_mfma_f32_16x16x32_bf16 v[20:23], a[244:247], v[68:71], v[20:23]\n"
  "v_rcp_f32 v81, v81\n"
  "v_mfma_f32_16x16x32_bf16 v[20:23], v[228:231], v[72:75], v[20:23]\n"
  "v_mul_f32 v82, v82, v80\n"
  "v_mul_f32 v83, v83, v81\n"
  "v_mfma_f32_16x16x32_bf16 v[20:23], v[244:247], v[76:79], v[20:23]\n"
  "ds_read_b128 v[48:51], v91 offset:4096\n"
  "v_cvt_pk_bf16_f32 v87, v82, v83\n"
  "v_mfma_f32_16x16x32_bf16 v[24:27], a[200:203], v[56:59], v[24:27]\n"
  "v_mfma_f32_16x16x32_bf16 v[24:27], a[216:219], v[60:63], v[24:27]\n"
  "v_mfma_f32_16x16x32_bf16 v[24:27], a[232:235], v[64:67], v[24:27]\n"
  "ds_read_b128 v[52:55], v91 offset:5120\n"
  "v_mfma_f32_16x16x32_bf16 v[24:27], a[248:251], v[68:71], v[24:27]\n"
  "v_mfma_f32_16x16x32_bf16 v[24:27], v[232:235], v[72:75], v[24:27]\n"
  "v_mfma_f32_16x16x32_bf16 v[24:27], v[248:251], v[76:79], v[24:27]\n"
  "v_mfma_f32_16x16x32_bf16 v[28:31], a[204:207], v[56:59], v[28:31]\n"
  "v_mfma_f32_16x16x32_bf16 v[28:31], a[220:223], v[60:63], v[28:31]\n"
  "v_mfma_f32_16x16x32_bf16 v[28:31], a[236:239], v[64:67], v[28:31]\n"
  "v_mfma_f32_16x16x32_bf16 v[28:31], a[252:255], v[68:71], v[28:31]\n"
  "buffer_store_dwordx4 v[84:87], v89, s[36:39], s55 offen sc1\n"
  "v_mfma_f32_16x16x32_bf16 v[28:31], v[236:239], v[72:75], v[28:31]\n"
  "s_add_u32 s55, s55, 147456\n"
  "v_mfma_f32_16x16x32_bf16 v[28:31], v[252:255], v[76:79], v[28:31]\n"
  "s_waitcnt lgkmcnt(0)\n"
  "v_mfma_f32_16x16x32_bf16 v[0:3], a[0:3], v[32:35], 0\n"
  "s_cmp_eq_u32 s55, s57\n"
  "s_cselect_b32 s55, s56, s55\n"
  "ds_read_b128 v[56:59], v91 offset:6144\n"
  "v_mfma_f32_16x16x32_bf16 v[0:3], a[16:19], v[36:39], v[0:3]\n"
  "v_mul_f32 v80, v16, v16\n"
  "v_mul_f32 v81, v17, v17\n"
  "v_mfma_f32_16x16x32_bf16 v[0:3], a[32:35], v[40:43], v[0:3]\n"
  "ds_read_b128 v[60:63], v91 offset:7168\n"
  "v_fmaak_f32 v80, v88, v80, 0xc0135761\n"
  "v_mfma_f32_16x16x32_bf16 v[0:3], a[48:51], v[44:47], v[0:3]\n"
  "v_fmaak_f32 v81, v88, v81, 0xc0135761\n"
  "v_mul_f32 v80, v16, v80\n"
  "v_mfma_f32_16x16x32_bf16 v[0:3], a[64:67], v[48:51], v[0:3]\n"
  "v_mul_f32 v81, v17, v81\n"
  "v_mfma_f32_16x16x32_bf16 v[0:3], a[80:83], v[52:55], v[0:3]\n"
  "ds_read_b128 v[64:67], v91 offset:8192\n"
  "v_mfma_f32_16x16x32_bf16 v[4:7], a[4:7], v[32:35], 0\n"
  "v_exp_f32 v80, v80\n"
  "v_mfma_f32_16x16x32_bf16 v[4:7], a[20:23], v[36:39], v[4:7]\n"
  "v_exp_f32 v81, v81\n"
  "v_mfma_f32_16x16x32_bf16 v[4:7], a[36:39], v[40:43], v[4:7]\n"
  "ds_read_b128 v[68:71], v91 offset:9216\n"
  "v_mul_f32 v82, v16, v24\n"
  "v_mfma_f32_16x16x32_bf16 v[4:7], a[52:55], v[44:47], v[4:7]\n"
  "v_mul_f32 v83, v17, v25\n"
  "v_add_f32 v80, 1.0, v80\n"
  "v_mfma_f32_16x16x32_bf16 v[4:7], a[68:71], v[48:51], v[4:7]\n"
  "v_add_f32 v81, 1.0, v81\n"
  "v_mfma_f32_16x16x32_bf16 v[4:7], a[84:87], v[52:55], v[4:7]\n"
  "ds_read_b128 v[72:75], v91 offset:10240\n"
  "v_mfma_f32_16x16x32_bf16 v[8:11], a[8:11], v[32:35], 0\n"
  "v_rcp_f32 v80, v80\n"
  "v_mfma_f32_16x16x32_bf16 v[8:11], a[24:27], v[36:39], v[8:11]\n"
  "v_rcp_f32 v81, v81\n"
  "v_mfma_f32_16x16x32_bf16 v[8:11], a[40:43], v[40:43], v[8:11]\n"
  "ds_read_b128 v[76:79], v91 offset:11264\n"
  "v_mul_f32 v82, v82, v80\n"
  "v_mfma_f32_16x16x32_bf16 v[8:11], a[56:59], v[44:47], v[8:11]\n"
  "v_mul_f32 v83, v83, v81\n"
  "v_cvt_pk_bf16_f32 v84, v82, v83\n"
  "v_mfma_f32_16x16x32_bf16 v[8:11], a[72:75], v[48:51], v[8:11]\n"
  "v_mul_f32 v80, v18, v18\n"
  "v_mul_f32 v81, v19, v19\n"
  "v_mfma_f32_16x16x32_bf16 v[8:11], a[88:91], v[52:55], v[8:11]\n"
  "v_fmaak_f32 v80, v88, v80, 0xc0135761\n"
  "v_fmaak_f32 v81, v88, v81, 0xc0135761\n"
  "v_mfma_f32_16x16x32_bf16 v[12:15], a[12:15], v[32:35], 0\n"
  "v_mul_f32 v80, v18, v80\n"
  "v_mul_f32 v81, v19, v81\n"
  "v_mfma_f32_16x16x32_bf16 v[12:15], a[28:31], v[36:39], v[12:15]\n"
  "v_exp_f32 v80, v80\n"
  "v_mfma_f32_16x16x32_bf16 v[12:15], a[44:47], v[40:43], v[12:15]\n"
  "v_exp_f32 v81, v81\n"
  "v_mfma_f32_16x16x32_bf16 v[12:15], a[60:63], v[44:47], v[12:15]\n"
  "v_mul_f32 v82, v18, v26\n"
  "v_mul_f32 v83, v19, v27\n"
  "v_mfma_f32_16x16x32_bf16 v[12:15], a[76:79], v[48:51], v[12:15]\n"
  "v_add_f32 v80, 1.0, v80\n"
  "v_add_f32 v81, 1.0, v81\n"
  "v_mfma_f32_16x16x32_bf16 v[12:15], a[92:95], v[52:55], v[12:15]\n"
  "s_waitcnt lgkmcnt(0)\n"
  "v_rcp_f32 v80, v80\n"
  "v_mfma_f32_16x16x32_bf16 v[0:3], a[96:99], v[56:59], v[0:3]\n"
  "ds_read_b128 v[32:35], v91 offset:12288\n"
  "v_mfma_f32_16x16x32_bf16 v[0:3], a[112:115], v[60:63], v[0:3]\n"
  "v_rcp_f32 v81, v81\n"
  "v_mfma_f32_16x16x32_bf16 v[0:3], a[128:131], v[64:67], v[0:3]\n"
  "ds_read_b128 v[36:39], v91 offset:13312\n"
  "v_mul_f32 v82, v82, v80\n"
  "v_mfma_f32_16x16x32_bf16 v[0:3], a[144:147], v[68:71], v[0:3]\n"
  "s_mov_b64 exec, 1\n"
  "buffer_load_dword v106, v105, s[24:27], 0 offen sc1\n"
  "s_mov_b64 exec, -1\n"
  "v_mfma_f32_16x16x32_bf16 v[0:3], a[160:163], v[72:75], v[0:3]\n"
  "v_mul_f32 v83, v83, v81\n"
  "v_cvt_pk_bf16_f32 v85, v82, v83\n"
  "v_mfma_f32_16x16x32_bf16 v[0:3], a[176:179], v[76:79], v[0:3]\n"
  "ds_read_b128 v[40:43], v91 offset:14336\n"
  "v_mul_f32 v80, v20, v20\n"
  "v_mfma_f32_16x16x32_bf16 v[4:7], a[100:103], v[56:59], v[4:7]\n"
  "v_mul_f32 v81, v21, v21\n"
  "v_fmaak_f32 v80, v88, v80, 0xc0135761\n"
  "v_mfma_f32_16x16x32_bf16 v[4:7], a[116:119], v[60:63], v[4:7]\n"
  "v_fmaak_f32 v81, v88, v81, 0xc0135761\n"
  "v_mul_f32 v80, v20, v80\n"
  "v_mfma_f32_16x16x32_bf16 v[4:7], a[132:135], v[64:67], v[4:7]\n"
  "ds_read_b128 v[44:47], v91 offset:15360\n"
  "v_mul_f32 v81, v21, v81\n"
  "v_mfma_f32_16x16x32_bf16 v[4:7], a[148:151], v[68:71], v[4:7]\n"
  "v_exp_f32 v80, v80\n"
  "v_mfma_f32_16x16x32_bf16 v[4:7], a[164:167], v[72:75], v[4:7]\n"
  "buffer_store_dwordx4 v[108:111], v107, s[36:39], s62 offen offset:0 sc1\n"
  "v_mfma_f32_16x16x32_bf16 v[4:7], a[180:183], v[76:79], v[4:7]\n"
  "ds_read_b128 v[48:51], v91 offset:20480\n"
  "v_mfma_f32_16x16x32_bf16 v[8:11], a[104:107], v[56:59], v[8:11]\n"
  "v_exp_f32 v81, v81\n"
  "v_mfma_f32_16x16x32_bf16 v[8:11], a[120:123], v[60:63], v[8:11]\n"
  "v_mul_f32 v82, v20, v28\n"
  "v_mul_f32 v83, v21, v29\n"
  "v_mfma_f32_16x16x32_bf16 v[8:11], a[136:139], v[64:67], v[8:11]\n"
  "ds_read_b128 v[52:55], v91 offset:21504\n"
  "v_add_f32 v80, 1.0, v80\n"
  "v_mfma_f32_16x16x32_bf16 v[8:11], a[152:155], v[68:71], v[8:11]\n"
  "v_add_f32 v81, 1.0, v81\n"
  "v_mfma_f32_16x16x32_bf16 v[8:11], a[168:171], v[72:75], v[8:11]\n"
  "v_rcp_f32 v80, v80\n"
  "v_mfma_f32_16x16x32_bf16 v[8:11], a[184:187], v[76:79], v[8:11]\n"
  "v_rcp_f32 v81, v81\n"
  "v_mfma_f32_16x16x32_bf16 v[12:15], a[108:111], v[56:59], v[12:15]\n"
  "v_mul_f32 v82, v82, v80\n"
  "v_mul_f32 v83, v83, v81\n"
  "v_mfma_f32_16x16x32_bf16 v[12:15], a[124:127], v[60:63], v[12:15]\n"
  "v_cvt_pk_bf16_f32 v86, v82, v83\n"
  "v_mul_f32 v80, v22, v22\n"
  "v_mfma_f32_16x16x32_bf16 v[12:15], a[140:143], v[64:67], v[12:15]\n"
  "v_mul_f32 v81, v23, v23\n"
  "v_fmaak_f32 v80, v88, v80, 0xc0135761\n"
  "v_mfma_f32_16x16x32_bf16 v[12:15], a[156:159], v[68:71], v[12:15]\n"
  "v_fmaak_f32 v81, v88, v81, 0xc0135761\n"
  "v_mul_f32 v80, v22, v80\n"
  "v_mfma_f32_16x16x32_bf16 v[12:15], a[172:175], v[72:75], v[12:15]\n"
  "v_mul_f32 v81, v23, v81\n"
  "v_mfma_f32_16x16x32_bf16 v[12:15], a[188:191], v[76:79], v[12:15]\n"
  "s_waitcnt lgkmcnt(0)\n"
  "v_exp_f32 v80, v80\n"
  "v_mfma_f32_16x16x32_bf16 v[0:3], a[192:195], v[32:35], v[0:3]\n"
  "ds_read_b128 v[56:59], v89 offset:32768\n"
  "v_mfma_f32_16x16x32_bf16 v[0:3], a[208:211], v[36:39], v[0:3]\n"
  "v_exp_f32 v81, v81\n"
  "v_mfma_f32_16x16x32_bf16 v[0:3], a[224:227], v[40:43], v[0:3]\n"
  "ds_read_b128 v[60:63], v89 offset:33792\n"
  "v_mul_f32 v82, v22, v30\n"
  "v_mfma_f32_16x16x32_bf16 v[0:3], a[240:243], v[44:47], v[0:3]\n"
  "v_mul_f32 v83, v23, v31\n"
  "v_add_f32 v80, 1.0, v80\n"
  "v_mfma_f32_16x16x32_bf16 v[0:3], v[224:227], v[48:51], v[0:3]\n"
  "v_add_f32 v81, 1.0, v81\n"
  "v_mfma_f32_16x16x32_bf16 v[0:3], v[240:243], v[52:55], v[0:3]\n"
  "ds_read_b128 v[64:67], v89 offset:34816\n"
  "v_mfma_f32_16x16x32_bf16 v[4:7], a[196:199], v[32:35], v[4:7]\n"
  "v_rcp_f32 v80, v80\n"
  "v_mfma_f32_16x16x32_bf16 v[4:7], a[212:215], v[36:39], v[4:7]\n"
  "v_rcp_f32 v81, v81\n"
  "v_mfma_f32_16x16x32_bf16 v[4:7], a[228:231], v[40:43], v[4:7]\n"
  "ds_read_b128 v[68:71], v89 offset:35840\n"
  "v_mul_f32 v82, v82, v80\n"
  "v_mfma_f32_16x16x32_bf16 v[4:7], a[244:247], v[44:47], v[4:7]\n"
  "v_mul_f32 v83, v83, v81\n"
  "v_cvt_pk_bf16_f32 v87, v82, v83\n"
  "v_mfma_f32_16x16x32_bf16 v[4:7], v[228:231], v[48:51], v[4:7]\n"
  "v_mfma_f32_16x16x32_bf16 v[4:7], v[244:247], v[52:55], v[4:7]\n"
  "ds_read_b128 v[72:75], v89 offset:36864\n"
  "v_mfma_f32_16x16x32_bf16 v[8:11], a[200:203], v[32:35], v[8:11]\n"
  "v_mfma_f32_16x16x32_bf16 v[8:11], a[216:219], v[36:39], v[8:11]\n"
  "v_mfma_f32_16x16x32_bf16 v[8:11], a[232:235], v[40:43], v[8:11]\n"
  "ds_read_b128 v[76:79], v89 offset:37888\n"
  "v_mfma_f32_16x16x32_bf16 v[8:11], a[248:251], v[44:47], v[8:11]\n"
  "v_mfma_f32_16x16x32_bf16 v[8:11], v[232:235], v[48:51], v[8:11]\n"
  "v_mfma_f32_16x16x32_bf16 v[8:11], v[248:251], v[52:55], v[8:11]\n"
  "v_mfma_f32_16x16x32_bf16 v[12:15], a[204:207], v[32:35], v[12:15]\n"
  "v_mfma_f32_16x16x32_bf16 v[12:15], a[220:223], v[36:39], v[12:15]\n"
  "v_mfma_f32_16x16x32_bf16 v[12:15], a[236:239], v[40:43], v[12:15]\n"
  "v_mfma_f32_16x16x32_bf16 v[12:15], a[252:255], v[44:47], v[12:15]\n"
  "buffer_store_dwordx4 v[84:87], v89, s[36:39], s55 offen sc1\n"
  "v_mfma_f32_16x16x32_bf16 v[12:15], v[236:239], v[48:51], v[12:15]\n"
  "s_add_u32 s55, s55, 147456\n"
  "v_mfma_f32_16x16x32_bf16 v[12:15], v[252:255], v[52:55], v[12:15]\n"
  "s_waitcnt lgkmcnt(0)\n"
  "v_mfma_f32_16x16x32_bf16 v[16:19], a[0:3], v[56:59], 0\n"
  "s_cmp_eq_u32 s55, s57\n"
  "s_cselect_b32 s55, s56, s55\n"
  "ds_read_b128 v[32:35], v89 offset:38912\n"
  "v_mfma_f32_16x16x32_bf16 v[16:19], a[16:19], v[60:63], v[16:19]\n"
  "v_mul_f32 v80, v0, v0\n"
  "v_mul_f32 v81, v1, v1\n"
  "v_mfma_f32_16x16x32_bf16 v[16:19], a[32:35], v[64:67], v[16:19]\n"
  "ds_read_b128 v[36:39], v89 offset:39936\n"
  "v_fmaak_f32 v80, v88, v80, 0xc0135761\n"
  "v_mfma_f32_16x16x32_bf16 v[16:19], a[48:51], v[68:71], v[16:19]\n"
  "v_fmaak_f32 v81, v88, v81, 0xc0135761\n"
  "v_mul_f32 v80, v0, v80\n"
  "v_mfma_f32_16x16x32_bf16 v[16:19], a[64:67], v[72:75], v[16:19]\n"
  "v_mul_f32 v81, v1, v81\n"
  "v_mfma_f32_16x16x32_bf16 v[16:19], a[80:83], v[76:79], v[16:19]\n"
  "ds_read_b128 v[40:43], v89 offset:40960\n"
  "v_mfma_f32_16x16x32_bf16 v[20:23], a[4:7], v[56:59], 0\n"
  "v_exp_f32 v80, v80\n"
  "v_mfma_f32_16x16x32_bf16 v[20:23], a[20:23], v[60:63], v[20:23]\n"
  "v_exp_f32 v81, v81\n"
  "v_mfma_f32_16x16x32_bf16 v[20:23], a[36:39], v[64:67], v[20:23]\n"
  "ds_read_b128 v[44:47], v89 offset:41984\n"
  "v_mul_f32 v82, v0, v8\n"
  "v_mfma_f32_16x16x32_bf16 v[20:23], a[52:55], v[68:71], v[20:23]\n"
  "v_mul_f32 v83, v1, v9\n"
  "v_add_f32 v80, 1.0, v80\n"
  "v_mfma_f32_16x16x32_bf16 v[20:23], a[68:71], v[72:75], v[20:23]\n"
  "v_add_f32 v81, 1.0, v81\n"
  "v_mfma_f32_16x16x32_bf16 v[20:23], a[84:87], v[76:79], v[20:23]\n"
  "ds_read_b128 v[48:51], v89 offset:43008\n"
  "v_mfma_f32_16x16x32_bf16 v[24:27], a[8:11], v[56:59], 0\n"
  "v_rcp_f32 v80, v80\n"
  "v_mfma_f32_16x16x32_bf16 v[24:27], a[24:27], v[60:63], v[24:27]\n"
  "v_rcp_f32 v81, v81\n"
  "v_mfma_f32_16x16x32_bf16 v[24:27], a[40:43], v[64:67], v[24:27]\n"
  "ds_read_b128 v[52:55], v89 offset:44032\n"
  "v_mul_f32 v82, v82, v80\n"
  "v_mfma_f32_16x16x32_bf16 v[24:27], a[56:59], v[68:71], v[24:27]\n"
  "v_mul_f32 v83, v83, v81\n"
  "v_cvt_pk_bf16_f32 v84, v82, v83\n"
  "v_mfma_f32_16x16x32_bf16 v[24:27], a[72:75], v[72:75], v[24:27]\n"
  "v_mul_f32 v80, v2, v2\n"
  "v_mul_f32 v81, v3, v3\n"
  "v_mfma_f32_16x16x32_bf16 v[24:27], a[88:91], v[76:79], v[24:27]\n"
  "v_fmaak_f32 v80, v88, v80, 0xc0135761\n"
  "v_fmaak_f32 v81, v88, v81, 0xc0135761\n"
  "v_mfma_f32_16x16x32_bf16 v[28:31], a[12:15], v[56:59], 0\n"
  "v_mul_f32 v80, v2, v80\n"
  "v_mul_f32 v81, v3, v81\n"
  "v_mfma_f32_16x16x32_bf16 v[28:31], a[28:31], v[60:63], v[28:31]\n"
  "v_exp_f32 v80, v80\n"
  "v_mfma_f32_16x16x32_bf16 v[28:31], a[44:47], v[64:67], v[28:31]\n"
  "v_exp_f32 v81, v81\n"
  "v_mfma_f32_16x16x32_bf16 v[28:31], a[60:63], v[68:71], v[28:31]\n"
  "v_mul_f32 v82, v2, v10\n"
  "v_mul_f32 v83, v3, v11\n"
  "v_mfma_f32_16x16x32_bf16 v[28:31], a[76:79], v[72:75], v[28:31]\n"
  "v_add_f32 v80, 1.0, v80\n"
  "v_add_f32 v81, 1.0, v81\n"
  "v_mfma_f32_16x16x32_bf16 v[28:31], a[92:95], v[76:79], v[28:31]\n"
  "s_waitcnt lgkmcnt(0)\n"
  "s_waitcnt vmcnt(2)\n"
  "v_cmp_gt_u32 vcc, 12, v106\n"
  "s_and_b64 vcc, vcc, 1\n"
  "s_cbranch_scc1 Lpollp0A%=\n"
  "Lpollrp0A%=:\n"
  "v_mfma_f32_16x16x32_bf16 v[16:19], a[96:99], v[32:35], v[16:19]\n"
  "s_add_u32 m0, s50, 155648\n"
  "ds_read_b128 v[56:59], v89 offset:45056\n"
  "v_mfma_f32_16x16x32_bf16 v[16:19], a[112:115], v[36:39], v[16:19]\n"
  "buffer_load_dwordx4 v98, s[36:39], s59 offen sc1 lds\n"
  "v_mfma_f32_16x16x32_bf16 v[16:19], a[128:131], v[40:43], v[16:19]\n"
  "ds_read_b128 v[60:63], v89 offset:46080\n"
  "v_mfma_f32_16x16x32_bf16 v[16:19], a[144:147], v[44:47], v[16:19]\n"
  "v_rcp_f32 v80, v80\n"
  "v_mfma_f32_16x16x32_bf16 v[16:19], a[160:163], v[48:51], v[16:19]\n"
  "v_rcp_f32 v81, v81\n"
  "v_mfma_f32_16x16x32_bf16 v[16:19], a[176:179], v[52:55], v[16:19]\n"
  "s_add_u32 m0, s48, 65536\n"
  "ds_read_b128 v[64:67], v89 offset:47104\n"
  "v_mul_f32 v82, v82, v80\n"
  "v_mfma_f32_16x16x32_bf16 v[20:23], a[100:103], v[32:35], v[20:23]\n"
  "buffer_load_dwordx4 v97, s[36:39], s59 offen offset:3072 sc1 lds\n"
  "v_mul_f32 v83, v83, v81\n"
  "v_mfma_f32_16x16x32_bf16 v[20:23], a[116:119], v[36:39], v[20:23]\n"
  "s_add_u32 s59, s59, 1024\n"
  "v_cvt_pk_bf16_f32 v85, v82, v83\n"
  "v_mul_f32 v80, v4, v4\n"
  "v_mfma_f32_16x16x32_bf16 v[20:23], a[132:135], v[40:43], v[20:23]\n"
  "ds_read_b128 v[68:71], v89 offset:48128\n"
  "v_mul_f32 v81, v5, v5\n"
  "v_mfma_f32_16x16x32_bf16 v[20:23], a[148:151], v[44:47], v[20:23]\n"
  "v_fmaak_f32 v80, v88, v80, 0xc0135761\n"
  "v_fmaak_f32 v81, v88, v81, 0xc0135761\n"
  "v_mfma_f32_16x16x32_bf16 v[20:23], a[164:167], v[48:51], v[20:23]\n"
  "buffer_store_dwordx4 v[108:111], v107, s[36:39], s62 offen offset:1024 sc1\n"
  "v_mul_f32 v80, v4, v80\n"
  "v_mfma_f32_16x16x32_bf16 v[20:23], a[180:183], v[52:55], v[20:23]\n"
  "buffer_load_dwordx4 v94, s[40:43], s60 offen offset:0 lds\n"
  "ds_read_b128 v[72:75], v91 offset:18432\n"
  "v_mfma_f32_16x16x32_bf16 v[24:27], a[104:107], v[32:35], v[24:27]\n"
  "v_mul_f32 v81, v5, v81\n"
  "v_mfma_f32_16x16x32_bf16 v[24:27], a[120:123], v[36:39], v[24:27]\n"
  "v_exp_f32 v80, v80\n"
  "v_mfma_f32_16x16x32_bf16 v[24:27], a[136:139], v[40:43], v[24:27]\n"
  "ds_read_b128 v[76:79], v91 offset:19456\n"
  "v_mfma_f32_16x16x32_bf16 v[24:27], a[152:155], v[44:47], v[24:27]\n"
  "v_exp_f32 v81, v81\n"
  "v_mfma_f32_16x16x32_bf16 v[24:27], a[168:171], v[48:51], v[24:27]\n"
  "buffer_load_dwordx4 v95, s[40:43], s60 offen offset:1024 lds\n"
  "v_mul_f32 v82, v4, v12\n"
  "v_mfma_f32_16x16x32_bf16 v[24:27], a[184:187], v[52:55], v[24:27]\n"
  "v_mul_f32 v83, v5, v13\n"
  "v_add_f32 v80, 1.0, v80\n"
  "v_mfma_f32_16x16x32_bf16 v[28:31], a[108:111], v[32:35], v[28:31]\n"
  "v_add_f32 v81, 1.0, v81\n"
  "v_mfma_f32_16x16x32_bf16 v[28:31], a[124:127], v[36:39], v[28:31]\n"
  "v_rcp_f32 v80, v80\n"
  "v_mfma_f32_16x16x32_bf16 v[28:31], a[140:143], v[40:43], v[28:31]\n"
  "v_rcp_f32 v81, v81\n"
  "v_mfma_f32_16x16x32_bf16 v[28:31], a[156:159], v[44:47], v[28:31]\n"
  "buffer_load_dwordx4 v96, s[40:43], s60 offen offset:2048 lds\n"
  "v_mul_f32 v82, v82, v80\n"
  "v_mfma_f32_16x16x32_bf16 v[28:31], a[172:175], v[48:51], v[28:31]\n"
  "s_add_u32 s60, s60, 36864\n"
  "v_mul_f32 v83, v83, v81\n"
  "v_cvt_pk_bf16_f32 v86, v82, v83\n"
  "v_mfma_f32_16x16x32_bf16 v[28:31], a[188:191], v[52:55], v[28:31]\n"
  "s_waitcnt vmcnt(25) lgkmcnt(0)\n"
  "s_barrier\n"
  "v_mfma_f32_16x16x32_bf16 v[16:19], a[192:195], v[56:59], v[16:19]\n"
  "ds_read_b128 v[32:35], v89 offset:16384\n"
  "v_mul_f32 v80, v6, v6\n"
  "v_mfma_f32_16x16x32_bf16 v[16:19], a[208:211], v[60:63], v[16:19]\n"
  "s_add_u32 m0, s48, 0\n"
  "v_mul_f32 v81, v7, v7\n"
  "v_fmaak_f32 v80, v88, v80, 0xc0135761\n"
  "v_mfma_f32_16x16x32_bf16 v[16:19], a[224:227], v[64:67], v[16:19]\n"
  "buffer_load_dwordx4 v97, s[36:39], s59 offen offset:3072 sc1 lds\n"
  "ds_read_b128 v[36:39], v89 offset:17408\n"
  "v_mfma_f32_16x16x32_bf16 v[16:19], a[240:243], v[68:71], v[16:19]\n"
  "s_add_u32 s59, s59, 1024\n"
  "v_fmaak_f32 v81, v88, v81, 0xc0135761\n"
  "v_mul_f32 v80, v6, v80\n"
  "v_mfma_f32_16x16x32_bf16 v[16:19], v[224:227], v[72:75], v[16:19]\n"
  "v_mul_f32 v81, v7, v81\n"
  "v_mfma_f32_16x16x32_bf16 v[16:19], v[240:243], v[76:79], v[16:19]\n"
  "ds_read_b128 v[40:43], v89 offset:18432\n"
  "v_mfma_f32_16x16x32_bf16 v[20:23], a[196:199], v[56:59], v[20:23]\n"
  "v_exp_f32 v80, v80\n"
  "v_mfma_f32_16x16x32_bf16 v[20:23], a[212:215], v[60:63], v[20:23]\n"
  "buffer_load_dwordx4 v94, s[40:43], s60 offen offset:0 lds\n"
  "v_mfma_f32_16x16x32_bf16 v[20:23], a[228:231], v[64:67], v[20:23]\n"
  "ds_read_b128 v[44:47], v89 offset:19456\n"
  "v_mfma_f32_16x16x32_bf16 v[20:23], a[244:247], v[68:71], v[20:23]\n"
  "v_exp_f32 v81, v81\n"
  "v_mfma_f32_16x16x32_bf16 v[20:23], v[228:231], v[72:75], v[20:23]\n"
  "v_mul_f32 v82, v6, v14\n"
  "v_mul_f32 v83, v7, v15\n"
  "v_mfma_f32_16x16x32_bf16 v[20:23], v[244:247], v[76:79], v[20:23]\n"
  "ds_read_b128 v[48:51], v89 offset:20480\n"
  "v_add_f32 v80, 1.0, v80\n"
  "v_mfma_f32_16x16x32_bf16 v[24:27], a[200:203], v[56:59], v[24:27]\n"
  "buffer_load_dwordx4 v95, s[40:43], s60 offen offset:1024 lds\n"
  "v_add_f32 v81, 1.0, v81\n"
  "v_mfma_f32_16x16x32_bf16 v[24:27], a[216:219], v[60:63], v[24:27]\n"
  "v_rcp_f32 v80, v80\n"
  "v_mfma_f32_16x16x32_bf16 v[24:27], a[232:235], v[64:67], v[24:27]\n"
  "ds_read_b128 v[52:55], v89 offset:21504\n"
  "v_mfma_f32_16x16x32_bf16 v[24:27], a[248:251], v[68:71], v[24:27]\n"
  "v_rcp_f32 v81, v81\n"
  "v_mfma_f32_16x16x32_bf16 v[24:27], v[232:235], v[72:75], v[24:27]\n"
  "v_mul_f32 v82, v82, v80\n"
  "v_mul_f32 v83, v83, v81\n"
  "v_mfma_f32_16x16x32_bf16 v[24:27], v[248:251], v[76:79], v[24:27]\n"
  "buffer_load_dwordx4 v96, s[40:43], s60 offen offset:2048 lds\n"
  "v_cvt_pk_bf16_f32 v87, v82, v83\n"
  "v_mfma_f32_16x16x32_bf16 v[28:31], a[204:207], v[56:59], v[28:31]\n"
  "s_add_u32 s60, s60, 36864\n"
  "v_mfma_f32_16x16x32_bf16 v[28:31], a[220:223], v[60:63], v[28:31]\n"
  "v_mfma_f32_16x16x32_bf16 v[28:31], a[236:239], v[64:67], v[28:31]\n"
  "v_mfma_f32_16x16x32_bf16 v[28:31], a[252:255], v[68:71], v[28:31]\n"
  "s_add_u32 m0, s48, 81920\n"
  "buffer_store_dwordx4 v[84:87], v89, s[36:39], s55 offen sc1\n"
  "v_mfma_f32_16x16x32_bf16 v[28:31], v[236:239], v[72:75], v[28:31]\n"
  "buffer_load_dwordx4 v92, s[36:39], s52 offen offset:0 sc1 lds\n"
  "v_mfma_f32_16x16x32_bf16 v[28:31], v[252:255], v[76:79], v[28:31]\n"
  "s_waitcnt lgkmcnt(0)\n"
  "v_mfma_f32_16x16x32_bf16 v[0:3], a[0:3], v[32:35], 0\n"
  "s_add_u32 s55, s55, 147456\n"
  "ds_read_b128 v[56:59], v89 offset:22528\n"
  "v_mfma_f32_16x16x32_bf16 v[0:3], a[16:19], v[36:39], v[0:3]\n"
  "s_cmp_eq_u32 s55, s57\n"
  "s_cselect_b32 s55, s56, s55\n"
  "v_mul_f32 v80, v16, v16\n"
  "v_mfma_f32_16x16x32_bf16 v[0:3], a[32:35], v[40:43], v[0:3]\n"
  "ds_read_b128 v[60:63], v89 offset:23552\n"
  "v_mul_f32 v81, v17, v17\n"
  "v_mfma_f32_16x16x32_bf16 v[0:3], a[48:51], v[44:47], v[0:3]\n"
  "buffer_load_dwordx4 v92, s[36:39], s52 offen offset:1024 sc1 lds\n"
  "v_fmaak_f32 v80, v88, v80, 0xc0135761\n"
  "v_mfma_f32_16x16x32_bf16 v[0:3], a[64:67], v[48:51], v[0:3]\n"
  "v_fmaak_f32 v81, v88, v81, 0xc0135761\n"
  "v_mul_f32 v80, v16, v80\n"
  "v_mfma_f32_16x16x32_bf16 v[0:3], a[80:83], v[52:55], v[0:3]\n"
  "ds_read_b128 v[64:67], v89 offset:24576\n"
  "v_mul_f32 v81, v17, v81\n"
  "v_mfma_f32_16x16x32_bf16 v[4:7], a[4:7], v[32:35], 0\n"
  "v_exp_f32 v80, v80\n"
  "v_mfma_f32_16x16x32_bf16 v[4:7], a[20:23], v[36:39], v[4:7]\n"
  "v_exp_f32 v81, v81\n"
  "v_mfma_f32_16x16x32_bf16 v[4:7], a[36:39], v[40:43], v[4:7]\n"
  "buffer_load_dwordx4 v92, s[36:39], s52 offen offset:2048 sc1 lds\n"
  "ds_read_b128 v[68:71], v89 offset:25600\n"
  "v_mfma_f32_16x16x32_bf16 v[4:7], a[52:55], v[44:47], v[4:7]\n"
  "v_mul_f32 v82, v16, v24\n"
  "v_mul_f32 v83, v17, v25\n"
  "v_mfma_f32_16x16x32_bf16 v[4:7], a[68:71], v[48:51], v[4:7]\n"
  "v_add_f32 v80, 1.0, v80\n"
  "v_add_f32 v81, 1.0, v81\n"
  "v_mfma_f32_16x16x32_bf16 v[4:7], a[84:87], v[52:55], v[4:7]\n"
  "ds_read_b128 v[72:75], v89 offset:26624\n"
  "v_mfma_f32_16x16x32_bf16 v[8:11], a[8:11], v[32:35], 0\n"
  "v_rcp_f32 v80, v80\n"
  "v_mfma_f32_16x16x32_bf16 v[8:11], a[24:27], v[36:39], v[8:11]\n"
  "buffer_load_dwordx4 v92, s[36:39], s52 offen offset:3072 sc1 lds\n"
  "v_mfma_f32_16x16x32_bf16 v[8:11], a[40:43], v[40:43], v[8:11]\n"
  "ds_read_b128 v[76:79], v89 offset:27648\n"
  "v_mfma_f32_16x16x32_bf16 v[8:11], a[56:59], v[44:47], v[8:11]\n"
  "v_rcp_f32 v81, v81\n"
  "v_mfma_f32_16x16x32_bf16 v[8:11], a[72:75], v[48:51], v[8:11]\n"
  "v_mul_f32 v82, v82, v80\n"
  "v_mul_f32 v83, v83, v81\n"
  "v_mfma_f32_16x16x32_bf16 v[8:11], a[88:91], v[52:55], v[8:11]\n"
  "s_add_u32 m0, s49, 143360\n"
  "v_cvt_pk_bf16_f32 v84, v82, v83\n"
  "v_mul_f32 v80, v18, v18\n"
  "v_mfma_f32_16x16x32_bf16 v[12:15], a[12:15], v[32:35], 0\n"
  "buffer_load_dwordx4 v93, s[36:39], s52 offen sc1 lds\n"
  "v_mul_f32 v81, v19, v19\n"
  "v_mfma_f32_16x16x32_bf16 v[12:15], a[28:31], v[36:39], v[12:15]\n"
  "s_add_u32 s52, s52, 18432\n"
  "v_fmaak_f32 v80, v88, v80, 0xc0135761\n"
  "v_fmaak_f32 v81, v88, v81, 0xc0135761\n"
  "v_mfma_f32_16x16x32_bf16 v[12:15], a[44:47], v[40:43], v[12:15]\n"
  "s_cmp_eq_u32 s52, s54\n"
  "s_cselect_b32 s52, s53, s52\n"
  "v_mul_f32 v80, v18, v80\n"
  "v_mfma_f32_16x16x32_bf16 v[12:15], a[60:63], v[44:47], v[12:15]\n"
  "v_mul_f32 v81, v19, v81\n"
  "v_mfma_f32_16x16x32_bf16 v[12:15], a[76:79], v[48:51], v[12:15]\n"
  "s_add_u32 m0, s48, 98304\n"
  "v_exp_f32 v80, v80\n"
  "v_mfma_f32_16x16x32_bf16 v[12:15], a[92:95], v[52:55], v[12:15]\n"
  "s_waitcnt lgkmcnt(0)\n"
  "v_exp_f32 v81, v81\n"
  "v_mfma_f32_16x16x32_bf16 v[0:3], a[96:99], v[56:59], v[0:3]\n"
  "buffer_load_dwordx4 v97, s[36:39], s59 offen offset:3072 sc1 lds\n"
  "ds_read_b128 v[32:35], v89 offset:28672\n"
  "v_mfma_f32_16x16x32_bf16 v[0:3], a[112:115], v[60:63], v[0:3]\n"
  "s_add_u32 s59, s59, 1024\n"
  "v_mul_f32 v82, v18, v26\n"
  "v_mul_f32 v83, v19, v27\n"
  "v_mfma_f32_16x16x32_bf16 v[0:3], a[128:131], v[64:67], v[0:3]\n"
  "ds_read_b128 v[36:39], v89 offset:29696\n"
  "v_add_f32 v80, 1.0, v80\n"
  "v_mfma_f32_16x16x32_bf16 v[0:3], a[144:147], v[68:71], v[0:3]\n"
  "v_add_f32 v81, 1.0, v81\n"
  "v_mfma_f32_16x16x32_bf16 v[0:3], a[160:163], v[72:75], v[0:3]\n"
  "buffer_load_dwordx4 v94, s[40:43], s60 offen offset:0 lds\n"
  "v_mfma_f32_16x16x32_bf16 v[0:3], a[176:179], v[76:79], v[0:3]\n"
  "ds_read_b128 v[40:43], v89 offset:30720\n"
  "v_mfma_f32_16x16x32_bf16 v[4:7], a[100:103], v[56:59], v[4:7]\n"
  "v_rcp_f32 v80, v80\n"
  "v_mfma_f32_16x16x32_bf16 v[4:7], a[116:119], v[60:63], v[4:7]\n"
  "v_rcp_f32 v81, v81\n"
  "v_mfma_f32_16x16x32_bf16 v[4:7], a[132:135], v[64:67], v[4:7]\n"
  "ds_read_b128 v[44:47], v89 offset:31744\n"
  "v_mul_f32 v82, v82, v80\n"
  "v_mfma_f32_16x16x32_bf16 v[4:7], a[148:151], v[68:71], v[4:7]\n"
  "buffer_load_dwordx4 v95, s[40:43], s60 offen offset:1024 lds\n"
  "v_mul_f32 v83, v83, v81\n"
  "v_mfma_f32_16x16x32_bf16 v[4:7], a[164:167], v[72:75], v[4:7]\n"
  "buffer_store_dwordx4 v[108:111], v107, s[36:39], s62 offen offset:2048 sc1\n"
  "v_cvt_pk_bf16_f32 v85, v82, v83\n"
  "v_mfma_f32_16x16x32_bf16 v[4:7], a[180:183], v[76:79], v[4:7]\n"
  "ds_read_b128 v[48:51], v91 offset:16384\n"
  "v_mul_f32 v80, v20, v20\n"
  "v_mfma_f32_16x16x32_bf16 v[8:11], a[104:107], v[56:59], v[8:11]\n"
  "v_mul_f32 v81, v21, v21\n"
  "v_fmaak_f32 v80, v88, v80, 0xc0135761\n"
  "v_mfma_f32_16x16x32_bf16 v[8:11], a[120:123], v[60:63], v[8:11]\n"
  "v_fmaak_f32 v81, v88, v81, 0xc0135761\n"
  "v_mul_f32 v80, v20, v80\n"
  "v_mfma_f32_16x16x32_bf16 v[8:11], a[136:139], v[64:67], v[8:11]\n"
  "buffer_load_dwordx4 v96, s[40:43], s60 offen offset:2048 lds\n"
  "ds_read_b128 v[52:55], v91 offset:17408\n"
  "v_mfma_f32_16x16x32_bf16 v[8:11], a[152:155], v[68:71], v[8:11]\n"
  "s_add_u32 s60, s60, 36864\n"
  "v_mul_f32 v81, v21, v81\n"
  "v_mfma_f32_16x16x32_bf16 v[8:11], a[168:171], v[72:75], v[8:11]\n"
  "v_exp_f32 v80, v80\n"
  "v_mfma_f32_16x16x32_bf16 v[8:11], a[184:187], v[76:79], v[8:11]\n"
  "v_exp_f32 v81, v81\n"
  "v_mfma_f32_16x16x32_bf16 v[12:15], a[108:111], v[56:59], v[12:15]\n"
  "s_add_u32 m0, s48, 114688\n"
  "v_mul_f32 v82, v20, v28\n"
  "v_mul_f32 v83, v21, v29\n"
  "v_mfma_f32_16x16x32_bf16 v[12:15], a[124:127], v[60:63], v[12:15]\n"
  "buffer_load_dwordx4 v97, s[36:39], s59 offen offset:3072 sc1 lds\n"
  "v_add_f32 v80, 1.0, v80\n"
  "v_mfma_f32_16x16x32_bf16 v[12:15], a[140:143], v[64:67], v[12:15]\n"
  "s_add_u32 s59, s59, 1024\n"
  "v_add_f32 v81, 1.0, v81\n"
  "v_mfma_f32_16x16x32_bf16 v[12:15], a[156:159], v[68:71], v[12:15]\n"
  "v_rcp_f32 v80, v80\n"
  "v_mfma_f32_16x16x32_bf16 v[12:15], a[172:175], v[72:75], v[12:15]\n"
  "v_rcp_f32 v81, v81\n"
  "v_mfma_f32_16x16x32_bf16 v[12:15], a[188:191], v[76:79], v[12:15]\n"
  "s_waitcnt vmcnt(12) lgkmcnt(0)\n"
  "s_barrier\n"
  "v_mfma_f32_16x16x32_bf16 v[0:3], a[192:195], v[32:35], v[0:3]\n"
  "buffer_load_dwordx4 v94, s[40:43], s60 offen offset:0 lds\n"
  "ds_read_b128 v[56:59], v89 offset:49152\n"
  "v_mfma_f32_16x16x32_bf16 v[0:3], a[208:211], v[36:39], v[0:3]\n"
  "v_mul_f32 v82, v82, v80\n"
  "v_mul_f32 v83, v83, v81\n"
  "v_mfma_f32_16x16x32_bf16 v[0:3], a[224:227], v[40:43], v[0:3]\n"
  "ds_read_b128 v[60:63], v89 offset:50176\n"
  "v_cvt_pk_bf16_f32 v86, v82, v83\n"
  "v_mfma_f32_16x16x32_bf16 v[0:3], a[240:243], v[44:47], v[0:3]\n"
  "v_mul_f32 v80, v22, v22\n"
  "v_mul_f32 v81, v23, v23\n"
  "v_mfma_f32_16x16x32_bf16 v[0:3], v[224:227], v[48:51], v[0:3]\n"
  "v_fmaak_f32 v80, v88, v80, 0xc0135761\n"
  "v_fmaak_f32 v81, v88, v81, 0xc0135761\n"
  "v_mfma_f32_16x16x32_bf16 v[0:3], v[240:243], v[52:55], v[0:3]\n"
  "buffer_load_dwordx4 v95, s[40:43], s60 offen offset:1024 lds\n"
  "ds_read_b128 v[64:67], v89 offset:51200\n"
  "v_mfma_f32_16x16x32_bf16 v[4:7], a[196:199], v[32:35], v[4:7]\n"
  "v_mul_f32 v80, v22, v80\n"
  "v_mul_f32 v81, v23, v81\n"
  "v_mfma_f32_16x16x32_bf16 v[4:7], a[212:215], v[36:39], v[4:7]\n"
  "v_exp_f32 v80, v80\n"
  "v_mfma_f32_16x16x32_bf16 v[4:7], a[228:231], v[40:43], v[4:7]\n"
  "ds_read_b128 v[68:71], v89 offset:52224\n"
  "v_mfma_f32_16x16x32_bf16 v[4:7], a[244:247], v[44:47], v[4:7]\n"
  "v_exp_f32 v81, v81\n"
  "v_mfma_f32_16x16x32_bf16 v[4:7], v[228:231], v[48:51], v[4:7]\n"
  "buffer_load_dwordx4 v96, s[40:43], s60 offen offset:2048 lds\n"
  "v_mul_f32 v82, v22, v30\n"
  "v_mfma_f32_16x16x32_bf16 v[4:7], v[244:247], v[52:55], v[4:7]\n"
  "s_add_u32 s60, s60, 36864\n"
  "ds_read_b128 v[72:75], v89 offset:53248\n"
  "v_mul_f32 v83, v23, v31\n"
  "v_mfma_f32_16x16x32_bf16 v[8:11], a[200:203], v[32:35], v[8:11]\n"
  "v_add_f32 v80, 1.0, v80\n"
  "v_add_f32 v81, 1.0, v81\n"
  "v_mfma_f32_16x16x32_bf16 v[8:11], a[216:219], v[36:39], v[8:11]\n"
  "v_rcp_f32 v80, v80\n"
  "v_mfma_f32_16x16x32_bf16 v[8:11], a[232:235], v[40:43], v[8:11]\n"
  "s_add_u32 m0, s48, 32768\n"
  "ds_read_b128 v[76:79], v89 offset:54272\n"
  "v_mfma_f32_16x16x32_bf16 v[8:11], a[248:251], v[44:47], v[8:11]\n"
  "buffer_load_dwordx4 v92, s[36:39], s52 offen offset:0 sc1 lds\n"
  "v_mfma_f32_16x16x32_bf16 v[8:11], v[232:235], v[48:51], v[8:11]\n"
  "v_rcp_f32 v81, v81\n"
  "v_mfma_f32_16x16x32_bf16 v[8:11], v[248:251], v[52:55], v[8:11]\n"
  "v_mul_f32 v82, v82, v80\n"
  "v_mul_f32 v83, v83, v81\n"
  "v_mfma_f32_16x16x32_bf16 v[12:15], a[204:207], v[32:35], v[12:15]\n"
  "v_cvt_pk_bf16_f32 v87, v82, v83\n"
  "v_mfma_f32_16x16x32_bf16 v[12:15], a[220:223], v[36:39], v[12:15]\n"
  "v_mfma_f32_16x16x32_bf16 v[12:15], a[236:239], v[40:43], v[12:15]\n"
  "buffer_load_dwordx4 v92, s[36:39], s52 offen offset:1024 sc1 lds\n"
  "v_mfma_f32_16x16x32_bf16 v[12:15], a[252:255], v[44:47], v[12:15]\n"
  "buffer_store_dwordx4 v[84:87], v89, s[36:39], s55 offen sc1\n"
  "v_mfma_f32_16x16x32_bf16 v[12:15], v[236:239], v[48:51], v[12:15]\n"
  "s_add_u32 s55, s55, 147456\n"
  "v_mfma_f32_16x16x32_bf16 v[12:15], v[252:255], v[52:55], v[12:15]\n"
  "s_waitcnt lgkmcnt(0)\n"
  "v_mfma_f32_16x16x32_bf16 v[16:19], a[0:3], v[56:59], 0\n"
  "s_cmp_eq_u32 s55, s57\n"
  "s_cselect_b32 s55, s56, s55\n"
  "ds_read_b64_tr_b16 v[112:113], v100 offset:0\n"
  "v_mfma_f32_16x16x32_bf16 v[16:19], a[16:19], v[60:63], v[16:19]\n"
  "buffer_load_dwordx4 v92, s[36:39], s52 offen offset:2048 sc1 lds\n"
  "ds_read_b64_tr_b16 v[114:115], v100 offset:8192\n"
  "v_mfma_f32_16x16x32_bf16 v[16:19], a[32:35], v[64:67], v[16:19]\n"
  "s_cbranch_execz Ltramp%=\n"
  "v_mul_f32 v80, v0, v0\n"
  "v_mul_f32 v81, v1, v1\n"
  "v_mfma_f32_16x16x32_bf16 v[16:19], a[48:51], v[68:71], v[16:19]\n"
  "ds_read_b128 v[124:127], v90 offset:3072\n"
  "v_fmaak_f32 v80, v88, v80, 0xc0135761\n"
  "v_mfma_f32_16x16x32_bf16 v[16:19], a[64:67], v[72:75], v[16:19]\n"
  "ds_read_b128 v[128:131], v90 offset:7168\n"
  "v_fmaak_f32 v81, v88, v81, 0xc0135761\n"
  "v_mfma_f32_16x16x32_bf16 v[16:19], a[80:83], v[76:79], v[16:19]\n"
  "v_mul_f32 v80, v0, v80\n"
  "v_mul_f32 v81, v1, v81\n"
  "v_mfma_f32_16x16x32_bf16 v[20:23], a[4:7], v[56:59], 0\n"
  "buffer_load_dwordx4 v92, s[36:39], s52 offen offset:3072 sc1 lds\n"
  "ds_read_b128 v[132:135], v90 offset:11264\n"
  "v_mfma_f32_16x16x32_bf16 v[20:23], a[20:23], v[60:63], v[20:23]\n"
  "ds_read_b128 v[136:139], v90 offset:15360\n"
  "v_mfma_f32_16x16x32_bf16 v[20:23], a[36:39], v[64:67], v[20:23]\n"
  "v_exp_f32 v80, v80\n"
  "v_mfma_f32_16x16x32_bf16 v[20:23], a[52:55], v[68:71], v[20:23]\n"
  "ds_read_b128 v[140:143], v91 offset:24576\n"
  "v_mfma_f32_16x16x32_bf16 v[20:23], a[68:71], v[72:75], v[20:23]\n"
  "s_add_u32 m0, s49, 137216\n"
  "ds_read_b64_tr_b16 v[116:117], v100 offset:1024\n"
  "v_mfma_f32_16x16x32_bf16 v[20:23], a[84:87], v[76:79], v[20:23]\n"
  "buffer_load_dwordx4 v93, s[36:39], s52 offen sc1 lds\n"
  "v_mfma_f32_16x16x32_bf16 v[24:27], a[8:11], v[56:59], 0\n"
  "s_add_u32 s52, s52, 18432\n"
  "ds_read_b64_tr_b16 v[118:119], v100 offset:9216\n"
  "v_mfma_f32_16x16x32_bf16 v[24:27], a[24:27], v[60:63], v[24:27]\n"
  "s_cmp_eq_u32 s52, s54\n"
  "s_cselect_b32 s52, s53, s52\n"
  "ds_read_b64_tr_b16 v[120:121], v100 offset:2048\n"
  "v_mfma_f32_16x16x32_bf16 v[24:27], a[40:43], v[64:67], v[24:27]\n"
  "v_exp_f32 v81, v81\n"
  "v_mfma_f32_16x16x32_bf16 v[24:27], a[56:59], v[68:71], v[24:27]\n"
  "s_add_u32 m0, s50, 147456\n"
  "ds_read_b64_tr_b16 v[122:123], v100 offset:10240\n"
  "v_mul_f32 v82, v0, v8\n"
  "v_mfma_f32_16x16x32_bf16 v[24:27], a[72:75], v[72:75], v[24:27]\n"
  "buffer_load_dwordx4 v98, s[36:39], s59 offen sc1 lds\n"
  "v_mul_f32 v83, v1, v9\n"
  "v_mfma_f32_16x16x32_bf16 v[24:27], a[88:91], v[76:79], v[24:27]\n"
  "v_add_f32 v80, 1.0, v80\n"
  "v_add_f32 v81, 1.0, v81\n"
  "v_mfma_f32_16x16x32_bf16 v[28:31], a[12:15], v[56:59], 0\n"
  "v_rcp_f32 v80, v80\n"
  "v_mfma_f32_16x16x32_bf16 v[28:31], a[28:31], v[60:63], v[28:31]\n"
  "v_rcp_f32 v81, v81\n"
  "v_mfma_f32_16x16x32_bf16 v[28:31], a[44:47], v[64:67], v[28:31]\n"
  "s_add_u32 m0, s48, 131072\n"
  "v_mul_f32 v82, v82, v80\n"
  "v_mul_f32 v83, v83, v81\n"
  "v_mfma_f32_16x16x32_bf16 v[28:31], a[60:63], v[68:71], v[28:31]\n"
  "buffer_load_dwordx4 v97, s[36:39], s59 offen offset:3072 sc1 lds\n"
  "v_cvt_pk_bf16_f32 v84, v82, v83\n"
  "v_mfma_f32_16x16x32_bf16 v[28:31], a[76:79], v[72:75], v[28:31]\n"
  "s_add_u32 s59, s59, 1024\n"
  "v_mul_f32 v80, v2, v2\n"
  "v_mul_f32 v81, v3, v3\n"
  "v_mfma_f32_16x16x32_bf16 v[28:31], a[92:95], v[76:79], v[28:31]\n"
  "s_waitcnt lgkmcnt(0)\n"
  "v_fmaak_f32 v80, v88, v80, 0xc0135761\n"
  "v_fmaak_f32 v81, v88, v81, 0xc0135761\n"
  "v_mfma_f32_16x16x32_bf16 v[144:147], v[112:115], v[124:127], 0\n"
  "ds_read_b128 v[32:35], v89 offset:55296\n"
  "v_mul_f32 v80, v2, v80\n"
  "v_mfma_f32_16x16x32_bf16 v[148:151], v[112:115], v[128:131], 0\n"
  "ds_read_b128 v[36:39], v89 offset:56320\n"
  "v_mul_f32 v81, v3, v81\n"
  "v_mfma_f32_16x16x32_bf16 v[152:155], v[112:115], v[132:135], 0\n"
  "buffer_load_dwordx4 v94, s[40:43], s60 offen offset:0 lds\n"
  "ds_read_b128 v[40:43], v89 offset:57344\n"
  "v_mfma_f32_16x16x32_bf16 v[156:159], v[112:115], v[136:139], 0\n"
  "v_exp_f32 v80, v80\n"
  "v_mfma_f32_16x16x32_bf16 v[160:163], v[112:115], v[140:143], 0\n"
  "ds_read_b128 v[44:47], v89 offset:58368\n"
  "v_mfma_f32_16x16x32_bf16 v[164:167], v[116:119], v[124:127], 0\n"
  "ds_read_b128 v[48:51], v89 offset:59392\n"
  "v_mfma_f32_16x16x32_bf16 v[168:171], v[116:119], v[128:131], 0\n"
  "ds_read_b128 v[52:55], v89 offset:60416\n"
  "v_mfma_f32_16x16x32_bf16 v[172:175], v[116:119], v[132:135], 0\n"
  "buffer_load_dwordx4 v95, s[40:43], s60 offen offset:1024 lds\n"
  "v_mfma_f32_16x16x32_bf16 v[176:179], v[116:119], v[136:139], 0\n"
  "v_exp_f32 v81, v81\n"
  "v_mfma_f32_16x16x32_bf16 v[180:183], v[116:119], v[140:143], 0\n"
  "v_mul_f32 v82, v2, v10\n"
  "v_mul_f32 v83, v3, v11\n"
  "v_mfma_f32_16x16x32_bf16 v[184:187], v[120:123], v[124:127], 0\n"
  "v_add_f32 v80, 1.0, v80\n"
  "v_add_f32 v81, 1.0, v81\n"
  "v_mfma_f32_16x16x32_bf16 v[188:191], v[120:123], v[128:131], 0\n"
  "v_rcp_f32 v80, v80\n"
  "v_mfma_f32_16x16x32_bf16 v[192:195], v[120:123], v[132:135], 0\n"
  "buffer_load_dwordx4 v96, s[40:43], s60 offen offset:2048 lds\n"
  "v_mfma_f32_16x16x32_bf16 v[196:199], v[120:123], v[136:139], 0\n"
  "s_add_u32 s60, s60, 36864\n"
  "v_rcp_f32 v81, v81\n"
  "v_mfma_f32_16x16x32_bf16 v[200:203], v[120:123], v[140:143], 0\n"
  "s_waitcnt lgkmcnt(0)\n"
  "v_mul_f32 v82, v82, v80\n"
  "v_mul_f32 v83, v83, v81\n"
  "v_mfma_f32_16x16x32_bf16 v[16:19], a[96:99], v[32:35], v[16:19]\n"
  "ds_read_b64_tr_b16 v[112:113], v99 offset:0\n"
  "v_cvt_pk_bf16_f32 v85, v82, v83\n"
  "v_mfma_f32_16x16x32_bf16 v[16:19], a[112:115], v[36:39], v[16:19]\n"
  "s_add_u32 m0, s48, 16384\n"
  "ds_read_b64_tr_b16 v[114:115], v99 offset:8192\n"
  "v_mul_f32 v80, v4, v4\n"
  "v_mfma_f32_16x16x32_bf16 v[16:19], a[128:131], v[40:43], v[16:19]\n"
  "buffer_load_dwordx4 v97, s[36:39], s59 offen offset:3072 sc1 lds\n"
  "v_mul_f32 v81, v5, v5\n"
  "v_mfma_f32_16x16x32_bf16 v[16:19], a[144:147], v[44:47], v[16:19]\n"
  "s_add_u32 s59, s59, 1024\n"
  "ds_read_b128 v[124:127], v89 offset:3072\n"
  "v_fmaak_f32 v80, v88, v80, 0xc0135761\n"
  "v_mfma_f32_16x16x32_bf16 v[16:19], a[160:163], v[48:51], v[16:19]\n"
  "ds_read_b128 v[128:131], v89 offset:7168\n"
  "v_fmaak_f32 v81, v88, v81, 0xc0135761\n"
  "v_mfma_f32_16x16x32_bf16 v[16:19], a[176:179], v[52:55], v[16:19]\n"
  "v_mul_f32 v80, v4, v80\n"
  "v_mul_f32 v81, v5, v81\n"
  "v_mfma_f32_16x16x32_bf16 v[20:23], a[100:103], v[32:35], v[20:23]\n"
  "ds_read_b128 v[132:135], v89 offset:11264\n"
  "v_mfma_f32_16x16x32_bf16 v[20:23], a[116:119], v[36:39], v[20:23]\n"
  "buffer_load_dwordx4 v94, s[40:43], s60 offen offset:0 lds\n"
  "ds_read_b128 v[136:139], v89 offset:15360\n"
  "v_mfma_f32_16x16x32_bf16 v[20:23], a[132:135], v[40:43], v[20:23]\n"
  "v_exp_f32 v80, v80\n"
  "v_mfma_f32_16x16x32_bf16 v[20:23], a[148:151], v[44:47], v[20:23]\n"
  "ds_read_b128 v[140:143], v91 offset:25600\n"
  "v_mfma_f32_16x16x32_bf16 v[20:23], a[164:167], v[48:51], v[20:23]\n"
  "ds_read_b64_tr_b16 v[116:117], v99 offset:1024\n"
  "v_mfma_f32_16x16x32_bf16 v[20:23], a[180:183], v[52:55], v[20:23]\n"
  "v_exp_f32 v81, v81\n"
  "v_mfma_f32_16x16x32_bf16 v[24:27], a[104:107], v[32:35], v[24:27]\n"
  "buffer_load_dwordx4 v95, s[40:43], s60 offen offset:1024 lds\n"
  "ds_read_b64_tr_b16 v[118:119], v99 offset:9216\n"
  "v_mfma_f32_16x16x32_bf16 v[24:27], a[120:123], v[36:39], v[24:27]\n"
  "ds_read_b64_tr_b16 v[120:121], v99 offset:2048\n"
  "v_mul_f32 v82, v4, v12\n"
  "v_mfma_f32_16x16x32_bf16 v[24:27], a[136:139], v[40:43], v[24:27]\n"
  "v_mul_f32 v83, v5, v13\n"
  "v_add_f32 v80, 1.0, v80\n"
  "v_mfma_f32_16x16x32_bf16 v[24:27], a[152:155], v[44:47], v[24:27]\n"
  "ds_read_b64_tr_b16 v[122:123], v99 offset:10240\n"
  "v_add_f32 v81, 1.0, v81\n"
  "v_mfma_f32_16x16x32_bf16 v[24:27], a[168:171], v[48:51], v[24:27]\n"
  "v_rcp_f32 v80, v80\n"
  "v_mfma_f32_16x16x32_bf16 v[24:27], a[184:187], v[52:55], v[24:27]\n"
  "buffer_load_dwordx4 v96, s[40:43], s60 offen offset:2048 lds\n"
  "v_mfma_f32_16x16x32_bf16 v[28:31], a[108:111], v[32:35], v[28:31]\n"
  "s_add_u32 s60, s60, 36864\n"
  "v_rcp_f32 v81, v81\n"
  "v_mfma_f32_16x16x32_bf16 v[28:31], a[124:127], v[36:39], v[28:31]\n"
  "v_mul_f32 v82, v82, v80\n"
  "v_mul_f32 v83, v83, v81\n"
  "v_mfma_f32_16x16x32_bf16 v[28:31], a[140:143], v[40:43], v[28:31]\n"
  "v_cvt_pk_bf16_f32 v86, v82, v83\n"
  "v_mul_f32 v80, v6, v6\n"
  "v_mfma_f32_16x16x32_bf16 v[28:31], a[156:159], v[44:47], v[28:31]\n"
  "v_mul_f32 v81, v7, v7\n"
  "v_fmaak_f32 v80, v88, v80, 0xc0135761\n"
  "v_mfma_f32_16x16x32_bf16 v[28:31], a[172:175], v[48:51], v[28:31]\n"
  "v_fmaak_f32 v81, v88, v81, 0xc0135761\n"
  "v_mul_f32 v80, v6, v80\n"
  "v_mfma_f32_16x16x32_bf16 v[28:31], a[188:191], v[52:55], v[28:31]\n"
  "s_waitcnt lgkmcnt(0)\n"
  "v_mul_f32 v81, v7, v81\n"
  "v_mfma_f32_16x16x32_bf16 v[144:147], v[112:115], v[124:127], v[144:147]\n"
  "ds_read_b128 v[56:59], v89 offset:61440\n"
  "v_mfma_f32_16x16x32_bf16 v[148:151], v[112:115], v[128:131], v[148:151]\n"
  "ds_read_b128 v[60:63], v89 offset:62464\n"
  "v_mfma_f32_16x16x32_bf16 v[152:155], v[112:115], v[132:135], v[152:155]\n"
  "ds_read_b128 v[64:67], v89 offset:63488\n"
  "v_mfma_f32_16x16x32_bf16 v[156:159], v[112:115], v[136:139], v[156:159]\n"
  "v_exp_f32 v80, v80\n"
  "v_mfma_f32_16x16x32_bf16 v[160:163], v[112:115], v[140:143], v[160:163]\n"
  "ds_read_b128 v[68:71], v89 offset:64512\n"
  "v_mfma_f32_16x16x32_bf16 v[164:167], v[116:119], v[124:127], v[164:167]\n"
  "ds_read_b128 v[72:75], v91 offset:30720\n"
  "v_mfma_f32_16x16x32_bf16 v[168:171], v[116:119], v[128:131], v[168:171]\n"
  "ds_read_b128 v[76:79], v91 offset:31744\n"
  "v_mfma_f32_16x16x32_bf16 v[172:175], v[116:119], v[132:135], v[172:175]\n"
  "v_exp_f32 v81, v81\n"
  "v_mfma_f32_16x16x32_bf16 v[176:179], v[116:119], v[136:139], v[176:179]\n"
  "v_mul_f32 v82, v6, v14\n"
  "v_mul_f32 v83, v7, v15\n"
  "v_mfma_f32_16x16x32_bf16 v[180:183], v[116:119], v[140:143], v[180:183]\n"
  "v_add_f32 v80, 1.0, v80\n"
  "v_add_f32 v81, 1.0, v81\n"
  "v_mfma_f32_16x16x32_bf16 v[184:187], v[120:123], v[124:127], v[184:187]\n"
  "v_rcp_f32 v80, v80\n"
  "v_mfma_f32_16x16x32_bf16 v[188:191], v[120:123], v[128:131], v[188:191]\n"
  "v_rcp_f32 v81, v81\n"
  "v_mfma_f32_16x16x32_bf16 v[192:195], v[120:123], v[132:135], v[192:195]\n"
  "s_waitcnt vmcnt(12)\n"
  "s_mov_b64 exec, 1\n"
  "buffer_atomic_add v104, off, s[24:27], s13 sc1\n"
  "s_mov_b64 exec, -1\n"
  "v_mfma_f32_16x16x32_bf16 v[196:199], v[120:123], v[136:139], v[196:199]\n"
  "v_mul_f32 v82, v82, v80\n"
  "v_mul_f32 v83, v83, v81\n"
  "v_mfma_f32_16x16x32_bf16 v[200:203], v[120:123], v[140:143], v[200:203]\n"
  "s_waitcnt vmcnt(16) lgkmcnt(0)\n"
  "s_barrier\n"
  "v_mfma_f32_16x16x32_bf16 v[16:19], a[192:195], v[56:59], v[16:19]\n"
  "s_add_u32 m0, s48, 0\n"
  "ds_read_b128 v[32:35], v90 offset:16384\n"
  "v_cvt_pk_bf16_f32 v87, v82, v83\n"
  "v_mfma_f32_16x16x32_bf16 v[16:19], a[208:211], v[60:63], v[16:19]\n"
  "buffer_load_dwordx4 v92, s[36:39], s52 offen offset:0 sc1 lds\n"
  "v_mfma_f32_16x16x32_bf16 v[16:19], a[224:227], v[64:67], v[16:19]\n"
  "ds_read_b128 v[36:39], v90 offset:17408\n"
  "v_mfma_f32_16x16x32_bf16 v[16:19], a[240:243], v[68:71], v[16:19]\n"
  "v_mfma_f32_16x16x32_bf16 v[16:19], v[224:227], v[72:75], v[16:19]\n"
  "v_mfma_f32_16x16x32_bf16 v[16:19], v[240:243], v[76:79], v[16:19]\n"
  "ds_read_b128 v[40:43], v90 offset:18432\n"
  "v_mfma_f32_16x16x32_bf16 v[20:23], a[196:199], v[56:59], v[20:23]\n"
  "buffer_load_dwordx4 v92, s[36:39], s52 offen offset:1024 sc1 lds\n"
  "v_mfma_f32_16x16x32_bf16 v[20:23], a[212:215], v[60:63], v[20:23]\n"
  "v_mfma_f32_16x16x32_bf16 v[20:23], a[228:231], v[64:67], v[20:23]\n"
  "ds_read_b128 v[44:47], v90 offset:19456\n"
  "v_mfma_f32_16x16x32_bf16 v[20:23], a[244:247], v[68:71], v[20:23]\n"
  "v_mfma_f32_16x16x32_bf16 v[20:23], v[228:231], v[72:75], v[20:23]\n"
  "v_mfma_f32_16x16x32_bf16 v[20:23], v[244:247], v[76:79], v[20:23]\n"
  "buffer_load_dwordx4 v92, s[36:39], s52 offen offset:2048 sc1 lds\n"
  "ds_read_b128 v[48:51], v90 offset:20480\n"
  "v_mfma_f32_16x16x32_bf16 v[24:27], a[200:203], v[56:59], v[24:27]\n"
  "v_mfma_f32_16x16x32_bf16 v[24:27], a[216:219], v[60:63], v[24:27]\n"
  "v_mfma_f32_16x16x32_bf16 v[24:27], a[232:235], v[64:67], v[24:27]\n"
  "ds_read_b128 v[52:55], v90 offset:21504\n"
  "v_mfma_f32_16x16x32_bf16 v[24:27], a[248:251], v[68:71], v[24:27]\n"
  "v_mfma_f32_16x16x32_bf16 v[24:27], v[232:235], v[72:75], v[24:27]\n"
  "buffer_load_dwordx4 v92, s[36:39], s52 offen offset:3072 sc1 lds\n"
  "v_mfma_f32_16x16x32_bf16 v[24:27], v[248:251], v[76:79], v[24:27]\n"
  "v_mfma_f32_16x16x32_bf16 v[28:31], a[204:207], v[56:59], v[28:31]\n"
  "v_mfma_f32_16x16x32_bf16 v[28:31], a[220:223], v[60:63], v[28:31]\n"
  "v_mfma_f32_16x16x32_bf16 v[28:31], a[236:239], v[64:67], v[28:31]\n"
  "s_add_u32 m0, s49, 135168\n"
  "v_mfma_f32_16x16x32_bf16 v[28:31], a[252:255], v[68:71], v[28:31]\n"
  "buffer_load_dwordx4 v93, s[36:39], s52 offen sc1 lds\n"
  "v_mfma_f32_16x16x32_bf16 v[28:31], v[236:239], v[72:75], v[28:31]\n"
  "s_add_u32 s52, s52, 18432\n"
  "buffer_store_dwordx4 v[84:87], v89, s[36:39], s55 offen sc1\n"
  "v_mfma_f32_16x16x32_bf16 v[28:31], v[252:255], v[76:79], v[28:31]\n"
  "s_waitcnt lgkmcnt(0)\n"
  "s_add_u32 s55, s55, 147456\n"
  "v_mfma_f32_16x16x32_bf16 v[0:3], a[0:3], v[32:35], 0\n"
  "s_cmp_eq_u32 s52, s54\n"
  "s_cselect_b32 s52, s53, s52\n"
  "ds_read_b64_tr_b16 v[112:113], v100 offset:32768\n"
  "v_mfma_f32_16x16x32_bf16 v[0:3], a[16:19], v[36:39], v[0:3]\n"
  "s_add_u32 m0, s48, 49152\n"
  "s_cmp_eq_u32 s55, s57\n"
  "s_cselect_b32 s55, s56, s55\n"
  "ds_read_b64_tr_b16 v[114:115], v100 offset:40960\n"
  "v_mfma_f32_16x16x32_bf16 v[0:3], a[32:35], v[40:43], v[0:3]\n"
  "buffer_load_dwordx4 v97, s[36:39], s59 offen offset:3072 sc1 lds\n"
  "v_mul_f32 v80, v16, v16\n"
  "v_mfma_f32_16x16x32_bf16 v[0:3], a[48:51], v[44:47], v[0:3]\n"
  "s_add_u32 s59, s59, 1024\n"
  "ds_read_b128 v[124:127], v90 offset:35840\n"
  "v_mul_f32 v81, v17, v17\n"
  "v_mfma_f32_16x16x32_bf16 v[0:3], a[64:67], v[48:51], v[0:3]\n"
  "ds_read_b128 v[128:131], v90 offset:39936\n"
  "v_fmaak_f32 v80, v88, v80, 0xc0135761\n"
  "v_mfma_f32_16x16x32_bf16 v[0:3], a[80:83], v[52:55], v[0:3]\n"
  "v_fmaak_f32 v81, v88, v81, 0xc0135761\n"
  "v_mul_f32 v80, v16, v80\n"
  "v_mfma_f32_16x16x32_bf16 v[4:7], a[4:7], v[32:35], 0\n"
  "ds_read_b128 v[132:135], v90 offset:44032\n"
  "v_mul_f32 v81, v17, v81\n"
  "v_mfma_f32_16x16x32_bf16 v[4:7], a[20:23], v[36:39], v[4:7]\n"
  "buffer_load_dwordx4 v94, s[40:43], s60 offen offset:0 lds\n"
  "ds_read_b128 v[136:139], v90 offset:48128\n"
  "v_mfma_f32_16x16x32_bf16 v[4:7], a[36:39], v[40:43], v[4:7]\n"
  "v_exp_f32 v80, v80\n"
  "v_mfma_f32_16x16x32_bf16 v[4:7], a[52:55], v[44:47], v[4:7]\n"
  "ds_read_b128 v[140:143], v91 offset:26624\n"
  "v_mfma_f32_16x16x32_bf16 v[4:7], a[68:71], v[48:51], v[4:7]\n"
  "ds_read_b64_tr_b16 v[116:117], v100 offset:33792\n"
  "v_mfma_f32_16x16x32_bf16 v[4:7], a[84:87], v[52:55], v[4:7]\n"
  "v_exp_f32 v81, v81\n"
  "v_mfma_f32_16x16x32_bf16 v[8:11], a[8:11], v[32:35], 0\n"
  "buffer_load_dwordx4 v95, s[40:43], s60 offen offset:1024 lds\n"
  "ds_read_b64_tr_b16 v[118:119], v100 offset:41984\n"
  "v_mfma_f32_16x16x32_bf16 v[8:11], a[24:27], v[36:39], v[8:11]\n"
  "ds_read_b64_tr_b16 v[120:121], v100 offset:34816\n"
  "v_mul_f32 v82, v16, v24\n"
  "v_mfma_f32_16x16x32_bf16 v[8:11], a[40:43], v[40:43], v[8:11]\n"
  "v_mul_f32 v83, v17, v25\n"
  "v_add_f32 v80, 1.0, v80\n"
  "v_mfma_f32_16x16x32_bf16 v[8:11], a[56:59], v[44:47], v[8:11]\n"
  "ds_read_b64_tr_b16 v[122:123], v100 offset:43008\n"
  "v_add_f32 v81, 1.0, v81\n"
  "v_mfma_f32_16x16x32_bf16 v[8:11], a[72:75], v[48:51], v[8:11]\n"
  "v_rcp_f32 v80, v80\n"
  "v_mfma_f32_16x16x32_bf16 v[8:11], a[88:91], v[52:55], v[8:11]\n"
  "buffer_load_dwordx4 v96, s[40:43], s60 offen offset:2048 lds\n"
  "v_mfma_f32_16x16x32_bf16 v[12:15], a[12:15], v[32:35], 0\n"
  "s_add_u32 s60, s60, 36864\n"
  "v_rcp_f32 v81, v81\n"
  "v_mfma_f32_16x16x32_bf16 v[12:15], a[28:31], v[36:39], v[12:15]\n"
  "v_mul_f32 v82, v82, v80\n"
  "v_mul_f32 v83, v83, v81\n"
  "v_mfma_f32_16x16x32_bf16 v[12:15], a[44:47], v[40:43], v[12:15]\n"
  "v_cvt_pk_bf16_f32 v84, v82, v83\n"
  "v_mul_f32 v80, v18, v18\n"
  "v_mfma_f32_16x16x32_bf16 v[12:15], a[60:63], v[44:47], v[12:15]\n"
  "s_add_u32 m0, s48, 65536\n"
  "v_mul_f32 v81, v19, v19\n"
  "v_fmaak_f32 v80, v88, v80, 0xc0135761\n"
  "v_mfma_f32_16x16x32_bf16 v[12:15], a[76:79], v[48:51], v[12:15]\n"
  "buffer_load_dwordx4 v97, s[36:39], s59 offen offset:3072 sc1 lds\n"
  "v_fmaak_f32 v81, v88, v81, 0xc0135761\n"
  "v_mfma_f32_16x16x32_bf16 v[12:15], a[92:95], v[52:55], v[12:15]\n"
  "s_waitcnt lgkmcnt(0)\n"
  "v_mul_f32 v80, v18, v80\n"
  "v_mul_f32 v81, v19, v81\n"
  "v_mfma_f32_16x16x32_bf16 v[144:147], v[112:115], v[124:127], v[144:147]\n"
  "s_add_u32 s59, s59, 1024\n"
  "ds_read_b128 v[56:59], v90 offset:22528\n"
  "v_mfma_f32_16x16x32_bf16 v[148:151], v[112:115], v[128:131], v[148:151]\n"
  "ds_read_b128 v[60:63], v90 offset:23552\n"
  "v_mfma_f32_16x16x32_bf16 v[152:155], v[112:115], v[132:135], v[152:155]\n"
  "ds_read_b128 v[64:67], v90 offset:24576\n"
  "v_mfma_f32_16x16x32_bf16 v[156:159], v[112:115], v[136:139], v[156:159]\n"
  "buffer_load_dwordx4 v94, s[40:43], s60 offen offset:0 lds\n"
  "v_mfma_f32_16x16x32_bf16 v[160:163], v[112:115], v[140:143], v[160:163]\n"
  "ds_read_b128 v[68:71], v90 offset:25600\n"
  "v_mfma_f32_16x16x32_bf16 v[164:167], v[116:119], v[124:127], v[164:167]\n"
  "ds_read_b128 v[72:75], v90 offset:26624\n"
  "v_mfma_f32_16x16x32_bf16 v[168:171], v[116:119], v[128:131], v[168:171]\n"
  "ds_read_b128 v[76:79], v90 offset:27648\n"
  "v_mfma_f32_16x16x32_bf16 v[172:175], v[116:119], v[132:135], v[172:175]\n"
  "v_exp_f32 v80, v80\n"
  "v_mfma_f32_16x16x32_bf16 v[176:179], v[116:119], v[136:139], v[176:179]\n"
  "buffer_load_dwordx4 v95, s[40:43], s60 offen offset:1024 lds\n"
  "v_mfma_f32_16x16x32_bf16 v[180:183], v[116:119], v[140:143], v[180:183]\n"
  "v_exp_f32 v81, v81\n"
  "v_mfma_f32_16x16x32_bf16 v[184:187], v[120:123], v[124:127], v[184:187]\n"
  "v_mul_f32 v82, v18, v26\n"
  "v_mul_f32 v83, v19, v27\n"
  "v_mfma_f32_16x16x32_bf16 v[188:191], v[120:123], v[128:131], v[188:191]\n"
  "v_add_f32 v80, 1.0, v80\n"
  "v_add_f32 v81, 1.0, v81\n"
  "v_mfma_f32_16x16x32_bf16 v[192:195], v[120:123], v[132:135], v[192:195]\n"
  "v_rcp_f32 v80, v80\n"
  "v_mfma_f32_16x16x32_bf16 v[196:199], v[120:123], v[136:139], v[196:199]\n"
  "buffer_load_dwordx4 v96, s[40:43], s60 offen offset:2048 lds\n"
  "v_mfma_f32_16x16x32_bf16 v[200:203], v[120:123], v[140:143], v[200:203]\n"
  "s_waitcnt lgkmcnt(0)\n"
  "v_rcp_f32 v81, v81\n"
  "v_mfma_f32_16x16x32_bf16 v[0:3], a[96:99], v[56:59], v[0:3]\n"
  "s_add_u32 s60, s60, 36864\n"
  "ds_read_b64_tr_b16 v[112:113], v100 offset:49152\n"
  "v_mul_f32 v82, v82, v80\n"
  "v_mfma_f32_16x16x32_bf16 v[0:3], a[112:115], v[60:63], v[0:3]\n"
  "ds_read_b64_tr_b16 v[114:115], v100 offset:57344\n"
  "v_mul_f32 v83, v83, v81\n"
  "v_mfma_f32_16x16x32_bf16 v[0:3], a[128:131], v[64:67], v[0:3]\n"
  "v_cvt_pk_bf16_f32 v85, v82, v83\n"
  "v_mul_f32 v80, v20, v20\n"
  "v_mfma_f32_16x16x32_bf16 v[0:3], a[144:147], v[68:71], v[0:3]\n"
  "ds_read_b128 v[124:127], v90 offset:52224\n"
  "v_mul_f32 v81, v21, v21\n"
  "v_mfma_f32_16x16x32_bf16 v[0:3], a[160:163], v[72:75], v[0:3]\n"
  "ds_read_b128 v[128:131], v90 offset:56320\n"
  "v_fmaak_f32 v80, v88, v80, 0xc0135761\n"
  "v_mfma_f32_16x16x32_bf16 v[0:3], a[176:179], v[76:79], v[0:3]\n"
  "v_fmaak_f32 v81, v88, v81, 0xc0135761\n"
  "v_mul_f32 v80, v20, v80\n"
  "v_mfma_f32_16x16x32_bf16 v[4:7], a[100:103], v[56:59], v[4:7]\n"
  "ds_read_b128 v[132:135], v90 offset:60416\n"
  "v_mul_f32 v81, v21, v81\n"
  "v_mfma_f32_16x16x32_bf16 v[4:7], a[116:119], v[60:63], v[4:7]\n"
  "ds_read_b128 v[136:139], v90 offset:64512\n"
  "v_mfma_f32_16x16x32_bf16 v[4:7], a[132:135], v[64:67], v[4:7]\n"
  "v_exp_f32 v80, v80\n"
  "v_mfma_f32_16x16x32_bf16 v[4:7], a[148:151], v[68:71], v[4:7]\n"
  "ds_read_b128 v[140:143], v91 offset:27648\n"
  "v_mfma_f32_16x16x32_bf16 v[4:7], a[164:167], v[72:75], v[4:7]\n"
  "ds_read_b64_tr_b16 v[116:117], v100 offset:50176\n"
  "v_mfma_f32_16x16x32_bf16 v[4:7], a[180:183], v[76:79], v[4:7]\n"
  "v_exp_f32 v81, v81\n"
  "v_mfma_f32_16x16x32_bf16 v[8:11], a[104:107], v[56:59], v[8:11]\n"
  "ds_read_b64_tr_b16 v[118:119], v100 offset:58368\n"
  "v_mul_f32 v82, v20, v28\n"
  "v_mfma_f32_16x16x32_bf16 v[8:11], a[120:123], v[60:63], v[8:11]\n"
  "ds_read_b64_tr_b16 v[120:121], v100 offset:51200\n"
  "v_mul_f32 v83, v21, v29\n"
  "v_mfma_f32_16x16x32_bf16 v[8:11], a[136:139], v[64:67], v[8:11]\n"
  "v_add_f32 v80, 1.0, v80\n"
  "v_add_f32 v81, 1.0, v81\n"
  "v_mfma_f32_16x16x32_bf16 v[8:11], a[152:155], v[68:71], v[8:11]\n"
  "ds_read_b64_tr_b16 v[122:123], v100 offset:59392\n"
  "v_mfma_f32_16x16x32_bf16 v[8:11], a[168:171], v[72:75], v[8:11]\n"
  "v_rcp_f32 v80, v80\n"
  "v_mfma_f32_16x16x32_bf16 v[8:11], a[184:187], v[76:79], v[8:11]\n"
  "v_rcp_f32 v81, v81\n"
  "v_mfma_f32_16x16x32_bf16 v[12:15], a[108:111], v[56:59], v[12:15]\n"
  "v_mul_f32 v82, v82, v80\n"
  "v_mul_f32 v83, v83, v81\n"
  "v_mfma_f32_16x16x32_bf16 v[12:15], a[124:127], v[60:63], v[12:15]\n"
  "v_cvt_pk_bf16_f32 v86, v82, v83\n"
  "v_mul_f32 v80, v22, v22\n"
  "v_mfma_f32_16x16x32_bf16 v[12:15], a[140:143], v[64:67], v[12:15]\n"
  "s_waitcnt vmcnt(35)\n"
  "s_mov_b64 exec, 1\n"
  "buffer_atomic_add v104, off, s[24:27], s58 sc1\n"
  "s_mov_b64 exec, -1\n"
  "v_mfma_f32_16x16x32_bf16 v[12:15], a[156:159], v[68:71], v[12:15]\n"
  "v_mul_f32 v81, v23, v23\n"
  "v_fmaak_f32 v80, v88, v80, 0xc0135761\n"
  "v_mfma_f32_16x16x32_bf16 v[12:15], a[172:175], v[72:75], v[12:15]\n"
  "v_fmaak_f32 v81, v88, v81, 0xc0135761\n"
  "v_mul_f32 v80, v22, v80\n"
  "v_mfma_f32_16x16x32_bf16 v[12:15], a[188:191], v[76:79], v[12:15]\n"
  "s_waitcnt lgkmcnt(0)\n"
  "v_mul_f32 v81, v23, v81\n"
  "v_mfma_f32_16x16x32_bf16 v[144:147], v[112:115], v[124:127], v[144:147]\n"
  "ds_read_b128 v[32:35], v90 offset:28672\n"
  "v_mfma_f32_16x16x32_bf16 v[148:151], v[112:115], v[128:131], v[148:151]\n"
  "ds_read_b128 v[36:39], v90 offset:29696\n"
  "v_mfma_f32_16x16x32_bf16 v[152:155], v[112:115], v[132:135], v[152:155]\n"
  "ds_read_b128 v[40:43], v90 offset:30720\n"
  "v_mfma_f32_16x16x32_bf16 v[156:159], v[112:115], v[136:139], v[156:159]\n"
  "v_exp_f32 v80, v80\n"
  "v_mfma_f32_16x16x32_bf16 v[160:163], v[112:115], v[140:143], v[160:163]\n"
  "ds_read_b128 v[44:47], v90 offset:31744\n"
  "v_mfma_f32_16x16x32_bf16 v[164:167], v[116:119], v[124:127], v[164:167]\n"
  "ds_read_b128 v[48:51], v91 offset:28672\n"
  "v_mfma_f32_16x16x32_bf16 v[168:171], v[116:119], v[128:131], v[168:171]\n"
  "ds_read_b128 v[52:55], v91 offset:29696\n"
  "v_mfma_f32_16x16x32_bf16 v[172:175], v[116:119], v[132:135], v[172:175]\n"
  "v_exp_f32 v81, v81\n"
  "v_mfma_f32_16x16x32_bf16 v[176:179], v[116:119], v[136:139], v[176:179]\n"
  "v_mul_f32 v82, v22, v30\n"
  "v_mul_f32 v83, v23, v31\n"
  "v_mfma_f32_16x16x32_bf16 v[180:183], v[116:119], v[140:143], v[180:183]\n"
  "v_add_f32 v80, 1.0, v80\n"
  "v_add_f32 v81, 1.0, v81\n"
  "v_mfma_f32_16x16x32_bf16 v[184:187], v[120:123], v[124:127], v[184:187]\n"
  "v_rcp_f32 v80, v80\n"
  "v_mfma_f32_16x16x32_bf16 v[188:191], v[120:123], v[128:131], v[188:191]\n"
  "v_rcp_f32 v81, v81\n"
  "v_mfma_f32_16x16x32_bf16 v[192:195], v[120:123], v[132:135], v[192:195]\n"
  "v_mul_f32 v82, v82, v80\n"
  "v_mul_f32 v83, v83, v81\n"
  "v_mfma_f32_16x16x32_bf16 v[196:199], v[120:123], v[136:139], v[196:199]\n"
  "v_cvt_pk_bf16_f32 v87, v82, v83\n"
  "v_mfma_f32_16x16x32_bf16 v[200:203], v[120:123], v[140:143], v[200:203]\n"
  "s_waitcnt vmcnt(16) lgkmcnt(0)\n"
  "s_barrier\n"
  "v_mfma_f32_16x16x32_bf16 v[0:3], a[192:195], v[32:35], v[0:3]\n"
  "s_add_u32 m0, s48, 81920\n"
  "ds_read_b128 v[56:59], v89 offset:32768\n"
  "v_mfma_f32_16x16x32_bf16 v[0:3], a[208:211], v[36:39], v[0:3]\n"
  "buffer_load_dwordx4 v92, s[36:39], s52 offen offset:0 sc1 lds\n"
  "v_mfma_f32_16x16x32_bf16 v[0:3], a[224:227], v[40:43], v[0:3]\n"
  "ds_read_b128 v[60:63], v89 offset:33792\n"
  "v_mfma_f32_16x16x32_bf16 v[0:3], a[240:243], v[44:47], v[0:3]\n"
  "v_mfma_f32_16x16x32_bf16 v[0:3], v[224:227], v[48:51], v[0:3]\n"
  "v_mfma_f32_16x16x32_bf16 v[0:3], v[240:243], v[52:55], v[0:3]\n"
  "ds_read_b128 v[64:67], v89 offset:34816\n"
  "v_mfma_f32_16x16x32_bf16 v[4:7], a[196:199], v[32:35], v[4:7]\n"
  "buffer_load_dwordx4 v92, s[36:39], s52 offen offset:1024 sc1 lds\n"
  "v_mfma_f32_16x16x32_bf16 v[4:7], a[212:215], v[36:39], v[4:7]\n"
  "v_mfma_f32_16x16x32_bf16 v[4:7], a[228:231], v[40:43], v[4:7]\n"
  "ds_read_b128 v[68:71], v89 offset:35840\n"
  "v_mfma_f32_16x16x32_bf16 v[4:7], a[244:247], v[44:47], v[4:7]\n"
  "v_mfma_f32_16x16x32_bf16 v[4:7], v[228:231], v[48:51], v[4:7]\n"
  "v_mfma_f32_16x16x32_bf16 v[4:7], v[244:247], v[52:55], v[4:7]\n"
  "buffer_load_dwordx4 v92, s[36:39], s52 offen offset:2048 sc1 lds\n"
  "ds_read_b128 v[72:75], v89 offset:36864\n"
  "v_mfma_f32_16x16x32_bf16 v[8:11], a[200:203], v[32:35], v[8:11]\n"
  "v_mfma_f32_16x16x32_bf16 v[8:11], a[216:219], v[36:39], v[8:11]\n"
  "v_mfma_f32_16x16x32_bf16 v[8:11], a[232:235], v[40:43], v[8:11]\n"
  "ds_read_b128 v[76:79], v89 offset:37888\n"
  "v_mfma_f32_16x16x32_bf16 v[8:11], a[248:251], v[44:47], v[8:11]\n"
  "v_mfma_f32_16x16x32_bf16 v[8:11], v[232:235], v[48:51], v[8:11]\n"
  "buffer_load_dwordx4 v92, s[36:39], s52 offen offset:3072 sc1 lds\n"
  "v_mfma_f32_16x16x32_bf16 v[8:11], v[248:251], v[52:55], v[8:11]\n"
  "v_mfma_f32_16x16x32_bf16 v[12:15], a[204:207], v[32:35], v[12:15]\n"
  "v_mfma_f32_16x16x32_bf16 v[12:15], a[220:223], v[36:39], v[12:15]\n"
  "v_mfma_f32_16x16x32_bf16 v[12:15], a[236:239], v[40:43], v[12:15]\n"
  "s_add_u32 m0, s49, 145408\n"
  "v_mfma_f32_16x16x32_bf16 v[12:15], a[252:255], v[44:47], v[12:15]\n"
  "buffer_load_dwordx4 v93, s[36:39], s52 offen sc1 lds\n"
  "v_mfma_f32_16x16x32_bf16 v[12:15], v[236:239], v[48:51], v[12:15]\n"
  "s_add_u32 s52, s52, 18432\n"
  "buffer_store_dwordx4 v[84:87], v89, s[36:39], s55 offen sc1\n"
  "v_mfma_f32_16x16x32_bf16 v[12:15], v[252:255], v[52:55], v[12:15]\n"
  "s_waitcnt lgkmcnt(0)\n"
  "s_add_u32 s55, s55, 147456\n"
  "v_mfma_f32_16x16x32_bf16 v[16:19], a[0:3], v[56:59], 0\n"
  "s_cmp_eq_u32 s52, s54\n"
  "s_cselect_b32 s52, s53, s52\n"
  "ds_read_b64_tr_b16 v[112:113], v101 offset:0\n"
  "v_mfma_f32_16x16x32_bf16 v[16:19], a[16:19], v[60:63], v[16:19]\n"
  "s_add_u32 m0, s50, 155648\n"
  "s_cmp_eq_u32 s55, s57\n"
  "s_cselect_b32 s55, s56, s55\n"
  "ds_read_b64_tr_b16 v[114:115], v101 offset:8192\n"
  "v_mfma_f32_16x16x32_bf16 v[16:19], a[32:35], v[64:67], v[16:19]\n"
  "buffer_load_dwordx4 v98, s[36:39], s59 offen sc1 lds\n"
  "v_mul_f32 v80, v0, v0\n"
  "v_mfma_f32_16x16x32_bf16 v[16:19], a[48:51], v[68:71], v[16:19]\n"
  "ds_read_b128 v[124:127], v91 offset:3072\n"
  "v_mul_f32 v81, v1, v1\n"
  "v_mfma_f32_16x16x32_bf16 v[16:19], a[64:67], v[72:75], v[16:19]\n"
  "ds_read_b128 v[128:131], v91 offset:7168\n"
  "v_fmaak_f32 v80, v88, v80, 0xc0135761\n"
  "v_mfma_f32_16x16x32_bf16 v[16:19], a[80:83], v[76:79], v[16:19]\n"
  "v_fmaak_f32 v81, v88, v81, 0xc0135761\n"
  "v_mul_f32 v80, v0, v80\n"
  "v_mfma_f32_16x16x32_bf16 v[20:23], a[4:7], v[56:59], 0\n"
  "s_add_u32 m0, s48, 98304\n"
  "ds_read_b128 v[132:135], v91 offset:11264\n"
  "v_mul_f32 v81, v1, v81\n"
  "v_mfma_f32_16x16x32_bf16 v[20:23], a[20:23], v[60:63], v[20:23]\n"
  "buffer_load_dwordx4 v97, s[36:39], s59 offen offset:3072 sc1 lds\n"
  "ds_read_b128 v[136:139], v91 offset:15360\n"
  "v_mfma_f32_16x16x32_bf16 v[20:23], a[36:39], v[64:67], v[20:23]\n"
  "s_add_u32 s59, s59, 1024\n"
  "v_exp_f32 v80, v80\n"
  "v_mfma_f32_16x16x32_bf16 v[20:23], a[52:55], v[68:71], v[20:23]\n"
  "ds_read_b128 v[140:143], v91 offset:16384\n"
  "v_mfma_f32_16x16x32_bf16 v[20:23], a[68:71], v[72:75], v[20:23]\n"
  "ds_read_b64_tr_b16 v[116:117], v101 offset:1024\n"
  "v_mfma_f32_16x16x32_bf16 v[20:23], a[84:87], v[76:79], v[20:23]\n"
  "v_exp_f32 v81, v81\n"
  "v_mfma_f32_16x16x32_bf16 v[24:27], a[8:11], v[56:59], 0\n"
  "buffer_load_dwordx4 v94, s[40:43], s60 offen offset:0 lds\n"
  "ds_read_b64_tr_b16 v[118:119], v101 offset:9216\n"
  "v_mfma_f32_16x16x32_bf16 v[24:27], a[24:27], v[60:63], v[24:27]\n"
  "ds_read_b64_tr_b16 v[120:121], v101 offset:2048\n"
  "v_mul_f32 v82, v0, v8\n"
  "v_mfma_f32_16x16x32_bf16 v[24:27], a[40:43], v[64:67], v[24:27]\n"
  "v_mul_f32 v83, v1, v9\n"
  "v_add_f32 v80, 1.0, v80\n"
  "v_mfma_f32_16x16x32_bf16 v[24:27], a[56:59], v[68:71], v[24:27]\n"
  "ds_read_b64_tr_b16 v[122:123], v101 offset:10240\n"
  "v_add_f32 v81, 1.0, v81\n"
  "v_mfma_f32_16x16x32_bf16 v[24:27], a[72:75], v[72:75], v[24:27]\n"
  "v_rcp_f32 v80, v80\n"
  "v_mfma_f32_16x16x32_bf16 v[24:27], a[88:91], v[76:79], v[24:27]\n"
  "buffer_load_dwordx4 v95, s[40:43], s60 offen offset:1024 lds\n"
  "v_mfma_f32_16x16x32_bf16 v[28:31], a[12:15], v[56:59], 0\n"
  "v_rcp_f32 v81, v81\n"
  "v_mfma_f32_16x16x32_bf16 v[28:31], a[28:31], v[60:63], v[28:31]\n"
  "v_mul_f32 v82, v82, v80\n"
  "v_mul_f32 v83, v83, v81\n"
  "v_mfma_f32_16x16x32_bf16 v[28:31], a[44:47], v[64:67], v[28:31]\n"
  "v_cvt_pk_bf16_f32 v84, v82, v83\n"
  "v_mul_f32 v80, v2, v2\n"
  "v_mfma_f32_16x16x32_bf16 v[28:31], a[60:63], v[68:71], v[28:31]\n"
  "v_mul_f32 v81, v3, v3\n"
  "v_fmaak_f32 v80, v88, v80, 0xc0135761\n"
  "v_mfma_f32_16x16x32_bf16 v[28:31], a[76:79], v[72:75], v[28:31]\n"
  "buffer_load_dwordx4 v96, s[40:43], s60 offen offset:2048 lds\n"
  "v_fmaak_f32 v81, v88, v81, 0xc0135761\n"
  "v_mfma_f32_16x16x32_bf16 v[28:31], a[92:95], v[76:79], v[28:31]\n"
  "s_waitcnt lgkmcnt(0)\n"
  "v_mul_f32 v80, v2, v80\n"
  "v_mul_f32 v81, v3, v81\n"
  "v_mfma_f32_16x16x32_bf16 v[144:147], v[112:115], v[124:127], v[144:147]\n"
  "s_add_u32 s60, s60, 36864\n"
  "ds_read_b128 v[32:35], v89 offset:38912\n"
  "v_mfma_f32_16x16x32_bf16 v[148:151], v[112:115], v[128:131], v[148:151]\n"
  "ds_read_b128 v[36:39], v89 offset:39936\n"
  "v_mfma_f32_16x16x32_bf16 v[152:155], v[112:115], v[132:135], v[152:155]\n"
  "s_add_u32 m0, s48, 114688\n"
  "ds_read_b128 v[40:43], v89 offset:40960\n"
  "v_mfma_f32_16x16x32_bf16 v[156:159], v[112:115], v[136:139], v[156:159]\n"
  "buffer_load_dwordx4 v97, s[36:39], s59 offen offset:3072 sc1 lds\n"
  "v_mfma_f32_16x16x32_bf16 v[160:163], v[112:115], v[140:143], v[160:163]\n"
  "s_add_u32 s59, s59, 1024\n"
  "ds_read_b128 v[44:47], v89 offset:41984\n"
  "v_mfma_f32_16x16x32_bf16 v[164:167], v[116:119], v[124:127], v[164:167]\n"
  "ds_read_b128 v[48:51], v89 offset:43008\n"
  "v_mfma_f32_16x16x32_bf16 v[168:171], v[116:119], v[128:131], v[168:171]\n"
  "ds_read_b128 v[52:55], v89 offset:44032\n"
  "v_mfma_f32_16x16x32_bf16 v[172:175], v[116:119], v[132:135], v[172:175]\n"
  "v_exp_f32 v80, v80\n"
  "v_mfma_f32_16x16x32_bf16 v[176:179], v[116:119], v[136:139], v[176:179]\n"
  "buffer_load_dwordx4 v94, s[40:43], s60 offen offset:0 lds\n"
  "v_mfma_f32_16x16x32_bf16 v[180:183], v[116:119], v[140:143], v[180:183]\n"
  "v_exp_f32 v81, v81\n"
  "v_mfma_f32_16x16x32_bf16 v[184:187], v[120:123], v[124:127], v[184:187]\n"
  "v_mul_f32 v82, v2, v10\n"
  "v_mul_f32 v83, v3, v11\n"
  "v_mfma_f32_16x16x32_bf16 v[188:191], v[120:123], v[128:131], v[188:191]\n"
  "v_add_f32 v80, 1.0, v80\n"
  "s_cbranch_execz Ltramp%=\n"
  "v_add_f32 v81, 1.0, v81\n"
  "v_mfma_f32_16x16x32_bf16 v[192:195], v[120:123], v[132:135], v[192:195]\n"
  "v_rcp_f32 v80, v80\n"
  "v_mfma_f32_16x16x32_bf16 v[196:199], v[120:123], v[136:139], v[196:199]\n"
  "buffer_load_dwordx4 v95, s[40:43], s60 offen offset:1024 lds\n"
  "v_mfma_f32_16x16x32_bf16 v[200:203], v[120:123], v[140:143], v[200:203]\n"
  "s_waitcnt lgkmcnt(0)\n"
  "v_rcp_f32 v81, v81\n"
  "v_mfma_f32_16x16x32_bf16 v[16:19], a[96:99], v[32:35], v[16:19]\n"
  "ds_read_b64_tr_b16 v[112:113], v99 offset:16384\n"
  "v_mul_f32 v82, v82, v80\n"
  "v_mfma_f32_16x16x32_bf16 v[16:19], a[112:115], v[36:39], v[16:19]\n"
  "ds_read_b64_tr_b16 v[114:115], v99 offset:24576\n"
  "v_mul_f32 v83, v83, v81\n"
  "v_mfma_f32_16x16x32_bf16 v[16:19], a[128:131], v[40:43], v[16:19]\n"
  "v_cvt_pk_bf16_f32 v85, v82, v83\n"
  "v_mul_f32 v80, v4, v4\n"
  "v_mfma_f32_16x16x32_bf16 v[16:19], a[144:147], v[44:47], v[16:19]\n"
  "buffer_load_dwordx4 v96, s[40:43], s60 offen offset:2048 lds\n"
  "ds_read_b128 v[124:127], v89 offset:19456\n"
  "v_mfma_f32_16x16x32_bf16 v[16:19], a[160:163], v[48:51], v[16:19]\n"
  "s_add_u32 s60, s60, 36864\n"
  "ds_read_b128 v[128:131], v89 offset:23552\n"
  "v_mul_f32 v81, v5, v5\n"
  "v_mfma_f32_16x16x32_bf16 v[16:19], a[176:179], v[52:55], v[16:19]\n"
  "v_fmaak_f32 v80, v88, v80, 0xc0135761\n"
  "v_fmaak_f32 v81, v88, v81, 0xc0135761\n"
  "v_mfma_f32_16x16x32_bf16 v[20:23], a[100:103], v[32:35], v[20:23]\n"
  "ds_read_b128 v[132:135], v89 offset:27648\n"
  "v_mul_f32 v80, v4, v80\n"
  "v_mfma_f32_16x16x32_bf16 v[20:23], a[116:119], v[36:39], v[20:23]\n"
  "ds_read_b128 v[136:139], v89 offset:31744\n"
  "v_mul_f32 v81, v5, v81\n"
  "v_mfma_f32_16x16x32_bf16 v[20:23], a[132:135], v[40:43], v[20:23]\n"
  "v_exp_f32 v80, v80\n"
  "v_mfma_f32_16x16x32_bf16 v[20:23], a[148:151], v[44:47], v[20:23]\n"
  "ds_read_b128 v[140:143], v91 offset:17408\n"
  "v_mfma_f32_16x16x32_bf16 v[20:23], a[164:167], v[48:51], v[20:23]\n"
  "ds_read_b64_tr_b16 v[116:117], v99 offset:17408\n"
  "v_mfma_f32_16x16x32_bf16 v[20:23], a[180:183], v[52:55], v[20:23]\n"
  "v_exp_f32 v81, v81\n"
  "v_mfma_f32_16x16x32_bf16 v[24:27], a[104:107], v[32:35], v[24:27]\n"
  "ds_read_b64_tr_b16 v[118:119], v99 offset:25600\n"
  "v_mul_f32 v82, v4, v12\n"
  "v_mfma_f32_16x16x32_bf16 v[24:27], a[120:123], v[36:39], v[24:27]\n"
  "ds_read_b64_tr_b16 v[120:121], v99 offset:18432\n"
  "v_mul_f32 v83, v5, v13\n"
  "v_mfma_f32_16x16x32_bf16 v[24:27], a[136:139], v[40:43], v[24:27]\n"
  "v_add_f32 v80, 1.0, v80\n"
  "v_add_f32 v81, 1.0, v81\n"
  "v_mfma_f32_16x16x32_bf16 v[24:27], a[152:155], v[44:47], v[24:27]\n"
  "ds_read_b64_tr_b16 v[122:123], v99 offset:26624\n"
  "v_mfma_f32_16x16x32_bf16 v[24:27], a[168:171], v[48:51], v[24:27]\n"
  "v_rcp_f32 v80, v80\n"
  "v_mfma_f32_16x16x32_bf16 v[24:27], a[184:187], v[52:55], v[24:27]\n"
  "v_rcp_f32 v81, v81\n"
  "v_mfma_f32_16x16x32_bf16 v[28:31], a[108:111], v[32:35], v[28:31]\n"
  "v_mul_f32 v82, v82, v80\n"
  "v_mul_f32 v83, v83, v81\n"
  "v_mfma_f32_16x16x32_bf16 v[28:31], a[124:127], v[36:39], v[28:31]\n"
  "v_cvt_pk_bf16_f32 v86, v82, v83\n"
  "v_mul_f32 v80, v6, v6\n"
  "v_mfma_f32_16x16x32_bf16 v[28:31], a[140:143], v[40:43], v[28:31]\n"
  "v_mul_f32 v81, v7, v7\n"
  "v_fmaak_f32 v80, v88, v80, 0xc0135761\n"
  "v_mfma_f32_16x16x32_bf16 v[28:31], a[156:159], v[44:47], v[28:31]\n"
  "v_fmaak_f32 v81, v88, v81, 0xc0135761\n"
  "v_mul_f32 v80, v6, v80\n"
  "v_mfma_f32_16x16x32_bf16 v[28:31], a[172:175], v[48:51], v[28:31]\n"
  "v_mul_f32 v81, v7, v81\n"
  "v_mfma_f32_16x16x32_bf16 v[28:31], a[188:191], v[52:55], v[28:31]\n"
  "s_waitcnt lgkmcnt(0)\n"
  "v_exp_f32 v80, v80\n"
  "v_mfma_f32_16x16x32_bf16 v[144:147], v[112:115], v[124:127], v[144:147]\n"
  "ds_read_b128 v[56:59], v89 offset:45056\n"
  "v_mfma_f32_16x16x32_bf16 v[148:151], v[112:115], v[128:131], v[148:151]\n"
  "ds_read_b128 v[60:63], v89 offset:46080\n"
  "v_mfma_f32_16x16x32_bf16 v[152:155], v[112:115], v[132:135], v[152:155]\n"
  "ds_read_b128 v[64:67], v89 offset:47104\n"
  "v_mfma_f32_16x16x32_bf16 v[156:159], v[112:115], v[136:139], v[156:159]\n"
  "v_exp_f32 v81, v81\n"
  "v_mfma_f32_16x16x32_bf16 v[160:163], v[112:115], v[140:143], v[160:163]\n"
  "ds_read_b128 v[68:71], v89 offset:48128\n"
  "v_mul_f32 v82, v6, v14\n"
  "v_mfma_f32_16x16x32_bf16 v[164:167], v[116:119], v[124:127], v[164:167]\n"
  "ds_read_b128 v[72:75], v91 offset:22528\n"
  "v_mul_f32 v83, v7, v15\n"
  "v_mfma_f32_16x16x32_bf16 v[168:171], v[116:119], v[128:131], v[168:171]\n"
  "ds_read_b128 v[76:79], v91 offset:23552\n"
  "v_add_f32 v80, 1.0, v80\n"
  "v_mfma_f32_16x16x32_bf16 v[172:175], v[116:119], v[132:135], v[172:175]\n"
  "v_add_f32 v81, 1.0, v81\n"
  "v_mfma_f32_16x16x32_bf16 v[176:179], v[116:119], v[136:139], v[176:179]\n"
  "v_rcp_f32 v80, v80\n"
  "v_mfma_f32_16x16x32_bf16 v[180:183], v[116:119], v[140:143], v[180:183]\n"
  "v_rcp_f32 v81, v81\n"
  "v_mfma_f32_16x16x32_bf16 v[184:187], v[120:123], v[124:127], v[184:187]\n"
  "v_mul_f32 v82, v82, v80\n"
  "v_mul_f32 v83, v83, v81\n"
  "v_mfma_f32_16x16x32_bf16 v[188:191], v[120:123], v[128:131], v[188:191]\n"
  "v_cvt_pk_bf16_f32 v87, v82, v83\n"
  "v_mfma_f32_16x16x32_bf16 v[192:195], v[120:123], v[132:135], v[192:195]\n"
  "v_mfma_f32_16x16x32_bf16 v[196:199], v[120:123], v[136:139], v[196:199]\n"
  "v_mfma_f32_16x16x32_bf16 v[200:203], v[120:123], v[140:143], v[200:203]\n"
  "s_waitcnt vmcnt(16) lgkmcnt(0)\n"
  "s_barrier\n"
  "v_mfma_f32_16x16x32_bf16 v[16:19], a[192:195], v[56:59], v[16:19]\n"
  "s_add_u32 m0, s48, 16384\n"
  "ds_read_b128 v[32:35], v89 offset:0\n"
  "v_mfma_f32_16x16x32_bf16 v[16:19], a[208:211], v[60:63], v[16:19]\n"
  "buffer_load_dwordx4 v92, s[36:39], s52 offen offset:0 sc1 lds\n"
  "v_mfma_f32_16x16x32_bf16 v[16:19], a[224:227], v[64:67], v[16:19]\n"
  "ds_read_b128 v[36:39], v89 offset:1024\n"
  "v_mfma_f32_16x16x32_bf16 v[16:19], a[240:243], v[68:71], v[16:19]\n"
  "v_mfma_f32_16x16x32_bf16 v[16:19], v[224:227], v[72:75], v[16:19]\n"
  "v_mfma_f32_16x16x32_bf16 v[16:19], v[240:243], v[76:79], v[16:19]\n"
  "ds_read_b128 v[40:43], v89 offset:2048\n"
  "v_mfma_f32_16x16x32_bf16 v[20:23], a[196:199], v[56:59], v[20:23]\n"
  "buffer_load_dwordx4 v92, s[36:39], s52 offen offset:1024 sc1 lds\n"
  "v_mfma_f32_16x16x32_bf16 v[20:23], a[212:215], v[60:63], v[20:23]\n"
  "v_mfma_f32_16x16x32_bf16 v[20:23], a[228:231], v[64:67], v[20:23]\n"
  "ds_read_b128 v[44:47], v89 offset:3072\n"
  "v_mfma_f32_16x16x32_bf16 v[20:23], a[244:247], v[68:71], v[20:23]\n"
  "v_mfma_f32_16x16x32_bf16 v[20:23], v[228:231], v[72:75], v[20:23]\n"
  "v_mfma_f32_16x16x32_bf16 v[20:23], v[244:247], v[76:79], v[20:23]\n"
  "buffer_load_dwordx4 v92, s[36:39], s52 offen offset:2048 sc1 lds\n"
  "ds_read_b128 v[48:51], v89 offset:4096\n"
  "v_mfma_f32_16x16x32_bf16 v[24:27], a[200:203], v[56:59], v[24:27]\n"
  "v_mfma_f32_16x16x32_bf16 v[24:27], a[216:219], v[60:63], v[24:27]\n"
  "v_mfma_f32_16x16x32_bf16 v[24:27], a[232:235], v[64:67], v[24:27]\n"
  "ds_read_b128 v[52:55], v89 offset:5120\n"
  "v_mfma_f32_16x16x32_bf16 v[24:27], a[248:251], v[68:71], v[24:27]\n"
  "v_mfma_f32_16x16x32_bf16 v[24:27], v[232:235], v[72:75], v[24:27]\n"
  "buffer_load_dwordx4 v92, s[36:39], s52 offen offset:3072 sc1 lds\n"
  "v_mfma_f32_16x16x32_bf16 v[24:27], v[248:251], v[76:79], v[24:27]\n"
  "v_mfma_f32_16x16x32_bf16 v[28:31], a[204:207], v[56:59], v[28:31]\n"
  "v_mfma_f32_16x16x32_bf16 v[28:31], a[220:223], v[60:63], v[28:31]\n"
  "v_mfma_f32_16x16x32_bf16 v[28:31], a[236:239], v[64:67], v[28:31]\n"
  "s_add_u32 m0, s49, 143360\n"
  "v_mfma_f32_16x16x32_bf16 v[28:31], a[252:255], v[68:71], v[28:31]\n"
  "buffer_load_dwordx4 v93, s[36:39], s52 offen sc1 lds\n"
  "v_mfma_f32_16x16x32_bf16 v[28:31], v[236:239], v[72:75], v[28:31]\n"
  "s_add_u32 s52, s52, 18432\n"
  "buffer_store_dwordx4 v[84:87], v89, s[36:39], s55 offen sc1\n"
  "v_mfma_f32_16x16x32_bf16 v[28:31], v[252:255], v[76:79], v[28:31]\n"
  "s_waitcnt lgkmcnt(0)\n"
  "s_add_u32 s55, s55, 147456\n"
  "v_mfma_f32_16x16x32_bf16 v[0:3], a[0:3], v[32:35], 0\n"
  "s_cmp_eq_u32 s52, s54\n"
  "s_cselect_b32 s52, s53, s52\n"
  "ds_read_b64_tr_b16 v[112:113], v99 offset:49152\n"
  "v_mfma_f32_16x16x32_bf16 v[0:3], a[16:19], v[36:39], v[0:3]\n"
  "s_add_u32 m0, s48, 32768\n"
  "s_cmp_eq_u32 s55, s57\n"
  "s_cselect_b32 s55, s56, s55\n"
  "ds_read_b64_tr_b16 v[114:115], v99 offset:57344\n"
  "v_mfma_f32_16x16x32_bf16 v[0:3], a[32:35], v[40:43], v[0:3]\n"
  "buffer_load_dwordx4 v97, s[36:39], s59 offen offset:3072 sc1 lds\n"
  "v_mul_f32 v80, v16, v16\n"
  "v_mfma_f32_16x16x32_bf16 v[0:3], a[48:51], v[44:47], v[0:3]\n"
  "s_add_u32 s59, s59, 1024\n"
  "ds_read_b128 v[124:127], v89 offset:52224\n"
  "v_mul_f32 v81, v17, v17\n"
  "v_mfma_f32_16x16x32_bf16 v[0:3], a[64:67], v[48:51], v[0:3]\n"
  "ds_read_b128 v[128:131], v89 offset:56320\n"
  "v_fmaak_f32 v80, v88, v80, 0xc0135761\n"
  "v_mfma_f32_16x16x32_bf16 v[0:3], a[80:83], v[52:55], v[0:3]\n"
  "v_fmaak_f32 v81, v88, v81, 0xc0135761\n"
  "v_mul_f32 v80, v16, v80\n"
  "v_mfma_f32_16x16x32_bf16 v[4:7], a[4:7], v[32:35], 0\n"
  "ds_read_b128 v[132:135], v89 offset:60416\n"
  "v_mul_f32 v81, v17, v81\n"
  "v_mfma_f32_16x16x32_bf16 v[4:7], a[20:23], v[36:39], v[4:7]\n"
  "buffer_load_dwordx4 v94, s[40:43], s60 offen offset:0 lds\n"
  "ds_read_b128 v[136:139], v89 offset:64512\n"
  "v_mfma_f32_16x16x32_bf16 v[4:7], a[36:39], v[40:43], v[4:7]\n"
  "v_exp_f32 v80, v80\n"
  "v_mfma_f32_16x16x32_bf16 v[4:7], a[52:55], v[44:47], v[4:7]\n"
  "ds_read_b128 v[140:143], v91 offset:18432\n"
  "v_mfma_f32_16x16x32_bf16 v[4:7], a[68:71], v[48:51], v[4:7]\n"
  "ds_read_b64_tr_b16 v[116:117], v99 offset:50176\n"
  "v_mfma_f32_16x16x32_bf16 v[4:7], a[84:87], v[52:55], v[4:7]\n"
  "v_exp_f32 v81, v81\n"
  "v_mfma_f32_16x16x32_bf16 v[8:11], a[8:11], v[32:35], 0\n"
  "buffer_load_dwordx4 v95, s[40:43], s60 offen offset:1024 lds\n"
  "ds_read_b64_tr_b16 v[118:119], v99 offset:58368\n"
  "v_mfma_f32_16x16x32_bf16 v[8:11], a[24:27], v[36:39], v[8:11]\n"
  "ds_read_b64_tr_b16 v[120:121], v99 offset:51200\n"
  "v_mul_f32 v82, v16, v24\n"
  "v_mfma_f32_16x16x32_bf16 v[8:11], a[40:43], v[40:43], v[8:11]\n"
  "v_mul_f32 v83, v17, v25\n"
  "v_add_f32 v80, 1.0, v80\n"
  "v_mfma_f32_16x16x32_bf16 v[8:11], a[56:59], v[44:47], v[8:11]\n"
  "ds_read_b64_tr_b16 v[122:123], v99 offset:59392\n"
  "v_add_f32 v81, 1.0, v81\n"
  "v_mfma_f32_16x16x32_bf16 v[8:11], a[72:75], v[48:51], v[8:11]\n"
  "v_rcp_f32 v80, v80\n"
  "v_mfma_f32_16x16x32_bf16 v[8:11], a[88:91], v[52:55], v[8:11]\n"
  "buffer_load_dwordx4 v96, s[40:43], s60 offen offset:2048 lds\n"
  "v_mfma_f32_16x16x32_bf16 v[12:15], a[12:15], v[32:35], 0\n"
  "s_add_u32 s60, s60, 36864\n"
  "v_rcp_f32 v81, v81\n"
  "v_mfma_f32_16x16x32_bf16 v[12:15], a[28:31], v[36:39], v[12:15]\n"
  "v_mul_f32 v82, v82, v80\n"
  "v_mul_f32 v83, v83, v81\n"
  "v_mfma_f32_16x16x32_bf16 v[12:15], a[44:47], v[40:43], v[12:15]\n"
  "v_cvt_pk_bf16_f32 v84, v82, v83\n"
  "v_mul_f32 v80, v18, v18\n"
  "v_mfma_f32_16x16x32_bf16 v[12:15], a[60:63], v[44:47], v[12:15]\n"
  "s_add_u32 m0, s48, 131072\n"
  "v_mul_f32 v81, v19, v19\n"
  "v_fmaak_f32 v80, v88, v80, 0xc0135761\n"
  "v_mfma_f32_16x16x32_bf16 v[12:15], a[76:79], v[48:51], v[12:15]\n"
  "buffer_load_dwordx4 v97, s[36:39], s59 offen offset:3072 sc1 lds\n"
  "v_fmaak_f32 v81, v88, v81, 0xc0135761\n"
  "v_mfma_f32_16x16x32_bf16 v[12:15], a[92:95], v[52:55], v[12:15]\n"
  "s_waitcnt lgkmcnt(0)\n"
  "v_mul_f32 v80, v18, v80\n"
  "v_mul_f32 v81, v19, v81\n"
  "v_mfma_f32_16x16x32_bf16 v[144:147], v[112:115], v[124:127], v[144:147]\n"
  "s_add_u32 s59, s59, 25600\n"
  "ds_read_b128 v[56:59], v89 offset:6144\n"
  "v_mfma_f32_16x16x32_bf16 v[148:151], v[112:115], v[128:131], v[148:151]\n"
  "ds_read_b128 v[60:63], v89 offset:7168\n"
  "v_mfma_f32_16x16x32_bf16 v[152:155], v[112:115], v[132:135], v[152:155]\n"
  "ds_read_b128 v[64:67], v89 offset:8192\n"
  "v_mfma_f32_16x16x32_bf16 v[156:159], v[112:115], v[136:139], v[156:159]\n"
  "buffer_load_dwordx4 v94, s[40:43], s60 offen offset:0 lds\n"
  "v_mfma_f32_16x16x32_bf16 v[160:163], v[112:115], v[140:143], v[160:163]\n"
  "ds_read_b128 v[68:71], v89 offset:9216\n"
  "v_mfma_f32_16x16x32_bf16 v[164:167], v[116:119], v[124:127], v[164:167]\n"
  "ds_read_b128 v[72:75], v89 offset:10240\n"
  "v_mfma_f32_16x16x32_bf16 v[168:171], v[116:119], v[128:131], v[168:171]\n"
  "ds_read_b128 v[76:79], v89 offset:11264\n"
  "v_mfma_f32_16x16x32_bf16 v[172:175], v[116:119], v[132:135], v[172:175]\n"
  "v_exp_f32 v80, v80\n"
  "v_mfma_f32_16x16x32_bf16 v[176:179], v[116:119], v[136:139], v[176:179]\n"
  "buffer_load_dwordx4 v95, s[40:43], s60 offen offset:1024 lds\n"
  "v_mfma_f32_16x16x32_bf16 v[180:183], v[116:119], v[140:143], v[180:183]\n"
  "v_exp_f32 v81, v81\n"
  "v_mfma_f32_16x16x32_bf16 v[184:187], v[120:123], v[124:127], v[184:187]\n"
  "s_mov_b64 exec, 3\n"
  "buffer_load_dword v106, v105, s[24:27], 0 offen sc1\n"
  "s_mov_b64 exec, -1\n"
  "v_mfma_f32_16x16x32_bf16 v[188:191], v[120:123], v[128:131], v[188:191]\n"
  "v_mul_f32 v82, v18, v26\n"
  "v_mul_f32 v83, v19, v27\n"
  "v_mfma_f32_16x16x32_bf16 v[192:195], v[120:123], v[132:135], v[192:195]\n"
  "v_add_f32 v80, 1.0, v80\n"
  "v_add_f32 v81, 1.0, v81\n"
  "v_mfma_f32_16x16x32_bf16 v[196:199], v[120:123], v[136:139], v[196:199]\n"
  "buffer_load_dwordx4 v96, s[40:43], s60 offen offset:2048 lds\n"
  "v_mfma_f32_16x16x32_bf16 v[200:203], v[120:123], v[140:143], v[200:203]\n"
  "s_waitcnt lgkmcnt(0)\n"
  "v_rcp_f32 v80, v80\n"
  "v_mfma_f32_16x16x32_bf16 v[0:3], a[96:99], v[56:59], v[0:3]\n"
  "s_add_u32 s60, s60, 921600\n"
  "ds_read_b64_tr_b16 v[112:113], v100 offset:0\n"
  "v_mfma_f32_16x16x32_bf16 v[0:3], a[112:115], v[60:63], v[0:3]\n"
  "s_cmp_ge_u32 s60, 5308416\n"
  "s_cselect_b32 s64, 5308416, 0\n"
  "s_cselect_b32 s65, 147456, 0\n"
  "s_sub_u32 s60, s60, s64\n"
  "s_sub_u32 s59, s59, s65\n"
  "ds_read_b64_tr_b16 v[114:115], v100 offset:8192\n"
  "v_mfma_f32_16x16x32_bf16 v[0:3], a[128:131], v[64:67], v[0:3]\n"
  "v_rcp_f32 v81, v81\n"
  "v_mfma_f32_16x16x32_bf16 v[0:3], a[144:147], v[68:71], v[0:3]\n"
  "ds_read_b128 v[124:127], v90 offset:3072\n"
  "v_mul_f32 v82, v82, v80\n"
  "v_mfma_f32_16x16x32_bf16 v[0:3], a[160:163], v[72:75], v[0:3]\n"
  "ds_read_b128 v[128:131], v90 offset:7168\n"
  "v_mul_f32 v83, v83, v81\n"
  "v_mfma_f32_16x16x32_bf16 v[0:3], a[176:179], v[76:79], v[0:3]\n"
  "v_cvt_pk_bf16_f32 v85, v82, v83\n"
  "v_mul_f32 v80, v20, v20\n"
  "v_mfma_f32_16x16x32_bf16 v[4:7], a[100:103], v[56:59], v[4:7]\n"
  "ds_read_b128 v[132:135], v90 offset:11264\n"
  "v_mul_f32 v81, v21, v21\n"
  "v_mfma_f32_16x16x32_bf16 v[4:7], a[116:119], v[60:63], v[4:7]\n"
  "ds_read_b128 v[136:139], v90 offset:15360\n"
  "v_fmaak_f32 v80, v88, v80, 0xc0135761\n"
  "v_mfma_f32_16x16x32_bf16 v[4:7], a[132:135], v[64:67], v[4:7]\n"
  "v_fmaak_f32 v81, v88, v81, 0xc0135761\n"
  "v_mul_f32 v80, v20, v80\n"
  "v_mfma_f32_16x16x32_bf16 v[4:7], a[148:151], v[68:71], v[4:7]\n"
  "ds_read_b128 v[140:143], v91 offset:19456\n"
  "v_mul_f32 v81, v21, v81\n"
  "v_mfma_f32_16x16x32_bf16 v[4:7], a[164:167], v[72:75], v[4:7]\n"
  "ds_read_b64_tr_b16 v[116:117], v100 offset:1024\n"
  "v_mfma_f32_16x16x32_bf16 v[4:7], a[180:183], v[76:79], v[4:7]\n"
  "v_exp_f32 v80, v80\n"
  "v_mfma_f32_16x16x32_bf16 v[8:11], a[104:107], v[56:59], v[8:11]\n"
  "ds_read_b64_tr_b16 v[118:119], v100 offset:9216\n"
  "v_mfma_f32_16x16x32_bf16 v[8:11], a[120:123], v[60:63], v[8:11]\n"
  "ds_read_b64_tr_b16 v[120:121], v100 offset:2048\n"
  "v_mfma_f32_16x16x32_bf16 v[8:11], a[136:139], v[64:67], v[8:11]\n"
  "v_exp_f32 v81, v81\n"
  "v_mfma_f32_16x16x32_bf16 v[8:11], a[152:155], v[68:71], v[8:11]\n"
  "ds_read_b64_tr_b16 v[122:123], v100 offset:10240\n"
  "v_mul_f32 v82, v20, v28\n"
  "v_mfma_f32_16x16x32_bf16 v[8:11], a[168:171], v[72:75], v[8:11]\n"
  "v_mul_f32 v83, v21, v29\n"
  "v_add_f32 v80, 1.0, v80\n"
  "v_mfma_f32_16x16x32_bf16 v[8:11], a[184:187], v[76:79], v[8:11]\n"
  "v_add_f32 v81, 1.0, v81\n"
  "v_mfma_f32_16x16x32_bf16 v[12:15], a[108:111], v[56:59], v[12:15]\n"
  "v_rcp_f32 v80, v80\n"
  "v_mfma_f32_16x16x32_bf16 v[12:15], a[124:127], v[60:63], v[12:15]\n"
  "v_rcp_f32 v81, v81\n"
  "v_mfma_f32_16x16x32_bf16 v[12:15], a[140:143], v[64:67], v[12:15]\n"
  "v_mul_f32 v82, v82, v80\n"
  "v_mul_f32 v83, v83, v81\n"
  "v_mfma_f32_16x16x32_bf16 v[12:15], a[156:159], v[68:71], v[12:15]\n"
  "v_cvt_pk_bf16_f32 v86, v82, v83\n"
  "v_mul_f32 v80, v22, v22\n"
  "v_mfma_f32_16x16x32_bf16 v[12:15], a[172:175], v[72:75], v[12:15]\n"
  "v_mul_f32 v81, v23, v23\n"
  "v_fmaak_f32 v80, v88, v80, 0xc0135761\n"
  "v_mfma_f32_16x16x32_bf16 v[12:15], a[188:191], v[76:79], v[12:15]\n"
  "s_waitcnt lgkmcnt(0)\n"
  "v_fmaak_f32 v81, v88, v81, 0xc0135761\n"
  "v_mul_f32 v80, v22, v80\n"
  "v_mfma_f32_16x16x32_bf16 v[144:147], v[112:115], v[124:127], v[144:147]\n"
  "ds_read_b128 v[32:35], v89 offset:12288\n"
  "v_mul_f32 v81, v23, v81\n"
  "v_mfma_f32_16x16x32_bf16 v[148:151], v[112:115], v[128:131], v[148:151]\n"
  "ds_read_b128 v[36:39], v89 offset:13312\n"
  "v_mfma_f32_16x16x32_bf16 v[152:155], v[112:115], v[132:135], v[152:155]\n"
  "ds_read_b128 v[40:43], v89 offset:14336\n"
  "v_mfma_f32_16x16x32_bf16 v[156:159], v[112:115], v[136:139], v[156:159]\n"
  "v_exp_f32 v80, v80\n"
  "v_mfma_f32_16x16x32_bf16 v[160:163], v[112:115], v[140:143], v[160:163]\n"
  "ds_read_b128 v[44:47], v89 offset:15360\n"
  "v_mfma_f32_16x16x32_bf16 v[164:167], v[116:119], v[124:127], v[164:167]\n"
  "ds_read_b128 v[48:51], v91 offset:20480\n"
  "v_mfma_f32_16x16x32_bf16 v[168:171], v[116:119], v[128:131], v[168:171]\n"
  "ds_read_b128 v[52:55], v91 offset:21504\n"
  "v_mfma_f32_16x16x32_bf16 v[172:175], v[116:119], v[132:135], v[172:175]\n"
  "v_exp_f32 v81, v81\n"
  "v_mfma_f32_16x16x32_bf16 v[176:179], v[116:119], v[136:139], v[176:179]\n"
  "v_mul_f32 v82, v22, v30\n"
  "v_mul_f32 v83, v23, v31\n"
  "v_mfma_f32_16x16x32_bf16 v[180:183], v[116:119], v[140:143], v[180:183]\n"
  "v_add_f32 v80, 1.0, v80\n"
  "v_add_f32 v81, 1.0, v81\n"
  "v_mfma_f32_16x16x32_bf16 v[184:187], v[120:123], v[124:127], v[184:187]\n"
  "v_rcp_f32 v80, v80\n"
  "v_mfma_f32_16x16x32_bf16 v[188:191], v[120:123], v[128:131], v[188:191]\n"
  "v_rcp_f32 v81, v81\n"
  "v_mfma_f32_16x16x32_bf16 v[192:195], v[120:123], v[132:135], v[192:195]\n"
  "v_mul_f32 v82, v82, v80\n"
  "v_mul_f32 v83, v83, v81\n"
  "v_mfma_f32_16x16x32_bf16 v[196:199], v[120:123], v[136:139], v[196:199]\n"
  "v_cvt_pk_bf16_f32 v87, v82, v83\n"
  "v_mfma_f32_16x16x32_bf16 v[200:203], v[120:123], v[140:143], v[200:203]\n"
  "s_waitcnt vmcnt(15) lgkmcnt(0)\n"
  "s_barrier\n"
  "v_mfma_f32_16x16x32_bf16 v[0:3], a[192:195], v[32:35], v[0:3]\n"
  "s_add_u32 m0, s48, 0\n"
  "ds_read_b128 v[56:59], v90 offset:16384\n"
  "v_mfma_f32_16x16x32_bf16 v[0:3], a[208:211], v[36:39], v[0:3]\n"
  "buffer_load_dwordx4 v92, s[36:39], s52 offen offset:0 sc1 lds\n"
  "v_mfma_f32_16x16x32_bf16 v[0:3], a[224:227], v[40:43], v[0:3]\n"
  "ds_read_b128 v[60:63], v90 offset:17408\n"
  "v_mfma_f32_16x16x32_bf16 v[0:3], a[240:243], v[44:47], v[0:3]\n"
  "v_mfma_f32_16x16x32_bf16 v[0:3], v[224:227], v[48:51], v[0:3]\n"
  "v_mfma_f32_16x16x32_bf16 v[0:3], v[240:243], v[52:55], v[0:3]\n"
  "ds_read_b128 v[64:67], v90 offset:18432\n"
  "v_mfma_f32_16x16x32_bf16 v[4:7], a[196:199], v[32:35], v[4:7]\n"
  "buffer_load_dwordx4 v92, s[36:39], s52 offen offset:1024 sc1 lds\n"
  "v_mfma_f32_16x16x32_bf16 v[4:7], a[212:215], v[36:39], v[4:7]\n"
  "v_mfma_f32_16x16x32_bf16 v[4:7], a[228:231], v[40:43], v[4:7]\n"
  "ds_read_b128 v[68:71], v90 offset:19456\n"
  "v_mfma_f32_16x16x32_bf16 v[4:7], a[244:247], v[44:47], v[4:7]\n"
  "v_mfma_f32_16x16x32_bf16 v[4:7], v[228:231], v[48:51], v[4:7]\n"
  "v_mfma_f32_16x16x32_bf16 v[4:7], v[244:247], v[52:55], v[4:7]\n"
  "buffer_load_dwordx4 v92, s[36:39], s52 offen offset:2048 sc1 lds\n"
  "ds_read_b128 v[72:75], v90 offset:20480\n"
  "v_mfma_f32_16x16x32_bf16 v[8:11], a[200:203], v[32:35], v[8:11]\n"
  "v_mfma_f32_16x16x32_bf16 v[8:11], a[216:219], v[36:39], v[8:11]\n"
  "v_mfma_f32_16x16x32_bf16 v[8:11], a[232:235], v[40:43], v[8:11]\n"
  "ds_read_b128 v[76:79], v90 offset:21504\n"
  "v_mfma_f32_16x16x32_bf16 v[8:11], a[248:251], v[44:47], v[8:11]\n"
  "v_mfma_f32_16x16x32_bf16 v[8:11], v[232:235], v[48:51], v[8:11]\n"
  "buffer_load_dwordx4 v92, s[36:39], s52 offen offset:3072 sc1 lds\n"
  "v_mfma_f32_16x16x32_bf16 v[8:11], v[248:251], v[52:55], v[8:11]\n"
  "v_mfma_f32_16x16x32_bf16 v[12:15], a[204:207], v[32:35], v[12:15]\n"
  "v_mfma_f32_16x16x32_bf16 v[12:15], a[220:223], v[36:39], v[12:15]\n"
  "v_mfma_f32_16x16x32_bf16 v[12:15], a[236:239], v[40:43], v[12:15]\n"
  "s_add_u32 m0, s49, 137216\n"
  "v_mfma_f32_16x16x32_bf16 v[12:15], a[252:255], v[44:47], v[12:15]\n"
  "buffer_load_dwordx4 v93, s[36:39], s52 offen sc1 lds\n"
  "v_mfma_f32_16x16x32_bf16 v[12:15], v[236:239], v[48:51], v[12:15]\n"
  "s_add_u32 s52, s52, 18432\n"
  "buffer_store_dwordx4 v[84:87], v89, s[36:39], s55 offen sc1\n"
  "v_mfma_f32_16x16x32_bf16 v[12:15], v[252:255], v[52:55], v[12:15]\n"
  "s_waitcnt lgkmcnt(0)\n"
  "s_add_u32 s55, s55, 147456\n"
  "v_mfma_f32_16x16x32_bf16 v[16:19], a[0:3], v[56:59], 0\n"
  "s_cmp_eq_u32 s52, s54\n"
  "s_cselect_b32 s52, s53, s52\n"
  "ds_read_b64_tr_b16 v[112:113], v100 offset:32768\n"
  "s_waitcnt vmcnt(7)\n"
  "v_cmp_gt_u32 vcc, 12, v106\n"
  "s_and_b64 vcc, vcc, 3\n"
  "s_cbranch_scc1 Lpollp12A%=\n"
  "Lpollrp12A%=:\n"
  "v_mfma_f32_16x16x32_bf16 v[16:19], a[16:19], v[60:63], v[16:19]\n"
  "s_add_u32 m0, s50, 147456\n"
  "s_cmp_eq_u32 s55, s57\n"
  "s_cselect_b32 s55, s56, s55\n"
  "ds_read_b64_tr_b16 v[114:115], v100 offset:40960\n"
  "v_mfma_f32_16x16x32_bf16 v[16:19], a[32:35], v[64:67], v[16:19]\n"
  "buffer_load_dwordx4 v98, s[36:39], s59 offen sc1 lds\n"
  "v_mul_f32 v80, v0, v0\n"
  "v_mfma_f32_16x16x32_bf16 v[16:19], a[48:51], v[68:71], v[16:19]\n"
  "ds_read_b128 v[124:127], v90 offset:35840\n"
  "v_mul_f32 v81, v1, v1\n"
  "v_mfma_f32_16x16x32_bf16 v[16:19], a[64:67], v[72:75], v[16:19]\n"
  "ds_read_b128 v[128:131], v90 offset:39936\n"
  "v_fmaak_f32 v80, v88, v80, 0xc0135761\n"
  "v_mfma_f32_16x16x32_bf16 v[16:19], a[80:83], v[76:79], v[16:19]\n"
  "v_fmaak_f32 v81, v88, v81, 0xc0135761\n"
  "v_mul_f32 v80, v0, v80\n"
  "v_mfma_f32_16x16x32_bf16 v[20:23], a[4:7], v[56:59], 0\n"
  "s_add_u32 m0, s48, 49152\n"
  "ds_read_b128 v[132:135], v90 offset:44032\n"
  "v_mul_f32 v81, v1, v81\n"
  "v_mfma_f32_16x16x32_bf16 v[20:23], a[20:23], v[60:63], v[20:23]\n"
  "buffer_load_dwordx4 v97, s[36:39], s59 offen offset:3072 sc1 lds\n"
  "ds_read_b128 v[136:139], v90 offset:48128\n"
  "v_mfma_f32_16x16x32_bf16 v[20:23], a[36:39], v[64:67], v[20:23]\n"
  "s_add_u32 s59, s59, 1024\n"
  "v_exp_f32 v80, v80\n"
  "v_mfma_f32_16x16x32_bf16 v[20:23], a[52:55], v[68:71], v[20:23]\n"
  "ds_read_b128 v[140:143], v91 offset:24576\n"
  "v_mfma_f32_16x16x32_bf16 v[20:23], a[68:71], v[72:75], v[20:23]\n"
  "ds_read_b64_tr_b16 v[116:117], v100 offset:33792\n"
  "v_mfma_f32_16x16x32_bf16 v[20:23], a[84:87], v[76:79], v[20:23]\n"
  "v_exp_f32 v81, v81\n"
  "v_mfma_f32_16x16x32_bf16 v[24:27], a[8:11], v[56:59], 0\n"
  "buffer_load_dwordx4 v94, s[40:43], s60 offen offset:0 lds\n"
  "ds_read_b64_tr_b16 v[118:119], v100 offset:41984\n"
  "v_mfma_f32_16x16x32_bf16 v[24:27], a[24:27], v[60:63], v[24:27]\n"
  "ds_read_b64_tr_b16 v[120:121], v100 offset:34816\n"
  "v_mul_f32 v82, v0, v8\n"
  "v_mfma_f32_16x16x32_bf16 v[24:27], a[40:43], v[64:67], v[24:27]\n"
  "v_mul_f32 v83, v1, v9\n"
  "v_add_f32 v80, 1.0, v80\n"
  "v_mfma_f32_16x16x32_bf16 v[24:27], a[56:59], v[68:71], v[24:27]\n"
  "ds_read_b64_tr_b16 v[122:123], v100 offset:43008\n"
  "v_add_f32 v81, 1.0, v81\n"
  "v_mfma_f32_16x16x32_bf16 v[24:27], a[72:75], v[72:75], v[24:27]\n"
  "v_rcp_f32 v80, v80\n"
  "v_mfma_f32_16x16x32_bf16 v[24:27], a[88:91], v[76:79], v[24:27]\n"
  "buffer_load_dwordx4 v95, s[40:43], s60 offen offset:1024 lds\n"
  "v_mfma_f32_16x16x32_bf16 v[28:31], a[12:15], v[56:59], 0\n"
  "v_rcp_f32 v81, v81\n"
  "v_mfma_f32_16x16x32_bf16 v[28:31], a[28:31], v[60:63], v[28:31]\n"
  "v_mul_f32 v82, v82, v80\n"
  "v_mul_f32 v83, v83, v81\n"
  "v_mfma_f32_16x16x32_bf16 v[28:31], a[44:47], v[64:67], v[28:31]\n"
  "v_cvt_pk_bf16_f32 v84, v82, v83\n"
  "v_mul_f32 v80, v2, v2\n"
  "v_mfma_f32_16x16x32_bf16 v[28:31], a[60:63], v[68:71], v[28:31]\n"
  "v_mul_f32 v81, v3, v3\n"
  "v_fmaak_f32 v80, v88, v80, 0xc0135761\n"
  "v_mfma_f32_16x16x32_bf16 v[28:31], a[76:79], v[72:75], v[28:31]\n"
  "buffer_load_dwordx4 v96, s[40:43], s60 offen offset:2048 lds\n"
  "v_fmaak_f32 v81, v88, v81, 0xc0135761\n"
  "v_mfma_f32_16x16x32_bf16 v[28:31], a[92:95], v[76:79], v[28:31]\n"
  "s_waitcnt lgkmcnt(0)\n"
  "v_mul_f32 v80, v2, v80\n"
  "v_mul_f32 v81, v3, v81\n"
  "v_mfma_f32_16x16x32_bf16 v[144:147], v[112:115], v[124:127], v[144:147]\n"
  "s_add_u32 s60, s60, 36864\n"
  "ds_read_b128 v[32:35], v90 offset:22528\n"
  "v_mfma_f32_16x16x32_bf16 v[148:151], v[112:115], v[128:131], v[148:151]\n"
  "ds_read_b128 v[36:39], v90 offset:23552\n"
  "v_mfma_f32_16x16x32_bf16 v[152:155], v[112:115], v[132:135], v[152:155]\n"
  "s_add_u32 m0, s48, 65536\n"
  "ds_read_b128 v[40:43], v90 offset:24576\n"
  "v_mfma_f32_16x16x32_bf16 v[156:159], v[112:115], v[136:139], v[156:159]\n"
  "buffer_load_dwordx4 v97, s[36:39], s59 offen offset:3072 sc1 lds\n"
  "v_mfma_f32_16x16x32_bf16 v[160:163], v[112:115], v[140:143], v[160:163]\n"
  "s_add_u32 s59, s59, 1024\n"
  "ds_read_b128 v[44:47], v90 offset:25600\n"
  "v_mfma_f32_16x16x32_bf16 v[164:167], v[116:119], v[124:127], v[164:167]\n"
  "ds_read_b128 v[48:51], v90 offset:26624\n"
  "v_mfma_f32_16x16x32_bf16 v[168:171], v[116:119], v[128:131], v[168:171]\n"
  "ds_read_b128 v[52:55], v90 offset:27648\n"
  "v_mfma_f32_16x16x32_bf16 v[172:175], v[116:119], v[132:135], v[172:175]\n"
  "v_exp_f32 v80, v80\n"
  "v_mfma_f32_16x16x32_bf16 v[176:179], v[116:119], v[136:139], v[176:179]\n"
  "buffer_load_dwordx4 v94, s[40:43], s60 offen offset:0 lds\n"
  "v_mfma_f32_16x16x32_bf16 v[180:183], v[116:119], v[140:143], v[180:183]\n"
  "v_exp_f32 v81, v81\n"
  "v_mfma_f32_16x16x32_bf16 v[184:187], v[120:123], v[124:127], v[184:187]\n"
  "v_mul_f32 v82, v2, v10\n"
  "v_mul_f32 v83, v3, v11\n"
  "v_mfma_f32_16x16x32_bf16 v[188:191], v[120:123], v[128:131], v[188:191]\n"
  "v_add_f32 v80, 1.0, v80\n"
  "v_add_f32 v81, 1.0, v81\n"
  "v_mfma_f32_16x16x32_bf16 v[192:195], v[120:123], v[132:135], v[192:195]\n"
  "v_rcp_f32 v80, v80\n"
  "v_mfma_f32_16x16x32_bf16 v[196:199], v[120:123], v[136:139], v[196:199]\n"
  "buffer_load_dwordx4 v95, s[40:43], s60 offen offset:1024 lds\n"
  "v_mfma_f32_16x16x32_bf16 v[200:203], v[120:123], v[140:143], v[200:203]\n"
  "s_waitcnt lgkmcnt(0)\n"
  "v_rcp_f32 v81, v81\n"
  "v_mfma_f32_16x16x32_bf16 v[16:19], a[96:99], v[32:35], v[16:19]\n"
  "ds_read_b64_tr_b16 v[112:113], v100 offset:49152\n"
  "v_mul_f32 v82, v82, v80\n"
  "v_mfma_f32_16x16x32_bf16 v[16:19], a[112:115], v[36:39], v[16:19]\n"
  "ds_read_b64_tr_b16 v[114:115], v100 offset:57344\n"
  "v_mul_f32 v83, v83, v81\n"
  "v_mfma_f32_16x16x32_bf16 v[16:19], a[128:131], v[40:43], v[16:19]\n"
  "v_cvt_pk_bf16_f32 v85, v82, v83\n"
  "v_mul_f32 v80, v4, v4\n"
  "v_mfma_f32_16x16x32_bf16 v[16:19], a[144:147], v[44:47], v[16:19]\n"
  "buffer_load_dwordx4 v96, s[40:43], s60 offen offset:2048 lds\n"
  "ds_read_b128 v[124:127], v90 offset:52224\n"
  "v_mfma_f32_16x16x32_bf16 v[16:19], a[160:163], v[48:51], v[16:19]\n"
  "s_add_u32 s60, s60, 36864\n"
  "ds_read_b128 v[128:131], v90 offset:56320\n"
  "v_mul_f32 v81, v5, v5\n"
  "v_mfma_f32_16x16x32_bf16 v[16:19], a[176:179], v[52:55], v[16:19]\n"
  "v_fmaak_f32 v80, v88, v80, 0xc0135761\n"
  "v_fmaak_f32 v81, v88, v81, 0xc0135761\n"
  "v_mfma_f32_16x16x32_bf16 v[20:23], a[100:103], v[32:35], v[20:23]\n"
  "ds_read_b128 v[132:135], v90 offset:60416\n"
  "v_mul_f32 v80, v4, v80\n"
  "v_mfma_f32_16x16x32_bf16 v[20:23], a[116:119], v[36:39], v[20:23]\n"
  "ds_read_b128 v[136:139], v90 offset:64512\n"
  "v_mul_f32 v81, v5, v81\n"
  "v_mfma_f32_16x16x32_bf16 v[20:23], a[132:135], v[40:43], v[20:23]\n"
  "v_exp_f32 v80, v80\n"
  "v_mfma_f32_16x16x32_bf16 v[20:23], a[148:151], v[44:47], v[20:23]\n"
  "ds_read_b128 v[140:143], v91 offset:25600\n"
  "v_mfma_f32_16x16x32_bf16 v[20:23], a[164:167], v[48:51], v[20:23]\n"
  "ds_read_b64_tr_b16 v[116:117], v100 offset:50176\n"
  "v_mfma_f32_16x16x32_bf16 v[20:23], a[180:183], v[52:55], v[20:23]\n"
  "v_exp_f32 v81, v81\n"
  "v_mfma_f32_16x16x32_bf16 v[24:27], a[104:107], v[32:35], v[24:27]\n"
  "ds_read_b64_tr_b16 v[118:119], v100 offset:58368\n"
  "v_mul_f32 v82, v4, v12\n"
  "v_mfma_f32_16x16x32_bf16 v[24:27], a[120:123], v[36:39], v[24:27]\n"
  "ds_read_b64_tr_b16 v[120:121], v100 offset:51200\n"
  "v_mul_f32 v83, v5, v13\n"
  "v_mfma_f32_16x16x32_bf16 v[24:27], a[136:139], v[40:43], v[24:27]\n"
  "v_add_f32 v80, 1.0, v80\n"
  "v_add_f32 v81, 1.0, v81\n"
  "v_mfma_f32_16x16x32_bf16 v[24:27], a[152:155], v[44:47], v[24:27]\n"
  "ds_read_b64_tr_b16 v[122:123], v100 offset:59392\n"
  "v_mfma_f32_16x16x32_bf16 v[24:27], a[168:171], v[48:51], v[24:27]\n"
  "v_rcp_f32 v80, v80\n"
  "v_mfma_f32_16x16x32_bf16 v[24:27], a[184:187], v[52:55], v[24:27]\n"
  "v_rcp_f32 v81, v81\n"
  "v_mfma_f32_16x16x32_bf16 v[28:31], a[108:111], v[32:35], v[28:31]\n"
  "v_mul_f32 v82, v82, v80\n"
  "v_mul_f32 v83, v83, v81\n"
  "v_mfma_f32_16x16x32_bf16 v[28:31], a[124:127], v[36:39], v[28:31]\n"
  "v_cvt_pk_bf16_f32 v86, v82, v83\n"
  "v_mul_f32 v80, v6, v6\n"
  "v_mfma_f32_16x16x32_bf16 v[28:31], a[140:143], v[40:43], v[28:31]\n"
  "v_mul_f32 v81, v7, v7\n"
  "v_fmaak_f32 v80, v88, v80, 0xc0135761\n"
  "v_mfma_f32_16x16x32_bf16 v[28:31], a[156:159], v[44:47], v[28:31]\n"
  "v_fmaak_f32 v81, v88, v81, 0xc0135761\n"
  "v_mul_f32 v80, v6, v80\n"
  "v_mfma_f32_16x16x32_bf16 v[28:31], a[172:175], v[48:51], v[28:31]\n"
  "v_mul_f32 v81, v7, v81\n"
  "v_mfma_f32_16x16x32_bf16 v[28:31], a[188:191], v[52:55], v[28:31]\n"
  "s_waitcnt lgkmcnt(0)\n"
  "v_exp_f32 v80, v80\n"
  "v_mfma_f32_16x16x32_bf16 v[144:147], v[112:115], v[124:127], v[144:147]\n"
  "ds_read_b128 v[56:59], v90 offset:28672\n"
  "v_mfma_f32_16x16x32_bf16 v[148:151], v[112:115], v[128:131], v[148:151]\n"
  "ds_read_b128 v[60:63], v90 offset:29696\n"
  "v_mfma_f32_16x16x32_bf16 v[152:155], v[112:115], v[132:135], v[152:155]\n"
  "ds_read_b128 v[64:67], v90 offset:30720\n"
  "v_mfma_f32_16x16x32_bf16 v[156:159], v[112:115], v[136:139], v[156:159]\n"
  "v_exp_f32 v81, v81\n"
  "v_mfma_f32_16x16x32_bf16 v[160:163], v[112:115], v[140:143], v[160:163]\n"
  "ds_read_b128 v[68:71], v90 offset:31744\n"
  "v_mul_f32 v82, v6, v14\n"
  "v_mfma_f32_16x16x32_bf16 v[164:167], v[116:119], v[124:127], v[164:167]\n"
  "ds_read_b128 v[72:75], v91 offset:30720\n"
  "v_mul_f32 v83, v7, v15\n"
  "v_mfma_f32_16x16x32_bf16 v[168:171], v[116:119], v[128:131], v[168:171]\n"
  "ds_read_b128 v[76:79], v91 offset:31744\n"
  "v_add_f32 v80, 1.0, v80\n"
  "v_mfma_f32_16x16x32_bf16 v[172:175], v[116:119], v[132:135], v[172:175]\n"
  "v_add_f32 v81, 1.0, v81\n"
  "v_mfma_f32_16x16x32_bf16 v[176:179], v[116:119], v[136:139], v[176:179]\n"
  "v_rcp_f32 v80, v80\n"
  "v_mfma_f32_16x16x32_bf16 v[180:183], v[116:119], v[140:143], v[180:183]\n"
  "v_rcp_f32 v81, v81\n"
  "s_nop 0\n"
  "s_cbranch_execz Ltramp%=\n"
  "v_mfma_f32_16x16x32_bf16 v[184:187], v[120:123], v[124:127], v[184:187]\n"
  "v_mul_f32 v82, v82, v80\n"
  "v_mul_f32 v83, v83, v81\n"
  "v_mfma_f32_16x16x32_bf16 v[188:191], v[120:123], v[128:131], v[188:191]\n"
  "v_cvt_pk_bf16_f32 v87, v82, v83\n"
  "v_mfma_f32_16x16x32_bf16 v[192:195], v[120:123], v[132:135], v[192:195]\n"
  "s_waitcnt vmcnt(9)\n"
  "s_mov_b64 exec, 1\n"
  "buffer_atomic_add v104, off, s[24:27], s14 sc1\n"
  "s_mov_b64 exec, -1\n"
  "v_mfma_f32_16x16x32_bf16 v[196:199], v[120:123], v[136:139], v[196:199]\n"
  "v_mfma_f32_16x16x32_bf16 v[200:203], v[120:123], v[140:143], v[200:203]\n"
  "s_waitcnt vmcnt(16) lgkmcnt(0)\n"
  "s_barrier\n"
  "v_mfma_f32_16x16x32_bf16 v[16:19], a[192:195], v[56:59], v[16:19]\n"
  "s_add_u32 m0, s48, 81920\n"
  "ds_read_b128 v[32:35], v89 offset:16384\n"
  "v_mfma_f32_16x16x32_bf16 v[16:19], a[208:211], v[60:63], v[16:19]\n"
  "buffer_load_dwordx4 v97, s[36:39], s59 offen offset:3072 sc1 lds\n"
  "v_mfma_f32_16x16x32_bf16 v[16:19], a[224:227], v[64:67], v[16:19]\n"
  "s_add_u32 s59, s59, 1024\n"
  "ds_read_b128 v[36:39], v89 offset:17408\n"
  "v_mfma_f32_16x16x32_bf16 v[16:19], a[240:243], v[68:71], v[16:19]\n"
  "v_mfma_f32_16x16x32_bf16 v[16:19], v[224:227], v[72:75], v[16:19]\n"
  "v_mfma_f32_16x16x32_bf16 v[16:19], v[240:243], v[76:79], v[16:19]\n"
  "ds_read_b128 v[40:43], v89 offset:18432\n"
  "v_mfma_f32_16x16x32_bf16 v[20:23], a[196:199], v[56:59], v[20:23]\n"
  "buffer_load_dwordx4 v94, s[40:43], s60 offen offset:0 lds\n"
  "v_mfma_f32_16x16x32_bf16 v[20:23], a[212:215], v[60:63], v[20:23]\n"
  "v_mfma_f32_16x16x32_bf16 v[20:23], a[228:231], v[64:67], v[20:23]\n"
  "ds_read_b128 v[44:47], v89 offset:19456\n"
  "v_mfma_f32_16x16x32_bf16 v[20:23], a[244:247], v[68:71], v[20:23]\n"
  "v_mfma_f32_16x16x32_bf16 v[20:23], v[228:231], v[72:75], v[20:23]\n"
  "v_mfma_f32_16x16x32_bf16 v[20:23], v[244:247], v[76:79], v[20:23]\n"
  "buffer_load_dwordx4 v95, s[40:43], s60 offen offset:1024 lds\n"
  "ds_read_b128 v[48:51], v89 offset:20480\n"
  "v_mfma_f32_16x16x32_bf16 v[24:27], a[200:203], v[56:59], v[24:27]\n"
  "v_mfma_f32_16x16x32_bf16 v[24:27], a[216:219], v[60:63], v[24:27]\n"
  "v_mfma_f32_16x16x32_bf16 v[24:27], a[232:235], v[64:67], v[24:27]\n"
  "ds_read_b128 v[52:55], v89 offset:21504\n"
  "v_mfma_f32_16x16x32_bf16 v[24:27], a[248:251], v[68:71], v[24:27]\n"
  "v_mfma_f32_16x16x32_bf16 v[24:27], v[232:235], v[72:75], v[24:27]\n"
  "buffer_load_dwordx4 v96, s[40:43], s60 offen offset:2048 lds\n"
  "v_mfma_f32_16x16x32_bf16 v[24:27], v[248:251], v[76:79], v[24:27]\n"
  "s_add_u32 s60, s60, 36864\n"
  "v_mfma_f32_16x16x32_bf16 v[28:31], a[204:207], v[56:59], v[28:31]\n"
  "v_mfma_f32_16x16x32_bf16 v[28:31], a[220:223], v[60:63], v[28:31]\n"
  "v_mfma_f32_16x16x32_bf16 v[28:31], a[236:239], v[64:67], v[28:31]\n"
  "s_add_u32 m0, s48, 98304\n"
  "v_mfma_f32_16x16x32_bf16 v[28:31], a[252:255], v[68:71], v[28:31]\n"
  "buffer_load_dwordx4 v97, s[36:39], s59 offen offset:3072 sc1 lds\n"
  "v_mfma_f32_16x16x32_bf16 v[28:31], v[236:239], v[72:75], v[28:31]\n"
  "s_add_u32 s59, s59, 1024\n"
  "buffer_store_dwordx4 v[84:87], v89, s[36:39], s55 offen sc1\n"
  "v_mfma_f32_16x16x32_bf16 v[28:31], v[252:255], v[76:79], v[28:31]\n"
  "s_waitcnt lgkmcnt(0)\n"
  "v_mfma_f32_16x16x32_bf16 v[0:3], a[0:3], v[32:35], 0\n"
  "s_add_u32 s55, s55, 147456\n"
  "ds_read_b64_tr_b16 v[112:113], v99 offset:32768\n"
  "v_mfma_f32_16x16x32_bf16 v[0:3], a[16:19], v[36:39], v[0:3]\n"
  "s_cmp_eq_u32 s55, s57\n"
  "s_cselect_b32 s55, s56, s55\n"
  "ds_read_b64_tr_b16 v[114:115], v99 offset:40960\n"
  "v_mfma_f32_16x16x32_bf16 v[0:3], a[32:35], v[40:43], v[0:3]\n"
  "buffer_load_dwordx4 v94, s[40:43], s60 offen offset:0 lds\n"
  "v_mul_f32 v80, v16, v16\n"
  "v_mfma_f32_16x16x32_bf16 v[0:3], a[48:51], v[44:47], v[0:3]\n"
  "ds_read_b128 v[124:127], v89 offset:35840\n"
  "v_mul_f32 v81, v17, v17\n"
  "v_mfma_f32_16x16x32_bf16 v[0:3], a[64:67], v[48:51], v[0:3]\n"
  "ds_read_b128 v[128:131], v89 offset:39936\n"
  "v_fmaak_f32 v80, v88, v80, 0xc0135761\n"
  "v_mfma_f32_16x16x32_bf16 v[0:3], a[80:83], v[52:55], v[0:3]\n"
  "v_fmaak_f32 v81, v88, v81, 0xc0135761\n"
  "v_mul_f32 v80, v16, v80\n"
  "v_mfma_f32_16x16x32_bf16 v[4:7], a[4:7], v[32:35], 0\n"
  "ds_read_b128 v[132:135], v89 offset:44032\n"
  "v_mul_f32 v81, v17, v81\n"
  "v_mfma_f32_16x16x32_bf16 v[4:7], a[20:23], v[36:39], v[4:7]\n"
  "buffer_load_dwordx4 v95, s[40:43], s60 offen offset:1024 lds\n"
  "ds_read_b128 v[136:139], v89 offset:48128\n"
  "v_mfma_f32_16x16x32_bf16 v[4:7], a[36:39], v[40:43], v[4:7]\n"
  "v_exp_f32 v80, v80\n"
  "v_mfma_f32_16x16x32_bf16 v[4:7], a[52:55], v[44:47], v[4:7]\n"
  "ds_read_b128 v[140:143], v91 offset:26624\n"
  "v_mfma_f32_16x16x32_bf16 v[4:7], a[68:71], v[48:51], v[4:7]\n"
  "ds_read_b64_tr_b16 v[116:117], v99 offset:33792\n"
  "v_mfma_f32_16x16x32_bf16 v[4:7], a[84:87], v[52:55], v[4:7]\n"
  "v_exp_f32 v81, v81\n"
  "v_mfma_f32_16x16x32_bf16 v[8:11], a[8:11], v[32:35], 0\n"
  "buffer_load_dwordx4 v96, s[40:43], s60 offen offset:2048 lds\n"
  "ds_read_b64_tr_b16 v[118:119], v99 offset:41984\n"
  "v_mfma_f32_16x16x32_bf16 v[8:11], a[24:27], v[36:39], v[8:11]\n"
  "s_add_u32 s60, s60, 36864\n"
  "ds_read_b64_tr_b16 v[120:121], v99 offset:34816\n"
  "v_mul_f32 v82, v16, v24\n"
  "v_mfma_f32_16x16x32_bf16 v[8:11], a[40:43], v[40:43], v[8:11]\n"
  "v_mul_f32 v83, v17, v25\n"
  "v_add_f32 v80, 1.0, v80\n"
  "v_mfma_f32_16x16x32_bf16 v[8:11], a[56:59], v[44:47], v[8:11]\n"
  "ds_read_b64_tr_b16 v[122:123], v99 offset:43008\n"
  "v_add_f32 v81, 1.0, v81\n"
  "v_mfma_f32_16x16x32_bf16 v[8:11], a[72:75], v[48:51], v[8:11]\n"
  "v_rcp_f32 v80, v80\n"
  "v_mfma_f32_16x16x32_bf16 v[8:11], a[88:91], v[52:55], v[8:11]\n"
  "v_rcp_f32 v81, v81\n"
  "v_mfma_f32_16x16x32_bf16 v[12:15], a[12:15], v[32:35], 0\n"
  "v_mul_f32 v82, v82, v80\n"
  "v_mul_f32 v83, v83, v81\n"
  "v_mfma_f32_16x16x32_bf16 v[12:15], a[28:31], v[36:39], v[12:15]\n"
  "v_cvt_pk_bf16_f32 v84, v82, v83\n"
  "v_mul_f32 v80, v18, v18\n"
  "v_mfma_f32_16x16x32_bf16 v[12:15], a[44:47], v[40:43], v[12:15]\n"
  "v_mul_f32 v81, v19, v19\n"
  "v_fmaak_f32 v80, v88, v80, 0xc0135761\n"
  "v_mfma_f32_16x16x32_bf16 v[12:15], a[60:63], v[44:47], v[12:15]\n"
  "v_fmaak_f32 v81, v88, v81, 0xc0135761\n"
  "v_mul_f32 v80, v18, v80\n"
  "v_mfma_f32_16x16x32_bf16 v[12:15], a[76:79], v[48:51], v[12:15]\n"
  "v_mul_f32 v81, v19, v81\n"
  "v_mfma_f32_16x16x32_bf16 v[12:15], a[92:95], v[52:55], v[12:15]\n"
  "s_waitcnt lgkmcnt(0)\n"
  "v_exp_f32 v80, v80\n"
  "v_mfma_f32_16x16x32_bf16 v[144:147], v[112:115], v[124:127], v[144:147]\n"
  "ds_read_b128 v[56:59], v89 offset:22528\n"
  "v_mfma_f32_16x16x32_bf16 v[148:151], v[112:115], v[128:131], v[148:151]\n"
  "ds_read_b128 v[60:63], v89 offset:23552\n"
  "v_mfma_f32_16x16x32_bf16 v[152:155], v[112:115], v[132:135], v[152:155]\n"
  "ds_read_b128 v[64:67], v89 offset:24576\n"
  "v_mfma_f32_16x16x32_bf16 v[156:159], v[112:115], v[136:139], v[156:159]\n"
  "v_exp_f32 v81, v81\n"
  "v_mfma_f32_16x16x32_bf16 v[160:163], v[112:115], v[140:143], v[160:163]\n"
  "ds_read_b128 v[68:71], v89 offset:25600\n"
  "v_mul_f32 v82, v18, v26\n"
  "v_mfma_f32_16x16x32_bf16 v[164:167], v[116:119], v[124:127], v[164:167]\n"
  "ds_read_b128 v[72:75], v89 offset:26624\n"
  "v_mul_f32 v83, v19, v27\n"
  "v_mfma_f32_16x16x32_bf16 v[168:171], v[116:119], v[128:131], v[168:171]\n"
  "ds_read_b128 v[76:79], v89 offset:27648\n"
  "v_add_f32 v80, 1.0, v80\n"
  "v_mfma_f32_16x16x32_bf16 v[172:175], v[116:119], v[132:135], v[172:175]\n"
  "v_add_f32 v81, 1.0, v81\n"
  "v_mfma_f32_16x16x32_bf16 v[176:179], v[116:119], v[136:139], v[176:179]\n"
  "v_rcp_f32 v80, v80\n"
  "v_mfma_f32_16x16x32_bf16 v[180:183], v[116:119], v[140:143], v[180:183]\n"
  "v_rcp_f32 v81, v81\n"
  "v_mfma_f32_16x16x32_bf16 v[184:187], v[120:123], v[124:127], v[184:187]\n"
  "v_mul_f32 v82, v82, v80\n"
  "v_mul_f32 v83, v83, v81\n"
  "v_mfma_f32_16x16x32_bf16 v[188:191], v[120:123], v[128:131], v[188:191]\n"
  "v_cvt_pk_bf16_f32 v85, v82, v83\n"
  "v_mul_f32 v80, v20, v20\n"
  "v_mfma_f32_16x16x32_bf16 v[192:195], v[120:123], v[132:135], v[192:195]\n"
  "v_mul_f32 v81, v21, v21\n"
  "v_fmaak_f32 v80, v88, v80, 0xc0135761\n"
  "v_mfma_f32_16x16x32_bf16 v[196:199], v[120:123], v[136:139], v[196:199]\n"
  "v_fmaak_f32 v81, v88, v81, 0xc0135761\n"
  "v_mul_f32 v80, v20, v80\n"
  "v_mfma_f32_16x16x32_bf16 v[200:203], v[120:123], v[140:143], v[200:203]\n"
  "s_waitcnt lgkmcnt(0)\n"
  "v_mul_f32 v81, v21, v81\n"
  "v_mfma_f32_16x16x32_bf16 v[0:3], a[96:99], v[56:59], v[0:3]\n"
  "ds_read_b64_tr_b16 v[112:113], v101 offset:0\n"
  "v_mfma_f32_16x16x32_bf16 v[0:3], a[112:115], v[60:63], v[0:3]\n"
  "ds_read_b64_tr_b16 v[114:115], v101 offset:8192\n"
  "v_mfma_f32_16x16x32_bf16 v[0:3], a[128:131], v[64:67], v[0:3]\n"
  "v_exp_f32 v80, v80\n"
  "v_mfma_f32_16x16x32_bf16 v[0:3], a[144:147], v[68:71], v[0:3]\n"
  "ds_read_b128 v[124:127], v91 offset:3072\n"
  "v_mfma_f32_16x16x32_bf16 v[0:3], a[160:163], v[72:75], v[0:3]\n"
  "ds_read_b128 v[128:131], v91 offset:7168\n"
  "v_mfma_f32_16x16x32_bf16 v[0:3], a[176:179], v[76:79], v[0:3]\n"
  "v_exp_f32 v81, v81\n"
  "v_mfma_f32_16x16x32_bf16 v[4:7], a[100:103], v[56:59], v[4:7]\n"
  "ds_read_b128 v[132:135], v91 offset:11264\n"
  "v_mul_f32 v82, v20, v28\n"
  "v_mfma_f32_16x16x32_bf16 v[4:7], a[116:119], v[60:63], v[4:7]\n"
  "ds_read_b128 v[136:139], v91 offset:15360\n"
  "v_mul_f32 v83, v21, v29\n"
  "v_mfma_f32_16x16x32_bf16 v[4:7], a[132:135], v[64:67], v[4:7]\n"
  "v_add_f32 v80, 1.0, v80\n"
  "v_add_f32 v81, 1.0, v81\n"
  "v_mfma_f32_16x16x32_bf16 v[4:7], a[148:151], v[68:71], v[4:7]\n"
  "ds_read_b128 v[140:143], v91 offset:27648\n"
  "v_mfma_f32_16x16x32_bf16 v[4:7], a[164:167], v[72:75], v[4:7]\n"
  "ds_read_b64_tr_b16 v[116:117], v101 offset:1024\n"
  "v_mfma_f32_16x16x32_bf16 v[4:7], a[180:183], v[76:79], v[4:7]\n"
  "v_rcp_f32 v80, v80\n"
  "v_mfma_f32_16x16x32_bf16 v[8:11], a[104:107], v[56:59], v[8:11]\n"
  "ds_read_b64_tr_b16 v[118:119], v101 offset:9216\n"
  "v_mfma_f32_16x16x32_bf16 v[8:11], a[120:123], v[60:63], v[8:11]\n"
  "ds_read_b64_tr_b16 v[120:121], v101 offset:2048\n"
  "v_mfma_f32_16x16x32_bf16 v[8:11], a[136:139], v[64:67], v[8:11]\n"
  "v_rcp_f32 v81, v81\n"
  "v_mfma_f32_16x16x32_bf16 v[8:11], a[152:155], v[68:71], v[8:11]\n"
  "ds_read_b64_tr_b16 v[122:123], v101 offset:10240\n"
  "v_mul_f32 v82, v82, v80\n"
  "v_mfma_f32_16x16x32_bf16 v[8:11], a[168:171], v[72:75], v[8:11]\n"
  "v_mul_f32 v83, v83, v81\n"
  "v_cvt_pk_bf16_f32 v86, v82, v83\n"
  "v_mfma_f32_16x16x32_bf16 v[8:11], a[184:187], v[76:79], v[8:11]\n"
  "v_mul_f32 v80, v22, v22\n"
  "v_mul_f32 v81, v23, v23\n"
  "v_mfma_f32_16x16x32_bf16 v[12:15], a[108:111], v[56:59], v[12:15]\n"
  "v_fmaak_f32 v80, v88, v80, 0xc0135761\n"
  "v_fmaak_f32 v81, v88, v81, 0xc0135761\n"
  "v_mfma_f32_16x16x32_bf16 v[12:15], a[124:127], v[60:63], v[12:15]\n"
  "v_mul_f32 v80, v22, v80\n"
  "v_mul_f32 v81, v23, v81\n"
  "v_mfma_f32_16x16x32_bf16 v[12:15], a[140:143], v[64:67], v[12:15]\n"
  "v_exp_f32 v80, v80\n"
  "v_mfma_f32_16x16x32_bf16 v[12:15], a[156:159], v[68:71], v[12:15]\n"
  "v_exp_f32 v81, v81\n"
  "v_mfma_f32_16x16x32_bf16 v[12:15], a[172:175], v[72:75], v[12:15]\n"
  "v_mul_f32 v82, v22, v30\n"
  "v_mul_f32 v83, v23, v31\n"
  "v_mfma_f32_16x16x32_bf16 v[12:15], a[188:191], v[76:79], v[12:15]\n"
  "s_waitcnt lgkmcnt(0)\n"
  "v_add_f32 v80, 1.0, v80\n"
  "v_add_f32 v81, 1.0, v81\n"
  "v_mfma_f32_16x16x32_bf16 v[144:147], v[112:115], v[124:127], v[144:147]\n"
  "ds_read_b128 v[32:35], v89 offset:28672\n"
  "v_mfma_f32_16x16x32_bf16 v[148:151], v[112:115], v[128:131], v[148:151]\n"
  "ds_read_b128 v[36:39], v89 offset:29696\n"
  "v_mfma_f32_16x16x32_bf16 v[152:155], v[112:115], v[132:135], v[152:155]\n"
  "ds_read_b128 v[40:43], v89 offset:30720\n"
  "v_mfma_f32_16x16x32_bf16 v[156:159], v[112:115], v[136:139], v[156:159]\n"
  "v_rcp_f32 v80, v80\n"
  "v_mfma_f32_16x16x32_bf16 v[160:163], v[112:115], v[140:143], v[160:163]\n"
  "ds_read_b128 v[44:47], v89 offset:31744\n"
  "v_mfma_f32_16x16x32_bf16 v[164:167], v[116:119], v[124:127], v[164:167]\n"
  "ds_read_b128 v[48:51], v91 offset:28672\n"
  "v_mfma_f32_16x16x32_bf16 v[168:171], v[116:119], v[128:131], v[168:171]\n"
  "ds_read_b128 v[52:55], v91 offset:29696\n"
  "v_mfma_f32_16x16x32_bf16 v[172:175], v[116:119], v[132:135], v[172:175]\n"
  "v_rcp_f32 v81, v81\n"
  "v_mfma_f32_16x16x32_bf16 v[176:179], v[116:119], v[136:139], v[176:179]\n"
  "v_mul_f32 v82, v82, v80\n"
  "v_mul_f32 v83, v83, v81\n"
  "v_mfma_f32_16x16x32_bf16 v[180:183], v[116:119], v[140:143], v[180:183]\n"
  "v_cvt_pk_bf16_f32 v87, v82, v83\n"
  "v_mfma_f32_16x16x32_bf16 v[184:187], v[120:123], v[124:127], v[184:187]\n"
  "v_mfma_f32_16x16x32_bf16 v[188:191], v[120:123], v[128:131], v[188:191]\n"
  "v_mfma_f32_16x16x32_bf16 v[192:195], v[120:123], v[132:135], v[192:195]\n"
  "v_mfma_f32_16x16x32_bf16 v[196:199], v[120:123], v[136:139], v[196:199]\n"
  "v_mfma_f32_16x16x32_bf16 v[200:203], v[120:123], v[140:143], v[200:203]\n"
  "s_waitcnt vmcnt(10) lgkmcnt(0)\n"
  "s_barrier\n"
  "v_mfma_f32_16x16x32_bf16 v[0:3], a[192:195], v[32:35], v[0:3]\n"
  "s_add_u32 m0, s50, 155648\n"
  "ds_read_b128 v[56:59], v89 offset:0\n"
  "v_mfma_f32_16x16x32_bf16 v[0:3], a[208:211], v[36:39], v[0:3]\n"
  "buffer_load_dwordx4 v98, s[36:39], s59 offen sc1 lds\n"
  "v_mfma_f32_16x16x32_bf16 v[0:3], a[224:227], v[40:43], v[0:3]\n"
  "ds_read_b128 v[60:63], v89 offset:1024\n"
  "v_mfma_f32_16x16x32_bf16 v[0:3], a[240:243], v[44:47], v[0:3]\n"
  "v_mfma_f32_16x16x32_bf16 v[0:3], v[224:227], v[48:51], v[0:3]\n"
  "v_mfma_f32_16x16x32_bf16 v[0:3], v[240:243], v[52:55], v[0:3]\n"
  "s_add_u32 m0, s48, 114688\n"
  "ds_read_b128 v[64:67], v89 offset:2048\n"
  "v_mfma_f32_16x16x32_bf16 v[4:7], a[196:199], v[32:35], v[4:7]\n"
  "buffer_load_dwordx4 v97, s[36:39], s59 offen offset:3072 sc1 lds\n"
  "v_mfma_f32_16x16x32_bf16 v[4:7], a[212:215], v[36:39], v[4:7]\n"
  "s_add_u32 s59, s59, 1024\n"
  "v_mfma_f32_16x16x32_bf16 v[4:7], a[228:231], v[40:43], v[4:7]\n"
  "ds_read_b128 v[68:71], v89 offset:3072\n"
  "v_mfma_f32_16x16x32_bf16 v[4:7], a[244:247], v[44:47], v[4:7]\n"
  "v_mfma_f32_16x16x32_bf16 v[4:7], v[228:231], v[48:51], v[4:7]\n"
  "v_mfma_f32_16x16x32_bf16 v[4:7], v[244:247], v[52:55], v[4:7]\n"
  "buffer_load_dwordx4 v94, s[40:43], s60 offen offset:0 lds\n"
  "ds_read_b128 v[72:75], v89 offset:4096\n"
  "v_mfma_f32_16x16x32_bf16 v[8:11], a[200:203], v[32:35], v[8:11]\n"
  "v_mfma_f32_16x16x32_bf16 v[8:11], a[216:219], v[36:39], v[8:11]\n"
  "v_mfma_f32_16x16x32_bf16 v[8:11], a[232:235], v[40:43], v[8:11]\n"
  "ds_read_b128 v[76:79], v89 offset:5120\n"
  "v_mfma_f32_16x16x32_bf16 v[8:11], a[248:251], v[44:47], v[8:11]\n"
  "v_mfma_f32_16x16x32_bf16 v[8:11], v[232:235], v[48:51], v[8:11]\n"
  "buffer_load_dwordx4 v95, s[40:43], s60 offen offset:1024 lds\n"
  "v_mfma_f32_16x16x32_bf16 v[8:11], v[248:251], v[52:55], v[8:11]\n"
  "v_mfma_f32_16x16x32_bf16 v[12:15], a[204:207], v[32:35], v[12:15]\n"
  "v_mfma_f32_16x16x32_bf16 v[12:15], a[220:223], v[36:39], v[12:15]\n"
  "v_mfma_f32_16x16x32_bf16 v[12:15], a[236:239], v[40:43], v[12:15]\n"
  "v_mfma_f32_16x16x32_bf16 v[12:15], a[252:255], v[44:47], v[12:15]\n"
  "buffer_load_dwordx4 v96, s[40:43], s60 offen offset:2048 lds\n"
  "v_mfma_f32_16x16x32_bf16 v[12:15], v[236:239], v[48:51], v[12:15]\n"
  "s_add_u32 s60, s60, 36864\n"
  "buffer_store_dwordx4 v[84:87], v89, s[36:39], s55 offen sc1\n"
  "v_mfma_f32_16x16x32_bf16 v[12:15], v[252:255], v[52:55], v[12:15]\n"
  "s_waitcnt lgkmcnt(0)\n"
  "v_mfma_f32_16x16x32_bf16 v[16:19], a[0:3], v[56:59], 0\n"
  "s_add_u32 s55, s55, 147456\n"
  "ds_read_b64_tr_b16 v[112:113], v99 offset:49152\n"
  "v_mfma_f32_16x16x32_bf16 v[16:19], a[16:19], v[60:63], v[16:19]\n"
  "s_add_u32 m0, s48, 16384\n"
  "ds_read_b64_tr_b16 v[114:115], v99 offset:57344\n"
  "v_mul_f32 v80, v0, v0\n"
  "v_mfma_f32_16x16x32_bf16 v[16:19], a[32:35], v[64:67], v[16:19]\n"
  "s_cmp_eq_u32 s55, s57\n"
  "s_cselect_b32 s55, s56, s55\n"
  "buffer_load_dwordx4 v97, s[36:39], s59 offen offset:3072 sc1 lds\n"
  "v_mfma_f32_16x16x32_bf16 v[16:19], a[48:51], v[68:71], v[16:19]\n"
  "s_add_u32 s59, s59, 1024\n"
  "ds_read_b128 v[124:127], v89 offset:52224\n"
  "v_mul_f32 v81, v1, v1\n"
  "v_mfma_f32_16x16x32_bf16 v[16:19], a[64:67], v[72:75], v[16:19]\n"
  "ds_read_b128 v[128:131], v89 offset:56320\n"
  "v_fmaak_f32 v80, v88, v80, 0xc0135761\n"
  "v_mfma_f32_16x16x32_bf16 v[16:19], a[80:83], v[76:79], v[16:19]\n"
  "v_fmaak_f32 v81, v88, v81, 0xc0135761\n"
  "v_mul_f32 v80, v0, v80\n"
  "v_mfma_f32_16x16x32_bf16 v[20:23], a[4:7], v[56:59], 0\n"
  "ds_read_b128 v[132:135], v89 offset:60416\n"
  "v_mul_f32 v81, v1, v81\n"
  "v_mfma_f32_16x16x32_bf16 v[20:23], a[20:23], v[60:63], v[20:23]\n"
  "buffer_load_dwordx4 v94, s[40:43], s60 offen offset:0 lds\n"
  "ds_read_b128 v[136:139], v89 offset:64512\n"
  "v_mfma_f32_16x16x32_bf16 v[20:23], a[36:39], v[64:67], v[20:23]\n"
  "v_exp_f32 v80, v80\n"
  "v_mfma_f32_16x16x32_bf16 v[20:23], a[52:55], v[68:71], v[20:23]\n"
  "ds_read_b128 v[140:143], v91 offset:16384\n"
  "v_mfma_f32_16x16x32_bf16 v[20:23], a[68:71], v[72:75], v[20:23]\n"
  "ds_read_b64_tr_b16 v[116:117], v99 offset:50176\n"
  "v_mfma_f32_16x16x32_bf16 v[20:23], a[84:87], v[76:79], v[20:23]\n"
  "v_exp_f32 v81, v81\n"
  "v_mfma_f32_16x16x32_bf16 v[24:27], a[8:11], v[56:59], 0\n"
  "buffer_load_dwordx4 v95, s[40:43], s60 offen offset:1024 lds\n"
  "ds_read_b64_tr_b16 v[118:119], v99 offset:58368\n"
  "v_mfma_f32_16x16x32_bf16 v[24:27], a[24:27], v[60:63], v[24:27]\n"
  "ds_read_b64_tr_b16 v[120:121], v99 offset:51200\n"
  "v_mul_f32 v82, v0, v8\n"
  "v_mfma_f32_16x16x32_bf16 v[24:27], a[40:43], v[64:67], v[24:27]\n"
  "v_mul_f32 v83, v1, v9\n"
  "v_add_f32 v80, 1.0, v80\n"
  "v_mfma_f32_16x16x32_bf16 v[24:27], a[56:59], v[68:71], v[24:27]\n"
  "ds_read_b64_tr_b16 v[122:123], v99 offset:59392\n"
  "v_add_f32 v81, 1.0, v81\n"
  "v_mfma_f32_16x16x32_bf16 v[24:27], a[72:75], v[72:75], v[24:27]\n"
  "v_rcp_f32 v80, v80\n"
  "v_mfma_f32_16x16x32_bf16 v[24:27], a[88:91], v[76:79], v[24:27]\n"
  "buffer_load_dwordx4 v96, s[40:43], s60 offen offset:2048 lds\n"
  "v_mfma_f32_16x16x32_bf16 v[28:31], a[12:15], v[56:59], 0\n"
  "s_add_u32 s60, s60, 36864\n"
  "v_rcp_f32 v81, v81\n"
  "v_mfma_f32_16x16x32_bf16 v[28:31], a[28:31], v[60:63], v[28:31]\n"
  "v_mul_f32 v82, v82, v80\n"
  "v_mul_f32 v83, v83, v81\n"
  "v_mfma_f32_16x16x32_bf16 v[28:31], a[44:47], v[64:67], v[28:31]\n"
  "v_cvt_pk_bf16_f32 v84, v82, v83\n"
  "v_mul_f32 v80, v2, v2\n"
  "v_mfma_f32_16x16x32_bf16 v[28:31], a[60:63], v[68:71], v[28:31]\n"
  "s_add_u32 m0, s48, 32768\n"
  "v_mul_f32 v81, v3, v3\n"
  "v_fmaak_f32 v80, v88, v80, 0xc0135761\n"
  "v_mfma_f32_16x16x32_bf16 v[28:31], a[76:79], v[72:75], v[28:31]\n"
  "buffer_load_dwordx4 v97, s[36:39], s59 offen offset:3072 sc1 lds\n"
  "v_fmaak_f32 v81, v88, v81, 0xc0135761\n"
  "v_mfma_f32_16x16x32_bf16 v[28:31], a[92:95], v[76:79], v[28:31]\n"
  "s_waitcnt lgkmcnt(0)\n"
  "v_mul_f32 v80, v2, v80\n"
  "v_mul_f32 v81, v3, v81\n"
  "v_mfma_f32_16x16x32_bf16 v[144:147], v[112:115], v[124:127], v[144:147]\n"
  "s_add_u32 s59, s59, 1024\n"
  "ds_read_b128 v[32:35], v89 offset:6144\n"
  "v_mfma_f32_16x16x32_bf16 v[148:151], v[112:115], v[128:131], v[148:151]\n"
  "ds_read_b128 v[36:39], v89 offset:7168\n"
  "v_mfma_f32_16x16x32_bf16 v[152:155], v[112:115], v[132:135], v[152:155]\n"
  "ds_read_b128 v[40:43], v89 offset:8192\n"
  "v_mfma_f32_16x16x32_bf16 v[156:159], v[112:115], v[136:139], v[156:159]\n"
  "buffer_load_dwordx4 v94, s[40:43], s60 offen offset:0 lds\n"
  "v_mfma_f32_16x16x32_bf16 v[160:163], v[112:115], v[140:143], v[160:163]\n"
  "ds_read_b128 v[44:47], v89 offset:9216\n"
  "v_mfma_f32_16x16x32_bf16 v[164:167], v[116:119], v[124:127], v[164:167]\n"
  "ds_read_b128 v[48:51], v89 offset:10240\n"
  "v_mfma_f32_16x16x32_bf16 v[168:171], v[116:119], v[128:131], v[168:171]\n"
  "ds_read_b128 v[52:55], v89 offset:11264\n"
  "v_mfma_f32_16x16x32_bf16 v[172:175], v[116:119], v[132:135], v[172:175]\n"
  "v_exp_f32 v80, v80\n"
  "v_mfma_f32_16x16x32_bf16 v[176:179], v[116:119], v[136:139], v[176:179]\n"
  "buffer_load_dwordx4 v95, s[40:43], s60 offen offset:1024 lds\n"
  "v_mfma_f32_16x16x32_bf16 v[180:183], v[116:119], v[140:143], v[180:183]\n"
  "v_exp_f32 v81, v81\n"
  "v_mfma_f32_16x16x32_bf16 v[184:187], v[120:123], v[124:127], v[184:187]\n"
  "v_mul_f32 v82, v2, v10\n"
  "v_mul_f32 v83, v3, v11\n"
  "v_mfma_f32_16x16x32_bf16 v[188:191], v[120:123], v[128:131], v[188:191]\n"
  "v_add_f32 v80, 1.0, v80\n"
  "v_add_f32 v81, 1.0, v81\n"
  "v_mfma_f32_16x16x32_bf16 v[192:195], v[120:123], v[132:135], v[192:195]\n"
  "v_rcp_f32 v80, v80\n"
  "v_mfma_f32_16x16x32_bf16 v[196:199], v[120:123], v[136:139], v[196:199]\n"
  "buffer_load_dwordx4 v96, s[40:43], s60 offen offset:2048 lds\n"
  "v_mfma_f32_16x16x32_bf16 v[200:203], v[120:123], v[140:143], v[200:203]\n"
  "s_waitcnt lgkmcnt(0)\n"
  "v_rcp_f32 v81, v81\n"
  "v_mfma_f32_16x16x32_bf16 v[16:19], a[96:99], v[32:35], v[16:19]\n"
  "s_add_u32 s60, s60, 36864\n"
  "ds_read_b64_tr_b16 v[112:113], v100 offset:0\n"
  "v_mul_f32 v82, v82, v80\n"
  "v_mfma_f32_16x16x32_bf16 v[16:19], a[112:115], v[36:39], v[16:19]\n"
  "ds_read_b64_tr_b16 v[114:115], v100 offset:8192\n"
  "v_mul_f32 v83, v83, v81\n"
  "v_mfma_f32_16x16x32_bf16 v[16:19], a[128:131], v[40:43], v[16:19]\n"
  "s_add_u32 m0, s48, 131072\n"
  "v_cvt_pk_bf16_f32 v85, v82, v83\n"
  "v_mul_f32 v80, v4, v4\n"
  "v_mfma_f32_16x16x32_bf16 v[16:19], a[144:147], v[44:47], v[16:19]\n"
  "buffer_load_dwordx4 v97, s[36:39], s59 offen offset:3072 sc1 lds\n"
  "ds_read_b128 v[124:127], v90 offset:3072\n"
  "v_mfma_f32_16x16x32_bf16 v[16:19], a[160:163], v[48:51], v[16:19]\n"
  "s_add_u32 s59, s59, 1024\n"
  "ds_read_b128 v[128:131], v90 offset:7168\n"
  "v_mul_f32 v81, v5, v5\n"
  "v_mfma_f32_16x16x32_bf16 v[16:19], a[176:179], v[52:55], v[16:19]\n"
  "v_fmaak_f32 v80, v88, v80, 0xc0135761\n"
  "v_fmaak_f32 v81, v88, v81, 0xc0135761\n"
  "v_mfma_f32_16x16x32_bf16 v[20:23], a[100:103], v[32:35], v[20:23]\n"
  "ds_read_b128 v[132:135], v90 offset:11264\n"
  "v_mul_f32 v80, v4, v80\n"
  "v_mfma_f32_16x16x32_bf16 v[20:23], a[116:119], v[36:39], v[20:23]\n"
  "ds_read_b128 v[136:139], v90 offset:15360\n"
  "v_mul_f32 v81, v5, v81\n"
  "v_mfma_f32_16x16x32_bf16 v[20:23], a[132:135], v[40:43], v[20:23]\n"
  "buffer_load_dwordx4 v94, s[40:43], s60 offen offset:0 lds\n"
  "v_mfma_f32_16x16x32_bf16 v[20:23], a[148:151], v[44:47], v[20:23]\n"
  "ds_read_b128 v[140:143], v91 offset:17408\n"
  "v_mfma_f32_16x16x32_bf16 v[20:23], a[164:167], v[48:51], v[20:23]\n"
  "ds_read_b64_tr_b16 v[116:117], v100 offset:1024\n"
  "v_mfma_f32_16x16x32_bf16 v[20:23], a[180:183], v[52:55], v[20:23]\n"
  "v_exp_f32 v80, v80\n"
  "v_mfma_f32_16x16x32_bf16 v[24:27], a[104:107], v[32:35], v[24:27]\n"
  "ds_read_b64_tr_b16 v[118:119], v100 offset:9216\n"
  "v_mfma_f32_16x16x32_bf16 v[24:27], a[120:123], v[36:39], v[24:27]\n"
  "buffer_load_dwordx4 v95, s[40:43], s60 offen offset:1024 lds\n"
  "ds_read_b64_tr_b16 v[120:121], v100 offset:2048\n"
  "v_mfma_f32_16x16x32_bf16 v[24:27], a[136:139], v[40:43], v[24:27]\n"
  "v_exp_f32 v81, v81\n"
  "v_mfma_f32_16x16x32_bf16 v[24:27], a[152:155], v[44:47], v[24:27]\n"
  "ds_read_b64_tr_b16 v[122:123], v100 offset:10240\n"
  "v_mul_f32 v82, v4, v12\n"
  "v_mfma_f32_16x16x32_bf16 v[24:27], a[168:171], v[48:51], v[24:27]\n"
  "v_mul_f32 v83, v5, v13\n"
  "v_add_f32 v80, 1.0, v80\n"
  "v_mfma_f32_16x16x32_bf16 v[24:27], a[184:187], v[52:55], v[24:27]\n"
  "v_add_f32 v81, 1.0, v81\n"
  "v_mfma_f32_16x16x32_bf16 v[28:31], a[108:111], v[32:35], v[28:31]\n"
  "buffer_load_dwordx4 v96, s[40:43], s60 offen offset:2048 lds\n"
  "v_mfma_f32_16x16x32_bf16 v[28:31], a[124:127], v[36:39], v[28:31]\n"
  "s_add_u32 s60, s60, 36864\n"
  "v_rcp_f32 v80, v80\n"
  "v_mfma_f32_16x16x32_bf16 v[28:31], a[140:143], v[40:43], v[28:31]\n"
  "v_rcp_f32 v81, v81\n"
  "v_mfma_f32_16x16x32_bf16 v[28:31], a[156:159], v[44:47], v[28:31]\n"
  "v_mul_f32 v82, v82, v80\n"
  "v_mul_f32 v83, v83, v81\n"
  "v_mfma_f32_16x16x32_bf16 v[28:31], a[172:175], v[48:51], v[28:31]\n"
  "v_cvt_pk_bf16_f32 v86, v82, v83\n"
  "v_mul_f32 v80, v6, v6\n"
  "v_mfma_f32_16x16x32_bf16 v[28:31], a[188:191], v[52:55], v[28:31]\n"
  "s_waitcnt lgkmcnt(0)\n"
  "v_mul_f32 v81, v7, v7\n"
  "v_fmaak_f32 v80, v88, v80, 0xc0135761\n"
  "v_mfma_f32_16x16x32_bf16 v[144:147], v[112:115], v[124:127], v[144:147]\n"
  "ds_read_b128 v[56:59], v89 offset:12288\n"
  "v_fmaak_f32 v81, v88, v81, 0xc0135761\n"
  "v_mfma_f32_16x16x32_bf16 v[148:151], v[112:115], v[128:131], v[148:151]\n"
  "ds_read_b128 v[60:63], v89 offset:13312\n"
  "v_mul_f32 v80, v6, v80\n"
  "v_mfma_f32_16x16x32_bf16 v[152:155], v[112:115], v[132:135], v[152:155]\n"
  "ds_read_b128 v[64:67], v89 offset:14336\n"
  "v_mul_f32 v81, v7, v81\n"
  "v_mfma_f32_16x16x32_bf16 v[156:159], v[112:115], v[136:139], v[156:159]\n"
  "v_exp_f32 v80, v80\n"
  "v_mfma_f32_16x16x32_bf16 v[160:163], v[112:115], v[140:143], v[160:163]\n"
  "ds_read_b128 v[68:71], v89 offset:15360\n"
  "v_mfma_f32_16x16x32_bf16 v[164:167], v[116:119], v[124:127], v[164:167]\n"
  "ds_read_b128 v[72:75], v91 offset:22528\n"
  "v_mfma_f32_16x16x32_bf16 v[168:171], v[116:119], v[128:131], v[168:171]\n"
  "ds_read_b128 v[76:79], v91 offset:23552\n"
  "v_mfma_f32_16x16x32_bf16 v[172:175], v[116:119], v[132:135], v[172:175]\n"
  "v_exp_f32 v81, v81\n"
  "v_mfma_f32_16x16x32_bf16 v[176:179], v[116:119], v[136:139], v[176:179]\n"
  "v_mul_f32 v82, v6, v14\n"
  "v_mul_f32 v83, v7, v15\n"
  "v_mfma_f32_16x16x32_bf16 v[180:183], v[116:119], v[140:143], v[180:183]\n"
  "v_add_f32 v80, 1.0, v80\n"
  "v_add_f32 v81, 1.0, v81\n"
  "v_mfma_f32_16x16x32_bf16 v[184:187], v[120:123], v[124:127], v[184:187]\n"
  "v_rcp_f32 v80, v80\n"
  "v_mfma_f32_16x16x32_bf16 v[188:191], v[120:123], v[128:131], v[188:191]\n"
  "v_rcp_f32 v81, v81\n"
  "v_mfma_f32_16x16x32_bf16 v[192:195], v[120:123], v[132:135], v[192:195]\n"
  "v_mul_f32 v82, v82, v80\n"
  "v_mul_f32 v83, v83, v81\n"
  "v_mfma_f32_16x16x32_bf16 v[196:199], v[120:123], v[136:139], v[196:199]\n"
  "v_cvt_pk_bf16_f32 v87, v82, v83\n"
  "v_mfma_f32_16x16x32_bf16 v[200:203], v[120:123], v[140:143], v[200:203]\n"
  "s_waitcnt vmcnt(23) lgkmcnt(0)\n"
  "s_barrier\n"
  "v_mfma_f32_16x16x32_bf16 v[16:19], a[192:195], v[56:59], v[16:19]\n"
  "s_add_u32 m0, s50, 159744\n"
  "ds_read_b64_tr_b16 v[112:113], v100 offset:16384\n"
  "v_mfma_f32_16x16x32_bf16 v[16:19], a[208:211], v[60:63], v[16:19]\n"
  "buffer_load_dwordx4 v98, s[36:39], s59 offen sc1 lds\n"
  "ds_read_b64_tr_b16 v[114:115], v100 offset:24576\n"
  "v_mfma_f32_16x16x32_bf16 v[16:19], a[224:227], v[64:67], v[16:19]\n"
  "v_mfma_f32_16x16x32_bf16 v[16:19], a[240:243], v[68:71], v[16:19]\n"
  "ds_read_b128 v[124:127], v90 offset:19456\n"
  "v_mfma_f32_16x16x32_bf16 v[16:19], v[224:227], v[72:75], v[16:19]\n"
  "ds_read_b128 v[128:131], v90 offset:23552\n"
  "v_mfma_f32_16x16x32_bf16 v[16:19], v[240:243], v[76:79], v[16:19]\n"
  "s_add_u32 m0, s48, 0\n"
  "v_mfma_f32_16x16x32_bf16 v[20:23], a[196:199], v[56:59], v[20:23]\n"
  "buffer_load_dwordx4 v97, s[36:39], s59 offen offset:3072 sc1 lds\n"
  "ds_read_b128 v[132:135], v90 offset:27648\n"
  "v_mfma_f32_16x16x32_bf16 v[20:23], a[212:215], v[60:63], v[20:23]\n"
  "s_add_u32 s59, s59, 1024\n"
  "ds_read_b128 v[136:139], v90 offset:31744\n"
  "v_mfma_f32_16x16x32_bf16 v[20:23], a[228:231], v[64:67], v[20:23]\n"
  "v_mfma_f32_16x16x32_bf16 v[20:23], a[244:247], v[68:71], v[20:23]\n"
  "ds_read_b128 v[140:143], v91 offset:18432\n"
  "v_mfma_f32_16x16x32_bf16 v[20:23], v[228:231], v[72:75], v[20:23]\n"
  "ds_read_b64_tr_b16 v[116:117], v100 offset:17408\n"
  "v_mfma_f32_16x16x32_bf16 v[20:23], v[244:247], v[76:79], v[20:23]\n"
  "buffer_load_dwordx4 v94, s[40:43], s60 offen offset:0 lds\n"
  "v_mfma_f32_16x16x32_bf16 v[24:27], a[200:203], v[56:59], v[24:27]\n"
  "ds_read_b64_tr_b16 v[118:119], v100 offset:25600\n"
  "v_mfma_f32_16x16x32_bf16 v[24:27], a[216:219], v[60:63], v[24:27]\n"
  "ds_read_b64_tr_b16 v[120:121], v100 offset:18432\n"
  "v_mfma_f32_16x16x32_bf16 v[24:27], a[232:235], v[64:67], v[24:27]\n"
  "v_mfma_f32_16x16x32_bf16 v[24:27], a[248:251], v[68:71], v[24:27]\n"
  "ds_read_b64_tr_b16 v[122:123], v100 offset:26624\n"
  "v_mfma_f32_16x16x32_bf16 v[24:27], v[232:235], v[72:75], v[24:27]\n"
  "buffer_load_dwordx4 v95, s[40:43], s60 offen offset:1024 lds\n"
  "v_mfma_f32_16x16x32_bf16 v[24:27], v[248:251], v[76:79], v[24:27]\n"
  "v_mfma_f32_16x16x32_bf16 v[28:31], a[204:207], v[56:59], v[28:31]\n"
  "v_mfma_f32_16x16x32_bf16 v[28:31], a[220:223], v[60:63], v[28:31]\n"
  "v_mfma_f32_16x16x32_bf16 v[28:31], a[236:239], v[64:67], v[28:31]\n"
  "v_mfma_f32_16x16x32_bf16 v[28:31], a[252:255], v[68:71], v[28:31]\n"
  "buffer_load_dwordx4 v96, s[40:43], s60 offen offset:2048 lds\n"
  "v_mfma_f32_16x16x32_bf16 v[28:31], v[236:239], v[72:75], v[28:31]\n"
  "s_add_u32 s60, s60, 36864\n"
  "buffer_store_dwordx4 v[84:87], v89, s[36:39], s55 offen sc1\n"
  "v_mfma_f32_16x16x32_bf16 v[28:31], v[252:255], v[76:79], v[28:31]\n"
  "s_waitcnt vmcnt(24) lgkmcnt(0)\n"
  "s_barrier\n"
  "v_mfma_f32_16x16x32_bf16 v[144:147], v[112:115], v[124:127], v[144:147]\n"
  "s_add_u32 s55, s55, 147456\n"
  "ds_read_b64_tr_b16 v[32:33], v100 offset:32768\n"
  "ds_read_b64_tr_b16 v[34:35], v100 offset:40960\n"
  "v_mfma_f32_16x16x32_bf16 v[148:151], v[112:115], v[128:131], v[148:151]\n"
  "s_add_u32 m0, s48, 49152\n"
  "ds_read_b128 v[44:47], v90 offset:35840\n"
  "v_mul_f32 v80, v16, v16\n"
  "v_mul_f32 v81, v17, v17\n"
  "v_fmaak_f32 v80, v88, v80, 0xc0135761\n"
  "v_mfma_f32_16x16x32_bf16 v[152:155], v[112:115], v[132:135], v[152:155]\n"
  "s_cmp_eq_u32 s55, s57\n"
  "s_cselect_b32 s55, s56, s55\n"
  "buffer_load_dwordx4 v97, s[36:39], s59 offen offset:3072 sc1 lds\n"
  "ds_read_b128 v[48:51], v90 offset:39936\n"
  "v_fmaak_f32 v81, v88, v81, 0xc0135761\n"
  "v_mfma_f32_16x16x32_bf16 v[156:159], v[112:115], v[136:139], v[156:159]\n"
  "s_add_u32 s59, s59, 1024\n"
  "ds_read_b128 v[52:55], v90 offset:44032\n"
  "v_mul_f32 v80, v16, v80\n"
  "v_mul_f32 v81, v17, v81\n"
  "v_mfma_f32_16x16x32_bf16 v[160:163], v[112:115], v[140:143], v[160:163]\n"
  "ds_read_b128 v[56:59], v90 offset:48128\n"
  "v_exp_f32 v80, v80\n"
  "v_mfma_f32_16x16x32_bf16 v[164:167], v[116:119], v[124:127], v[164:167]\n"
  "ds_read_b128 v[60:63], v91 offset:19456\n"
  "v_exp_f32 v81, v81\n"
  "v_mul_f32 v82, v16, v24\n"
  "v_mfma_f32_16x16x32_bf16 v[168:171], v[116:119], v[128:131], v[168:171]\n"
  "ds_read_b64_tr_b16 v[36:37], v100 offset:33792\n"
  "v_mul_f32 v83, v17, v25\n"
  "v_add_f32 v80, 1.0, v80\n"
  "v_add_f32 v81, 1.0, v81\n"
  "v_mfma_f32_16x16x32_bf16 v[172:175], v[116:119], v[132:135], v[172:175]\n"
  "buffer_load_dwordx4 v94, s[40:43], s60 offen offset:0 lds\n"
  "ds_read_b64_tr_b16 v[38:39], v100 offset:41984\n"
  "v_rcp_f32 v80, v80\n"
  "v_mfma_f32_16x16x32_bf16 v[176:179], v[116:119], v[136:139], v[176:179]\n"
  "ds_read_b64_tr_b16 v[40:41], v100 offset:34816\n"
  "v_rcp_f32 v81, v81\n"
  "v_mul_f32 v82, v82, v80\n"
  "v_mfma_f32_16x16x32_bf16 v[180:183], v[116:119], v[140:143], v[180:183]\n"
  "ds_read_b64_tr_b16 v[42:43], v100 offset:43008\n"
  "s_cbranch_execz Ltramp%=\n"
  "v_mul_f32 v83, v83, v81\n"
  "v_cvt_pk_bf16_f32 v84, v82, v83\n"
  "v_mul_f32 v80, v18, v18\n"
  "v_mfma_f32_16x16x32_bf16 v[184:187], v[120:123], v[124:127], v[184:187]\n"
  "v_mul_f32 v81, v19, v19\n"
  "v_fmaak_f32 v80, v88, v80, 0xc0135761\n"
  "v_fmaak_f32 v81, v88, v81, 0xc0135761\n"
  "v_mul_f32 v80, v18, v80\n"
  "v_mfma_f32_16x16x32_bf16 v[188:191], v[120:123], v[128:131], v[188:191]\n"
  "v_mul_f32 v81, v19, v81\n"
  "v_exp_f32 v80, v80\n"
  "v_mfma_f32_16x16x32_bf16 v[192:195], v[120:123], v[132:135], v[192:195]\n"
  "buffer_load_dwordx4 v95, s[40:43], s60 offen offset:1024 lds\n"
  "v_exp_f32 v81, v81\n"
  "v_mul_f32 v82, v18, v26\n"
  "v_mfma_f32_16x16x32_bf16 v[196:199], v[120:123], v[136:139], v[196:199]\n"
  "v_mul_f32 v83, v19, v27\n"
  "v_add_f32 v80, 1.0, v80\n"
  "v_add_f32 v81, 1.0, v81\n"
  "v_mfma_f32_16x16x32_bf16 v[200:203], v[120:123], v[140:143], v[200:203]\n"
  "s_waitcnt vmcnt(22) lgkmcnt(0)\n"
  "s_barrier\n"
  "v_mfma_f32_16x16x32_bf16 v[144:147], v[32:35], v[44:47], v[144:147]\n"
  "ds_read_b64_tr_b16 v[112:113], v100 offset:49152\n"
  "ds_read_b64_tr_b16 v[114:115], v100 offset:57344\n"
  "v_rcp_f32 v80, v80\n"
  "v_mfma_f32_16x16x32_bf16 v[148:151], v[32:35], v[48:51], v[148:151]\n"
  "ds_read_b128 v[124:127], v90 offset:52224\n"
  "v_rcp_f32 v81, v81\n"
  "v_mul_f32 v82, v82, v80\n"
  "v_mfma_f32_16x16x32_bf16 v[152:155], v[32:35], v[52:55], v[152:155]\n"
  "buffer_load_dwordx4 v96, s[40:43], s60 offen offset:2048 lds\n"
  "ds_read_b128 v[128:131], v90 offset:56320\n"
  "v_mul_f32 v83, v83, v81\n"
  "v_cvt_pk_bf16_f32 v85, v82, v83\n"
  "v_mfma_f32_16x16x32_bf16 v[156:159], v[32:35], v[56:59], v[156:159]\n"
  "s_add_u32 s60, s60, 36864\n"
  "ds_read_b128 v[132:135], v90 offset:60416\n"
  "v_mul_f32 v80, v20, v20\n"
  "v_mul_f32 v81, v21, v21\n"
  "v_fmaak_f32 v80, v88, v80, 0xc0135761\n"
  "v_mfma_f32_16x16x32_bf16 v[160:163], v[32:35], v[60:63], v[160:163]\n"
  "ds_read_b128 v[136:139], v90 offset:64512\n"
  "v_fmaak_f32 v81, v88, v81, 0xc0135761\n"
  "v_mul_f32 v80, v20, v80\n"
  "v_mul_f32 v81, v21, v81\n"
  "v_mfma_f32_16x16x32_bf16 v[164:167], v[36:39], v[44:47], v[164:167]\n"
  "ds_read_b128 v[140:143], v91 offset:24576\n"
  "v_exp_f32 v80, v80\n"
  "v_mfma_f32_16x16x32_bf16 v[168:171], v[36:39], v[48:51], v[168:171]\n"
  "s_add_u32 m0, s48, 65536\n"
  "ds_read_b64_tr_b16 v[116:117], v100 offset:50176\n"
  "v_exp_f32 v81, v81\n"
  "v_mul_f32 v82, v20, v28\n"
  "v_mfma_f32_16x16x32_bf16 v[172:175], v[36:39], v[52:55], v[172:175]\n"
  "buffer_load_dwordx4 v97, s[36:39], s59 offen offset:3072 sc1 lds\n"
  "ds_read_b64_tr_b16 v[118:119], v100 offset:58368\n"
  "v_mul_f32 v83, v21, v29\n"
  "v_add_f32 v80, 1.0, v80\n"
  "v_mfma_f32_16x16x32_bf16 v[176:179], v[36:39], v[56:59], v[176:179]\n"
  "s_add_u32 s59, s59, 1024\n"
  "ds_read_b64_tr_b16 v[120:121], v100 offset:51200\n"
  "s_mov_b64 exec, 7\n"
  "buffer_load_dword v106, v105, s[24:27], 0 offen sc1\n"
  "s_mov_b64 exec, -1\n"
  "v_mfma_f32_16x16x32_bf16 v[180:183], v[36:39], v[60:63], v[180:183]\n"
  "ds_read_b64_tr_b16 v[122:123], v100 offset:59392\n"
  "v_add_f32 v81, 1.0, v81\n"
  "v_rcp_f32 v80, v80\n"
  "v_mfma_f32_16x16x32_bf16 v[184:187], v[40:43], v[44:47], v[184:187]\n"
  "v_rcp_f32 v81, v81\n"
  "v_mul_f32 v82, v82, v80\n"
  "v_mul_f32 v83, v83, v81\n"
  "v_mfma_f32_16x16x32_bf16 v[188:191], v[40:43], v[48:51], v[188:191]\n"
  "v_cvt_pk_bf16_f32 v86, v82, v83\n"
  "v_mul_f32 v80, v22, v22\n"
  "v_mul_f32 v81, v23, v23\n"
  "v_fmaak_f32 v80, v88, v80, 0xc0135761\n"
  "v_mfma_f32_16x16x32_bf16 v[192:195], v[40:43], v[52:55], v[192:195]\n"
  "buffer_load_dwordx4 v94, s[40:43], s60 offen offset:0 lds\n"
  "v_fmaak_f32 v81, v88, v81, 0xc0135761\n"
  "v_mul_f32 v80, v22, v80\n"
  "v_mul_f32 v81, v23, v81\n"
  "v_mfma_f32_16x16x32_bf16 v[196:199], v[40:43], v[56:59], v[196:199]\n"
  "v_exp_f32 v80, v80\n"
  "v_exp_f32 v81, v81\n"
  "v_mfma_f32_16x16x32_bf16 v[200:203], v[40:43], v[60:63], v[200:203]\n"
  "s_waitcnt vmcnt(21) lgkmcnt(0)\n"
  "s_barrier\n"
  "v_mfma_f32_16x16x32_bf16 v[144:147], v[112:115], v[124:127], v[144:147]\n"
  "ds_read_b64_tr_b16 v[32:33], v99 offset:16384\n"
  "ds_read_b64_tr_b16 v[34:35], v99 offset:24576\n"
  "v_mul_f32 v82, v22, v30\n"
  "v_mul_f32 v83, v23, v31\n"
  "v_mfma_f32_16x16x32_bf16 v[148:151], v[112:115], v[128:131], v[148:151]\n"
  "ds_read_b128 v[44:47], v89 offset:19456\n"
  "v_add_f32 v80, 1.0, v80\n"
  "v_add_f32 v81, 1.0, v81\n"
  "v_mfma_f32_16x16x32_bf16 v[152:155], v[112:115], v[132:135], v[152:155]\n"
  "buffer_load_dwordx4 v95, s[40:43], s60 offen offset:1024 lds\n"
  "ds_read_b128 v[48:51], v89 offset:23552\n"
  "v_rcp_f32 v80, v80\n"
  "v_mfma_f32_16x16x32_bf16 v[156:159], v[112:115], v[136:139], v[156:159]\n"
  "ds_read_b128 v[52:55], v89 offset:27648\n"
  "v_rcp_f32 v81, v81\n"
  "v_mul_f32 v82, v82, v80\n"
  "v_mfma_f32_16x16x32_bf16 v[160:163], v[112:115], v[140:143], v[160:163]\n"
  "ds_read_b128 v[56:59], v89 offset:31744\n"
  "v_mul_f32 v83, v83, v81\n"
  "v_cvt_pk_bf16_f32 v87, v82, v83\n"
  "v_mfma_f32_16x16x32_bf16 v[164:167], v[116:119], v[124:127], v[164:167]\n"
  "ds_read_b128 v[60:63], v91 offset:25600\n"
  "v_mfma_f32_16x16x32_bf16 v[168:171], v[116:119], v[128:131], v[168:171]\n"
  "ds_read_b64_tr_b16 v[36:37], v99 offset:17408\n"
  "v_mfma_f32_16x16x32_bf16 v[172:175], v[116:119], v[132:135], v[172:175]\n"
  "buffer_load_dwordx4 v96, s[40:43], s60 offen offset:2048 lds\n"
  "ds_read_b64_tr_b16 v[38:39], v99 offset:25600\n"
  "v_mfma_f32_16x16x32_bf16 v[176:179], v[116:119], v[136:139], v[176:179]\n"
  "s_add_u32 s60, s60, 36864\n"
  "ds_read_b64_tr_b16 v[40:41], v99 offset:18432\n"
  "v_mfma_f32_16x16x32_bf16 v[180:183], v[116:119], v[140:143], v[180:183]\n"
  "ds_read_b64_tr_b16 v[42:43], v99 offset:26624\n"
  "v_mfma_f32_16x16x32_bf16 v[184:187], v[120:123], v[124:127], v[184:187]\n"
  "v_mfma_f32_16x16x32_bf16 v[188:191], v[120:123], v[128:131], v[188:191]\n"
  "s_add_u32 m0, s48, 81920\n"
  "v_mfma_f32_16x16x32_bf16 v[192:195], v[120:123], v[132:135], v[192:195]\n"
  "buffer_load_dwordx4 v97, s[36:39], s59 offen offset:3072 sc1 lds\n"
  "v_mfma_f32_16x16x32_bf16 v[196:199], v[120:123], v[136:139], v[196:199]\n"
  "s_add_u32 s59, s59, 25600\n"
  "v_mfma_f32_16x16x32_bf16 v[200:203], v[120:123], v[140:143], v[200:203]\n"
  "s_waitcnt vmcnt(20) lgkmcnt(0)\n"
  "s_barrier\n"
  "v_mfma_f32_16x16x32_bf16 v[144:147], v[32:35], v[44:47], v[144:147]\n"
  "ds_read_b64_tr_b16 v[112:113], v99 offset:32768\n"
  "ds_read_b64_tr_b16 v[114:115], v99 offset:40960\n"
  "v_mfma_f32_16x16x32_bf16 v[148:151], v[32:35], v[48:51], v[148:151]\n"
  "buffer_store_dwordx4 v[84:87], v89, s[36:39], s55 offen sc1\n"
  "ds_read_b128 v[124:127], v89 offset:35840\n"
  "v_mfma_f32_16x16x32_bf16 v[152:155], v[32:35], v[52:55], v[152:155]\n"
  "buffer_load_dwordx4 v94, s[40:43], s60 offen offset:0 lds\n"
  "ds_read_b128 v[128:131], v89 offset:39936\n"
  "v_mfma_f32_16x16x32_bf16 v[156:159], v[32:35], v[56:59], v[156:159]\n"
  "ds_read_b128 v[132:135], v89 offset:44032\n"
  "v_mfma_f32_16x16x32_bf16 v[160:163], v[32:35], v[60:63], v[160:163]\n"
  "ds_read_b128 v[136:139], v89 offset:48128\n"
  "v_mfma_f32_16x16x32_bf16 v[164:167], v[36:39], v[44:47], v[164:167]\n"
  "ds_read_b128 v[140:143], v91 offset:26624\n"
  "v_mfma_f32_16x16x32_bf16 v[168:171], v[36:39], v[48:51], v[168:171]\n"
  "ds_read_b64_tr_b16 v[116:117], v99 offset:33792\n"
  "v_mfma_f32_16x16x32_bf16 v[172:175], v[36:39], v[52:55], v[172:175]\n"
  "buffer_load_dwordx4 v95, s[40:43], s60 offen offset:1024 lds\n"
  "ds_read_b64_tr_b16 v[118:119], v99 offset:41984\n"
  "v_mfma_f32_16x16x32_bf16 v[176:179], v[36:39], v[56:59], v[176:179]\n"
  "ds_read_b64_tr_b16 v[120:121], v99 offset:34816\n"
  "v_mfma_f32_16x16x32_bf16 v[180:183], v[36:39], v[60:63], v[180:183]\n"
  "ds_read_b64_tr_b16 v[122:123], v99 offset:43008\n"
  "v_mfma_f32_16x16x32_bf16 v[184:187], v[40:43], v[44:47], v[184:187]\n"
  "v_mfma_f32_16x16x32_bf16 v[188:191], v[40:43], v[48:51], v[188:191]\n"
  "v_mfma_f32_16x16x32_bf16 v[192:195], v[40:43], v[52:55], v[192:195]\n"
  "buffer_load_dwordx4 v96, s[40:43], s60 offen offset:2048 lds\n"
  "v_mfma_f32_16x16x32_bf16 v[196:199], v[40:43], v[56:59], v[196:199]\n"
  "s_add_u32 s60, s60, 921600\n"
  "v_mfma_f32_16x16x32_bf16 v[200:203], v[40:43], v[60:63], v[200:203]\n"
  "s_waitcnt vmcnt(20) lgkmcnt(0)\n"
  "s_barrier\n"
  "v_mfma_f32_16x16x32_bf16 v[144:147], v[112:115], v[124:127], v[144:147]\n"
  "s_cmp_ge_u32 s60, 5308416\n"
  "s_cselect_b32 s64, 5308416, 0\n"
  "s_cselect_b32 s65, 147456, 0\n"
  "s_sub_u32 s60, s60, s64\n"
  "s_sub_u32 s59, s59, s65\n"
  "ds_read_b64_tr_b16 v[32:33], v101 offset:0\n"
  "ds_read_b64_tr_b16 v[34:35], v101 offset:8192\n"
  "s_waitcnt vmcnt(8)\n"
  "v_cmp_gt_u32 vcc, 12, v106\n"
  "s_and_b64 vcc, vcc, 7\n"
  "s_cbranch_scc1 Lpollp24A%=\n"
  "Lpollrp24A%=:\n"
  "v_mfma_f32_16x16x32_bf16 v[148:151], v[112:115], v[128:131], v[148:151]\n"
  "s_add_u32 m0, s50, 151552\n"
  "ds_read_b128 v[44:47], v91 offset:3072\n"
  "v_mfma_f32_16x16x32_bf16 v[152:155], v[112:115], v[132:135], v[152:155]\n"
  "buffer_load_dwordx4 v98, s[36:39], s59 offen sc1 lds\n"
  "ds_read_b128 v[48:51], v91 offset:7168\n"
  "v_mfma_f32_16x16x32_bf16 v[156:159], v[112:115], v[136:139], v[156:159]\n"
  "ds_read_b128 v[52:55], v91 offset:11264\n"
  "v_mfma_f32_16x16x32_bf16 v[160:163], v[112:115], v[140:143], v[160:163]\n"
  "ds_read_b128 v[56:59], v91 offset:15360\n"
  "v_mfma_f32_16x16x32_bf16 v[164:167], v[116:119], v[124:127], v[164:167]\n"
  "ds_read_b128 v[60:63], v91 offset:27648\n"
  "v_mfma_f32_16x16x32_bf16 v[168:171], v[116:119], v[128:131], v[168:171]\n"
  "s_add_u32 m0, s48, 98304\n"
  "ds_read_b64_tr_b16 v[36:37], v101 offset:1024\n"
  "v_mfma_f32_16x16x32_bf16 v[172:175], v[116:119], v[132:135], v[172:175]\n"
  "buffer_load_dwordx4 v97, s[36:39], s59 offen offset:3072 sc1 lds\n"
  "ds_read_b64_tr_b16 v[38:39], v101 offset:9216\n"
  "v_mfma_f32_16x16x32_bf16 v[176:179], v[116:119], v[136:139], v[176:179]\n"
  "s_add_u32 s59, s59, 1024\n"
  "ds_read_b64_tr_b16 v[40:41], v101 offset:2048\n"
  "v_mfma_f32_16x16x32_bf16 v[180:183], v[116:119], v[140:143], v[180:183]\n"
  "ds_read_b64_tr_b16 v[42:43], v101 offset:10240\n"
  "v_mfma_f32_16x16x32_bf16 v[184:187], v[120:123], v[124:127], v[184:187]\n"
  "v_mfma_f32_16x16x32_bf16 v[188:191], v[120:123], v[128:131], v[188:191]\n"
  "v_mfma_f32_16x16x32_bf16 v[192:195], v[120:123], v[132:135], v[192:195]\n"
  "buffer_load_dwordx4 v94, s[40:43], s60 offen offset:0 lds\n"
  "v_mfma_f32_16x16x32_bf16 v[196:199], v[120:123], v[136:139], v[196:199]\n"
  "v_mfma_f32_16x16x32_bf16 v[200:203], v[120:123], v[140:143], v[200:203]\n"
  "s_waitcnt vmcnt(18) lgkmcnt(0)\n"
  "s_barrier\n"
  "v_mfma_f32_16x16x32_bf16 v[144:147], v[32:35], v[44:47], v[144:147]\n"
  "ds_read_b64_tr_b16 v[112:113], v99 offset:0\n"
  "ds_read_b64_tr_b16 v[114:115], v99 offset:8192\n"
  "v_mfma_f32_16x16x32_bf16 v[148:151], v[32:35], v[48:51], v[148:151]\n"
  "ds_read_b128 v[124:127], v89 offset:3072\n"
  "v_mfma_f32_16x16x32_bf16 v[152:155], v[32:35], v[52:55], v[152:155]\n"
  "buffer_load_dwordx4 v95, s[40:43], s60 offen offset:1024 lds\n"
  "ds_read_b128 v[128:131], v89 offset:7168\n"
  "v_mfma_f32_16x16x32_bf16 v[156:159], v[32:35], v[56:59], v[156:159]\n"
  "ds_read_b128 v[132:135], v89 offset:11264\n"
  "v_mfma_f32_16x16x32_bf16 v[160:163], v[32:35], v[60:63], v[160:163]\n"
  "ds_read_b128 v[136:139], v89 offset:15360\n"
  "v_mfma_f32_16x16x32_bf16 v[164:167], v[36:39], v[44:47], v[164:167]\n"
  "ds_read_b128 v[140:143], v91 offset:28672\n"
  "v_mfma_f32_16x16x32_bf16 v[168:171], v[36:39], v[48:51], v[168:171]\n"
  "buffer_load_dwordx4 v96, s[40:43], s60 offen offset:2048 lds\n"
  "ds_read_b64_tr_b16 v[116:117], v99 offset:1024\n"
  "v_mfma_f32_16x16x32_bf16 v[172:175], v[36:39], v[52:55], v[172:175]\n"
  "s_add_u32 s60, s60, 36864\n"
  "ds_read_b64_tr_b16 v[118:119], v99 offset:9216\n"
  "v_mfma_f32_16x16x32_bf16 v[176:179], v[36:39], v[56:59], v[176:179]\n"
  "s_add_u32 m0, s48, 114688\n"
  "ds_read_b64_tr_b16 v[120:121], v99 offset:2048\n"
  "v_mfma_f32_16x16x32_bf16 v[180:183], v[36:39], v[60:63], v[180:183]\n"
  "buffer_load_dwordx4 v97, s[36:39], s59 offen offset:3072 sc1 lds\n"
  "ds_read_b64_tr_b16 v[122:123], v99 offset:10240\n"
  "v_mfma_f32_16x16x32_bf16 v[184:187], v[40:43], v[44:47], v[184:187]\n"
  "s_add_u32 s59, s59, 1024\n"
  "v_mfma_f32_16x16x32_bf16 v[188:191], v[40:43], v[48:51], v[188:191]\n"
  "v_mfma_f32_16x16x32_bf16 v[192:195], v[40:43], v[52:55], v[192:195]\n"
  "buffer_load_dwordx4 v94, s[40:43], s60 offen offset:0 lds\n"
  "v_mfma_f32_16x16x32_bf16 v[196:199], v[40:43], v[56:59], v[196:199]\n"
  "v_mfma_f32_16x16x32_bf16 v[200:203], v[40:43], v[60:63], v[200:203]\n"
  "s_waitcnt vmcnt(17) lgkmcnt(0)\n"
  "s_barrier\n"
  "v_mfma_f32_16x16x32_bf16 v[144:147], v[112:115], v[124:127], v[144:147]\n"
  "ds_read_b64_tr_b16 v[32:33], v99 offset:49152\n"
  "ds_read_b64_tr_b16 v[34:35], v99 offset:57344\n"
  "v_mfma_f32_16x16x32_bf16 v[148:151], v[112:115], v[128:131], v[148:151]\n"
  "buffer_load_dwordx4 v95, s[40:43], s60 offen offset:1024 lds\n"
  "ds_read_b128 v[44:47], v89 offset:52224\n"
  "v_mfma_f32_16x16x32_bf16 v[152:155], v[112:115], v[132:135], v[152:155]\n"
  "buffer_load_dwordx4 v96, s[40:43], s60 offen offset:2048 lds\n"
  "ds_read_b128 v[48:51], v89 offset:56320\n"
  "v_mfma_f32_16x16x32_bf16 v[156:159], v[112:115], v[136:139], v[156:159]\n"
  "s_add_u32 s60, s60, 36864\n"
  "ds_read_b128 v[52:55], v89 offset:60416\n"
  "s_waitcnt vmcnt(12)\n"
  "s_mov_b64 exec, 1\n"
  "buffer_atomic_add v104, off, s[24:27], s15 sc1\n"
  "s_mov_b64 exec, -1\n"
  "v_mfma_f32_16x16x32_bf16 v[160:163], v[112:115], v[140:143], v[160:163]\n"
  "s_add_u32 m0, s48, 16384\n"
  "ds_read_b128 v[56:59], v89 offset:64512\n"
  "v_mfma_f32_16x16x32_bf16 v[164:167], v[116:119], v[124:127], v[164:167]\n"
  "buffer_load_dwordx4 v97, s[36:39], s59 offen offset:3072 sc1 lds\n"
  "ds_read_b128 v[60:63], v91 offset:29696\n"
  "v_mfma_f32_16x16x32_bf16 v[168:171], v[116:119], v[128:131], v[168:171]\n"
  "s_add_u32 s59, s59, 1024\n"
  "ds_read_b64_tr_b16 v[36:37], v99 offset:50176\n"
  "v_mfma_f32_16x16x32_bf16 v[172:175], v[116:119], v[132:135], v[172:175]\n"
  "ds_read_b64_tr_b16 v[38:39], v99 offset:58368\n"
  "v_mfma_f32_16x16x32_bf16 v[176:179], v[116:119], v[136:139], v[176:179]\n"
  "buffer_load_dwordx4 v94, s[40:43], s60 offen offset:0 lds\n"
  "ds_read_b64_tr_b16 v[40:41], v99 offset:51200\n"
  "v_mfma_f32_16x16x32_bf16 v[180:183], v[116:119], v[140:143], v[180:183]\n"
  "ds_read_b64_tr_b16 v[42:43], v99 offset:59392\n"
  "v_mfma_f32_16x16x32_bf16 v[184:187], v[120:123], v[124:127], v[184:187]\n"
  "buffer_load_dwordx4 v[224:227], v207, s[36:39], s63 offen sc1\n"
  "v_mfma_f32_16x16x32_bf16 v[188:191], v[120:123], v[128:131], v[188:191]\n"
  "buffer_load_dwordx4 v95, s[40:43], s60 offen offset:1024 lds\n"
  "v_mfma_f32_16x16x32_bf16 v[192:195], v[120:123], v[132:135], v[192:195]\n"
  "v_mfma_f32_16x16x32_bf16 v[196:199], v[120:123], v[136:139], v[196:199]\n"
  "v_mfma_f32_16x16x32_bf16 v[200:203], v[120:123], v[140:143], v[200:203]\n"
  "s_waitcnt vmcnt(19) lgkmcnt(0)\n"
  "s_barrier\n"
  "v_mfma_f32_16x16x32_bf16 v[144:147], v[32:35], v[44:47], v[144:147]\n"
  "ds_read_b64_tr_b16 v[112:113], v100 offset:0\n"
  "ds_read_b64_tr_b16 v[114:115], v100 offset:8192\n"
  "v_mfma_f32_16x16x32_bf16 v[148:151], v[32:35], v[48:51], v[148:151]\n"
  "buffer_load_dwordx4 v96, s[40:43], s60 offen offset:2048 lds\n"
  "ds_read_b128 v[124:127], v90 offset:3072\n"
  "v_mfma_f32_16x16x32_bf16 v[152:155], v[32:35], v[52:55], v[152:155]\n"
  "s_add_u32 s60, s60, 36864\n"
  "ds_read_b128 v[128:131], v90 offset:7168\n"
  "v_mfma_f32_16x16x32_bf16 v[156:159], v[32:35], v[56:59], v[156:159]\n"
  "s_add_u32 m0, s48, 32768\n"
  "ds_read_b128 v[132:135], v90 offset:11264\n"
  "v_mfma_f32_16x16x32_bf16 v[160:163], v[32:35], v[60:63], v[160:163]\n"
  "buffer_load_dwordx4 v97, s[36:39], s59 offen offset:3072 sc1 lds\n"
  "ds_read_b128 v[136:139], v90 offset:15360\n"
  "v_mfma_f32_16x16x32_bf16 v[164:167], v[36:39], v[44:47], v[164:167]\n"
  "s_add_u32 s59, s59, 1024\n"
  "ds_read_b128 v[140:143], v91 offset:30720\n"
  "v_mfma_f32_16x16x32_bf16 v[168:171], v[36:39], v[48:51], v[168:171]\n"
  "buffer_load_dwordx4 v94, s[40:43], s60 offen offset:0 lds\n"
  "ds_read_b64_tr_b16 v[116:117], v100 offset:1024\n"
  "v_mfma_f32_16x16x32_bf16 v[172:175], v[36:39], v[52:55], v[172:175]\n"
  "buffer_load_dwordx4 v95, s[40:43], s60 offen offset:1024 lds\n"
  "ds_read_b64_tr_b16 v[118:119], v100 offset:9216\n"
  "v_mfma_f32_16x16x32_bf16 v[176:179], v[36:39], v[56:59], v[176:179]\n"
  "ds_read_b64_tr_b16 v[120:121], v100 offset:2048\n"
  "v_mfma_f32_16x16x32_bf16 v[180:183], v[36:39], v[60:63], v[180:183]\n"
  "ds_read_b64_tr_b16 v[122:123], v100 offset:10240\n"
  "v_mfma_f32_16x16x32_bf16 v[184:187], v[40:43], v[44:47], v[184:187]\n"
  "buffer_load_dwordx4 v96, s[40:43], s60 offen offset:2048 lds\n"
  "v_mfma_f32_16x16x32_bf16 v[188:191], v[40:43], v[48:51], v[188:191]\n"
  "s_add_u32 s60, s60, 36864\n"
  "v_mfma_f32_16x16x32_bf16 v[192:195], v[40:43], v[52:55], v[192:195]\n"
  "s_add_u32 m0, s50, 147456\n"
  "v_mfma_f32_16x16x32_bf16 v[196:199], v[40:43], v[56:59], v[196:199]\n"
  "buffer_load_dwordx4 v98, s[36:39], s59 offen sc1 lds\n"
  "v_mfma_f32_16x16x32_bf16 v[200:203], v[40:43], v[60:63], v[200:203]\n"
  "s_waitcnt vmcnt(20) lgkmcnt(0)\n"
  "s_barrier\n"
  "v_mfma_f32_16x16x32_bf16 v[144:147], v[112:115], v[124:127], v[144:147]\n"
  "s_add_u32 m0, s48, 131072\n"
  "ds_read_b64_tr_b16 v[32:33], v100 offset:16384\n"
  "ds_read_b64_tr_b16 v[34:35], v100 offset:24576\n"
  "v_mfma_f32_16x16x32_bf16 v[148:151], v[112:115], v[128:131], v[148:151]\n"
  "buffer_load_dwordx4 v97, s[36:39], s59 offen offset:3072 sc1 lds\n"
  "ds_read_b128 v[44:47], v90 offset:19456\n"
  "v_mfma_f32_16x16x32_bf16 v[152:155], v[112:115], v[132:135], v[152:155]\n"
  "s_add_u32 s59, s59, 1024\n"
  "buffer_load_dwordx4 v[228:231], v208, s[36:39], s63 offen sc1\n"
  "ds_read_b128 v[48:51], v90 offset:23552\n"
  "v_mfma_f32_16x16x32_bf16 v[156:159], v[112:115], v[136:139], v[156:159]\n"
  "buffer_load_dwordx4 v94, s[40:43], s60 offen offset:0 lds\n"
  "ds_read_b128 v[52:55], v90 offset:27648\n"
  "v_mfma_f32_16x16x32_bf16 v[160:163], v[112:115], v[140:143], v[160:163]\n"
  "ds_read_b128 v[56:59], v90 offset:31744\n"
  "v_mfma_f32_16x16x32_bf16 v[164:167], v[116:119], v[124:127], v[164:167]\n"
  "ds_read_b128 v[60:63], v91 offset:31744\n"
  "v_mfma_f32_16x16x32_bf16 v[168:171], v[116:119], v[128:131], v[168:171]\n"
  "buffer_load_dwordx4 v95, s[40:43], s60 offen offset:1024 lds\n"
  "ds_read_b64_tr_b16 v[36:37], v100 offset:17408\n"
  "v_mfma_f32_16x16x32_bf16 v[172:175], v[116:119], v[132:135], v[172:175]\n"
  "ds_read_b64_tr_b16 v[38:39], v100 offset:25600\n"
  "v_mfma_f32_16x16x32_bf16 v[176:179], v[116:119], v[136:139], v[176:179]\n"
  "ds_read_b64_tr_b16 v[40:41], v100 offset:18432\n"
  "v_mfma_f32_16x16x32_bf16 v[180:183], v[116:119], v[140:143], v[180:183]\n"
  "buffer_load_dwordx4 v96, s[40:43], s60 offen offset:2048 lds\n"
  "ds_read_b64_tr_b16 v[42:43], v100 offset:26624\n"
  "v_mfma_f32_16x16x32_bf16 v[184:187], v[120:123], v[124:127], v[184:187]\n"
  "s_add_u32 s60, s60, 36864\n"
  "v_mfma_f32_16x16x32_bf16 v[188:191], v[120:123], v[128:131], v[188:191]\n"
  "s_add_u32 m0, s48, 0\n"
  "v_mfma_f32_16x16x32_bf16 v[192:195], v[120:123], v[132:135], v[192:195]\n"
  "buffer_load_dwordx4 v97, s[36:39], s59 offen offset:3072 sc1 lds\n"
  "v_mfma_f32_16x16x32_bf16 v[196:199], v[120:123], v[136:139], v[196:199]\n"
  "s_add_u32 s59, s59, 1024\n"
  "v_mfma_f32_16x16x32_bf16 v[200:203], v[120:123], v[140:143], v[200:203]\n"
  "s_waitcnt vmcnt(21) lgkmcnt(0)\n"
  "s_barrier\n"
  "v_mfma_f32_16x16x32_bf16 v[144:147], v[32:35], v[44:47], v[144:147]\n"
  "ds_read_b64_tr_b16 v[112:113], v100 offset:32768\n"
  "ds_read_b64_tr_b16 v[114:115], v100 offset:40960\n"
  "v_mfma_f32_16x16x32_bf16 v[148:151], v[32:35], v[48:51], v[148:151]\n"
  "buffer_load_dwordx4 v94, s[40:43], s60 offen offset:0 lds\n"
  "ds_read_b128 v[124:127], v90 offset:35840\n"
  "v_mfma_f32_16x16x32_bf16 v[152:155], v[32:35], v[52:55], v[152:155]\n"
  "buffer_load_dwordx4 v95, s[40:43], s60 offen offset:1024 lds\n"
  "ds_read_b128 v[128:131], v90 offset:39936\n"
  "v_mfma_f32_16x16x32_bf16 v[156:159], v[32:35], v[56:59], v[156:159]\n"
  "ds_read_b128 v[132:135], v90 offset:44032\n"
  "v_mfma_f32_16x16x32_bf16 v[160:163], v[32:35], v[60:63], v[160:163]\n"
  "ds_read_b128 v[136:139], v90 offset:48128\n"
  "v_mfma_f32_16x16x32_bf16 v[164:167], v[36:39], v[44:47], v[164:167]\n"
  "buffer_load_dwordx4 v96, s[40:43], s60 offen offset:2048 lds\n"
  "ds_read_b128 v[140:143], v91 offset:20480\n"
  "v_mfma_f32_16x16x32_bf16 v[168:171], v[36:39], v[48:51], v[168:171]\n"
  "s_add_u32 s60, s60, 36864\n"
  "buffer_load_dwordx4 v[232:235], v209, s[36:39], s63 offen sc1\n"
  "ds_read_b64_tr_b16 v[116:117], v100 offset:33792\n"
  "v_mfma_f32_16x16x32_bf16 v[172:175], v[36:39], v[52:55], v[172:175]\n"
  "s_add_u32 m0, s48, 49152\n"
  "ds_read_b64_tr_b16 v[118:119], v100 offset:41984\n"
  "v_mfma_f32_16x16x32_bf16 v[176:179], v[36:39], v[56:59], v[176:179]\n"
  "buffer_load_dwordx4 v97, s[36:39], s59 offen offset:3072 sc1 lds\n"
  "ds_read_b64_tr_b16 v[120:121], v100 offset:34816\n"
  "v_mfma_f32_16x16x32_bf16 v[180:183], v[36:39], v[60:63], v[180:183]\n"
  "s_add_u32 s59, s59, 1024\n"
  "ds_read_b64_tr_b16 v[122:123], v100 offset:43008\n"
  "v_mfma_f32_16x16x32_bf16 v[184:187], v[40:43], v[44:47], v[184:187]\n"
  "v_mfma_f32_16x16x32_bf16 v[188:191], v[40:43], v[48:51], v[188:191]\n"
  "buffer_load_dwordx4 v94, s[40:43], s60 offen offset:0 lds\n"
  "v_mfma_f32_16x16x32_bf16 v[192:195], v[40:43], v[52:55], v[192:195]\n"
  "v_mfma_f32_16x16x32_bf16 v[196:199], v[40:43], v[56:59], v[196:199]\n"
  "buffer_load_dwordx4 v95, s[40:43], s60 offen offset:1024 lds\n"
  "v_mfma_f32_16x16x32_bf16 v[200:203], v[40:43], v[60:63], v[200:203]\n"
  "s_waitcnt vmcnt(24) lgkmcnt(0)\n"
  "s_barrier\n"
  "v_mfma_f32_16x16x32_bf16 v[144:147], v[112:115], v[124:127], v[144:147]\n"
  "ds_read_b64_tr_b16 v[32:33], v100 offset:49152\n"
  "ds_read_b64_tr_b16 v[34:35], v100 offset:57344\n"
  "v_mfma_f32_16x16x32_bf16 v[148:151], v[112:115], v[128:131], v[148:151]\n"
  "buffer_load_dwordx4 v96, s[40:43], s60 offen offset:2048 lds\n"
  "ds_read_b128 v[44:47], v90 offset:52224\n"
  "v_mfma_f32_16x16x32_bf16 v[152:155], v[112:115], v[132:135], v[152:155]\n"
  "s_add_u32 s60, s60, 36864\n"
  "ds_read_b128 v[48:51], v90 offset:56320\n"
  "v_mfma_f32_16x16x32_bf16 v[156:159], v[112:115], v[136:139], v[156:159]\n"
  "s_add_u32 m0, s48, 65536\n"
  "ds_read_b128 v[52:55], v90 offset:60416\n"
  "v_mfma_f32_16x16x32_bf16 v[160:163], v[112:115], v[140:143], v[160:163]\n"
  "buffer_load_dwordx4 v97, s[36:39], s59 offen offset:3072 sc1 lds\n"
  "ds_read_b128 v[56:59], v90 offset:64512\n"
  "v_mfma_f32_16x16x32_bf16 v[164:167], v[116:119], v[124:127], v[164:167]\n"
  "s_add_u32 s59, s59, 1024\n"
  "ds_read_b128 v[60:63], v91 offset:21504\n"
  "v_mfma_f32_16x16x32_bf16 v[168:171], v[116:119], v[128:131], v[168:171]\n"
  "ds_read_b64_tr_b16 v[36:37], v100 offset:50176\n"
  "v_mfma_f32_16x16x32_bf16 v[172:175], v[116:119], v[132:135], v[172:175]\n"
  "buffer_load_dwordx4 v94, s[40:43], s60 offen offset:0 lds\n"
  "ds_read_b64_tr_b16 v[38:39], v100 offset:58368\n"
  "v_mfma_f32_16x16x32_bf16 v[176:179], v[116:119], v[136:139], v[176:179]\n"
  "ds_read_b64_tr_b16 v[40:41], v100 offset:51200\n"
  "v_mfma_f32_16x16x32_bf16 v[180:183], v[116:119], v[140:143], v[180:183]\n"
  "ds_read_b64_tr_b16 v[42:43], v100 offset:59392\n"
  "v_mfma_f32_16x16x32_bf16 v[184:187], v[120:123], v[124:127], v[184:187]\n"
  "buffer_load_dwordx4 v95, s[40:43], s60 offen offset:1024 lds\n"
  "v_mfma_f32_16x16x32_bf16 v[188:191], v[120:123], v[128:131], v[188:191]\n"
  "v_mfma_f32_16x16x32_bf16 v[192:195], v[120:123], v[132:135], v[192:195]\n"
  "buffer_load_dwordx4 v96, s[40:43], s60 offen offset:2048 lds\n"
  "v_mfma_f32_16x16x32_bf16 v[196:199], v[120:123], v[136:139], v[196:199]\n"
  "s_add_u32 s60, s60, 36864\n"
  "v_mfma_f32_16x16x32_bf16 v[200:203], v[120:123], v[140:143], v[200:203]\n"
  "s_waitcnt vmcnt(23) lgkmcnt(0)\n"
  "s_barrier\n"
  "v_mfma_f32_16x16x32_bf16 v[144:147], v[32:35], v[44:47], v[144:147]\n"
  "s_add_u32 m0, s50, 155648\n"
  "ds_read_b64_tr_b16 v[112:113], v99 offset:16384\n"
  "ds_read_b64_tr_b16 v[114:115], v99 offset:24576\n"
  "v_mfma_f32_16x16x32_bf16 v[148:151], v[32:35], v[48:51], v[148:151]\n"
  "buffer_load_dwordx4 v98, s[36:39], s59 offen sc1 lds\n"
  "ds_read_b128 v[124:127], v89 offset:19456\n"
  "v_mfma_f32_16x16x32_bf16 v[152:155], v[32:35], v[52:55], v[152:155]\n"
  "s_add_u32 m0, s48, 81920\n"
  "s_add_u32 s51, s59, 3072\n"
  "ds_read_b128 v[128:131], v89 offset:23552\n"
  "v_mfma_f32_16x16x32_bf16 v[156:159], v[32:35], v[56:59], v[156:159]\n"
  "buffer_load_dwordx4 v97, s[36:39], s51 offen offset:0 sc1 lds\n"
  "ds_read_b128 v[132:135], v89 offset:27648\n"
  "v_mfma_f32_16x16x32_bf16 v[160:163], v[32:35], v[60:63], v[160:163]\n"
  "ds_read_b128 v[136:139], v89 offset:31744\n"
  "v_mfma_f32_16x16x32_bf16 v[164:167], v[36:39], v[44:47], v[164:167]\n"
  "ds_read_b128 v[140:143], v91 offset:22528\n"
  "v_mfma_f32_16x16x32_bf16 v[168:171], v[36:39], v[48:51], v[168:171]\n"
  "buffer_load_dwordx4 v97, s[36:39], s51 offen offset:1024 sc1 lds\n"
  "ds_read_b64_tr_b16 v[116:117], v99 offset:17408\n"
  "v_mfma_f32_16x16x32_bf16 v[172:175], v[36:39], v[52:55], v[172:175]\n"
  "s_add_u32 s53, s60, 1032192\n"
  "s_add_u32 s52, s59, 28672\n"
  "ds_read_b64_tr_b16 v[118:119], v99 offset:25600\n"
  "v_mfma_f32_16x16x32_bf16 v[176:179], v[36:39], v[56:59], v[176:179]\n"
  "s_cmp_ge_u32 s53, 5308416\n"
  "s_cselect_b32 s64, 5308416, 0\n"
  "s_cselect_b32 s65, 147456, 0\n"
  "s_sub_u32 s53, s53, s64\n"
  "s_sub_u32 s52, s52, s65\n"
  "ds_read_b64_tr_b16 v[120:121], v99 offset:18432\n"
  "v_mfma_f32_16x16x32_bf16 v[180:183], v[36:39], v[60:63], v[180:183]\n"
  "buffer_load_dwordx4 v97, s[36:39], s51 offen offset:2048 sc1 lds\n"
  "ds_read_b64_tr_b16 v[122:123], v99 offset:26624\n"
  "v_mfma_f32_16x16x32_bf16 v[184:187], v[40:43], v[44:47], v[184:187]\n"
  "v_mfma_f32_16x16x32_bf16 v[188:191], v[40:43], v[48:51], v[188:191]\n"
  "buffer_load_dwordx4 v97, s[36:39], s51 offen offset:3072 sc1 lds\n"
  "v_mfma_f32_16x16x32_bf16 v[192:195], v[40:43], v[52:55], v[192:195]\n"
  "v_mfma_f32_16x16x32_bf16 v[196:199], v[40:43], v[56:59], v[196:199]\n"
  "s_add_u32 m0, s48, 98304\n"
  "s_add_u32 s51, s60, 0\n"
  "v_mfma_f32_16x16x32_bf16 v[200:203], v[40:43], v[60:63], v[200:203]\n"
  "s_waitcnt vmcnt(24) lgkmcnt(0)\n"
  "s_barrier\n"
  "v_mfma_f32_16x16x32_bf16 v[144:147], v[112:115], v[124:127], v[144:147]\n"
  "ds_read_b64_tr_b16 v[32:33], v99 offset:32768\n"
  "ds_read_b64_tr_b16 v[34:35], v99 offset:40960\n"
  "v_mfma_f32_16x16x32_bf16 v[148:151], v[112:115], v[128:131], v[148:151]\n"
  "buffer_load_dwordx4 v94, s[40:43], s51 offen offset:0 lds\n"
  "ds_read_b128 v[44:47], v89 offset:35840\n"
  "v_mfma_f32_16x16x32_bf16 v[152:155], v[112:115], v[132:135], v[152:155]\n"
  "s_add_u32 s51, s60, 35840\n"
  "ds_read_b128 v[48:51], v89 offset:39936\n"
  "v_mfma_f32_16x16x32_bf16 v[156:159], v[112:115], v[136:139], v[156:159]\n"
  "buffer_load_dwordx4 v94, s[40:43], s51 offen offset:1024 lds\n"
  "ds_read_b128 v[52:55], v89 offset:44032\n"
  "v_mfma_f32_16x16x32_bf16 v[160:163], v[112:115], v[140:143], v[160:163]\n"
  "s_add_u32 s51, s60, 71680\n"
  "ds_read_b128 v[56:59], v89 offset:48128\n"
  "v_mfma_f32_16x16x32_bf16 v[164:167], v[116:119], v[124:127], v[164:167]\n"
  "buffer_load_dwordx4 v94, s[40:43], s51 offen offset:2048 lds\n"
  "ds_read_b128 v[60:63], v91 offset:23552\n"
  "v_mfma_f32_16x16x32_bf16 v[168:171], v[116:119], v[128:131], v[168:171]\n"
  "ds_read_b64_tr_b16 v[36:37], v99 offset:33792\n"
  "v_mfma_f32_16x16x32_bf16 v[172:175], v[116:119], v[132:135], v[172:175]\n"
  "s_add_u32 s51, s60, 107520\n"
  "ds_read_b64_tr_b16 v[38:39], v99 offset:41984\n"
  "v_mfma_f32_16x16x32_bf16 v[176:179], v[116:119], v[136:139], v[176:179]\n"
  "buffer_load_dwordx4 v94, s[40:43], s51 offen offset:3072 lds\n"
  "ds_read_b64_tr_b16 v[40:41], v99 offset:34816\n"
  "v_mfma_f32_16x16x32_bf16 v[180:183], v[116:119], v[140:143], v[180:183]\n"
  "s_add_u32 m0, s48, 114688\n"
  "s_add_u32 s51, s60, 1024\n"
  "ds_read_b64_tr_b16 v[42:43], v99 offset:43008\n"
  "v_mfma_f32_16x16x32_bf16 v[184:187], v[120:123], v[124:127], v[184:187]\n"
  "buffer_load_dwordx4 v95, s[40:43], s51 offen offset:0 lds\n"
  "v_mfma_f32_16x16x32_bf16 v[188:191], v[120:123], v[128:131], v[188:191]\n"
  "v_mfma_f32_16x16x32_bf16 v[192:195], v[120:123], v[132:135], v[192:195]\n"
  "s_add_u32 s51, s60, 36864\n"
  "v_mfma_f32_16x16x32_bf16 v[196:199], v[120:123], v[136:139], v[196:199]\n"
  "buffer_load_dwordx4 v95, s[40:43], s51 offen offset:1024 lds\n"
  "v_mfma_f32_16x16x32_bf16 v[200:203], v[120:123], v[140:143], v[200:203]\n"
  "s_waitcnt vmcnt(24) lgkmcnt(0)\n"
  "s_barrier\n"
  "v_mfma_f32_16x16x32_bf16 v[144:147], v[32:35], v[44:47], v[144:147]\n"
  "s_add_u32 s51, s60, 72704\n"
  "ds_read_b64_tr_b16 v[112:113], v101 offset:0\n"
  "ds_read_b64_tr_b16 v[114:115], v101 offset:8192\n"
  "v_mfma_f32_16x16x32_bf16 v[148:151], v[32:35], v[48:51], v[148:151]\n"
  "buffer_load_dwordx4 v95, s[40:43], s51 offen offset:2048 lds\n"
  "ds_read_b128 v[124:127], v91 offset:3072\n"
  "v_mfma_f32_16x16x32_bf16 v[152:155], v[32:35], v[52:55], v[152:155]\n"
  "ds_read_b128 v[128:131], v91 offset:7168\n"
  "s_mov_b64 exec, 31\n"
  "buffer_load_dword v106, v105, s[24:27], 0 offen sc1\n"
  "s_mov_b64 exec, -1\n"
  "v_mfma_f32_16x16x32_bf16 v[156:159], v[32:35], v[56:59], v[156:159]\n"
  "s_add_u32 s51, s60, 108544\n"
  "ds_read_b128 v[132:135], v91 offset:11264\n"
  "v_mfma_f32_16x16x32_bf16 v[160:163], v[32:35], v[60:63], v[160:163]\n"
  "buffer_load_dwordx4 v95, s[40:43], s51 offen offset:3072 lds\n"
  "ds_read_b128 v[136:139], v91 offset:15360\n"
  "v_mfma_f32_16x16x32_bf16 v[164:167], v[36:39], v[44:47], v[164:167]\n"
  "ds_read_b128 v[140:143], v91 offset:16384\n"
  "v_add_u32 v210, 0, v210\n"
  "v_add_u32 v211, 0, v211\n"
  "v_mfma_f32_16x16x32_bf16 v[168:171], v[36:39], v[48:51], v[168:171]\n"
  "s_add_u32 m0, s48, 16384\n"
  "s_add_u32 s51, s53, 0\n"
  "ds_read_b64_tr_b16 v[116:117], v101 offset:1024\n"
  "v_mfma_f32_16x16x32_bf16 v[172:175], v[36:39], v[52:55], v[172:175]\n"
  "buffer_load_dwordx4 v94, s[40:43], s51 offen offset:0 lds\n"
  "ds_read_b64_tr_b16 v[118:119], v101 offset:9216\n"
  "v_mfma_f32_16x16x32_bf16 v[176:179], v[36:39], v[56:59], v[176:179]\n"
  "s_add_u32 s51, s53, 35840\n"
  "ds_read_b64_tr_b16 v[120:121], v101 offset:2048\n"
  "v_mfma_f32_16x16x32_bf16 v[180:183], v[36:39], v[60:63], v[180:183]\n"
  "buffer_load_dwordx4 v94, s[40:43], s51 offen offset:1024 lds\n"
  "s_cbranch_execz Ltramp%=\n"
  "ds_read_b64_tr_b16 v[122:123], v101 offset:10240\n"
  "v_mfma_f32_16x16x32_bf16 v[184:187], v[40:43], v[44:47], v[184:187]\n"
  "v_mfma_f32_16x16x32_bf16 v[188:191], v[40:43], v[48:51], v[188:191]\n"
  "s_add_u32 s51, s53, 71680\n"
  "v_mfma_f32_16x16x32_bf16 v[192:195], v[40:43], v[52:55], v[192:195]\n"
  "buffer_load_dwordx4 v94, s[40:43], s51 offen offset:2048 lds\n"
  "v_mfma_f32_16x16x32_bf16 v[196:199], v[40:43], v[56:59], v[196:199]\n"
  "v_mfma_f32_16x16x32_bf16 v[200:203], v[40:43], v[60:63], v[200:203]\n"
  "s_waitcnt vmcnt(26) lgkmcnt(0)\n"
  "s_barrier\n"
  "v_mfma_f32_16x16x32_bf16 v[144:147], v[112:115], v[124:127], v[144:147]\n"
  "s_add_u32 s51, s53, 107520\n"
  "ds_read_b64_tr_b16 v[32:33], v99 offset:0\n"
  "ds_read_b64_tr_b16 v[34:35], v99 offset:8192\n"
  "v_mfma_f32_16x16x32_bf16 v[148:151], v[112:115], v[128:131], v[148:151]\n"
  "buffer_load_dwordx4 v94, s[40:43], s51 offen offset:3072 lds\n"
  "ds_read_b128 v[44:47], v89 offset:3072\n"
  "v_mfma_f32_16x16x32_bf16 v[152:155], v[112:115], v[132:135], v[152:155]\n"
  "s_add_u32 m0, s48, 32768\n"
  "s_add_u32 s51, s53, 147456\n"
  "ds_read_b128 v[48:51], v89 offset:7168\n"
  "v_mfma_f32_16x16x32_bf16 v[156:159], v[112:115], v[136:139], v[156:159]\n"
  "buffer_load_dwordx4 v94, s[40:43], s51 offen offset:0 lds\n"
  "ds_read_b128 v[52:55], v89 offset:11264\n"
  "v_mfma_f32_16x16x32_bf16 v[160:163], v[112:115], v[140:143], v[160:163]\n"
  "ds_read_b128 v[56:59], v89 offset:15360\n"
  "v_mfma_f32_16x16x32_bf16 v[164:167], v[116:119], v[124:127], v[164:167]\n"
  "s_add_u32 s51, s53, 183296\n"
  "ds_read_b128 v[60:63], v91 offset:17408\n"
  "v_mfma_f32_16x16x32_bf16 v[168:171], v[116:119], v[128:131], v[168:171]\n"
  "buffer_load_dwordx4 v94, s[40:43], s51 offen offset:1024 lds\n"
  "ds_read_b64_tr_b16 v[36:37], v99 offset:1024\n"
  "v_mfma_f32_16x16x32_bf16 v[172:175], v[116:119], v[132:135], v[172:175]\n"
  "s_add_u32 s51, s53, 219136\n"
  "ds_read_b64_tr_b16 v[38:39], v99 offset:9216\n"
  "v_mfma_f32_16x16x32_bf16 v[176:179], v[116:119], v[136:139], v[176:179]\n"
  "buffer_load_dwordx4 v94, s[40:43], s51 offen offset:2048 lds\n"
  "ds_read_b64_tr_b16 v[40:41], v99 offset:2048\n"
  "v_mfma_f32_16x16x32_bf16 v[180:183], v[116:119], v[140:143], v[180:183]\n"
  "ds_read_b64_tr_b16 v[42:43], v99 offset:10240\n"
  "v_mfma_f32_16x16x32_bf16 v[184:187], v[120:123], v[124:127], v[184:187]\n"
  "s_add_u32 s51, s53, 254976\n"
  "v_mfma_f32_16x16x32_bf16 v[188:191], v[120:123], v[128:131], v[188:191]\n"
  "buffer_load_dwordx4 v94, s[40:43], s51 offen offset:3072 lds\n"
  "v_mfma_f32_16x16x32_bf16 v[192:195], v[120:123], v[132:135], v[192:195]\n"
  "v_mfma_f32_16x16x32_bf16 v[196:199], v[120:123], v[136:139], v[196:199]\n"
  "s_add_u32 m0, s48, 131072\n"
  "s_add_u32 s51, s53, 294912\n"
  "v_mfma_f32_16x16x32_bf16 v[200:203], v[120:123], v[140:143], v[200:203]\n"
  "s_waitcnt vmcnt(26) lgkmcnt(0)\n"
  "s_barrier\n"
  "v_mfma_f32_16x16x32_bf16 v[144:147], v[32:35], v[44:47], v[144:147]\n"
  "ds_read_b64_tr_b16 v[112:113], v99 offset:49152\n"
  "ds_read_b64_tr_b16 v[114:115], v99 offset:57344\n"
  "v_mfma_f32_16x16x32_bf16 v[148:151], v[32:35], v[48:51], v[148:151]\n"
  "buffer_load_dwordx4 v94, s[40:43], s51 offen offset:0 lds\n"
  "ds_read_b128 v[124:127], v89 offset:52224\n"
  "v_mfma_f32_16x16x32_bf16 v[152:155], v[32:35], v[52:55], v[152:155]\n"
  "s_add_u32 s51, s53, 330752\n"
  "ds_read_b128 v[128:131], v89 offset:56320\n"
  "v_mfma_f32_16x16x32_bf16 v[156:159], v[32:35], v[56:59], v[156:159]\n"
  "buffer_load_dwordx4 v94, s[40:43], s51 offen offset:1024 lds\n"
  "ds_read_b128 v[132:135], v89 offset:60416\n"
  "v_mfma_f32_16x16x32_bf16 v[160:163], v[32:35], v[60:63], v[160:163]\n"
  "s_add_u32 s51, s53, 366592\n"
  "ds_read_b128 v[136:139], v89 offset:64512\n"
  "v_mfma_f32_16x16x32_bf16 v[164:167], v[36:39], v[44:47], v[164:167]\n"
  "buffer_load_dwordx4 v94, s[40:43], s51 offen offset:2048 lds\n"
  "ds_read_b128 v[140:143], v91 offset:18432\n"
  "v_mfma_f32_16x16x32_bf16 v[168:171], v[36:39], v[48:51], v[168:171]\n"
  "s_add_u32 s51, s53, 402432\n"
  "ds_read_b64_tr_b16 v[116:117], v99 offset:50176\n"
  "v_mfma_f32_16x16x32_bf16 v[172:175], v[36:39], v[52:55], v[172:175]\n"
  "buffer_load_dwordx4 v94, s[40:43], s51 offen offset:3072 lds\n"
  "ds_read_b64_tr_b16 v[118:119], v99 offset:58368\n"
  "v_mfma_f32_16x16x32_bf16 v[176:179], v[36:39], v[56:59], v[176:179]\n"
  "ds_read_b64_tr_b16 v[120:121], v99 offset:51200\n"
  "s_waitcnt vmcnt(13)\n"
  "v_cmp_gt_u32 vcc, 12, v106\n"
  "s_and_b64 vcc, vcc, 31\n"
  "s_cbranch_scc1 Lpollp127A%=\n"
  "Lpollrp127A%=:\n"
  "v_mfma_f32_16x16x32_bf16 v[180:183], v[36:39], v[60:63], v[180:183]\n"
  "s_add_u32 m0, s50, 159744\n"
  "ds_read_b64_tr_b16 v[122:123], v99 offset:59392\n"
  "v_mfma_f32_16x16x32_bf16 v[184:187], v[40:43], v[44:47], v[184:187]\n"
  "buffer_load_dwordx4 v98, s[36:39], s52 offen sc1 lds\n"
  "v_mfma_f32_16x16x32_bf16 v[188:191], v[40:43], v[48:51], v[188:191]\n"
  "v_mfma_f32_16x16x32_bf16 v[192:195], v[40:43], v[52:55], v[192:195]\n"
  "s_add_u32 m0, s48, 0\n"
  "s_add_u32 s51, s52, 3072\n"
  "v_mfma_f32_16x16x32_bf16 v[196:199], v[40:43], v[56:59], v[196:199]\n"
  "buffer_load_dwordx4 v97, s[36:39], s51 offen offset:0 sc1 lds\n"
  "v_mfma_f32_16x16x32_bf16 v[200:203], v[40:43], v[60:63], v[200:203]\n"
  "s_waitcnt vmcnt(28) lgkmcnt(0)\n"
  "s_barrier\n"
  "v_mfma_f32_16x16x32_bf16 v[144:147], v[112:115], v[124:127], v[144:147]\n"
  "ds_read_b64_tr_b16 v[32:33], v100 offset:0\n"
  "ds_read_b64_tr_b16 v[34:35], v100 offset:8192\n"
  "v_mfma_f32_16x16x32_bf16 v[148:151], v[112:115], v[128:131], v[148:151]\n"
  "buffer_load_dwordx4 v97, s[36:39], s51 offen offset:1024 sc1 lds\n"
  "ds_read_b128 v[44:47], v90 offset:3072\n"
  "v_mfma_f32_16x16x32_bf16 v[152:155], v[112:115], v[132:135], v[152:155]\n"
  "ds_read_b128 v[48:51], v90 offset:7168\n"
  "v_mfma_f32_16x16x32_bf16 v[156:159], v[112:115], v[136:139], v[156:159]\n"
  "ds_read_b128 v[52:55], v90 offset:11264\n"
  "v_mfma_f32_16x16x32_bf16 v[160:163], v[112:115], v[140:143], v[160:163]\n"
  "buffer_load_dwordx4 v97, s[36:39], s51 offen offset:2048 sc1 lds\n"
  "ds_read_b128 v[56:59], v90 offset:15360\n"
  "v_mfma_f32_16x16x32_bf16 v[164:167], v[116:119], v[124:127], v[164:167]\n"
  "ds_read_b128 v[60:63], v91 offset:19456\n"
  "v_mfma_f32_16x16x32_bf16 v[168:171], v[116:119], v[128:131], v[168:171]\n"
  "buffer_load_dwordx4 v97, s[36:39], s51 offen offset:3072 sc1 lds\n"
  "ds_read_b64_tr_b16 v[36:37], v100 offset:1024\n"
  "v_mfma_f32_16x16x32_bf16 v[172:175], v[116:119], v[132:135], v[172:175]\n"
  "ds_read_b64_tr_b16 v[38:39], v100 offset:9216\n"
  "v_mfma_f32_16x16x32_bf16 v[176:179], v[116:119], v[136:139], v[176:179]\n"
  "s_add_u32 m0, s50, 151552\n"
  "s_add_u32 s51, s52, 4096\n"
  "ds_read_b64_tr_b16 v[40:41], v100 offset:2048\n"
  "v_mfma_f32_16x16x32_bf16 v[180:183], v[116:119], v[140:143], v[180:183]\n"
  "buffer_load_dwordx4 v98, s[36:39], s51 offen sc1 lds\n"
  "ds_read_b64_tr_b16 v[42:43], v100 offset:10240\n"
  "v_mfma_f32_16x16x32_bf16 v[184:187], v[120:123], v[124:127], v[184:187]\n"
  "v_mfma_f32_16x16x32_bf16 v[188:191], v[120:123], v[128:131], v[188:191]\n"
  "s_add_u32 m0, s48, 49152\n"
  "s_add_u32 s51, s52, 7168\n"
  "v_mfma_f32_16x16x32_bf16 v[192:195], v[120:123], v[132:135], v[192:195]\n"
  "buffer_load_dwordx4 v97, s[36:39], s51 offen offset:0 sc1 lds\n"
  "v_mfma_f32_16x16x32_bf16 v[196:199], v[120:123], v[136:139], v[196:199]\n"
  "v_mfma_f32_16x16x32_bf16 v[200:203], v[120:123], v[140:143], v[200:203]\n"
  "s_waitcnt vmcnt(24) lgkmcnt(0)\n"
  "s_barrier\n"
  "v_mfma_f32_16x16x32_bf16 v[144:147], v[32:35], v[44:47], v[144:147]\n"
  "ds_read_b64_tr_b16 v[112:113], v100 offset:32768\n"
  "ds_read_b64_tr_b16 v[114:115], v100 offset:40960\n"
  "ds_read_b64_tr_b16 v[116:117], v100 offset:33792\n"
  "v_mfma_f32_16x16x32_bf16 v[148:151], v[32:35], v[48:51], v[148:151]\n"
  "buffer_load_dwordx4 v97, s[36:39], s51 offen offset:1024 sc1 lds\n"
  "ds_read_b64_tr_b16 v[118:119], v100 offset:41984\n"
  "ds_read_b64_tr_b16 v[120:121], v100 offset:34816\n"
  "v_mfma_f32_16x16x32_bf16 v[152:155], v[32:35], v[52:55], v[152:155]\n"
  "ds_read_b64_tr_b16 v[122:123], v100 offset:43008\n"
  "ds_read_b64_tr_b16 v[124:125], v100 offset:35840\n"
  "v_mfma_f32_16x16x32_bf16 v[156:159], v[32:35], v[56:59], v[156:159]\n"
  "buffer_load_dwordx4 v97, s[36:39], s51 offen offset:2048 sc1 lds\n"
  "ds_read_b64_tr_b16 v[126:127], v100 offset:44032\n"
  "ds_read_b128 a[160:163], v90 offset:16384\n"
  "v_mfma_f32_16x16x32_bf16 v[160:163], v[32:35], v[60:63], v[160:163]\n"
  "ds_read_b128 a[180:183], v90 offset:17408\n"
  "ds_read_b128 a[200:203], v90 offset:18432\n"
  "v_mfma_f32_16x16x32_bf16 v[164:167], v[36:39], v[44:47], v[164:167]\n"
  "buffer_load_dwordx4 v97, s[36:39], s51 offen offset:3072 sc1 lds\n"
  "ds_read_b128 a[220:223], v90 offset:19456\n"
  "ds_read_b128 a[164:167], v90 offset:20480\n"
  "v_mfma_f32_16x16x32_bf16 v[168:171], v[36:39], v[48:51], v[168:171]\n"
  "ds_read_b128 a[184:187], v90 offset:21504\n"
  "ds_read_b128 a[204:207], v90 offset:22528\n"
  "ds_read_b128 a[224:227], v90 offset:23552\n"
  "v_mfma_f32_16x16x32_bf16 v[172:175], v[36:39], v[52:55], v[172:175]\n"
  "s_add_u32 m0, s50, 147456\n"
  "s_add_u32 s51, s52, 8192\n"
  "ds_read_b128 a[168:171], v90 offset:24576\n"
  "ds_read_b128 a[188:191], v90 offset:25600\n"
  "v_mfma_f32_16x16x32_bf16 v[176:179], v[36:39], v[56:59], v[176:179]\n"
  "buffer_load_dwordx4 v98, s[36:39], s51 offen sc1 lds\n"
  "ds_read_b128 a[208:211], v90 offset:26624\n"
  "ds_read_b128 a[228:231], v90 offset:27648\n"
  "v_mfma_f32_16x16x32_bf16 v[180:183], v[36:39], v[60:63], v[180:183]\n"
  "ds_read_b128 a[172:175], v90 offset:28672\n"
  "ds_read_b128 a[192:195], v90 offset:29696\n"
  "v_mfma_f32_16x16x32_bf16 v[184:187], v[40:43], v[44:47], v[184:187]\n"
  "s_add_u32 m0, s48, 65536\n"
  "s_add_u32 s51, s52, 11264\n"
  "ds_read_b128 a[212:215], v90 offset:30720\n"
  "ds_read_b128 a[232:235], v90 offset:31744\n"
  "v_mfma_f32_16x16x32_bf16 v[188:191], v[40:43], v[48:51], v[188:191]\n"
  "ds_read_b128 a[176:179], v91 offset:24576\n"
  "ds_read_b128 a[196:199], v91 offset:25600\n"
  "v_mfma_f32_16x16x32_bf16 v[192:195], v[40:43], v[52:55], v[192:195]\n"
  "ds_read_b128 a[216:219], v91 offset:26624\n"
  "ds_read_b128 a[236:239], v91 offset:27648\n"
  "v_mfma_f32_16x16x32_bf16 v[196:199], v[40:43], v[56:59], v[196:199]\n"
  "buffer_load_dwordx4 v97, s[36:39], s51 offen offset:0 sc1 lds\n"
  "v_mfma_f32_16x16x32_bf16 v[200:203], v[40:43], v[60:63], v[200:203]\n"
  "s_waitcnt vmcnt(24) lgkmcnt(0)\n"
  "s_barrier\n"
  "v_mfma_f32_16x16x32_bf16 v[144:147], v[112:115], a[160:163], v[144:147]\n"
  "buffer_load_dwordx4 v97, s[36:39], s51 offen offset:1024 sc1 lds\n"
  "ds_read_b64_tr_b16 v[32:33], v100 offset:49152\n"
  "v_mfma_f32_16x16x32_bf16 v[144:147], v[116:119], a[180:183], v[144:147]\n"
  "ds_read_b64_tr_b16 v[34:35], v100 offset:57344\n"
  "v_mfma_f32_16x16x32_bf16 v[144:147], v[120:123], a[200:203], v[144:147]\n"
  "buffer_load_dwordx4 v97, s[36:39], s51 offen offset:2048 sc1 lds\n"
  "v_mfma_f32_16x16x32_bf16 v[144:147], v[124:127], a[220:223], v[144:147]\n"
  "ds_read_b64_tr_b16 v[36:37], v100 offset:50176\n"
  "v_mfma_f32_16x16x32_bf16 v[148:151], v[112:115], a[164:167], v[148:151]\n"
  "buffer_load_dwordx4 v97, s[36:39], s51 offen offset:3072 sc1 lds\n"
  "ds_read_b64_tr_b16 v[38:39], v100 offset:58368\n"
  "v_mfma_f32_16x16x32_bf16 v[148:151], v[116:119], a[184:187], v[148:151]\n"
  "v_mfma_f32_16x16x32_bf16 v[148:151], v[120:123], a[204:207], v[148:151]\n"
  "s_add_u32 m0, s48, 98304\n"
  "s_add_u32 s51, s53, 1024\n"
  "ds_read_b64_tr_b16 v[40:41], v100 offset:51200\n"
  "v_mfma_f32_16x16x32_bf16 v[148:151], v[124:127], a[224:227], v[148:151]\n"
  "buffer_load_dwordx4 v95, s[40:43], s51 offen offset:0 lds\n"
  "v_mfma_f32_16x16x32_bf16 v[152:155], v[112:115], a[168:171], v[152:155]\n"
  "ds_read_b64_tr_b16 v[42:43], v100 offset:59392\n"
  "v_mfma_f32_16x16x32_bf16 v[152:155], v[116:119], a[188:191], v[152:155]\n"
  "s_add_u32 s51, s53, 36864\n"
  "ds_read_b64_tr_b16 v[44:45], v100 offset:52224\n"
  "v_mfma_f32_16x16x32_bf16 v[152:155], v[120:123], a[208:211], v[152:155]\n"
  "buffer_load_dwordx4 v95, s[40:43], s51 offen offset:1024 lds\n"
  "v_mfma_f32_16x16x32_bf16 v[152:155], v[124:127], a[228:231], v[152:155]\n"
  "ds_read_b64_tr_b16 v[46:47], v100 offset:60416\n"
  "v_mfma_f32_16x16x32_bf16 v[156:159], v[112:115], a[172:175], v[156:159]\n"
  "s_add_u32 s51, s53, 72704\n"
  "v_mfma_f32_16x16x32_bf16 v[156:159], v[116:119], a[192:195], v[156:159]\n"
  "buffer_load_dwordx4 v95, s[40:43], s51 offen offset:2048 lds\n"
  "v_mfma_f32_16x16x32_bf16 v[156:159], v[120:123], a[212:215], v[156:159]\n"
  "v_mfma_f32_16x16x32_bf16 v[156:159], v[124:127], a[232:235], v[156:159]\n"
  "s_add_u32 s51, s53, 108544\n"
  "v_mfma_f32_16x16x32_bf16 v[160:163], v[112:115], a[176:179], v[160:163]\n"
  "buffer_load_dwordx4 v95, s[40:43], s51 offen offset:3072 lds\n"
  "v_mfma_f32_16x16x32_bf16 v[160:163], v[116:119], a[196:199], v[160:163]\n"
  "v_mfma_f32_16x16x32_bf16 v[160:163], v[120:123], a[216:219], v[160:163]\n"
  "v_mfma_f32_16x16x32_bf16 v[160:163], v[124:127], a[236:239], v[160:163]\n"
  "s_waitcnt vmcnt(14) lgkmcnt(0)\n"
  "s_barrier\n"
  "v_mfma_f32_16x16x32_bf16 v[164:167], v[32:35], a[160:163], v[164:167]\n"
  "s_add_u32 m0, s48, 114688\n"
  "s_add_u32 s51, s53, 148480\n"
  "ds_read_b64_tr_b16 v[112:113], v99 offset:16384\n"
  "ds_read_b64_tr_b16 v[114:115], v99 offset:24576\n"
  "v_mfma_f32_16x16x32_bf16 v[164:167], v[36:39], a[180:183], v[164:167]\n"
  "ds_read_b64_tr_b16 v[116:117], v99 offset:17408\n"
  "ds_read_b64_tr_b16 v[118:119], v99 offset:25600\n"
  "v_mfma_f32_16x16x32_bf16 v[164:167], v[40:43], a[200:203], v[164:167]\n"
  "ds_read_b64_tr_b16 v[120:121], v99 offset:18432\n"
  "ds_read_b64_tr_b16 v[122:123], v99 offset:26624\n"
  "v_mfma_f32_16x16x32_bf16 v[164:167], v[44:47], a[220:223], v[164:167]\n"
  "buffer_load_dwordx4 v95, s[40:43], s51 offen offset:0 lds\n"
  "ds_read_b64_tr_b16 v[124:125], v99 offset:19456\n"
  "v_mfma_f32_16x16x32_bf16 v[168:171], v[32:35], a[164:167], v[168:171]\n"
  "ds_read_b64_tr_b16 v[126:127], v99 offset:27648\n"
  "ds_read_b128 a[0:3], v89 offset:0\n"
  "v_mfma_f32_16x16x32_bf16 v[168:171], v[36:39], a[184:187], v[168:171]\n"
  "s_add_u32 s51, s53, 184320\n"
  "ds_read_b128 a[20:23], v89 offset:1024\n"
  "ds_read_b128 a[40:43], v89 offset:2048\n"
  "v_mfma_f32_16x16x32_bf16 v[168:171], v[40:43], a[204:207], v[168:171]\n"
  "ds_read_b128 a[60:63], v89 offset:3072\n"
  "ds_read_b128 a[4:7], v89 offset:4096\n"
  "v_mfma_f32_16x16x32_bf16 v[168:171], v[44:47], a[224:227], v[168:171]\n"
  "buffer_load_dwordx4 v95, s[40:43], s51 offen offset:1024 lds\n"
  "ds_read_b128 a[24:27], v89 offset:5120\n"
  "v_mfma_f32_16x16x32_bf16 v[172:175], v[32:35], a[168:171], v[172:175]\n"
  "ds_read_b128 a[44:47], v89 offset:6144\n"
  "ds_read_b128 a[64:67], v89 offset:7168\n"
  "v_mfma_f32_16x16x32_bf16 v[172:175], v[36:39], a[188:191], v[172:175]\n"
  "ds_read_b128 a[8:11], v89 offset:8192\n"
  "ds_read_b128 a[28:31], v89 offset:9216\n"
  "v_mfma_f32_16x16x32_bf16 v[172:175], v[40:43], a[208:211], v[172:175]\n"
  "s_add_u32 s51, s53, 220160\n"
  "ds_read_b128 a[48:51], v89 offset:10240\n"
  "ds_read_b128 a[68:71], v89 offset:11264\n"
  "v_mfma_f32_16x16x32_bf16 v[172:175], v[44:47], a[228:231], v[172:175]\n"
  "buffer_load_dwordx4 v95, s[40:43], s51 offen offset:2048 lds\n"
  "ds_read_b128 a[12:15], v89 offset:12288\n"
  "v_mfma_f32_16x16x32_bf16 v[176:179], v[32:35], a[172:175], v[176:179]\n"
  "ds_read_b128 a[32:35], v89 offset:13312\n"
  "ds_read_b128 a[52:55], v89 offset:14336\n"
  "v_mfma_f32_16x16x32_bf16 v[176:179], v[36:39], a[192:195], v[176:179]\n"
  "ds_read_b128 a[72:75], v89 offset:15360\n"
  "ds_read_b128 a[16:19], v91 offset:28672\n"
  "v_mfma_f32_16x16x32_bf16 v[176:179], v[40:43], a[212:215], v[176:179]\n"
  "ds_read_b128 a[36:39], v91 offset:29696\n"
  "ds_read_b128 a[56:59], v91 offset:30720\n"
  "v_mfma_f32_16x16x32_bf16 v[176:179], v[44:47], a[232:235], v[176:179]\n"
  "s_add_u32 s51, s53, 256000\n"
  "ds_read_b128 a[76:79], v91 offset:31744\n"
  "v_mfma_f32_16x16x32_bf16 v[180:183], v[32:35], a[176:179], v[180:183]\n"
  "buffer_load_dwordx4 v95, s[40:43], s51 offen offset:3072 lds\n"
  "v_mfma_f32_16x16x32_bf16 v[180:183], v[36:39], a[196:199], v[180:183]\n"
  "v_mfma_f32_16x16x32_bf16 v[180:183], v[40:43], a[216:219], v[180:183]\n"
  "v_mfma_f32_16x16x32_bf16 v[180:183], v[44:47], a[236:239], v[180:183]\n"
  "s_waitcnt vmcnt(13) lgkmcnt(0)\n"
  "s_barrier\n"
  "v_mfma_f32_16x16x32_bf16 v[144:147], v[112:115], a[0:3], v[144:147]\n"
  "s_add_u32 m0, s48, 0\n"
  "s_add_u32 s51, s53, 295936\n"
  "ds_read_b64_tr_b16 v[32:33], v99 offset:32768\n"
  "ds_read_b64_tr_b16 v[34:35], v99 offset:40960\n"
  "v_mfma_f32_16x16x32_bf16 v[144:147], v[116:119], a[20:23], v[144:147]\n"
  "ds_read_b64_tr_b16 v[36:37], v99 offset:33792\n"
  "ds_read_b64_tr_b16 v[38:39], v99 offset:41984\n"
  "v_mfma_f32_16x16x32_bf16 v[144:147], v[120:123], a[40:43], v[144:147]\n"
  "ds_read_b64_tr_b16 v[40:41], v99 offset:34816\n"
  "ds_read_b64_tr_b16 v[42:43], v99 offset:43008\n"
  "v_mfma_f32_16x16x32_bf16 v[144:147], v[124:127], a[60:63], v[144:147]\n"
  "buffer_load_dwordx4 v95, s[40:43], s51 offen offset:0 lds\n"
  "ds_read_b64_tr_b16 v[44:45], v99 offset:35840\n"
  "v_mfma_f32_16x16x32_bf16 v[148:151], v[112:115], a[4:7], v[148:151]\n"
  "s_add_u32 s51, s53, 331776\n"
  "ds_read_b64_tr_b16 v[46:47], v99 offset:44032\n"
  "ds_read_b128 a[80:83], v89 offset:49152\n"
  "v_mfma_f32_16x16x32_bf16 v[148:151], v[116:119], a[24:27], v[148:151]\n"
  "ds_read_b128 a[100:103], v89 offset:50176\n"
  "ds_read_b128 a[120:123], v89 offset:51200\n"
  "v_mfma_f32_16x16x32_bf16 v[148:151], v[120:123], a[44:47], v[148:151]\n"
  "ds_read_b128 a[140:143], v89 offset:52224\n"
  "ds_read_b128 a[84:87], v89 offset:53248\n"
  "v_mfma_f32_16x16x32_bf16 v[148:151], v[124:127], a[64:67], v[148:151]\n"
  "buffer_load_dwordx4 v95, s[40:43], s51 offen offset:1024 lds\n"
  "ds_read_b128 a[104:107], v89 offset:54272\n"
  "v_mfma_f32_16x16x32_bf16 v[152:155], v[112:115], a[8:11], v[152:155]\n"
  "s_add_u32 s51, s53, 367616\n"
  "ds_read_b128 a[124:127], v89 offset:55296\n"
  "ds_read_b128 a[144:147], v89 offset:56320\n"
  "v_mfma_f32_16x16x32_bf16 v[152:155], v[116:119], a[28:31], v[152:155]\n"
  "ds_read_b128 a[88:91], v89 offset:57344\n"
  "ds_read_b128 a[108:111], v89 offset:58368\n"
  "v_mfma_f32_16x16x32_bf16 v[152:155], v[120:123], a[48:51], v[152:155]\n"
  "ds_read_b128 a[128:131], v89 offset:59392\n"
  "ds_read_b128 a[148:151], v89 offset:60416\n"
  "v_mfma_f32_16x16x32_bf16 v[152:155], v[124:127], a[68:71], v[152:155]\n"
  "buffer_load_dwordx4 v95, s[40:43], s51 offen offset:2048 lds\n"
  "ds_read_b128 a[92:95], v89 offset:61440\n"
  "v_mfma_f32_16x16x32_bf16 v[156:159], v[112:115], a[12:15], v[156:159]\n"
  "s_add_u32 s51, s53, 403456\n"
  "ds_read_b128 a[112:115], v89 offset:62464\n"
  "ds_read_b128 a[132:135], v89 offset:63488\n"
  "v_mfma_f32_16x16x32_bf16 v[156:159], v[116:119], a[32:35], v[156:159]\n"
  "ds_read_b128 a[152:155], v89 offset:64512\n"
  "ds_read_b128 a[96:99], v91 offset:20480\n"
  "v_mfma_f32_16x16x32_bf16 v[156:159], v[120:123], a[52:55], v[156:159]\n"
  "ds_read_b128 a[116:119], v91 offset:21504\n"
  "ds_read_b128 a[136:139], v91 offset:22528\n"
  "v_mfma_f32_16x16x32_bf16 v[156:159], v[124:127], a[72:75], v[156:159]\n"
  "buffer_load_dwordx4 v95, s[40:43], s51 offen offset:3072 lds\n"
  "ds_read_b128 a[156:159], v91 offset:23552\n"
  "v_mfma_f32_16x16x32_bf16 v[160:163], v[112:115], a[16:19], v[160:163]\n"
  "s_add_u32 m0, s48, 16384\n"
  "s_add_u32 s51, s53, 2048\n"
  "v_mfma_f32_16x16x32_bf16 v[160:163], v[116:119], a[36:39], v[160:163]\n"
  "buffer_load_dwordx4 v96, s[40:43], s51 offen offset:0 lds\n"
  "v_mfma_f32_16x16x32_bf16 v[160:163], v[120:123], a[56:59], v[160:163]\n"
  "s_add_u32 s51, s53, 37888\n"
  "v_mfma_f32_16x16x32_bf16 v[160:163], v[124:127], a[76:79], v[160:163]\n"
  "s_waitcnt vmcnt(13) lgkmcnt(0)\n"
  "s_barrier\n"
  "v_mfma_f32_16x16x32_bf16 v[144:147], v[32:35], a[80:83], v[144:147]\n"
  "buffer_load_dwordx4 v96, s[40:43], s51 offen offset:1024 lds\n"
  "ds_read_b64_tr_b16 v[112:113], v101 offset:0\n"
  "ds_read_b64_tr_b16 v[114:115], v101 offset:8192\n"
  "v_mfma_f32_16x16x32_bf16 v[144:147], v[36:39], a[100:103], v[144:147]\n"
  "s_add_u32 s51, s53, 73728\n"
  "ds_read_b64_tr_b16 v[116:117], v101 offset:1024\n"
  "ds_read_b64_tr_b16 v[118:119], v101 offset:9216\n"
  "v_mfma_f32_16x16x32_bf16 v[144:147], v[40:43], a[120:123], v[144:147]\n"
  "ds_read_b64_tr_b16 v[120:121], v101 offset:2048\n"
  "ds_read_b64_tr_b16 v[122:123], v101 offset:10240\n"
  "v_mfma_f32_16x16x32_bf16 v[144:147], v[44:47], a[140:143], v[144:147]\n"
  "buffer_load_dwordx4 v96, s[40:43], s51 offen offset:2048 lds\n"
  "ds_read_b64_tr_b16 v[124:125], v101 offset:3072\n"
  "v_mfma_f32_16x16x32_bf16 v[148:151], v[32:35], a[84:87], v[148:151]\n"
  "ds_read_b64_tr_b16 v[126:127], v101 offset:11264\n"
  "ds_read_b128 a[160:163], v90 offset:0\n"
  "v_mfma_f32_16x16x32_bf16 v[148:151], v[36:39], a[104:107], v[148:151]\n"
  "s_add_u32 s51, s53, 109568\n"
  "ds_read_b128 a[180:183], v90 offset:1024\n"
  "ds_read_b128 a[200:203], v90 offset:2048\n"
  "v_mfma_f32_16x16x32_bf16 v[148:151], v[40:43], a[124:127], v[148:151]\n"
  "ds_read_b128 a[220:223], v90 offset:3072\n"
  "ds_read_b128 a[164:167], v90 offset:4096\n"
  "v_mfma_f32_16x16x32_bf16 v[148:151], v[44:47], a[144:147], v[148:151]\n"
  "buffer_load_dwordx4 v96, s[40:43], s51 offen offset:3072 lds\n"
  "ds_read_b128 a[184:187], v90 offset:5120\n"
  "v_mfma_f32_16x16x32_bf16 v[152:155], v[32:35], a[88:91], v[152:155]\n"
  "ds_read_b128 a[204:207], v90 offset:6144\n"
  "ds_read_b128 a[224:227], v90 offset:7168\n"
  "v_mfma_f32_16x16x32_bf16 v[152:155], v[36:39], a[108:111], v[152:155]\n"
  "ds_read_b128 a[168:171], v90 offset:8192\n"
  "ds_read_b128 a[188:191], v90 offset:9216\n"
  "v_mfma_f32_16x16x32_bf16 v[152:155], v[40:43], a[128:131], v[152:155]\n"
  "s_add_u32 m0, s48, 32768\n"
  "s_add_u32 s51, s53, 149504\n"
  "ds_read_b128 a[208:211], v90 offset:10240\n"
  "ds_read_b128 a[228:231], v90 offset:11264\n"
  "v_mfma_f32_16x16x32_bf16 v[152:155], v[44:47], a[148:151], v[152:155]\n"
  "buffer_load_dwordx4 v96, s[40:43], s51 offen offset:0 lds\n"
  "ds_read_b128 a[172:175], v90 offset:12288\n"
  "v_mfma_f32_16x16x32_bf16 v[156:159], v[32:35], a[92:95], v[156:159]\n"
  "ds_read_b128 a[192:195], v90 offset:13312\n"
  "ds_read_b128 a[212:215], v90 offset:14336\n"
  "v_mfma_f32_16x16x32_bf16 v[156:159], v[36:39], a[112:115], v[156:159]\n"
  "ds_read_b128 a[232:235], v90 offset:15360\n"
  "ds_read_b128 a[176:179], v91 offset:16384\n"
  "v_mfma_f32_16x16x32_bf16 v[156:159], v[40:43], a[132:135], v[156:159]\n"
  "ds_read_b128 a[196:199], v91 offset:17408\n"
  "ds_read_b128 a[216:219], v91 offset:18432\n"
  "v_mfma_f32_16x16x32_bf16 v[156:159], v[44:47], a[152:155], v[156:159]\n"
  "s_add_u32 s51, s53, 185344\n"
  "ds_read_b128 a[236:239], v91 offset:19456\n"
  "v_mfma_f32_16x16x32_bf16 v[160:163], v[32:35], a[96:99], v[160:163]\n"
  "buffer_load_dwordx4 v96, s[40:43], s51 offen offset:1024 lds\n"
  "v_mfma_f32_16x16x32_bf16 v[160:163], v[36:39], a[116:119], v[160:163]\n"
  "v_mfma_f32_16x16x32_bf16 v[160:163], v[40:43], a[136:139], v[160:163]\n"
  "v_mfma_f32_16x16x32_bf16 v[160:163], v[44:47], a[156:159], v[160:163]\n"
  "s_waitcnt vmcnt(14) lgkmcnt(0)\n"
  "s_barrier\n"
  "v_mfma_f32_16x16x32_bf16 v[144:147], v[112:115], a[160:163], v[144:147]\n"
  "s_add_u32 s51, s53, 221184\n"
  "ds_read_b64_tr_b16 v[32:33], v100 offset:32768\n"
  "v_mfma_f32_16x16x32_bf16 v[144:147], v[116:119], a[180:183], v[144:147]\n"
  "buffer_load_dwordx4 v96, s[40:43], s51 offen offset:2048 lds\n"
  "ds_read_b64_tr_b16 v[34:35], v100 offset:40960\n"
  "v_mfma_f32_16x16x32_bf16 v[144:147], v[120:123], a[200:203], v[144:147]\n"
  "v_mfma_f32_16x16x32_bf16 v[144:147], v[124:127], a[220:223], v[144:147]\n"
  "ds_read_b64_tr_b16 v[36:37], v100 offset:33792\n"
  "v_mfma_f32_16x16x32_bf16 v[148:151], v[112:115], a[164:167], v[148:151]\n"
  "ds_read_b64_tr_b16 v[38:39], v100 offset:41984\n"
  "v_mfma_f32_16x16x32_bf16 v[148:151], v[116:119], a[184:187], v[148:151]\n"
  "s_add_u32 s51, s53, 257024\n"
  "v_mfma_f32_16x16x32_bf16 v[148:151], v[120:123], a[204:207], v[148:151]\n"
  "buffer_load_dwordx4 v96, s[40:43], s51 offen offset:3072 lds\n"
  "ds_read_b64_tr_b16 v[40:41], v100 offset:34816\n"
  "v_mfma_f32_16x16x32_bf16 v[148:151], v[124:127], a[224:227], v[148:151]\n"
  "v_mfma_f32_16x16x32_bf16 v[152:155], v[112:115], a[168:171], v[152:155]\n"
  "ds_read_b64_tr_b16 v[42:43], v100 offset:43008\n"
  "v_mfma_f32_16x16x32_bf16 v[152:155], v[116:119], a[188:191], v[152:155]\n"
  "ds_read_b64_tr_b16 v[44:45], v100 offset:35840\n"
  "v_mfma_f32_16x16x32_bf16 v[152:155], v[120:123], a[208:211], v[152:155]\n"
  "s_add_u32 m0, s48, 49152\n"
  "s_add_u32 s51, s53, 296960\n"
  "v_mfma_f32_16x16x32_bf16 v[152:155], v[124:127], a[228:231], v[152:155]\n"
  "buffer_load_dwordx4 v96, s[40:43], s51 offen offset:0 lds\n"
  "ds_read_b64_tr_b16 v[46:47], v100 offset:44032\n"
  "v_mfma_f32_16x16x32_bf16 v[156:159], v[112:115], a[172:175], v[156:159]\n"
  "v_cvt_pk_bf16_f32 v0, v144, v145\n"
  "v_cvt_pk_bf16_f32 v1, v146, v147\n"
  "v_mfma_f32_16x16x32_bf16 v[156:159], v[116:119], a[192:195], v[156:159]\n"
  "v_cvt_pk_bf16_f32 v2, v148, v149\n"
  "v_cvt_pk_bf16_f32 v3, v150, v151\n"
  "v_mfma_f32_16x16x32_bf16 v[156:159], v[120:123], a[212:215], v[156:159]\n"
  "v_permlane16_swap_b32 v0, v2\n"
  "v_mfma_f32_16x16x32_bf16 v[156:159], v[124:127], a[232:235], v[156:159]\n"
  "s_add_u32 s51, s53, 332800\n"
  "v_permlane16_swap_b32 v1, v3\n"
  "v_mfma_f32_16x16x32_bf16 v[160:163], v[112:115], a[176:179], v[160:163]\n"
  "buffer_load_dwordx4 v96, s[40:43], s51 offen offset:1024 lds\n"
  "buffer_store_dwordx4 v[0:3], v89, s[36:39], s16 offen offset:0 sc1\n"
  "v_mfma_f32_16x16x32_bf16 v[160:163], v[116:119], a[196:199], v[160:163]\n"
  "v_mfma_f32_16x16x32_bf16 v[160:163], v[120:123], a[216:219], v[160:163]\n"
  "v_mfma_f32_16x16x32_bf16 v[160:163], v[124:127], a[236:239], v[160:163]\n"
  "s_waitcnt vmcnt(15) lgkmcnt(0)\n"
  "s_barrier\n"
  "v_mfma_f32_16x16x32_bf16 v[164:167], v[32:35], a[0:3], v[164:167]\n"
  "s_add_u32 s51, s53, 368640\n"
  "ds_read_b64_tr_b16 v[112:113], v100 offset:49152\n"
  "v_cvt_pk_bf16_f32 v4, v152, v153\n"
  "v_mfma_f32_16x16x32_bf16 v[164:167], v[36:39], a[20:23], v[164:167]\n"
  "buffer_load_dwordx4 v96, s[40:43], s51 offen offset:2048 lds\n"
  "ds_read_b64_tr_b16 v[114:115], v100 offset:57344\n"
  "v_mfma_f32_16x16x32_bf16 v[164:167], v[40:43], a[40:43], v[164:167]\n"
  "v_cvt_pk_bf16_f32 v5, v154, v155\n"
  "v_cvt_pk_bf16_f32 v6, v156, v157\n"
  "v_mfma_f32_16x16x32_bf16 v[164:167], v[44:47], a[60:63], v[164:167]\n"
  "ds_read_b64_tr_b16 v[116:117], v100 offset:50176\n"
  "v_cvt_pk_bf16_f32 v7, v158, v159\n"
  "v_mfma_f32_16x16x32_bf16 v[168:171], v[32:35], a[4:7], v[168:171]\n"
  "ds_read_b64_tr_b16 v[118:119], v100 offset:58368\n"
  "v_cvt_pk_bf16_f32 v8, v160, v161\n"
  "v_mfma_f32_16x16x32_bf16 v[168:171], v[36:39], a[24:27], v[168:171]\n"
  "s_add_u32 s51, s53, 404480\n"
  "v_permlane16_swap_b32 v4, v6\n"
  "v_mfma_f32_16x16x32_bf16 v[168:171], v[40:43], a[44:47], v[168:171]\n"
  "buffer_load_dwordx4 v96, s[40:43], s51 offen offset:3072 lds\n"
  "ds_read_b64_tr_b16 v[120:121], v100 offset:51200\n"
  "v_mfma_f32_16x16x32_bf16 v[168:171], v[44:47], a[64:67], v[168:171]\n"
  "v_permlane16_swap_b32 v5, v7\n"
  "v_mfma_f32_16x16x32_bf16 v[172:175], v[32:35], a[8:11], v[172:175]\n"
  "ds_read_b64_tr_b16 v[122:123], v100 offset:59392\n"
  "buffer_store_dwordx4 v[4:7], v89, s[36:39], s16 offen offset:1024 sc1\n"
  "v_mfma_f32_16x16x32_bf16 v[172:175], v[36:39], a[28:31], v[172:175]\n"
  "ds_read_b64_tr_b16 v[124:125], v100 offset:52224\n"
  "v_cvt_pk_bf16_f32 v9, v162, v163\n"
  "v_mfma_f32_16x16x32_bf16 v[172:175], v[40:43], a[48:51], v[172:175]\n"
  "s_add_u32 m0, s48, 65536\n"
  "s_add_u32 s51, s60, 2048\n"
  "v_cvt_pk_bf16_f32 v10, v160, v161\n"
  "v_mfma_f32_16x16x32_bf16 v[172:175], v[44:47], a[68:71], v[172:175]\n"
  "buffer_load_dwordx4 v96, s[40:43], s51 offen offset:0 lds\n"
  "ds_read_b64_tr_b16 v[126:127], v100 offset:60416\n"
  "v_mfma_f32_16x16x32_bf16 v[176:179], v[32:35], a[12:15], v[176:179]\n"
  "v_cvt_pk_bf16_f32 v11, v162, v163\n"
  "v_mfma_f32_16x16x32_bf16 v[176:179], v[36:39], a[32:35], v[176:179]\n"
  "v_permlane16_swap_b32 v8, v10\n"
  "v_mfma_f32_16x16x32_bf16 v[176:179], v[40:43], a[52:55], v[176:179]\n"
  "v_permlane16_swap_b32 v9, v11\n"
  "v_mfma_f32_16x16x32_bf16 v[176:179], v[44:47], a[72:75], v[176:179]\n"
  "s_add_u32 s51, s60, 37888\n"
  "buffer_store_dwordx4 v[8:11], v89, s[36:39], s16 offen offset:2048 sc1\n"
  "v_mfma_f32_16x16x32_bf16 v[180:183], v[32:35], a[16:19], v[180:183]\n"
  "buffer_load_dwordx4 v96, s[40:43], s51 offen offset:1024 lds\n"
  "v_mfma_f32_16x16x32_bf16 v[180:183], v[36:39], a[36:39], v[180:183]\n"
  "v_mfma_f32_16x16x32_bf16 v[180:183], v[40:43], a[56:59], v[180:183]\n"
  "v_mfma_f32_16x16x32_bf16 v[180:183], v[44:47], a[76:79], v[180:183]\n"
  "s_waitcnt vmcnt(17) lgkmcnt(0)\n"
  "s_barrier\n"
  "v_mfma_f32_16x16x32_bf16 v[164:167], v[112:115], a[80:83], v[164:167]\n"
  "s_add_u32 s51, s60, 73728\n"
  "ds_read_b64_tr_b16 v[32:33], v99 offset:0\n"
  "v_mfma_f32_16x16x32_bf16 v[164:167], v[116:119], a[100:103], v[164:167]\n"
  "buffer_load_dwordx4 v96, s[40:43], s51 offen offset:2048 lds\n"
  "ds_read_b64_tr_b16 v[34:35], v99 offset:8192\n"
  "v_mfma_f32_16x16x32_bf16 v[164:167], v[120:123], a[120:123], v[164:167]\n"
  "v_mfma_f32_16x16x32_bf16 v[164:167], v[124:127], a[140:143], v[164:167]\n"
  "ds_read_b64_tr_b16 v[36:37], v99 offset:1024\n"
  "v_mfma_f32_16x16x32_bf16 v[168:171], v[112:115], a[84:87], v[168:171]\n"
  "ds_read_b64_tr_b16 v[38:39], v99 offset:9216\n"
  "v_mfma_f32_16x16x32_bf16 v[168:171], v[116:119], a[104:107], v[168:171]\n"
  "s_add_u32 s51, s60, 109568\n"
  "v_mfma_f32_16x16x32_bf16 v[168:171], v[120:123], a[124:127], v[168:171]\n"
  "buffer_load_dwordx4 v96, s[40:43], s51 offen offset:3072 lds\n"
  "ds_read_b64_tr_b16 v[40:41], v99 offset:2048\n"
  "v_mfma_f32_16x16x32_bf16 v[168:171], v[124:127], a[144:147], v[168:171]\n"
  "v_mfma_f32_16x16x32_bf16 v[172:175], v[112:115], a[88:91], v[172:175]\n"
  "ds_read_b64_tr_b16 v[42:43], v99 offset:10240\n"
  "s_nop 0\n"
  "s_cbranch_execz Ltramp%=\n"
  "v_mfma_f32_16x16x32_bf16 v[172:175], v[116:119], a[108:111], v[172:175]\n"
  "ds_read_b64_tr_b16 v[44:45], v99 offset:3072\n"
  "v_mfma_f32_16x16x32_bf16 v[172:175], v[120:123], a[128:131], v[172:175]\n"
  "v_mfma_f32_16x16x32_bf16 v[172:175], v[124:127], a[148:151], v[172:175]\n"
  "ds_read_b64_tr_b16 v[46:47], v99 offset:11264\n"
  "v_mfma_f32_16x16x32_bf16 v[176:179], v[112:115], a[92:95], v[176:179]\n"
  "v_mfma_f32_16x16x32_bf16 v[176:179], v[116:119], a[112:115], v[176:179]\n"
  "v_mfma_f32_16x16x32_bf16 v[176:179], v[120:123], a[132:135], v[176:179]\n"
  "v_mfma_f32_16x16x32_bf16 v[176:179], v[124:127], a[152:155], v[176:179]\n"
  "v_mfma_f32_16x16x32_bf16 v[180:183], v[112:115], a[96:99], v[180:183]\n"
  "v_mfma_f32_16x16x32_bf16 v[180:183], v[116:119], a[116:119], v[180:183]\n"
  "v_mfma_f32_16x16x32_bf16 v[180:183], v[120:123], a[136:139], v[180:183]\n"
  "v_mfma_f32_16x16x32_bf16 v[180:183], v[124:127], a[156:159], v[180:183]\n"
  "s_waitcnt vmcnt(15) lgkmcnt(0)\n"
  "s_barrier\n"
  "v_mfma_f32_16x16x32_bf16 v[164:167], v[32:35], a[160:163], v[164:167]\n"
  "ds_read_b64_tr_b16 v[112:113], v99 offset:16384\n"
  "v_mfma_f32_16x16x32_bf16 v[164:167], v[36:39], a[180:183], v[164:167]\n"
  "ds_read_b64_tr_b16 v[114:115], v99 offset:24576\n"
  "v_mfma_f32_16x16x32_bf16 v[164:167], v[40:43], a[200:203], v[164:167]\n"
  "v_mfma_f32_16x16x32_bf16 v[164:167], v[44:47], a[220:223], v[164:167]\n"
  "ds_read_b64_tr_b16 v[116:117], v99 offset:17408\n"
  "v_mfma_f32_16x16x32_bf16 v[168:171], v[32:35], a[164:167], v[168:171]\n"
  "ds_read_b64_tr_b16 v[118:119], v99 offset:25600\n"
  "v_mfma_f32_16x16x32_bf16 v[168:171], v[36:39], a[184:187], v[168:171]\n"
  "v_mfma_f32_16x16x32_bf16 v[168:171], v[40:43], a[204:207], v[168:171]\n"
  "ds_read_b64_tr_b16 v[120:121], v99 offset:18432\n"
  "v_mfma_f32_16x16x32_bf16 v[168:171], v[44:47], a[224:227], v[168:171]\n"
  "v_mfma_f32_16x16x32_bf16 v[172:175], v[32:35], a[168:171], v[172:175]\n"
  "ds_read_b64_tr_b16 v[122:123], v99 offset:26624\n"
  "v_mfma_f32_16x16x32_bf16 v[172:175], v[36:39], a[188:191], v[172:175]\n"
  "ds_read_b64_tr_b16 v[124:125], v99 offset:19456\n"
  "v_mfma_f32_16x16x32_bf16 v[172:175], v[40:43], a[208:211], v[172:175]\n"
  "v_mfma_f32_16x16x32_bf16 v[172:175], v[44:47], a[228:231], v[172:175]\n"
  "ds_read_b64_tr_b16 v[126:127], v99 offset:27648\n"
  "v_mfma_f32_16x16x32_bf16 v[176:179], v[32:35], a[172:175], v[176:179]\n"
  "v_cvt_pk_bf16_f32 v12, v164, v165\n"
  "v_cvt_pk_bf16_f32 v13, v166, v167\n"
  "v_mfma_f32_16x16x32_bf16 v[176:179], v[36:39], a[192:195], v[176:179]\n"
  "v_cvt_pk_bf16_f32 v14, v168, v169\n"
  "v_cvt_pk_bf16_f32 v15, v170, v171\n"
  "v_mfma_f32_16x16x32_bf16 v[176:179], v[40:43], a[212:215], v[176:179]\n"
  "v_permlane16_swap_b32 v12, v14\n"
  "v_mfma_f32_16x16x32_bf16 v[176:179], v[44:47], a[232:235], v[176:179]\n"
  "v_permlane16_swap_b32 v13, v15\n"
  "v_mfma_f32_16x16x32_bf16 v[180:183], v[32:35], a[176:179], v[180:183]\n"
  "buffer_store_dwordx4 v[12:15], v89, s[36:39], s17 offen offset:0 sc1\n"
  "v_mfma_f32_16x16x32_bf16 v[180:183], v[36:39], a[196:199], v[180:183]\n"
  "v_mfma_f32_16x16x32_bf16 v[180:183], v[40:43], a[216:219], v[180:183]\n"
  "v_mfma_f32_16x16x32_bf16 v[180:183], v[44:47], a[236:239], v[180:183]\n"
  "s_waitcnt vmcnt(12) lgkmcnt(0)\n"
  "s_barrier\n"
  "v_mfma_f32_16x16x32_bf16 v[184:187], v[112:115], a[0:3], v[184:187]\n"
  "ds_read_b64_tr_b16 v[32:33], v99 offset:32768\n"
  "v_cvt_pk_bf16_f32 v16, v172, v173\n"
  "v_mfma_f32_16x16x32_bf16 v[184:187], v[116:119], a[20:23], v[184:187]\n"
  "ds_read_b64_tr_b16 v[34:35], v99 offset:40960\n"
  "v_cvt_pk_bf16_f32 v17, v174, v175\n"
  "v_mfma_f32_16x16x32_bf16 v[184:187], v[120:123], a[40:43], v[184:187]\n"
  "v_cvt_pk_bf16_f32 v18, v176, v177\n"
  "v_cvt_pk_bf16_f32 v19, v178, v179\n"
  "v_mfma_f32_16x16x32_bf16 v[184:187], v[124:127], a[60:63], v[184:187]\n"
  "ds_read_b64_tr_b16 v[36:37], v99 offset:33792\n"
  "v_mfma_f32_16x16x32_bf16 v[188:191], v[112:115], a[4:7], v[188:191]\n"
  "ds_read_b64_tr_b16 v[38:39], v99 offset:41984\n"
  "v_cvt_pk_bf16_f32 v20, v180, v181\n"
  "v_mfma_f32_16x16x32_bf16 v[188:191], v[116:119], a[24:27], v[188:191]\n"
  "v_permlane16_swap_b32 v16, v18\n"
  "v_mfma_f32_16x16x32_bf16 v[188:191], v[120:123], a[44:47], v[188:191]\n"
  "ds_read_b64_tr_b16 v[40:41], v99 offset:34816\n"
  "v_cvt_pk_bf16_f32 v21, v182, v183\n"
  "v_mfma_f32_16x16x32_bf16 v[188:191], v[124:127], a[64:67], v[188:191]\n"
  "v_permlane16_swap_b32 v17, v19\n"
  "v_mfma_f32_16x16x32_bf16 v[192:195], v[112:115], a[8:11], v[192:195]\n"
  "ds_read_b64_tr_b16 v[42:43], v99 offset:43008\n"
  "buffer_store_dwordx4 v[16:19], v89, s[36:39], s17 offen offset:1024 sc1\n"
  "v_mfma_f32_16x16x32_bf16 v[192:195], v[116:119], a[28:31], v[192:195]\n"
  "ds_read_b64_tr_b16 v[44:45], v99 offset:35840\n"
  "v_cvt_pk_bf16_f32 v22, v180, v181\n"
  "v_mfma_f32_16x16x32_bf16 v[192:195], v[120:123], a[48:51], v[192:195]\n"
  "v_cvt_pk_bf16_f32 v23, v182, v183\n"
  "v_mfma_f32_16x16x32_bf16 v[192:195], v[124:127], a[68:71], v[192:195]\n"
  "ds_read_b64_tr_b16 v[46:47], v99 offset:44032\n"
  "v_mfma_f32_16x16x32_bf16 v[196:199], v[112:115], a[12:15], v[196:199]\n"
  "v_permlane16_swap_b32 v20, v22\n"
  "v_mfma_f32_16x16x32_bf16 v[196:199], v[116:119], a[32:35], v[196:199]\n"
  "v_permlane16_swap_b32 v21, v23\n"
  "v_mfma_f32_16x16x32_bf16 v[196:199], v[120:123], a[52:55], v[196:199]\n"
  "buffer_store_dwordx4 v[20:23], v89, s[36:39], s17 offen offset:2048 sc1\n"
  "v_mfma_f32_16x16x32_bf16 v[196:199], v[124:127], a[72:75], v[196:199]\n"
  "v_mfma_f32_16x16x32_bf16 v[200:203], v[112:115], a[16:19], v[200:203]\n"
  "v_mfma_f32_16x16x32_bf16 v[200:203], v[116:119], a[36:39], v[200:203]\n"
  "v_mfma_f32_16x16x32_bf16 v[200:203], v[120:123], a[56:59], v[200:203]\n"
  "v_mfma_f32_16x16x32_bf16 v[200:203], v[124:127], a[76:79], v[200:203]\n"
  "s_waitcnt vmcnt(9) lgkmcnt(0)\n"
  "s_barrier\n"
  "v_mfma_f32_16x16x32_bf16 v[184:187], v[32:35], a[80:83], v[184:187]\n"
  "ds_read_b64_tr_b16 v[112:113], v99 offset:49152\n"
  "v_mfma_f32_16x16x32_bf16 v[184:187], v[36:39], a[100:103], v[184:187]\n"
  "ds_read_b64_tr_b16 v[114:115], v99 offset:57344\n"
  "v_mfma_f32_16x16x32_bf16 v[184:187], v[40:43], a[120:123], v[184:187]\n"
  "v_mfma_f32_16x16x32_bf16 v[184:187], v[44:47], a[140:143], v[184:187]\n"
  "ds_read_b64_tr_b16 v[116:117], v99 offset:50176\n"
  "v_mfma_f32_16x16x32_bf16 v[188:191], v[32:35], a[84:87], v[188:191]\n"
  "ds_read_b64_tr_b16 v[118:119], v99 offset:58368\n"
  "v_mfma_f32_16x16x32_bf16 v[188:191], v[36:39], a[104:107], v[188:191]\n"
  "v_mfma_f32_16x16x32_bf16 v[188:191], v[40:43], a[124:127], v[188:191]\n"
  "ds_read_b64_tr_b16 v[120:121], v99 offset:51200\n"
  "v_mfma_f32_16x16x32_bf16 v[188:191], v[44:47], a[144:147], v[188:191]\n"
  "v_mfma_f32_16x16x32_bf16 v[192:195], v[32:35], a[88:91], v[192:195]\n"
  "ds_read_b64_tr_b16 v[122:123], v99 offset:59392\n"
  "v_mfma_f32_16x16x32_bf16 v[192:195], v[36:39], a[108:111], v[192:195]\n"
  "ds_read_b64_tr_b16 v[124:125], v99 offset:52224\n"
  "v_mfma_f32_16x16x32_bf16 v[192:195], v[40:43], a[128:131], v[192:195]\n"
  "v_mfma_f32_16x16x32_bf16 v[192:195], v[44:47], a[148:151], v[192:195]\n"
  "ds_read_b64_tr_b16 v[126:127], v99 offset:60416\n"
  "v_mfma_f32_16x16x32_bf16 v[196:199], v[32:35], a[92:95], v[196:199]\n"
  "v_mfma_f32_16x16x32_bf16 v[196:199], v[36:39], a[112:115], v[196:199]\n"
  "v_mfma_f32_16x16x32_bf16 v[196:199], v[40:43], a[132:135], v[196:199]\n"
  "v_mfma_f32_16x16x32_bf16 v[196:199], v[44:47], a[152:155], v[196:199]\n"
  "v_mfma_f32_16x16x32_bf16 v[200:203], v[32:35], a[96:99], v[200:203]\n"
  "v_mfma_f32_16x16x32_bf16 v[200:203], v[36:39], a[116:119], v[200:203]\n"
  "v_mfma_f32_16x16x32_bf16 v[200:203], v[40:43], a[136:139], v[200:203]\n"
  "v_mfma_f32_16x16x32_bf16 v[200:203], v[44:47], a[156:159], v[200:203]\n"
  "s_waitcnt vmcnt(3) lgkmcnt(0)\n"
  "s_barrier\n"
  "v_mfma_f32_16x16x32_bf16 v[184:187], v[112:115], a[160:163], v[184:187]\n"
  "ds_read_b64_tr_b16 v[32:33], v100 offset:0\n"
  "ds_read_b64_tr_b16 v[34:35], v100 offset:8192\n"
  "v_mfma_f32_16x16x32_bf16 v[184:187], v[116:119], a[180:183], v[184:187]\n"
  "ds_read_b64_tr_b16 v[36:37], v100 offset:1024\n"
  "ds_read_b64_tr_b16 v[38:39], v100 offset:9216\n"
  "v_mfma_f32_16x16x32_bf16 v[184:187], v[120:123], a[200:203], v[184:187]\n"
  "ds_read_b64_tr_b16 v[40:41], v100 offset:2048\n"
  "ds_read_b64_tr_b16 v[42:43], v100 offset:10240\n"
  "v_mfma_f32_16x16x32_bf16 v[184:187], v[124:127], a[220:223], v[184:187]\n"
  "ds_read_b64_tr_b16 v[44:45], v100 offset:3072\n"
  "v_mfma_f32_16x16x32_bf16 v[188:191], v[112:115], a[164:167], v[188:191]\n"
  "ds_read_b64_tr_b16 v[46:47], v100 offset:11264\n"
  "ds_read_b128 a[0:3], v90 offset:16384\n"
  "v_mfma_f32_16x16x32_bf16 v[188:191], v[116:119], a[184:187], v[188:191]\n"
  "ds_read_b128 a[20:23], v90 offset:17408\n"
  "ds_read_b128 a[40:43], v90 offset:18432\n"
  "v_mfma_f32_16x16x32_bf16 v[188:191], v[120:123], a[204:207], v[188:191]\n"
  "ds_read_b128 a[60:63], v90 offset:19456\n"
  "ds_read_b128 a[4:7], v90 offset:20480\n"
  "v_mfma_f32_16x16x32_bf16 v[188:191], v[124:127], a[224:227], v[188:191]\n"
  "ds_read_b128 a[24:27], v90 offset:21504\n"
  "v_mfma_f32_16x16x32_bf16 v[192:195], v[112:115], a[168:171], v[192:195]\n"
  "buffer_load_dwordx4 v[64:67], v212, s[36:39], s61 offen offset:0 sc1\n"
  "ds_read_b128 a[44:47], v90 offset:22528\n"
  "ds_read_b128 a[64:67], v90 offset:23552\n"
  "v_mfma_f32_16x16x32_bf16 v[192:195], v[116:119], a[188:191], v[192:195]\n"
  "ds_read_b128 a[8:11], v90 offset:24576\n"
  "ds_read_b128 a[28:31], v90 offset:25600\n"
  "v_mfma_f32_16x16x32_bf16 v[192:195], v[120:123], a[208:211], v[192:195]\n"
  "ds_read_b128 a[48:51], v90 offset:26624\n"
  "ds_read_b128 a[68:71], v90 offset:27648\n"
  "v_mfma_f32_16x16x32_bf16 v[192:195], v[124:127], a[228:231], v[192:195]\n"
  "buffer_load_dwordx4 v[68:71], v212, s[36:39], s61 offen offset:1024 sc1\n"
  "ds_read_b128 a[12:15], v90 offset:28672\n"
  "v_mfma_f32_16x16x32_bf16 v[196:199], v[112:115], a[172:175], v[196:199]\n"
  "ds_read_b128 a[32:35], v90 offset:29696\n"
  "ds_read_b128 a[52:55], v90 offset:30720\n"
  "v_mfma_f32_16x16x32_bf16 v[196:199], v[116:119], a[192:195], v[196:199]\n"
  "ds_read_b128 a[72:75], v90 offset:31744\n"
  "ds_read_b128 a[16:19], v91 offset:24576\n"
  "v_mfma_f32_16x16x32_bf16 v[196:199], v[120:123], a[212:215], v[196:199]\n"
  "ds_read_b128 a[36:39], v91 offset:25600\n"
  "ds_read_b128 a[56:59], v91 offset:26624\n"
  "v_mfma_f32_16x16x32_bf16 v[196:199], v[124:127], a[232:235], v[196:199]\n"
  "buffer_load_dwordx4 v[72:75], v213, s[36:39], s61 offen offset:2048 sc1\n"
  "ds_read_b128 a[76:79], v91 offset:27648\n"
  "v_mfma_f32_16x16x32_bf16 v[200:203], v[112:115], a[176:179], v[200:203]\n"
  "v_mfma_f32_16x16x32_bf16 v[200:203], v[116:119], a[196:199], v[200:203]\n"
  "v_mfma_f32_16x16x32_bf16 v[200:203], v[120:123], a[216:219], v[200:203]\n"
  "v_mfma_f32_16x16x32_bf16 v[200:203], v[124:127], a[236:239], v[200:203]\n"
  "s_waitcnt lgkmcnt(0)\n"
  "v_mfma_f32_16x16x32_bf16 v[184:187], v[32:35], a[0:3], v[184:187]\n"
  "v_mfma_f32_16x16x32_bf16 v[184:187], v[36:39], a[20:23], v[184:187]\n"
  "v_mfma_f32_16x16x32_bf16 v[184:187], v[40:43], a[40:43], v[184:187]\n"
  "v_mfma_f32_16x16x32_bf16 v[184:187], v[44:47], a[60:63], v[184:187]\n"
  "v_mfma_f32_16x16x32_bf16 v[188:191], v[32:35], a[4:7], v[188:191]\n"
  "v_mfma_f32_16x16x32_bf16 v[188:191], v[36:39], a[24:27], v[188:191]\n"
  "v_mfma_f32_16x16x32_bf16 v[188:191], v[40:43], a[44:47], v[188:191]\n"
  "buffer_load_dwordx4 v[76:79], v212, s[36:39], s62 offen offset:0 sc1\n"
  "v_mfma_f32_16x16x32_bf16 v[188:191], v[44:47], a[64:67], v[188:191]\n"
  "v_mfma_f32_16x16x32_bf16 v[192:195], v[32:35], a[8:11], v[192:195]\n"
  "v_mfma_f32_16x16x32_bf16 v[192:195], v[36:39], a[28:31], v[192:195]\n"
  "buffer_load_dwordx4 v[80:83], v212, s[36:39], s62 offen offset:1024 sc1\n"
  "v_mfma_f32_16x16x32_bf16 v[192:195], v[40:43], a[48:51], v[192:195]\n"
  "v_mfma_f32_16x16x32_bf16 v[192:195], v[44:47], a[68:71], v[192:195]\n"
  "ds_write_b128 v210, v[184:187] offset:0\n"
  "v_mfma_f32_16x16x32_bf16 v[196:199], v[32:35], a[12:15], v[196:199]\n"
  "buffer_load_dwordx4 v[84:87], v213, s[36:39], s62 offen offset:2048 sc1\n"
  "v_mfma_f32_16x16x32_bf16 v[196:199], v[36:39], a[32:35], v[196:199]\n"
  "v_mfma_f32_16x16x32_bf16 v[196:199], v[40:43], a[52:55], v[196:199]\n"
  "v_mfma_f32_16x16x32_bf16 v[196:199], v[44:47], a[72:75], v[196:199]\n"
  "ds_write_b128 v210, v[188:191] offset:4096\n"
  "v_mfma_f32_16x16x32_bf16 v[200:203], v[32:35], a[16:19], v[200:203]\n"
  "v_mfma_f32_16x16x32_bf16 v[200:203], v[36:39], a[36:39], v[200:203]\n"
  "v_mfma_f32_16x16x32_bf16 v[200:203], v[40:43], a[56:59], v[200:203]\n"
  "v_mfma_f32_16x16x32_bf16 v[200:203], v[44:47], a[76:79], v[200:203]\n"
  "ds_write_b128 v210, v[192:195] offset:8192\n"
  "s_nop 1\n"
  "ds_write_b128 v210, v[196:199] offset:12288\n"
  "s_nop 3\n"
  "ds_write_b128 v210, v[200:203] offset:16384\n"
  "s_waitcnt lgkmcnt(0)\n"
  "s_barrier\n"
  "ds_read_b128 v[0:3], v211 offset:0\n"
  "ds_read_b128 v[4:7], v211 offset:256\n"
  "ds_read_b128 v[8:11], v211 offset:8192\n"
  "ds_read_b128 v[12:15], v211 offset:8448\n"
  "v_mov_b32 v24, 0x3f80\n"
  "v_mov_b32 v25, 0x3f800000\n"
  "ds_read_b128 v[16:19], v211 offset:16384\n"
  "ds_read_b128 v[20:23], v211 offset:16640\n"
  "s_waitcnt lgkmcnt(5)\n"
  "v_dot2c_f32_bf16 v3, v25, v225\n"
  "v_dot2c_f32_bf16 v2, v24, v225\n"
  "v_dot2c_f32_bf16 v0, v24, v224\n"
  "v_dot2c_f32_bf16 v1, v25, v224\n"
  "s_waitcnt lgkmcnt(4)\n"
  "v_dot2c_f32_bf16 v7, v25, v227\n"
  "v_dot2c_f32_bf16 v6, v24, v227\n"
  "v_dot2c_f32_bf16 v4, v24, v226\n"
  "v_dot2c_f32_bf16 v5, v25, v226\n"
  "s_waitcnt lgkmcnt(3)\n"
  "v_dot2c_f32_bf16 v10, v24, v229\n"
  "v_dot2c_f32_bf16 v11, v25, v229\n"
  "v_dot2c_f32_bf16 v9, v25, v228\n"
  "v_dot2c_f32_bf16 v8, v24, v228\n"
  "s_waitcnt lgkmcnt(2)\n"
  "v_dot2c_f32_bf16 v13, v25, v230\n"
  "v_dot2c_f32_bf16 v14, v24, v231\n"
  "v_dot2c_f32_bf16 v15, v25, v231\n"
  "v_dot2c_f32_bf16 v12, v24, v230\n"
  "s_waitcnt lgkmcnt(0)\n"
  "v_dot2c_f32_bf16 v21, v25, v234\n"
  "v_dot2c_f32_bf16 v19, v25, v233\n"
  "v_dot2c_f32_bf16 v22, v24, v235\n"
  "v_dot2c_f32_bf16 v17, v25, v232\n"
  "v_dot2c_f32_bf16 v20, v24, v234\n"
  "v_dot2c_f32_bf16 v23, v25, v235\n"
  "v_dot2c_f32_bf16 v18, v24, v233\n"
  "v_dot2c_f32_bf16 v16, v24, v232\n"
  "s_waitcnt vmcnt(5)\n"
  "v_max_u32 v26, v64, v65\n"
  "v_max3_u32 v26, v66, v67, v26\n"
  "s_waitcnt vmcnt(4)\n"
  "v_max3_u32 v26, v68, v69, v26\n"
  "v_max3_u32 v26, v70, v71, v26\n"
  "s_waitcnt vmcnt(3)\n"
  "v_max3_u32 v26, v72, v73, v26\n"
  "v_max3_u32 v26, v74, v75, v26\n"
  "v_cmp_eq_u32 vcc, -1, v26\n"
  "s_cmp_eq_u64 vcc, 0\n"
  "s_cbranch_scc0 Lx0A%=\n"
  "Lxok0A%=:\n"
  "v_dot2c_f32_bf16 v0, v24, v64\n"
  "v_dot2c_f32_bf16 v1, v25, v64\n"
  "v_dot2c_f32_bf16 v2, v24, v65\n"
  "v_dot2c_f32_bf16 v3, v25, v65\n"
  "v_dot2c_f32_bf16 v4, v24, v66\n"
  "v_dot2c_f32_bf16 v5, v25, v66\n"
  "v_dot2c_f32_bf16 v6, v24, v67\n"
  "v_dot2c_f32_bf16 v7, v25, v67\n"
  "v_dot2c_f32_bf16 v8, v24, v68\n"
  "v_dot2c_f32_bf16 v9, v25, v68\n"
  "v_dot2c_f32_bf16 v10, v24, v69\n"
  "v_dot2c_f32_bf16 v11, v25, v69\n"
  "v_dot2c_f32_bf16 v12, v24, v70\n"
  "v_dot2c_f32_bf16 v13, v25, v70\n"
  "v_dot2c_f32_bf16 v14, v24, v71\n"
  "v_dot2c_f32_bf16 v15, v25, v71\n"
  "v_dot2c_f32_bf16 v16, v24, v72\n"
  "v_dot2c_f32_bf16 v17, v25, v72\n"
  "v_dot2c_f32_bf16 v18, v24, v73\n"
  "v_dot2c_f32_bf16 v19, v25, v73\n"
  "v_dot2c_f32_bf16 v20, v24, v74\n"
  "v_dot2c_f32_bf16 v21, v25, v74\n"
  "v_dot2c_f32_bf16 v22, v24, v75\n"
  "v_dot2c_f32_bf16 v23, v25, v75\n"
  "s_waitcnt vmcnt(2)\n"
  "v_max_u32 v26, v76, v77\n"
  "v_max3_u32 v26, v78, v79, v26\n"
  "s_waitcnt vmcnt(1)\n"
  "v_max3_u32 v26, v80, v81, v26\n"
  "v_max3_u32 v26, v82, v83, v26\n"
  "s_waitcnt vmcnt(0)\n"
  "v_max3_u32 v26, v84, v85, v26\n"
  "v_max3_u32 v26, v86, v87, v26\n"
  "v_cmp_eq_u32 vcc, -1, v26\n"
  "s_cmp_eq_u64 vcc, 0\n"
  "s_cbranch_scc0 Lx1A%=\n"
  "Lxok1A%=:\n"
  "v_dot2c_f32_bf16 v0, v24, v76\n"
  "v_dot2c_f32_bf16 v1, v25, v76\n"
  "v_dot2c_f32_bf16 v2, v24, v77\n"
  "v_dot2c_f32_bf16 v4, v24, v78\n"
  "v_dot2c_f32_bf16 v3, v25, v77\n"
  "v_dot2c_f32_bf16 v5, v25, v78\n"
  "v_dot2c_f32_bf16 v6, v24, v79\n"
  "v_dot2c_f32_bf16 v7, v25, v79\n"
  "v_cvt_pk_bf16_f32 v236, v0, v1\n"
  "v_cvt_pk_bf16_f32 v237, v2, v3\n"
  "v_cvt_pk_bf16_f32 v238, v4, v5\n"
  "v_cvt_pk_bf16_f32 v239, v6, v7\n"
  "buffer_store_dwordx4 v[236:239], v204, s[44:47], 0 offen sc1\n"
  "v_dot2c_f32_bf16 v9, v25, v80\n"
  "v_dot2c_f32_bf16 v8, v24, v80\n"
  "v_dot2c_f32_bf16 v10, v24, v81\n"
  "v_dot2c_f32_bf16 v11, v25, v81\n"
  "v_dot2c_f32_bf16 v12, v24, v82\n"
  "v_dot2c_f32_bf16 v13, v25, v82\n"
  "v_dot2c_f32_bf16 v14, v24, v83\n"
  "v_dot2c_f32_bf16 v15, v25, v83\n"
  "v_cvt_pk_bf16_f32 v240, v8, v9\n"
  "v_cvt_pk_bf16_f32 v241, v10, v11\n"
  "v_cvt_pk_bf16_f32 v242, v12, v13\n"
  "v_dot2c_f32_bf16 v16, v24, v84\n"
  "v_cvt_pk_bf16_f32 v243, v14, v15\n"
  "buffer_store_dwordx4 v[240:243], v205, s[44:47], 0 offen sc1\n"
  "v_dot2c_f32_bf16 v17, v25, v84\n"
  "v_dot2c_f32_bf16 v18, v24, v85\n"
  "v_dot2c_f32_bf16 v19, v25, v85\n"
  "v_dot2c_f32_bf16 v20, v24, v86\n"
  "v_dot2c_f32_bf16 v21, v25, v86\n"
  "v_dot2c_f32_bf16 v22, v24, v87\n"
  "v_dot2c_f32_bf16 v23, v25, v87\n"
  "v_cvt_pk_bf16_f32 v244, v16, v17\n"
  "v_cvt_pk_bf16_f32 v245, v18, v19\n"
  "v_cvt_pk_bf16_f32 v246, v20, v21\n"
  "v_cvt_pk_bf16_f32 v247, v22, v23\n"
  "buffer_store_dwordx4 v[244:247], v206, s[44:47], 0 offen sc1\n"
  "s_endpgm\n"
  "Lpollc%=:\n"
  "s_mov_b32 s64, 262144\n"
  "Lpollsc%=:\n"
  "buffer_load_dword v106, v105, s[24:27], 0 offen sc1\n"
  "s_waitcnt vmcnt(0)\n"
  "v_cmp_gt_u32 vcc, 1, v106\n"
  "s_and_b64 vcc, vcc, -1\n"
  "s_cbranch_scc0 Lpollrc%=\n"
  "s_sleep 1\n"
  "s_sub_u32 s64, s64, 1\n"
  "s_cbranch_scc0 Lpollsc%=\n"
  "s_branch Lpollrc%=\n"
  "Lpollp0A%=:\n"
  "s_mov_b32 s64, 262144\n"
  "Lpollsp0A%=:\n"
  "buffer_load_dword v106, v105, s[24:27], 0 offen sc1\n"
  "s_waitcnt vmcnt(0)\n"
  "v_cmp_gt_u32 vcc, 12, v106\n"
  "s_and_b64 vcc, vcc, 1\n"
  "s_cbranch_scc0 Lpollrp0A%=\n"
  "s_sleep 1\n"
  "s_sub_u32 s64, s64, 1\n"
  "s_cbranch_scc0 Lpollsp0A%=\n"
  "s_branch Lpollrp0A%=\n"
  "Lpollp12A%=:\n"
  "s_mov_b32 s64, 262144\n"
  "Lpollsp12A%=:\n"
  "buffer_load_dword v106, v105, s[24:27], 0 offen sc1\n"
  "s_waitcnt vmcnt(0)\n"
  "v_cmp_gt_u32 vcc, 12, v106\n"
  "s_and_b64 vcc, vcc, 3\n"
  "s_cbranch_scc0 Lpollrp12A%=\n"
  "s_sleep 1\n"
  "s_sub_u32 s64, s64, 1\n"
  "s_cbranch_scc0 Lpollsp12A%=\n"
  "s_branch Lpollrp12A%=\n"
  "Lpollp24A%=:\n"
  "s_mov_b32 s64, 262144\n"
  "Lpollsp24A%=:\n"
  "buffer_load_dword v106, v105, s[24:27], 0 offen sc1\n"
  "s_waitcnt vmcnt(0)\n"
  "v_cmp_gt_u32 vcc, 12, v106\n"
  "s_and_b64 vcc, vcc, 7\n"
  "s_cbranch_scc0 Lpollrp24A%=\n"
  "s_sleep 1\n"
  "s_sub_u32 s64, s64, 1\n"
  "s_cbranch_scc0 Lpollsp24A%=\n"
  "s_branch Lpollrp24A%=\n"
  "Lpollp127A%=:\n"
  "s_mov_b32 s64, 262144\n"
  "Lpollsp127A%=:\n"
  "buffer_load_dword v106, v105, s[24:27], 0 offen sc1\n"
  "s_waitcnt vmcnt(0)\n"
  "v_cmp_gt_u32 vcc, 12, v106\n"
  "s_and_b64 vcc, vcc, 31\n"
  "s_cbranch_scc0 Lpollrp127A%=\n"
  "s_sleep 1\n"
  "s_sub_u32 s64, s64, 1\n"
  "s_cbranch_scc0 Lpollsp127A%=\n"
  "s_branch Lpollrp127A%=\n"
  "Lx0A%=:\n"
  "s_mov_b32 s64, 262144\n"
  "Lxs0A%=:\n"
  "buffer_load_dwordx4 v[64:67], v212, s[36:39], s61 offen offset:0 sc1\n"
  "buffer_load_dwordx4 v[68:71], v212, s[36:39], s61 offen offset:1024 sc1\n"
  "buffer_load_dwordx4 v[72:75], v213, s[36:39], s61 offen offset:2048 sc1\n"
  "s_waitcnt vmcnt(0)\n"
  "v_max_u32 v26, v64, v65\n"
  "v_max3_u32 v26, v66, v67, v26\n"
  "v_max3_u32 v26, v68, v69, v26\n"
  "v_max3_u32 v26, v70, v71, v26\n"
  "v_max3_u32 v26, v72, v73, v26\n"
  "v_max3_u32 v26, v74, v75, v26\n"
  "v_cmp_eq_u32 vcc, -1, v26\n"
  "s_cmp_eq_u64 vcc, 0\n"
  "s_cbranch_scc1 Lxok0A%=\n"
  "s_sub_u32 s64, s64, 1\n"
  "s_cbranch_scc0 Lxs0A%=\n"
  "s_branch Lxok0A%=\n"
  "Lx1A%=:\n"
  "s_mov_b32 s64, 262144\n"
  "Lxs1A%=:\n"
  "buffer_load_dwordx4 v[76:79], v212, s[36:39], s62 offen offset:0 sc1\n"
  "buffer_load_dwordx4 v[80:83], v212, s[36:39], s62 offen offset:1024 sc1\n"
  "buffer_load_dwordx4 v[84:87], v213, s[36:39], s62 offen offset:2048 sc1\n"
  "s_waitcnt vmcnt(0)\n"
  "v_max_u32 v26, v76, v77\n"
  "v_max3_u32 v26, v78, v79, v26\n"
  "v_max3_u32 v26, v80, v81, v26\n"
  "v_max3_u32 v26, v82, v83, v26\n"
  "v_max3_u32 v26, v84, v85, v26\n"
  "v_max3_u32 v26, v86, v87, v26\n"
  "v_cmp_eq_u32 vcc, -1, v26\n"
  "s_cmp_eq_u64 vcc, 0\n"
  "s_cbranch_scc1 Lxok1A%=\n"
  "s_sub_u32 s64, s64, 1\n"
  "s_cbranch_scc0 Lxs1A%=\n"
  "s_branch Lxok1A%=\n"
  "Lpollp0B%=:\n"
  "s_mov_b32 s64, 262144\n"
  "Lpollsp0B%=:\n"
  "buffer_load_dword v106, v105, s[24:27], 0 offen sc1\n"
  "s_waitcnt vmcnt(0)\n"
  "v_cmp_gt_u32 vcc, 12, v106\n"
  "s_and_b64 vcc, vcc, 1\n"
  "s_cbranch_scc0 Lpollrp0B%=\n"
  "s_sleep 1\n"
  "s_sub_u32 s64, s64, 1\n"
  "s_cbranch_scc0 Lpollsp0B%=\n"
  "s_branch Lpollrp0B%=\n"
  "Lpollp12B%=:\n"
  "s_mov_b32 s64, 262144\n"
  "Lpollsp12B%=:\n"
  "buffer_load_dword v106, v105, s[24:27], 0 offen sc1\n"
  "s_waitcnt vmcnt(0)\n"
  "v_cmp_gt_u32 vcc, 12, v106\n"
  "s_and_b64 vcc, vcc, 3\n"
  "s_cbranch_scc0 Lpollrp12B%=\n"
  "s_sleep 1\n"
  "s_sub_u32 s64, s64, 1\n"
  "s_cbranch_scc0 Lpollsp12B%=\n"
  "s_branch Lpollrp12B%=\n"
  "Lpollp24B%=:\n"
  "s_mov_b32 s64, 262144\n"
  "Lpollsp24B%=:\n"
  "buffer_load_dword v106, v105, s[24:27], 0 offen sc1\n"
  "s_waitcnt vmcnt(0)\n"
  "v_cmp_gt_u32 vcc, 12, v106\n"
  "s_and_b64 vcc, vcc, 7\n"
  "s_cbranch_scc0 Lpollrp24B%=\n"
  "s_sleep 1\n"
  "s_sub_u32 s64, s64, 1\n"
  "s_cbranch_scc0 Lpollsp24B%=\n"
  "s_branch Lpollrp24B%=\n"
  "Lpollp127B%=:\n"
  "s_mov_b32 s64, 262144\n"
  "Lpollsp127B%=:\n"
  "buffer_load_dword v106, v105, s[24:27], 0 offen sc1\n"
  "s_waitcnt vmcnt(0)\n"
  "v_cmp_gt_u32 vcc, 12, v106\n"
  "s_and_b64 vcc, vcc, 31\n"
  "s_cbranch_scc0 Lpollrp127B%=\n"
  "s_sleep 1\n"
  "s_sub_u32 s64, s64, 1\n"
  "s_cbranch_scc0 Lpollsp127B%=\n"
  "s_branch Lpollrp127B%=\n"
  "Lx0B%=:\n"
  "s_mov_b32 s64, 262144\n"
  "Lxs0B%=:\n"
  "buffer_load_dwordx4 v[64:67], v212, s[36:39], s61 offen offset:0 sc1\n"
  "buffer_load_dwordx4 v[68:71], v212, s[36:39], s61 offen offset:1024 sc1\n"
  "s_waitcnt vmcnt(0)\n"
  "v_max_u32 v26, v64, v65\n"
  "v_max3_u32 v26, v66, v67, v26\n"
  "v_max3_u32 v26, v68, v69, v26\n"
  "v_max3_u32 v26, v70, v71, v26\n"
  "v_cmp_eq_u32 vcc, -1, v26\n"
  "s_cmp_eq_u64 vcc, 0\n"
  "s_cbranch_scc1 Lxok0B%=\n"
  "s_sub_u32 s64, s64, 1\n"
  "s_cbranch_scc0 Lxs0B%=\n"
  "s_branch Lxok0B%=\n"
  "Lx1B%=:\n"
  "s_mov_b32 s64, 262144\n"
  "Lxs1B%=:\n"
  "buffer_load_dwordx4 v[76:79], v212, s[36:39], s62 offen offset:0 sc1\n"
  "buffer_load_dwordx4 v[80:83], v212, s[36:39], s62 offen offset:1024 sc1\n"
  "s_waitcnt vmcnt(0)\n"
  "v_max_u32 v26, v76, v77\n"
  "v_max3_u32 v26, v78, v79, v26\n"
  "v_max3_u32 v26, v80, v81, v26\n"
  "v_max3_u32 v26, v82, v83, v26\n"
  "v_cmp_eq_u32 vcc, -1, v26\n"
  "s_cmp_eq_u64 vcc, 0\n"
  "s_cbranch_scc1 Lxok1B%=\n"
  "s_sub_u32 s64, s64, 1\n"
  "s_cbranch_scc0 Lxs1B%=\n"
  "s_branch Lxok1B%=\n"
  "Lp19%=:\n"
  "ds_read_b128 v[32:35], v89 offset:0\n"
  "ds_read_b128 v[36:39], v89 offset:1024\n"
  "ds_read_b128 v[40:43], v89 offset:2048\n"
  "ds_read_b128 v[44:47], v89 offset:3072\n"
  "ds_read_b128 v[48:51], v89 offset:4096\n"
  "ds_read_b128 v[52:55], v89 offset:5120\n"
  "s_waitcnt lgkmcnt(0)\n"
  "v_mfma_f32_16x16x32_bf16 v[0:3], a[0:3], v[32:35], 0\n"
  "s_add_u32 m0, s48, 32768\n"
  "ds_read_b128 v[56:59], v89 offset:6144\n"
  "ds_read_b128 v[60:63], v89 offset:7168\n"
  "v_mfma_f32_16x16x32_bf16 v[0:3], a[16:19], v[36:39], v[0:3]\n"
  "buffer_load_dwordx4 v92, s[36:39], s52 offen offset:0 sc1 lds\n"
  "ds_read_b128 v[64:67], v89 offset:8192\n"
  "v_mfma_f32_16x16x32_bf16 v[0:3], a[32:35], v[40:43], v[0:3]\n"
  "s_sub_u32 s6, 4, s66\n"
  "s_and_b32 s6, s6, 3\n"
  "ds_read_b128 v[68:71], v89 offset:9216\n"
  "v_mfma_f32_16x16x32_bf16 v[0:3], a[48:51], v[44:47], v[0:3]\n"
  "s_mul_i32 s7, s67, 11\n"
  "ds_read_b128 v[72:75], v89 offset:10240\n"
  "ds_read_b128 v[76:79], v89 offset:11264\n"
  "v_mfma_f32_16x16x32_bf16 v[0:3], a[64:67], v[48:51], v[0:3]\n"
  "s_lshr_b32 s7, s7, 5\n"
  "s_mul_i32 s8, s7, 3\n"
  "ds_read_b64_tr_b16 a[224:225], v220 offset:32768\n"
  "v_mfma_f32_16x16x32_bf16 v[0:3], a[80:83], v[52:55], v[0:3]\n"
  "s_sub_u32 s8, s67, s8\n"
  "ds_read_b64_tr_b16 a[226:227], v220 offset:33280\n"
  "v_mfma_f32_16x16x32_bf16 v[4:7], a[4:7], v[32:35], 0\n"
  "ds_read_b64_tr_b16 a[228:229], v221 offset:32768\n"
  "ds_read_b64_tr_b16 a[230:231], v221 offset:33280\n"
  "v_mfma_f32_16x16x32_bf16 v[4:7], a[20:23], v[36:39], v[4:7]\n"
  "buffer_load_dwordx4 v92, s[36:39], s52 offen offset:1024 sc1 lds\n"
  "ds_read_b64_tr_b16 a[232:233], v220 offset:40960\n"
  "v_mfma_f32_16x16x32_bf16 v[4:7], a[36:39], v[40:43], v[4:7]\n"
  "s_sub_u32 s40, s18, 2048\n"
  "s_subb_u32 s41, s19, 0\n"
  "ds_read_b64_tr_b16 a[234:235], v220 offset:41472\n"
  "v_mfma_f32_16x16x32_bf16 v[4:7], a[52:55], v[44:47], v[4:7]\n"
  "s_mul_i32 s9, s6, 3\n"
  "ds_read_b64_tr_b16 a[236:237], v221 offset:40960\n"
  "v_lshrrev_b32 v200, 4, v89\n"
  "v_mfma_f32_16x16x32_bf16 v[4:7], a[68:71], v[48:51], v[4:7]\n"
  "s_add_u32 s9, s9, s7\n"
  "ds_read_b64_tr_b16 a[238:239], v221 offset:41472\n"
  "ds_read_b64_tr_b16 a[240:241], v220 offset:49152\n"
  "v_mfma_f32_16x16x32_bf16 v[4:7], a[84:87], v[52:55], v[4:7]\n"
  "buffer_load_dwordx4 v92, s[36:39], s52 offen offset:2048 sc1 lds\n"
  "ds_read_b64_tr_b16 a[242:243], v220 offset:49664\n"
  "v_mfma_f32_16x16x32_bf16 v[8:11], a[8:11], v[32:35], 0\n"
  "s_lshl2_add_u32 s22, s68, s10\n"
  "ds_read_b64_tr_b16 a[244:245], v221 offset:49152\n"
  "v_lshrrev_b32 v201, 2, v200\n"
  "v_mfma_f32_16x16x32_bf16 v[8:11], a[24:27], v[36:39], v[8:11]\n"
  "s_mul_i32 s22, s22, 14\n"
  "ds_read_b64_tr_b16 a[246:247], v221 offset:49664\n"
  "ds_read_b64_tr_b16 a[248:249], v220 offset:57344\n"
  "v_mfma_f32_16x16x32_bf16 v[8:11], a[40:43], v[40:43], v[8:11]\n"
  "ds_read_b64_tr_b16 a[250:251], v220 offset:57856\n"
  "v_min_u32 v202, 6, v201\n"
  "v_mfma_f32_16x16x32_bf16 v[8:11], a[56:59], v[44:47], v[8:11]\n"
  "ds_read_b64_tr_b16 a[252:253], v221 offset:57344\n"
  "v_add_u32 v112, s22, v202\n"
  "v_mfma_f32_16x16x32_bf16 v[8:11], a[72:75], v[48:51], v[8:11]\n"
  "buffer_load_dwordx4 v92, s[36:39], s52 offen offset:3072 sc1 lds\n"
  "ds_read_b64_tr_b16 a[254:255], v221 offset:57856\n"
  "v_mfma_f32_16x16x32_bf16 v[8:11], a[88:91], v[52:55], v[8:11]\n"
  "v_and_b32 v113, 3, v200\n"
  "v_min_u32 v114, 383, v112\n"
  "v_mfma_f32_16x16x32_bf16 v[12:15], a[12:15], v[32:35], 0\n"
  "s_mul_i32 s23, s8, 384\n"
  "v_min_u32 v115, 2, v113\n"
  "v_mul_u32_u24 v116, 1152, v114\n"
  "v_mfma_f32_16x16x32_bf16 v[12:15], a[28:31], v[36:39], v[12:15]\n"
  "s_mul_i32 s60, s9, 442368\n"
  "v_lshl_add_u32 v117, v115, 7, v116\n"
  "v_mfma_f32_16x16x32_bf16 v[12:15], a[44:47], v[40:43], v[12:15]\n"
  "s_add_u32 m0, s49, 2048\n"
  "s_add_u32 s34, s23, 2048\n"
  "s_mov_b32 s42, 5310464\n"
  "v_mfma_f32_16x16x32_bf16 v[12:15], a[60:63], v[44:47], v[12:15]\n"
  "buffer_load_dwordx4 v93, s[36:39], s52 offen sc1 lds\n"
  "v_mfma_f32_16x16x32_bf16 v[12:15], a[76:79], v[48:51], v[12:15]\n"
  "s_add_u32 s52, s52, 18432\n"
  "s_mov_b32 s43, 0x20000\n"
  "s_mul_i32 s9, s9, 12288\n"
  "v_mfma_f32_16x16x32_bf16 v[12:15], a[92:95], v[52:55], v[12:15]\n"
  "s_waitcnt vmcnt(5) lgkmcnt(0)\n"
  "s_barrier\n"
  "v_mfma_f32_16x16x32_bf16 v[0:3], a[96:99], v[56:59], v[0:3]\n"
  "s_cmp_eq_u32 s52, s54\n"
  "s_cselect_b32 s52, s53, s52\n"
  "ds_read_b128 v[32:35], v89 offset:12288\n"
  "ds_read_b128 v[36:39], v89 offset:13312\n"
  "v_mfma_f32_16x16x32_bf16 v[0:3], a[112:115], v[60:63], v[0:3]\n"
  "s_add_u32 m0, s48, 49152\n"
  "ds_read_b128 v[40:43], v89 offset:14336\n"
  "v_min_u32 v118, 13, v200\n"
  "v_mfma_f32_16x16x32_bf16 v[0:3], a[128:131], v[64:67], v[0:3]\n"
  "buffer_load_dwordx4 v92, s[36:39], s52 offen offset:0 sc1 lds\n"
  "ds_read_b128 v[44:47], v89 offset:15360\n"
  "v_mfma_f32_16x16x32_bf16 v[0:3], a[144:147], v[68:71], v[0:3]\n"
  "ds_read_b128 v[48:51], v89 offset:16384\n"
  "ds_read_b128 v[52:55], v89 offset:17408\n"
  "v_mfma_f32_16x16x32_bf16 v[0:3], a[160:163], v[72:75], v[0:3]\n"
  "ds_read_b64_tr_b16 v[224:225], v222 offset:0\n"
  "v_add_u32 v102, s34, v117\n"
  "v_mfma_f32_16x16x32_bf16 v[0:3], a[176:179], v[76:79], v[0:3]\n"
  "s_lshr_b32 s35, s10, 1\n"
  "s_and_b32 s70, s10, 1\n"
  "ds_read_b64_tr_b16 v[226:227], v222 offset:512\n"
  "v_mfma_f32_16x16x32_bf16 v[4:7], a[100:103], v[56:59], v[4:7]\n"
  "s_lshl4_add_u32 s22, s68, s66\n"
  "ds_read_b64_tr_b16 v[228:229], v223 offset:0\n"
  "ds_read_b64_tr_b16 v[230:231], v223 offset:512\n"
  "v_mfma_f32_16x16x32_bf16 v[4:7], a[116:119], v[60:63], v[4:7]\n"
  "buffer_load_dwordx4 v92, s[36:39], s52 offen offset:1024 sc1 lds\n"
  "ds_read_b64_tr_b16 v[232:233], v222 offset:8192\n"
  "v_mfma_f32_16x16x32_bf16 v[4:7], a[132:135], v[64:67], v[4:7]\n"
  "s_lshl2_add_u32 s22, s22, s7\n"
  "s_lshl_b32 s22, s22, 7\n"
  "ds_read_b64_tr_b16 v[234:235], v222 offset:8704\n"
  "v_mfma_f32_16x16x32_bf16 v[4:7], a[148:151], v[68:71], v[4:7]\n"
  "s_add_u32 s22, s22, 7081984\n"
  "s_add_u32 s12, s6, 0\n"
  "ds_read_b64_tr_b16 v[236:237], v223 offset:8192\n"
  "v_mfma_f32_16x16x32_bf16 v[4:7], a[164:167], v[72:75], v[4:7]\n"
  "s_and_b32 s12, s12, 3\n"
  "ds_read_b64_tr_b16 v[238:239], v223 offset:8704\n"
  "ds_read_b64_tr_b16 v[240:241], v222 offset:16384\n"
  "v_mfma_f32_16x16x32_bf16 v[4:7], a[180:183], v[76:79], v[4:7]\n"
  "s_lshl_b32 s12, s12, 11\n"
  "s_add_u32 s12, s12, s22\n"
  "ds_read_b64_tr_b16 v[242:243], v222 offset:16896\n"
  "v_mfma_f32_16x16x32_bf16 v[8:11], a[104:107], v[56:59], v[8:11]\n"
  "buffer_load_dwordx4 v92, s[36:39], s52 offen offset:2048 sc1 lds\n"
  "ds_read_b64_tr_b16 v[244:245], v223 offset:16384\n"
  "v_mfma_f32_16x16x32_bf16 v[8:11], a[120:123], v[60:63], v[8:11]\n"
  "s_add_u32 s13, s6, 1\n"
  "ds_read_b64_tr_b16 v[246:247], v223 offset:16896\n"
  "ds_read_b64_tr_b16 v[248:249], v222 offset:24576\n"
  "v_mfma_f32_16x16x32_bf16 v[8:11], a[136:139], v[64:67], v[8:11]\n"
  "s_and_b32 s13, s13, 3\n"
  "s_lshl_b32 s13, s13, 11\n"
  "ds_read_b64_tr_b16 v[250:251], v222 offset:25088\n"
  "v_mfma_f32_16x16x32_bf16 v[8:11], a[152:155], v[68:71], v[8:11]\n"
  "s_add_u32 s13, s13, s22\n"
  "s_add_u32 s14, s6, 2\n"
  "ds_read_b64_tr_b16 v[252:253], v223 offset:24576\n"
  "v_mfma_f32_16x16x32_bf16 v[8:11], a[168:171], v[72:75], v[8:11]\n"
  "s_and_b32 s14, s14, 3\n"
  "s_cbranch_execz Ltramp%=\n"
  "s_lshl_b32 s14, s14, 11\n"
  "ds_read_b64_tr_b16 v[254:255], v223 offset:25088\n"
  "v_mfma_f32_16x16x32_bf16 v[8:11], a[184:187], v[76:79], v[8:11]\n"
  "buffer_load_dwordx4 v92, s[36:39], s52 offen offset:3072 sc1 lds\n"
  "v_mfma_f32_16x16x32_bf16 v[12:15], a[108:111], v[56:59], v[12:15]\n"
  "s_add_u32 s14, s14, s22\n"
  "s_add_u32 s15, s6, 3\n"
  "s_and_b32 s15, s15, 3\n"
  "v_mfma_f32_16x16x32_bf16 v[12:15], a[124:127], v[60:63], v[12:15]\n"
  "s_lshl_b32 s15, s15, 11\n"
  "s_add_u32 s15, s15, s22\n"
  "v_mfma_f32_16x16x32_bf16 v[12:15], a[140:143], v[64:67], v[12:15]\n"
  "s_cmp_eq_u32 s66, 0\n"
  "s_cselect_b32 s34, 106173216, 0\n"
  "v_mfma_f32_16x16x32_bf16 v[12:15], a[156:159], v[68:71], v[12:15]\n"
  "s_add_u32 m0, s49, 4096\n"
  "s_cmp_eq_u32 s66, 1\n"
  "s_cselect_b32 s34, 103941441, s34\n"
  "v_mfma_f32_16x16x32_bf16 v[12:15], a[172:175], v[72:75], v[12:15]\n"
  "buffer_load_dwordx4 v93, s[36:39], s52 offen sc1 lds\n"
  "v_mfma_f32_16x16x32_bf16 v[12:15], a[188:191], v[76:79], v[12:15]\n"
  "s_waitcnt vmcnt(5) lgkmcnt(0)\n"
  "s_barrier\n"
  "v_mfma_f32_16x16x32_bf16 v[0:3], a[192:195], v[32:35], v[0:3]\n"
  "s_add_u32 s52, s52, 18432\n"
  "ds_read_b128 v[56:59], v89 offset:32768\n"
  "v_mfma_f32_16x16x32_bf16 v[0:3], a[208:211], v[36:39], v[0:3]\n"
  "s_cmp_eq_u32 s52, s54\n"
  "s_cselect_b32 s52, s53, s52\n"
  "v_mfma_f32_16x16x32_bf16 v[0:3], a[224:227], v[40:43], v[0:3]\n"
  "s_add_u32 m0, s48, 65536\n"
  "ds_read_b128 v[60:63], v89 offset:33792\n"
  "v_mfma_f32_16x16x32_bf16 v[0:3], a[240:243], v[44:47], v[0:3]\n"
  "buffer_load_dwordx4 v92, s[36:39], s52 offen offset:0 sc1 lds\n"
  "v_mfma_f32_16x16x32_bf16 v[0:3], v[224:227], v[48:51], v[0:3]\n"
  "s_cmp_eq_u32 s66, 2\n"
  "s_cselect_b32 s34, 87098406, s34\n"
  "v_mfma_f32_16x16x32_bf16 v[0:3], v[240:243], v[52:55], v[0:3]\n"
  "s_cmp_eq_u32 s66, 3\n"
  "s_cselect_b32 s34, 69273171, s34\n"
  "ds_read_b128 v[64:67], v89 offset:34816\n"
  "v_mfma_f32_16x16x32_bf16 v[4:7], a[196:199], v[32:35], v[4:7]\n"
  "s_lshl_b32 s71, s68, 2\n"
  "s_lshr_b32 s34, s34, s71\n"
  "s_and_b32 s34, s34, 15\n"
  "v_mfma_f32_16x16x32_bf16 v[4:7], a[212:215], v[36:39], v[4:7]\n"
  "s_min_u32 s84, s34, 2\n"
  "s_mul_i32 s28, s34, 18\n"
  "s_add_u32 s84, s84, s28\n"
  "v_mfma_f32_16x16x32_bf16 v[4:7], a[228:231], v[40:43], v[4:7]\n"
  "buffer_load_dwordx4 v92, s[36:39], s52 offen offset:1024 sc1 lds\n"
  "ds_read_b128 v[68:71], v89 offset:35840\n"
  "v_mfma_f32_16x16x32_bf16 v[4:7], a[244:247], v[44:47], v[4:7]\n"
  "s_lshl_b32 s29, s34, 2\n"
  "s_lshr_b32 s29, 0xCA69521, s29\n"
  "s_bfm_b32 s22, s66, 0\n"
  "v_mfma_f32_16x16x32_bf16 v[4:7], v[228:231], v[48:51], v[4:7]\n"
  "s_and_b32 s22, s22, s29\n"
  "s_bcnt1_i32_b32 s22, s22\n"
  "s_mul_i32 s30, s66, 5\n"
  "v_mfma_f32_16x16x32_bf16 v[4:7], v[244:247], v[52:55], v[4:7]\n"
  "s_add_u32 s30, s30, s84\n"
  "s_sub_u32 s30, s30, s22\n"
  "ds_read_b128 v[72:75], v89 offset:36864\n"
  "v_mfma_f32_16x16x32_bf16 v[8:11], a[200:203], v[32:35], v[8:11]\n"
  "s_lshr_b32 s31, s29, s66\n"
  "s_lshl_b32 s31, s31, 31\n"
  "s_mul_i32 s59, s30, 147456\n"
  "v_mfma_f32_16x16x32_bf16 v[8:11], a[216:219], v[36:39], v[8:11]\n"
  "buffer_load_dwordx4 v92, s[36:39], s52 offen offset:2048 sc1 lds\n"
  "v_mfma_f32_16x16x32_bf16 v[8:11], a[232:235], v[40:43], v[8:11]\n"
  "s_add_u32 s59, s59, s9\n"
  "s_add_u32 s59, s59, 4715520\n"
  "ds_read_b128 v[76:79], v89 offset:37888\n"
  "v_mfma_f32_16x16x32_bf16 v[8:11], a[248:251], v[44:47], v[8:11]\n"
  "s_or_b32 s71, s31, s50\n"
  "s_add_u32 s71, s71, 592896\n"
  "s_mul_i32 s28, s10, 9216\n"
  "v_mfma_f32_16x16x32_bf16 v[8:11], v[232:235], v[48:51], v[8:11]\n"
  "s_add_u32 s28, s28, s23\n"
  "s_lshl_b32 s72, s7, 1\n"
  "s_lshr_b32 s72, 0x249, s72\n"
  "v_mfma_f32_16x16x32_bf16 v[8:11], v[248:251], v[52:55], v[8:11]\n"
  "s_mul_i32 s73, s66, 7\n"
  "s_add_u32 s73, s73, s68\n"
  "s_mul_i32 s73, s73, 3\n"
  "v_mfma_f32_16x16x32_bf16 v[12:15], a[204:207], v[32:35], v[12:15]\n"
  "buffer_load_dwordx4 v92, s[36:39], s52 offen offset:3072 sc1 lds\n"
  "v_mfma_f32_16x16x32_bf16 v[12:15], a[220:223], v[36:39], v[12:15]\n"
  "s_add_u32 s73, s73, s8\n"
  "s_mul_i32 s73, s73, 3\n"
  "s_bfe_u32 s22, s72, 131072\n"
  "v_mfma_f32_16x16x32_bf16 v[12:15], a[236:239], v[40:43], v[12:15]\n"
  "s_add_u32 s16, s22, s73\n"
  "s_mul_i32 s29, s10, 3072\n"
  "s_mul_i32 s16, s16, 24576\n"
  "v_mfma_f32_16x16x32_bf16 v[12:15], a[252:255], v[44:47], v[12:15]\n"
  "s_add_u32 s16, s16, s29\n"
  "s_add_u32 s16, s16, 23592960\n"
  "s_lshl_b32 s22, s22, 7\n"
  "v_mfma_f32_16x16x32_bf16 v[12:15], v[236:239], v[48:51], v[12:15]\n"
  "s_add_u32 m0, s49, 145408\n"
  "s_add_u32 s22, s22, 2048\n"
  "s_bfe_u32 s9, s72, 131074\n"
  "v_mfma_f32_16x16x32_bf16 v[12:15], v[252:255], v[52:55], v[12:15]\n"
  "s_waitcnt lgkmcnt(0)\n"
  "s_add_u32 s17, s9, s73\n"
  "s_mul_i32 s17, s17, 24576\n"
  "v_mfma_f32_16x16x32_bf16 v[16:19], a[0:3], v[56:59], 0\n"
  "buffer_load_dwordx4 v93, s[36:39], s52 offen sc1 lds\n"
  "ds_read_b128 v[32:35], v89 offset:38912\n"
  "v_mfma_f32_16x16x32_bf16 v[16:19], a[16:19], v[60:63], v[16:19]\n"
  "s_add_u32 s52, s52, 18432\n"
  "v_mul_f32 v80, v0, v0\n"
  "v_mul_f32 v81, v1, v1\n"
  "v_mfma_f32_16x16x32_bf16 v[16:19], a[32:35], v[64:67], v[16:19]\n"
  "s_cmp_eq_u32 s52, s54\n"
  "s_cselect_b32 s52, s53, s52\n"
  "ds_read_b128 v[36:39], v89 offset:39936\n"
  "v_mfma_f32_16x16x32_bf16 v[16:19], a[48:51], v[68:71], v[16:19]\n"
  "s_add_u32 m0, s48, 81920\n"
  "v_fmaak_f32 v80, v88, v80, 0xc0135761\n"
  "v_fmaak_f32 v81, v88, v81, 0xc0135761\n"
  "v_mfma_f32_16x16x32_bf16 v[16:19], a[64:67], v[72:75], v[16:19]\n"
  "buffer_load_dwordx4 v92, s[36:39], s52 offen offset:0 sc1 lds\n"
  "v_mul_f32 v80, v0, v80\n"
  "v_mfma_f32_16x16x32_bf16 v[16:19], a[80:83], v[76:79], v[16:19]\n"
  "s_add_u32 s17, s17, s29\n"
  "ds_read_b128 v[40:43], v89 offset:40960\n"
  "v_mul_f32 v81, v1, v81\n"
  "v_mfma_f32_16x16x32_bf16 v[20:23], a[4:7], v[56:59], 0\n"
  "s_add_u32 s17, s17, 23605248\n"
  "v_exp_f32 v80, v80\n"
  "v_mfma_f32_16x16x32_bf16 v[20:23], a[20:23], v[60:63], v[20:23]\n"
  "s_lshl_b32 s9, s9, 7\n"
  "v_exp_f32 v81, v81\n"
  "v_mfma_f32_16x16x32_bf16 v[20:23], a[36:39], v[64:67], v[20:23]\n"
  "s_add_u32 s9, s9, 1024\n"
  "ds_read_b128 v[44:47], v89 offset:41984\n"
  "v_mul_f32 v82, v0, v8\n"
  "v_mfma_f32_16x16x32_bf16 v[20:23], a[52:55], v[68:71], v[20:23]\n"
  "buffer_load_dwordx4 v92, s[36:39], s52 offen offset:1024 sc1 lds\n"
  "v_mul_f32 v83, v1, v9\n"
  "v_mfma_f32_16x16x32_bf16 v[20:23], a[68:71], v[72:75], v[20:23]\n"
  "s_bfe_u32 s23, s72, 131076\n"
  "v_add_f32 v80, 1.0, v80\n"
  "v_add_f32 v81, 1.0, v81\n"
  "v_mfma_f32_16x16x32_bf16 v[20:23], a[84:87], v[76:79], v[20:23]\n"
  "s_lshl_b32 s23, s23, 7\n"
  "s_add_u32 s23, s23, 0\n"
  "ds_read_b128 v[48:51], v89 offset:43008\n"
  "v_mfma_f32_16x16x32_bf16 v[24:27], a[8:11], v[56:59], 0\n"
  "s_add_u32 s61, s73, s7\n"
  "v_rcp_f32 v80, v80\n"
  "v_mfma_f32_16x16x32_bf16 v[24:27], a[24:27], v[60:63], v[24:27]\n"
  "s_mul_i32 s61, s61, 24576\n"
  "v_rcp_f32 v81, v81\n"
  "v_mfma_f32_16x16x32_bf16 v[24:27], a[40:43], v[64:67], v[24:27]\n"
  "buffer_load_dwordx4 v92, s[36:39], s52 offen offset:2048 sc1 lds\n"
  "ds_read_b128 v[52:55], v89 offset:44032\n"
  "v_mfma_f32_16x16x32_bf16 v[24:27], a[56:59], v[68:71], v[24:27]\n"
  "s_add_u32 s61, s61, 23592960\n"
  "v_mul_f32 v82, v82, v80\n"
  "v_mul_f32 v83, v83, v81\n"
  "v_mfma_f32_16x16x32_bf16 v[24:27], a[72:75], v[72:75], v[24:27]\n"
  "s_add_u32 s62, s61, 12288\n"
  "v_cvt_pk_bf16_f32 v84, v82, v83\n"
  "v_mul_f32 v80, v2, v2\n"
  "v_mfma_f32_16x16x32_bf16 v[24:27], a[88:91], v[76:79], v[24:27]\n"
  "s_lshl_b32 s84, s10, 30\n"
  "v_mul_f32 v81, v3, v3\n"
  "v_fmaak_f32 v80, v88, v80, 0xc0135761\n"
  "v_mfma_f32_16x16x32_bf16 v[28:31], a[12:15], v[56:59], 0\n"
  "s_or_b32 s31, s31, s84\n"
  "v_fmaak_f32 v81, v88, v81, 0xc0135761\n"
  "v_mul_f32 v80, v2, v80\n"
  "v_mfma_f32_16x16x32_bf16 v[28:31], a[28:31], v[60:63], v[28:31]\n"
  "buffer_load_dwordx4 v92, s[36:39], s52 offen offset:3072 sc1 lds\n"
  "v_mul_f32 v81, v3, v81\n"
  "v_mfma_f32_16x16x32_bf16 v[28:31], a[44:47], v[64:67], v[28:31]\n"
  "s_and_b32 s31, s31, 0x80000000\n"
  "v_exp_f32 v80, v80\n"
  "v_mfma_f32_16x16x32_bf16 v[28:31], a[60:63], v[68:71], v[28:31]\n"
  "s_mul_i32 s74, s8, 3\n"
  "v_exp_f32 v81, v81\n"
  "v_mfma_f32_16x16x32_bf16 v[28:31], a[76:79], v[72:75], v[28:31]\n"
  "s_add_u32 s74, s74, s7\n"
  "v_mul_f32 v82, v2, v10\n"
  "v_mul_f32 v83, v3, v11\n"
  "v_mfma_f32_16x16x32_bf16 v[28:31], a[92:95], v[76:79], v[28:31]\n"
  "s_waitcnt lgkmcnt(0)\n"
  "v_add_f32 v80, 1.0, v80\n"
  "v_add_f32 v81, 1.0, v81\n"
  "v_mfma_f32_16x16x32_bf16 v[16:19], a[96:99], v[32:35], v[16:19]\n"
  "s_add_u32 m0, s49, 143360\n"
  "s_lshl_b32 s75, s74, 7\n"
  "ds_read_b128 v[56:59], v89 offset:45056\n"
  "v_mfma_f32_16x16x32_bf16 v[16:19], a[112:115], v[36:39], v[16:19]\n"
  "buffer_load_dwordx4 v93, s[36:39], s52 offen sc1 lds\n"
  "v_mfma_f32_16x16x32_bf16 v[16:19], a[128:131], v[40:43], v[16:19]\n"
  "s_add_u32 s52, s52, 18432\n"
  "s_lshl1_add_u32 s76, s30, s10\n"
  "ds_read_b128 v[60:63], v89 offset:46080\n"
  "v_mfma_f32_16x16x32_bf16 v[16:19], a[144:147], v[44:47], v[16:19]\n"
  "s_cmp_eq_u32 s52, s54\n"
  "s_cselect_b32 s52, s53, s52\n"
  "s_lshr_b32 s77, s76, 4\n"
  "v_mfma_f32_16x16x32_bf16 v[16:19], a[160:163], v[48:51], v[16:19]\n"
  "s_add_u32 m0, s48, 0\n"
  "v_rcp_f32 v80, v80\n"
  "v_mfma_f32_16x16x32_bf16 v[16:19], a[176:179], v[52:55], v[16:19]\n"
  "buffer_load_dwordx4 v92, s[36:39], s52 offen offset:0 sc1 lds\n"
  "ds_read_b128 v[64:67], v89 offset:47104\n"
  "v_mfma_f32_16x16x32_bf16 v[20:23], a[100:103], v[32:35], v[20:23]\n"
  "s_lshl_b32 s77, s77, 6\n"
  "v_rcp_f32 v81, v81\n"
  "v_mfma_f32_16x16x32_bf16 v[20:23], a[116:119], v[36:39], v[20:23]\n"
  "s_and_b32 s72, s76, 15\n"
  "v_mul_f32 v82, v82, v80\n"
  "v_mul_f32 v83, v83, v81\n"
  "v_mfma_f32_16x16x32_bf16 v[20:23], a[132:135], v[40:43], v[20:23]\n"
  "s_add_u32 s77, s77, s72\n"
  "ds_read_b128 v[68:71], v89 offset:48128\n"
  "v_cvt_pk_bf16_f32 v85, v82, v83\n"
  "v_mfma_f32_16x16x32_bf16 v[20:23], a[148:151], v[44:47], v[20:23]\n"
  "s_mul_i32 s77, s77, 2304\n"
  "v_mul_f32 v80, v4, v4\n"
  "v_mul_f32 v81, v5, v5\n"
  "v_mfma_f32_16x16x32_bf16 v[20:23], a[164:167], v[48:51], v[20:23]\n"
  "buffer_load_dwordx4 v92, s[36:39], s52 offen offset:1024 sc1 lds\n"
  "v_fmaak_f32 v80, v88, v80, 0xc0135761\n"
  "v_mfma_f32_16x16x32_bf16 v[20:23], a[180:183], v[52:55], v[20:23]\n"
  "s_add_u32 s77, s77, s75\n"
  "ds_read_b128 v[72:75], v89 offset:18432\n"
  "v_fmaak_f32 v81, v88, v81, 0xc0135761\n"
  "v_mfma_f32_16x16x32_bf16 v[24:27], a[104:107], v[32:35], v[24:27]\n"
  "s_add_u32 s76, s76, 4\n"
  "v_mul_f32 v80, v4, v80\n"
  "v_mul_f32 v81, v5, v81\n"
  "v_mfma_f32_16x16x32_bf16 v[24:27], a[120:123], v[36:39], v[24:27]\n"
  "s_lshr_b32 s73, s76, 4\n"
  "v_exp_f32 v80, v80\n"
  "v_mfma_f32_16x16x32_bf16 v[24:27], a[136:139], v[40:43], v[24:27]\n"
  "s_lshl_b32 s73, s73, 6\n"
  "s_and_b32 s78, s76, 15\n"
  "ds_read_b128 v[76:79], v89 offset:19456\n"
  "v_mfma_f32_16x16x32_bf16 v[24:27], a[152:155], v[44:47], v[24:27]\n"
  "buffer_load_dwordx4 v92, s[36:39], s52 offen offset:2048 sc1 lds\n"
  "v_mfma_f32_16x16x32_bf16 v[24:27], a[168:171], v[48:51], v[24:27]\n"
  "s_add_u32 s73, s73, s78\n"
  "v_exp_f32 v81, v81\n"
  "v_mfma_f32_16x16x32_bf16 v[24:27], a[184:187], v[52:55], v[24:27]\n"
  "s_mul_i32 s73, s73, 2304\n"
  "v_mul_f32 v82, v4, v12\n"
  "v_mul_f32 v83, v5, v13\n"
  "v_mfma_f32_16x16x32_bf16 v[28:31], a[108:111], v[32:35], v[28:31]\n"
  "s_add_u32 s73, s73, s75\n"
  "v_add_f32 v80, 1.0, v80\n"
  "v_add_f32 v81, 1.0, v81\n"
  "v_mfma_f32_16x16x32_bf16 v[28:31], a[124:127], v[36:39], v[28:31]\n"
  "s_add_u32 s76, s76, 4\n"
  "v_rcp_f32 v80, v80\n"
  "v_mfma_f32_16x16x32_bf16 v[28:31], a[140:143], v[40:43], v[28:31]\n"
  "buffer_load_dwordx4 v92, s[36:39], s52 offen offset:3072 sc1 lds\n"
  "v_mfma_f32_16x16x32_bf16 v[28:31], a[156:159], v[44:47], v[28:31]\n"
  "s_lshr_b32 s74, s76, 4\n"
  "v_rcp_f32 v81, v81\n"
  "v_mfma_f32_16x16x32_bf16 v[28:31], a[172:175], v[48:51], v[28:31]\n"
  "s_lshl_b32 s74, s74, 6\n"
  "v_mul_f32 v82, v82, v80\n"
  "v_mul_f32 v83, v83, v81\n"
  "v_mfma_f32_16x16x32_bf16 v[28:31], a[188:191], v[52:55], v[28:31]\n"
  "s_waitcnt vmcnt(9) lgkmcnt(0)\n"
  "s_barrier\n"
  "v_mfma_f32_16x16x32_bf16 v[16:19], a[192:195], v[56:59], v[16:19]\n"
  "s_add_u32 m0, s49, 141312\n"
  "ds_read_b128 v[32:35], v89 offset:49152\n"
  "v_cvt_pk_bf16_f32 v86, v82, v83\n"
  "v_mfma_f32_16x16x32_bf16 v[16:19], a[208:211], v[60:63], v[16:19]\n"
  "buffer_load_dwordx4 v93, s[36:39], s52 offen sc1 lds\n"
  "v_mul_f32 v80, v6, v6\n"
  "v_mfma_f32_16x16x32_bf16 v[16:19], a[224:227], v[64:67], v[16:19]\n"
  "s_add_u32 s52, s52, 18432\n"
  "ds_read_b128 v[36:39], v89 offset:50176\n"
  "v_mul_f32 v81, v7, v7\n"
  "v_mfma_f32_16x16x32_bf16 v[16:19], a[240:243], v[68:71], v[16:19]\n"
  "s_cmp_eq_u32 s52, s54\n"
  "s_cselect_b32 s52, s53, s52\n"
  "v_fmaak_f32 v80, v88, v80, 0xc0135761\n"
  "v_mfma_f32_16x16x32_bf16 v[16:19], v[224:227], v[72:75], v[16:19]\n"
  "s_and_b32 s79, s76, 15\n"
  "v_fmaak_f32 v81, v88, v81, 0xc0135761\n"
  "v_mul_f32 v80, v6, v80\n"
  "v_mfma_f32_16x16x32_bf16 v[16:19], v[240:243], v[76:79], v[16:19]\n"
  "s_add_u32 m0, s48, 98304\n"
  "ds_read_b128 v[40:43], v89 offset:51200\n"
  "v_mul_f32 v81, v7, v81\n"
  "v_mfma_f32_16x16x32_bf16 v[20:23], a[196:199], v[56:59], v[20:23]\n"
  "buffer_load_dwordx4 v92, s[36:39], s52 offen offset:0 sc1 lds\n"
  "v_mfma_f32_16x16x32_bf16 v[20:23], a[212:215], v[60:63], v[20:23]\n"
  "s_add_u32 s74, s74, s79\n"
  "v_exp_f32 v80, v80\n"
  "v_mfma_f32_16x16x32_bf16 v[20:23], a[228:231], v[64:67], v[20:23]\n"
  "s_mul_i32 s74, s74, 2304\n"
  "s_add_u32 s74, s74, s75\n"
  "ds_read_b128 v[44:47], v89 offset:52224\n"
  "v_mfma_f32_16x16x32_bf16 v[20:23], a[244:247], v[68:71], v[20:23]\n"
  "s_or_b32 s74, s74, s31\n"
  "v_exp_f32 v81, v81\n"
  "v_mfma_f32_16x16x32_bf16 v[20:23], v[228:231], v[72:75], v[20:23]\n"
  "s_mul_i32 s63, s30, 18432\n"
  "v_mul_f32 v82, v6, v14\n"
  "v_mul_f32 v83, v7, v15\n"
  "v_mfma_f32_16x16x32_bf16 v[20:23], v[244:247], v[76:79], v[20:23]\n"
  "buffer_load_dwordx4 v92, s[36:39], s52 offen offset:1024 sc1 lds\n"
  "ds_read_b128 v[48:51], v89 offset:53248\n"
  "v_mfma_f32_16x16x32_bf16 v[24:27], a[200:203], v[56:59], v[24:27]\n"
  "s_lshl4_add_u32 s63, s75, s63\n"
  "v_add_f32 v80, 1.0, v80\n"
  "v_add_f32 v81, 1.0, v81\n"
  "v_mfma_f32_16x16x32_bf16 v[24:27], a[216:219], v[60:63], v[24:27]\n"
  "s_add_u32 s63, s63, 2359296\n"
  "v_rcp_f32 v80, v80\n"
  "v_mfma_f32_16x16x32_bf16 v[24:27], a[232:235], v[64:67], v[24:27]\n"
  "s_lshl2_add_u32 s72, s34, s66\n"
  "s_lshl4_add_u32 s72, s72, s7\n"
  "ds_read_b128 v[52:55], v89 offset:54272\n"
  "v_mfma_f32_16x16x32_bf16 v[24:27], a[248:251], v[68:71], v[24:27]\n"
  "s_lshl_b32 s72, s72, 7\n"
  "v_rcp_f32 v81, v81\n"
  "v_mfma_f32_16x16x32_bf16 v[24:27], v[232:235], v[72:75], v[24:27]\n"
  "buffer_load_dwordx4 v92, s[36:39], s52 offen offset:2048 sc1 lds\n"
  "v_mul_f32 v82, v82, v80\n"
  "v_mfma_f32_16x16x32_bf16 v[24:27], v[248:251], v[76:79], v[24:27]\n"
  "v_mul_f32 v83, v83, v81\n"
  "v_cvt_pk_bf16_f32 v87, v82, v83\n"
  "v_mfma_f32_16x16x32_bf16 v[28:31], a[204:207], v[56:59], v[28:31]\n"
  "v_and_b32 v201, 7, v200\n"
  "v_mfma_f32_16x16x32_bf16 v[28:31], a[220:223], v[60:63], v[28:31]\n"
  "s_add_u32 s72, s72, 7081984\n"
  "v_lshrrev_b32 v202, 5, v200\n"
  "v_mov_b32 v112, s35\n"
  "v_mfma_f32_16x16x32_bf16 v[28:31], a[236:239], v[64:67], v[28:31]\n"
  "v_lshrrev_b32 v113, 1, v201\n"
  "v_mfma_f32_16x16x32_bf16 v[28:31], a[252:255], v[68:71], v[28:31]\n"
  "buffer_load_dwordx4 v92, s[36:39], s52 offen offset:3072 sc1 lds\n"
  "v_mfma_f32_16x16x32_bf16 v[28:31], v[236:239], v[72:75], v[28:31]\n"
  "buffer_store_dwordx4 v[84:87], v89, s[36:39], s55 offen sc1\n"
  "v_mfma_f32_16x16x32_bf16 v[28:31], v[252:255], v[76:79], v[28:31]\n"
  "s_waitcnt lgkmcnt(0)\n"
  "s_lshl2_add_u32 s58, s68, s66\n"
  "v_lshrrev_b32 v114, 3, v200\n"
  "v_mfma_f32_16x16x32_bf16 v[0:3], a[0:3], v[32:35], 0\n"
  "s_add_u32 s55, s55, 147456\n"
  "ds_read_b128 v[56:59], v89 offset:55296\n"
  "v_and_b32 v115, 3, v200\n"
  "v_mfma_f32_16x16x32_bf16 v[0:3], a[16:19], v[36:39], v[0:3]\n"
  "s_add_u32 m0, s49, 139264\n"
  "v_mul_f32 v80, v16, v16\n"
  "v_mul_f32 v81, v17, v17\n"
  "v_mfma_f32_16x16x32_bf16 v[0:3], a[32:35], v[40:43], v[0:3]\n"
  "s_cmp_eq_u32 s55, s57\n"
  "s_cselect_b32 s55, s56, s55\n"
  "buffer_load_dwordx4 v93, s[36:39], s52 offen sc1 lds\n"
  "ds_read_b128 v[60:63], v89 offset:56320\n"
  "v_mfma_f32_16x16x32_bf16 v[0:3], a[48:51], v[44:47], v[0:3]\n"
  "s_add_u32 s52, s52, 18432\n"
  "v_fmaak_f32 v80, v88, v80, 0xc0135761\n"
  "v_fmaak_f32 v81, v88, v81, 0xc0135761\n"
  "v_mfma_f32_16x16x32_bf16 v[0:3], a[64:67], v[48:51], v[0:3]\n"
  "s_cmp_eq_u32 s52, s54\n"
  "s_cselect_b32 s52, s53, s52\n"
  "v_mul_f32 v80, v16, v80\n"
  "v_mfma_f32_16x16x32_bf16 v[0:3], a[80:83], v[52:55], v[0:3]\n"
  "ds_read_b128 v[64:67], v89 offset:57344\n"
  "v_mul_f32 v81, v17, v81\n"
  "v_mfma_f32_16x16x32_bf16 v[4:7], a[4:7], v[32:35], 0\n"
  "s_add_u32 m0, s48, 114688\n"
  "v_exp_f32 v80, v80\n"
  "v_mfma_f32_16x16x32_bf16 v[4:7], a[20:23], v[36:39], v[4:7]\n"
  "buffer_load_dwordx4 v92, s[36:39], s52 offen offset:0 sc1 lds\n"
  "v_lshrrev_b32 v116, 4, v200\n"
  "v_mfma_f32_16x16x32_bf16 v[4:7], a[36:39], v[40:43], v[4:7]\n"
  "ds_read_b128 v[68:71], v89 offset:58368\n"
  "v_and_b32 v117, 1, v200\n"
  "v_mfma_f32_16x16x32_bf16 v[4:7], a[52:55], v[44:47], v[4:7]\n"
  "v_exp_f32 v81, v81\n"
  "v_mfma_f32_16x16x32_bf16 v[4:7], a[68:71], v[48:51], v[4:7]\n"
  "v_mul_f32 v82, v16, v24\n"
  "v_mul_f32 v83, v17, v25\n"
  "v_mfma_f32_16x16x32_bf16 v[4:7], a[84:87], v[52:55], v[4:7]\n"
  "ds_read_b128 v[72:75], v89 offset:59392\n"
  "v_add_f32 v80, 1.0, v80\n"
  "v_mfma_f32_16x16x32_bf16 v[8:11], a[8:11], v[32:35], 0\n"
  "buffer_load_dwordx4 v92, s[36:39], s52 offen offset:1024 sc1 lds\n"
  "v_add_f32 v81, 1.0, v81\n"
  "v_mfma_f32_16x16x32_bf16 v[8:11], a[24:27], v[36:39], v[8:11]\n"
  "v_rcp_f32 v80, v80\n"
  "v_mfma_f32_16x16x32_bf16 v[8:11], a[40:43], v[40:43], v[8:11]\n"
  "ds_read_b128 v[76:79], v89 offset:60416\n"
  "v_mov_b32 v118, s70\n"
  "v_mfma_f32_16x16x32_bf16 v[8:11], a[56:59], v[44:47], v[8:11]\n"
  "v_rcp_f32 v81, v81\n"
  "v_mfma_f32_16x16x32_bf16 v[8:11], a[72:75], v[48:51], v[8:11]\n"
  "v_mul_f32 v82, v82, v80\n"
  "v_mul_f32 v83, v83, v81\n"
  "v_mfma_f32_16x16x32_bf16 v[8:11], a[88:91], v[52:55], v[8:11]\n"
  "buffer_load_dwordx4 v92, s[36:39], s52 offen offset:2048 sc1 lds\n"
  "v_cvt_pk_bf16_f32 v84, v82, v83\n"
  "v_mfma_f32_16x16x32_bf16 v[12:15], a[12:15], v[32:35], 0\n"
  "v_mul_f32 v80, v18, v18\n"
  "v_mul_f32 v81, v19, v19\n"
  "v_mfma_f32_16x16x32_bf16 v[12:15], a[28:31], v[36:39], v[12:15]\n"
  "v_fmaak_f32 v80, v88, v80, 0xc0135761\n"
  "v_fmaak_f32 v81, v88, v81, 0xc0135761\n"
  "v_mfma_f32_16x16x32_bf16 v[12:15], a[44:47], v[40:43], v[12:15]\n"
  "v_mul_f32 v80, v18, v80\n"
  "v_mul_f32 v81, v19, v81\n"
  "v_mfma_f32_16x16x32_bf16 v[12:15], a[60:63], v[44:47], v[12:15]\n"
  "v_exp_f32 v80, v80\n"
  "v_mfma_f32_16x16x32_bf16 v[12:15], a[76:79], v[48:51], v[12:15]\n"
  "buffer_load_dwordx4 v92, s[36:39], s52 offen offset:3072 sc1 lds\n"
  "v_lshlrev_b32 v119, 12, v202\n"
  "v_mfma_f32_16x16x32_bf16 v[12:15], a[92:95], v[52:55], v[12:15]\n"
  "s_waitcnt lgkmcnt(0)\n"
  "v_exp_f32 v81, v81\n"
  "v_mfma_f32_16x16x32_bf16 v[0:3], a[96:99], v[56:59], v[0:3]\n"
  "ds_read_b128 v[32:35], v89 offset:61440\n"
  "v_mul_f32 v82, v18, v26\n"
  "v_mfma_f32_16x16x32_bf16 v[0:3], a[112:115], v[60:63], v[0:3]\n"
  "v_mul_f32 v83, v19, v27\n"
  "v_add_f32 v80, 1.0, v80\n"
  "v_mfma_f32_16x16x32_bf16 v[0:3], a[128:131], v[64:67], v[0:3]\n"
  "s_add_u32 m0, s49, 137216\n"
  "ds_read_b128 v[36:39], v89 offset:62464\n"
  "v_add_f32 v81, 1.0, v81\n"
  "v_mfma_f32_16x16x32_bf16 v[0:3], a[144:147], v[68:71], v[0:3]\n"
  "buffer_load_dwordx4 v93, s[36:39], s52 offen sc1 lds\n"
  "v_bfe_u32 v120, v200, 2, 3\n"
  "v_mfma_f32_16x16x32_bf16 v[0:3], a[160:163], v[72:75], v[0:3]\n"
  "s_add_u32 s52, s52, 18432\n"
  "v_rcp_f32 v80, v80\n"
  "v_mfma_f32_16x16x32_bf16 v[0:3], a[176:179], v[76:79], v[0:3]\n"
  "s_cmp_eq_u32 s52, s54\n"
  "s_cselect_b32 s52, s53, s52\n"
  "ds_read_b128 v[40:43], v89 offset:63488\n"
  "v_mfma_f32_16x16x32_bf16 v[4:7], a[100:103], v[56:59], v[4:7]\n"
  "v_rcp_f32 v81, v81\n"
  "v_mfma_f32_16x16x32_bf16 v[4:7], a[116:119], v[60:63], v[4:7]\n"
  "s_add_u32 m0, s48, 131072\n"
  "v_mul_f32 v82, v82, v80\n"
  "v_mul_f32 v83, v83, v81\n"
  "v_mfma_f32_16x16x32_bf16 v[4:7], a[132:135], v[64:67], v[4:7]\n"
  "buffer_load_dwordx4 v92, s[36:39], s52 offen offset:0 sc1 lds\n"
  "ds_read_b128 v[44:47], v89 offset:64512\n"
  "v_mfma_f32_16x16x32_bf16 v[4:7], a[148:151], v[68:71], v[4:7]\n"
  "v_cvt_pk_bf16_f32 v85, v82, v83\n"
  "v_mul_f32 v80, v20, v20\n"
  "v_mfma_f32_16x16x32_bf16 v[4:7], a[164:167], v[72:75], v[4:7]\n"
  "v_mul_f32 v81, v21, v21\n"
  "v_fmaak_f32 v80, v88, v80, 0xc0135761\n"
  "v_mfma_f32_16x16x32_bf16 v[4:7], a[180:183], v[76:79], v[4:7]\n"
  "ds_read_b128 v[48:51], v89 offset:20480\n"
  "v_fmaak_f32 v81, v88, v81, 0xc0135761\n"
  "v_mfma_f32_16x16x32_bf16 v[8:11], a[104:107], v[56:59], v[8:11]\n"
  "v_mul_f32 v80, v20, v80\n"
  "v_mul_f32 v81, v21, v81\n"
  "v_mfma_f32_16x16x32_bf16 v[8:11], a[120:123], v[60:63], v[8:11]\n"
  "buffer_load_dwordx4 v92, s[36:39], s52 offen offset:1024 sc1 lds\n"
  "v_bfe_u32 v121, v200, 3, 2\n"
  "v_mfma_f32_16x16x32_bf16 v[8:11], a[136:139], v[64:67], v[8:11]\n"
  "ds_read_b128 v[52:55], v89 offset:21504\n"
  "v_mul_u32_u24 v122, 18432, v112\n"
  "v_mfma_f32_16x16x32_bf16 v[8:11], a[152:155], v[68:71], v[8:11]\n"
  "v_exp_f32 v80, v80\n"
  "v_mfma_f32_16x16x32_bf16 v[8:11], a[168:171], v[72:75], v[8:11]\n"
  "v_exp_f32 v81, v81\n"
  "v_mfma_f32_16x16x32_bf16 v[8:11], a[184:187], v[76:79], v[8:11]\n"
  "v_mul_f32 v82, v20, v28\n"
  "v_mul_f32 v83, v21, v29\n"
  "v_mfma_f32_16x16x32_bf16 v[12:15], a[108:111], v[56:59], v[12:15]\n"
  "buffer_load_dwordx4 v92, s[36:39], s52 offen offset:2048 sc1 lds\n"
  "v_add_f32 v80, 1.0, v80\n"
  "v_mfma_f32_16x16x32_bf16 v[12:15], a[124:127], v[60:63], v[12:15]\n"
  "v_add_f32 v81, 1.0, v81\n"
  "v_mul_u32_u24 v123, 3072, v113\n"
  "v_mfma_f32_16x16x32_bf16 v[12:15], a[140:143], v[64:67], v[12:15]\n"
  "s_lshl2_add_u32 s58, s58, s8\n"
  "v_rcp_f32 v80, v80\n"
  "v_mfma_f32_16x16x32_bf16 v[12:15], a[156:159], v[68:71], v[12:15]\n"
  "v_rcp_f32 v81, v81\n"
  "v_mfma_f32_16x16x32_bf16 v[12:15], a[172:175], v[72:75], v[12:15]\n"
  "v_mul_f32 v82, v82, v80\n"
  "v_mul_f32 v83, v83, v81\n"
  "v_mfma_f32_16x16x32_bf16 v[12:15], a[188:191], v[76:79], v[12:15]\n"
  "s_waitcnt lgkmcnt(0)\n"
  "v_cvt_pk_bf16_f32 v86, v82, v83\n"
  "v_mul_f32 v80, v22, v22\n"
  "v_mfma_f32_16x16x32_bf16 v[0:3], a[192:195], v[32:35], v[0:3]\n"
  "buffer_load_dwordx4 v92, s[36:39], s52 offen offset:3072 sc1 lds\n"
  "ds_read_b128 v[56:59], v90 offset:0\n"
  "v_mfma_f32_16x16x32_bf16 v[0:3], a[208:211], v[36:39], v[0:3]\n"
  "v_mul_f32 v81, v23, v23\n"
  "v_fmaak_f32 v80, v88, v80, 0xc0135761\n"
  "v_mfma_f32_16x16x32_bf16 v[0:3], a[224:227], v[40:43], v[0:3]\n"
  "ds_read_b128 v[60:63], v90 offset:1024\n"
  "v_fmaak_f32 v81, v88, v81, 0xc0135761\n"
  "v_mfma_f32_16x16x32_bf16 v[0:3], a[240:243], v[44:47], v[0:3]\n"
  "s_add_u32 m0, s49, 135168\n"
  "v_mul_f32 v80, v22, v80\n"
  "v_mul_f32 v81, v23, v81\n"
  "v_mfma_f32_16x16x32_bf16 v[0:3], v[224:227], v[48:51], v[0:3]\n"
  "buffer_load_dwordx4 v93, s[36:39], s52 offen sc1 lds\n"
  "v_and_b32 v124, 0x70, v89\n"
  "v_mfma_f32_16x16x32_bf16 v[0:3], v[240:243], v[52:55], v[0:3]\n"
  "s_add_u32 s52, s52, 18432\n"
  "ds_read_b128 v[64:67], v90 offset:2048\n"
  "v_lshlrev_b32 v125, 5, v116\n"
  "v_mfma_f32_16x16x32_bf16 v[4:7], a[196:199], v[32:35], v[4:7]\n"
  "s_cmp_eq_u32 s52, s54\n"
  "s_cselect_b32 s52, s53, s52\n"
  "v_lshl_add_u32 v126, v120, 7, v119\n"
  "v_mfma_f32_16x16x32_bf16 v[4:7], a[212:215], v[36:39], v[4:7]\n"
  "v_exp_f32 v80, v80\n"
  "v_mfma_f32_16x16x32_bf16 v[4:7], a[228:231], v[40:43], v[4:7]\n"
  "s_add_u32 m0, s48, 32768\n"
  "ds_read_b128 v[68:71], v90 offset:3072\n"
  "v_lshl_add_u32 v127, v121, 5, v202\n"
  "v_mfma_f32_16x16x32_bf16 v[4:7], a[244:247], v[44:47], v[4:7]\n"
  "buffer_load_dwordx4 v92, s[36:39], s52 offen offset:0 sc1 lds\n"
  "v_lshl_add_u32 v128, v118, 3, v114\n"
  "v_mfma_f32_16x16x32_bf16 v[4:7], v[228:231], v[48:51], v[4:7]\n"
  "v_exp_f32 v81, v81\n"
  "v_mfma_f32_16x16x32_bf16 v[4:7], v[244:247], v[52:55], v[4:7]\n"
  "ds_read_b128 v[72:75], v90 offset:4096\n"
  "v_mul_f32 v82, v22, v30\n"
  "v_mfma_f32_16x16x32_bf16 v[8:11], a[200:203], v[32:35], v[8:11]\n"
  "v_mul_f32 v83, v23, v31\n"
  "v_add_f32 v80, 1.0, v80\n"
  "v_mfma_f32_16x16x32_bf16 v[8:11], a[216:219], v[36:39], v[8:11]\n"
  "v_add_f32 v81, 1.0, v81\n"
  "v_lshl_add_u32 v129, v201, 8, v122\n"
  "v_mfma_f32_16x16x32_bf16 v[8:11], a[232:235], v[40:43], v[8:11]\n"
  "buffer_load_dwordx4 v92, s[36:39], s52 offen offset:1024 sc1 lds\n"
  "ds_read_b128 v[76:79], v90 offset:5120\n"
  "v_mfma_f32_16x16x32_bf16 v[8:11], a[248:251], v[44:47], v[8:11]\n"
  "v_rcp_f32 v80, v80\n"
  "v_mfma_f32_16x16x32_bf16 v[8:11], v[232:235], v[48:51], v[8:11]\n"
  "v_rcp_f32 v81, v81\n"
  "v_mfma_f32_16x16x32_bf16 v[8:11], v[248:251], v[52:55], v[8:11]\n"
  "v_mul_f32 v82, v82, v80\n"
  "v_mul_f32 v83, v83, v81\n"
  "v_mfma_f32_16x16x32_bf16 v[12:15], a[204:207], v[32:35], v[12:15]\n"
  "v_cvt_pk_bf16_f32 v87, v82, v83\n"
  "v_lshl_add_u32 v130, v118, 1, v202\n"
  "v_mfma_f32_16x16x32_bf16 v[12:15], a[220:223], v[36:39], v[12:15]\n"
  "buffer_load_dwordx4 v92, s[36:39], s52 offen offset:2048 sc1 lds\n"
  "v_lshl_add_u32 v131, v117, 9, v123\n"
  "v_mfma_f32_16x16x32_bf16 v[12:15], a[236:239], v[40:43], v[12:15]\n"
  "s_lshl_b32 s58, s58, 9\n"
  "v_add_u32 v132, s6, v115\n"
  "v_mul_u32_u24 v133, 1152, v114\n"
  "v_mfma_f32_16x16x32_bf16 v[12:15], a[252:255], v[44:47], v[12:15]\n"
  "buffer_store_dwordx4 v[84:87], v89, s[36:39], s55 offen sc1\n"
  "v_xor_b32 v134, v124, v125\n"
  "v_mfma_f32_16x16x32_bf16 v[12:15], v[236:239], v[48:51], v[12:15]\n"
  "s_add_u32 s55, s55, 147456\n"
  "v_lshl_add_u32 v135, v115, 3, v126\n"
  "v_xor_b32 v200, s10, v121\n"
  "v_mfma_f32_16x16x32_bf16 v[12:15], v[252:255], v[52:55], v[12:15]\n"
  "s_waitcnt lgkmcnt(0)\n"
  "v_mul_u32_u24 v113, 1152, v127\n"
  "v_lshl_add_u32 v207, v128, 4, v129\n"
  "v_mfma_f32_16x16x32_bf16 v[16:19], a[0:3], v[56:59], 0\n"
  "s_cmp_eq_u32 s55, s57\n"
  "s_cselect_b32 s55, s56, s55\n"
  "buffer_load_dwordx4 v92, s[36:39], s52 offen offset:3072 sc1 lds\n"
  "ds_read_b128 v[32:35], v90 offset:6144\n"
  "v_mfma_f32_16x16x32_bf16 v[16:19], a[16:19], v[60:63], v[16:19]\n"
  "v_mul_f32 v80, v0, v0\n"
  "v_mul_f32 v81, v1, v1\n"
  "v_mfma_f32_16x16x32_bf16 v[16:19], a[32:35], v[64:67], v[16:19]\n"
  "ds_read_b128 v[36:39], v90 offset:7168\n"
  "v_fmaak_f32 v80, v88, v80, 0xc0135761\n"
  "v_mfma_f32_16x16x32_bf16 v[16:19], a[48:51], v[68:71], v[16:19]\n"
  "v_fmaak_f32 v81, v88, v81, 0xc0135761\n"
  "v_mul_f32 v80, v0, v80\n"
  "v_mfma_f32_16x16x32_bf16 v[16:19], a[64:67], v[72:75], v[16:19]\n"
  "s_add_u32 m0, s49, 133120\n"
  "v_mul_f32 v81, v1, v81\n"
  "v_lshlrev_b32 v136, 6, v130\n"
  "v_mfma_f32_16x16x32_bf16 v[16:19], a[80:83], v[76:79], v[16:19]\n"
  "buffer_load_dwordx4 v93, s[36:39], s52 offen sc1 lds\n"
  "ds_read_b128 v[40:43], v90 offset:8192\n"
  "v_mfma_f32_16x16x32_bf16 v[20:23], a[4:7], v[56:59], 0\n"
  "s_add_u32 s52, s52, 18432\n"
  "v_exp_f32 v80, v80\n"
  "v_mfma_f32_16x16x32_bf16 v[20:23], a[20:23], v[60:63], v[20:23]\n"
  "s_cmp_eq_u32 s52, s54\n"
  "s_cselect_b32 s52, s53, s52\n"
  "v_lshlrev_b32 v137, 12, v112\n"
  "v_mfma_f32_16x16x32_bf16 v[20:23], a[36:39], v[64:67], v[20:23]\n"
  "ds_read_b128 v[44:47], v90 offset:9216\n"
  "v_lshlrev_b32 v138, 2, v115\n"
  "v_mfma_f32_16x16x32_bf16 v[20:23], a[52:55], v[68:71], v[20:23]\n"
  "v_exp_f32 v81, v81\n"
  "v_mfma_f32_16x16x32_bf16 v[20:23], a[68:71], v[72:75], v[20:23]\n"
  "v_mul_f32 v82, v0, v8\n"
  "v_mul_f32 v83, v1, v9\n"
  "v_mfma_f32_16x16x32_bf16 v[20:23], a[84:87], v[76:79], v[20:23]\n"
  "ds_read_b128 v[48:51], v90 offset:10240\n"
  "v_add_f32 v80, 1.0, v80\n"
  "v_mfma_f32_16x16x32_bf16 v[24:27], a[8:11], v[56:59], 0\n"
  "v_add_f32 v81, 1.0, v81\n"
  "v_lshl_add_u32 v139, v112, 8, v131\n"
  "v_mfma_f32_16x16x32_bf16 v[24:27], a[24:27], v[60:63], v[24:27]\n"
  "s_add_u32 s58, s58, 7082368\n"
  "v_rcp_f32 v80, v80\n"
  "v_mfma_f32_16x16x32_bf16 v[24:27], a[40:43], v[64:67], v[24:27]\n"
  "ds_read_b128 v[52:55], v90 offset:11264\n"
  "v_and_b32 v116, 3, v132\n"
  "v_mfma_f32_16x16x32_bf16 v[24:27], a[56:59], v[68:71], v[24:27]\n"
  "s_mul_i32 s7, s10, 147456\n"
  "v_rcp_f32 v81, v81\n"
  "v_mfma_f32_16x16x32_bf16 v[24:27], a[72:75], v[72:75], v[24:27]\n"
  "v_mul_f32 v82, v82, v80\n"
  "v_mul_f32 v83, v83, v81\n"
  "v_mfma_f32_16x16x32_bf16 v[24:27], a[88:91], v[76:79], v[24:27]\n"
  "v_cvt_pk_bf16_f32 v84, v82, v83\n"
  "v_mul_f32 v80, v2, v2\n"
  "v_mfma_f32_16x16x32_bf16 v[28:31], a[12:15], v[56:59], 0\n"
  "v_mul_f32 v81, v3, v3\n"
  "v_fmaak_f32 v80, v88, v80, 0xc0135761\n"
  "v_mfma_f32_16x16x32_bf16 v[28:31], a[28:31], v[60:63], v[28:31]\n"
  "v_fmaak_f32 v81, v88, v81, 0xc0135761\n"
  "v_mul_f32 v80, v2, v80\n"
  "v_mfma_f32_16x16x32_bf16 v[28:31], a[44:47], v[64:67], v[28:31]\n"
  "v_mul_f32 v81, v3, v81\n"
  "s_cbranch_execz Ltramp%=\n"
  "v_add3_u32 v117, v133, v134, s28\n"
  "v_mfma_f32_16x16x32_bf16 v[28:31], a[60:63], v[68:71], v[28:31]\n"
  "v_exp_f32 v80, v80\n"
  "v_mfma_f32_16x16x32_bf16 v[28:31], a[76:79], v[72:75], v[28:31]\n"
  "v_exp_f32 v81, v81\n"
  "v_mfma_f32_16x16x32_bf16 v[28:31], a[92:95], v[76:79], v[28:31]\n"
  "s_waitcnt lgkmcnt(0)\n"
  "v_mul_f32 v82, v2, v10\n"
  "v_mul_f32 v83, v3, v11\n"
  "v_mfma_f32_16x16x32_bf16 v[16:19], a[96:99], v[32:35], v[16:19]\n"
  "ds_read_b128 v[56:59], v90 offset:12288\n"
  "v_add_f32 v80, 1.0, v80\n"
  "v_mfma_f32_16x16x32_bf16 v[16:19], a[112:115], v[36:39], v[16:19]\n"
  "v_add_f32 v81, 1.0, v81\n"
  "v_lshl_add_u32 v99, v200, 5, v135\n"
  "v_mfma_f32_16x16x32_bf16 v[16:19], a[128:131], v[40:43], v[16:19]\n"
  "ds_read_b128 v[60:63], v90 offset:13312\n"
  "v_lshl_add_u32 v118, v201, 4, v113\n"
  "v_mfma_f32_16x16x32_bf16 v[16:19], a[144:147], v[44:47], v[16:19]\n"
  "v_rcp_f32 v80, v80\n"
  "v_mfma_f32_16x16x32_bf16 v[16:19], a[160:163], v[48:51], v[16:19]\n"
  "v_rcp_f32 v81, v81\n"
  "v_mfma_f32_16x16x32_bf16 v[16:19], a[176:179], v[52:55], v[16:19]\n"
  "ds_read_b128 v[64:67], v90 offset:14336\n"
  "v_mul_f32 v82, v82, v80\n"
  "v_mfma_f32_16x16x32_bf16 v[20:23], a[100:103], v[32:35], v[20:23]\n"
  "v_mul_f32 v83, v83, v81\n"
  "v_cvt_pk_bf16_f32 v85, v82, v83\n"
  "v_mfma_f32_16x16x32_bf16 v[20:23], a[116:119], v[36:39], v[20:23]\n"
  "v_mul_f32 v80, v4, v4\n"
  "v_mul_f32 v81, v5, v5\n"
  "v_mfma_f32_16x16x32_bf16 v[20:23], a[132:135], v[40:43], v[20:23]\n"
  "ds_read_b128 v[68:71], v90 offset:15360\n"
  "v_fmaak_f32 v80, v88, v80, 0xc0135761\n"
  "v_mfma_f32_16x16x32_bf16 v[20:23], a[148:151], v[44:47], v[20:23]\n"
  "v_fmaak_f32 v81, v88, v81, 0xc0135761\n"
  "v_mul_f32 v80, v4, v80\n"
  "v_mfma_f32_16x16x32_bf16 v[20:23], a[164:167], v[48:51], v[20:23]\n"
  "v_mul_f32 v81, v5, v81\n"
  "v_add_u32 v119, 73728, v207\n"
  "v_mfma_f32_16x16x32_bf16 v[20:23], a[180:183], v[52:55], v[20:23]\n"
  "ds_read_b128 v[72:75], v91 offset:30720\n"
  "v_xor_b32 v202, v136, v89\n"
  "v_mfma_f32_16x16x32_bf16 v[24:27], a[104:107], v[32:35], v[24:27]\n"
  "v_exp_f32 v80, v80\n"
  "v_mfma_f32_16x16x32_bf16 v[24:27], a[120:123], v[36:39], v[24:27]\n"
  "v_exp_f32 v81, v81\n"
  "v_mfma_f32_16x16x32_bf16 v[24:27], a[136:139], v[40:43], v[24:27]\n"
  "ds_read_b128 v[76:79], v91 offset:31744\n"
  "v_mul_f32 v82, v4, v12\n"
  "v_mfma_f32_16x16x32_bf16 v[24:27], a[152:155], v[44:47], v[24:27]\n"
  "v_mul_f32 v83, v5, v13\n"
  "v_add_f32 v80, 1.0, v80\n"
  "v_mfma_f32_16x16x32_bf16 v[24:27], a[168:171], v[48:51], v[24:27]\n"
  "v_add_f32 v81, 1.0, v81\n"
  "v_lshl_add_u32 v114, v201, 9, v137\n"
  "v_mfma_f32_16x16x32_bf16 v[24:27], a[184:187], v[52:55], v[24:27]\n"
  "v_rcp_f32 v80, v80\n"
  "v_mfma_f32_16x16x32_bf16 v[28:31], a[108:111], v[32:35], v[28:31]\n"
  "v_rcp_f32 v81, v81\n"
  "v_mfma_f32_16x16x32_bf16 v[28:31], a[124:127], v[36:39], v[28:31]\n"
  "v_mul_f32 v82, v82, v80\n"
  "v_mul_f32 v83, v83, v81\n"
  "v_mfma_f32_16x16x32_bf16 v[28:31], a[140:143], v[40:43], v[28:31]\n"
  "v_cvt_pk_bf16_f32 v86, v82, v83\n"
  "v_mul_f32 v80, v6, v6\n"
  "v_mfma_f32_16x16x32_bf16 v[28:31], a[156:159], v[44:47], v[28:31]\n"
  "v_mul_f32 v81, v7, v7\n"
  "v_fmaak_f32 v80, v88, v80, 0xc0135761\n"
  "v_mfma_f32_16x16x32_bf16 v[28:31], a[172:175], v[48:51], v[28:31]\n"
  "v_fmaak_f32 v81, v88, v81, 0xc0135761\n"
  "v_mul_f32 v80, v6, v80\n"
  "v_mfma_f32_16x16x32_bf16 v[28:31], a[188:191], v[52:55], v[28:31]\n"
  "s_waitcnt vmcnt(22) lgkmcnt(0)\n"
  "s_barrier\n"
  "v_mfma_f32_16x16x32_bf16 v[16:19], a[192:195], v[56:59], v[16:19]\n"
  "s_add_u32 m0, s48, 16384\n"
  "ds_read_b128 v[32:35], v90 offset:16384\n"
  "v_mul_f32 v81, v7, v81\n"
  "v_mfma_f32_16x16x32_bf16 v[16:19], a[208:211], v[60:63], v[16:19]\n"
  "buffer_load_dwordx4 v92, s[36:39], s52 offen offset:0 sc1 lds\n"
  "v_xor_b32 v120, v138, v128\n"
  "v_mfma_f32_16x16x32_bf16 v[16:19], a[224:227], v[64:67], v[16:19]\n"
  "ds_read_b128 v[36:39], v90 offset:17408\n"
  "v_lshl_add_u32 v212, v128, 4, v139\n"
  "v_mfma_f32_16x16x32_bf16 v[16:19], a[240:243], v[68:71], v[16:19]\n"
  "v_exp_f32 v80, v80\n"
  "v_mfma_f32_16x16x32_bf16 v[16:19], v[224:227], v[72:75], v[16:19]\n"
  "v_exp_f32 v81, v81\n"
  "v_mfma_f32_16x16x32_bf16 v[16:19], v[240:243], v[76:79], v[16:19]\n"
  "ds_read_b128 v[40:43], v90 offset:18432\n"
  "v_mul_f32 v82, v6, v14\n"
  "v_mfma_f32_16x16x32_bf16 v[20:23], a[196:199], v[56:59], v[20:23]\n"
  "buffer_load_dwordx4 v92, s[36:39], s52 offen offset:1024 sc1 lds\n"
  "v_mul_f32 v83, v7, v15\n"
  "v_mfma_f32_16x16x32_bf16 v[20:23], a[212:215], v[60:63], v[20:23]\n"
  "v_add_f32 v80, 1.0, v80\n"
  "v_add_f32 v81, 1.0, v81\n"
  "v_mfma_f32_16x16x32_bf16 v[20:23], a[228:231], v[64:67], v[20:23]\n"
  "ds_read_b128 v[44:47], v90 offset:19456\n"
  "v_lshl_add_u32 v121, v116, 9, s72\n"
  "v_mfma_f32_16x16x32_bf16 v[20:23], a[244:247], v[68:71], v[20:23]\n"
  "v_rcp_f32 v80, v80\n"
  "v_mfma_f32_16x16x32_bf16 v[20:23], v[228:231], v[72:75], v[20:23]\n"
  "v_rcp_f32 v81, v81\n"
  "v_mfma_f32_16x16x32_bf16 v[20:23], v[244:247], v[76:79], v[20:23]\n"
  "buffer_load_dwordx4 v92, s[36:39], s52 offen offset:2048 sc1 lds\n"
  "ds_read_b128 v[48:51], v90 offset:20480\n"
  "v_mfma_f32_16x16x32_bf16 v[24:27], a[200:203], v[56:59], v[24:27]\n"
  "v_mul_f32 v82, v82, v80\n"
  "v_mul_f32 v83, v83, v81\n"
  "v_mfma_f32_16x16x32_bf16 v[24:27], a[216:219], v[60:63], v[24:27]\n"
  "v_cvt_pk_bf16_f32 v87, v82, v83\n"
  "v_mov_b32 v122, s58\n"
  "v_mfma_f32_16x16x32_bf16 v[24:27], a[232:235], v[64:67], v[24:27]\n"
  "s_mov_b64 s[34:35], 16\n"
  "s_mov_b64 s[44:45], s[20:21]\n"
  "ds_read_b128 v[52:55], v90 offset:21504\n"
  "v_mfma_f32_16x16x32_bf16 v[24:27], a[248:251], v[68:71], v[24:27]\n"
  "v_add_u32 v97, s7, v89\n"
  "v_add_u32 v98, s71, v89\n"
  "v_mfma_f32_16x16x32_bf16 v[24:27], v[232:235], v[72:75], v[24:27]\n"
  "buffer_load_dwordx4 v92, s[36:39], s52 offen offset:3072 sc1 lds\n"
  "v_add_u32 v107, s29, v89\n"
  "v_mfma_f32_16x16x32_bf16 v[24:27], v[248:251], v[76:79], v[24:27]\n"
  "v_add_u32 v94, s22, v117\n"
  "v_add_u32 v95, s9, v117\n"
  "v_mfma_f32_16x16x32_bf16 v[28:31], a[204:207], v[56:59], v[28:31]\n"
  "v_add_u32 v96, s23, v117\n"
  "v_mov_b32 v108, -1\n"
  "v_mfma_f32_16x16x32_bf16 v[28:31], a[220:223], v[60:63], v[28:31]\n"
  "v_mov_b32 v109, -1\n"
  "v_mov_b32 v110, -1\n"
  "v_mfma_f32_16x16x32_bf16 v[28:31], a[236:239], v[64:67], v[28:31]\n"
  "s_add_u32 m0, s49, 131072\n"
  "v_mov_b32 v111, -1\n"
  "v_add_u32 v100, 0x10000, v99\n"
  "v_mfma_f32_16x16x32_bf16 v[28:31], a[252:255], v[68:71], v[28:31]\n"
  "buffer_load_dwordx4 v93, s[36:39], s52 offen sc1 lds\n"
  "v_add_u32 v101, 0x20000, v99\n"
  "v_mfma_f32_16x16x32_bf16 v[28:31], v[236:239], v[72:75], v[28:31]\n"
  "s_add_u32 s52, s52, 18432\n"
  "buffer_store_dwordx4 v[84:87], v89, s[36:39], s55 offen sc1\n"
  "v_add_u32 v204, s77, v118\n"
  "v_mfma_f32_16x16x32_bf16 v[28:31], v[252:255], v[76:79], v[28:31]\n"
  "s_waitcnt lgkmcnt(0)\n"
  "s_add_u32 s55, s55, 147456\n"
  "v_add_u32 v205, s73, v118\n"
  "v_mfma_f32_16x16x32_bf16 v[0:3], a[0:3], v[32:35], 0\n"
  "s_cmp_eq_u32 s52, s54\n"
  "s_cselect_b32 s52, s53, s52\n"
  "ds_read_b128 v[56:59], v90 offset:22528\n"
  "v_mfma_f32_16x16x32_bf16 v[0:3], a[16:19], v[36:39], v[0:3]\n"
  "s_add_u32 m0, s48, 49152\n"
  "s_cmp_eq_u32 s55, s57\n"
  "s_cselect_b32 s55, s56, s55\n"
  "v_mfma_f32_16x16x32_bf16 v[0:3], a[32:35], v[40:43], v[0:3]\n"
  "buffer_load_dwordx4 v92, s[36:39], s52 offen offset:0 sc1 lds\n"
  "ds_read_b128 v[60:63], v90 offset:23552\n"
  "v_mfma_f32_16x16x32_bf16 v[0:3], a[48:51], v[44:47], v[0:3]\n"
  "v_mul_f32 v80, v16, v16\n"
  "v_mul_f32 v81, v17, v17\n"
  "v_mfma_f32_16x16x32_bf16 v[0:3], a[64:67], v[48:51], v[0:3]\n"
  "v_fmaak_f32 v80, v88, v80, 0xc0135761\n"
  "v_fmaak_f32 v81, v88, v81, 0xc0135761\n"
  "v_mfma_f32_16x16x32_bf16 v[0:3], a[80:83], v[52:55], v[0:3]\n"
  "ds_read_b128 v[64:67], v90 offset:24576\n"
  "v_mul_f32 v80, v16, v80\n"
  "v_mfma_f32_16x16x32_bf16 v[4:7], a[4:7], v[32:35], 0\n"
  "v_mul_f32 v81, v17, v81\n"
  "v_add_u32 v206, s74, v118\n"
  "v_mfma_f32_16x16x32_bf16 v[4:7], a[20:23], v[36:39], v[4:7]\n"
  "buffer_load_dwordx4 v92, s[36:39], s52 offen offset:1024 sc1 lds\n"
  "v_add_u32 v208, 36864, v207\n"
  "v_mfma_f32_16x16x32_bf16 v[4:7], a[36:39], v[40:43], v[4:7]\n"
  "ds_read_b128 v[68:71], v90 offset:25600\n"
  "v_or_b32 v209, s31, v119\n"
  "v_mfma_f32_16x16x32_bf16 v[4:7], a[52:55], v[44:47], v[4:7]\n"
  "v_exp_f32 v80, v80\n"
  "v_mfma_f32_16x16x32_bf16 v[4:7], a[68:71], v[48:51], v[4:7]\n"
  "v_exp_f32 v81, v81\n"
  "v_mfma_f32_16x16x32_bf16 v[4:7], a[84:87], v[52:55], v[4:7]\n"
  "ds_read_b128 v[72:75], v90 offset:26624\n"
  "v_mul_f32 v82, v16, v24\n"
  "v_mfma_f32_16x16x32_bf16 v[8:11], a[8:11], v[32:35], 0\n"
  "buffer_load_dwordx4 v92, s[36:39], s52 offen offset:2048 sc1 lds\n"
  "v_mul_f32 v83, v17, v25\n"
  "v_mfma_f32_16x16x32_bf16 v[8:11], a[24:27], v[36:39], v[8:11]\n"
  "v_add_f32 v80, 1.0, v80\n"
  "v_add_f32 v81, 1.0, v81\n"
  "v_mfma_f32_16x16x32_bf16 v[8:11], a[40:43], v[40:43], v[8:11]\n"
  "ds_read_b128 v[76:79], v90 offset:27648\n"
  "v_add_u32 v210, s50, v202\n"
  "v_mfma_f32_16x16x32_bf16 v[8:11], a[56:59], v[44:47], v[8:11]\n"
  "v_rcp_f32 v80, v80\n"
  "v_mfma_f32_16x16x32_bf16 v[8:11], a[72:75], v[48:51], v[8:11]\n"
  "v_rcp_f32 v81, v81\n"
  "v_mfma_f32_16x16x32_bf16 v[8:11], a[88:91], v[52:55], v[8:11]\n"
  "buffer_load_dwordx4 v92, s[36:39], s52 offen offset:3072 sc1 lds\n"
  "v_mul_f32 v82, v82, v80\n"
  "v_mfma_f32_16x16x32_bf16 v[12:15], a[12:15], v[32:35], 0\n"
  "v_mul_f32 v83, v83, v81\n"
  "v_cvt_pk_bf16_f32 v84, v82, v83\n"
  "v_mfma_f32_16x16x32_bf16 v[12:15], a[28:31], v[36:39], v[12:15]\n"
  "v_mul_f32 v80, v18, v18\n"
  "v_mul_f32 v81, v19, v19\n"
  "v_mfma_f32_16x16x32_bf16 v[12:15], a[44:47], v[40:43], v[12:15]\n"
  "v_fmaak_f32 v80, v88, v80, 0xc0135761\n"
  "v_fmaak_f32 v81, v88, v81, 0xc0135761\n"
  "v_mfma_f32_16x16x32_bf16 v[12:15], a[60:63], v[44:47], v[12:15]\n"
  "s_add_u32 m0, s49, 145408\n"
  "v_mul_f32 v80, v18, v80\n"
  "v_mul_f32 v81, v19, v81\n"
  "v_mfma_f32_16x16x32_bf16 v[12:15], a[76:79], v[48:51], v[12:15]\n"
  "buffer_load_dwordx4 v93, s[36:39], s52 offen sc1 lds\n"
  "v_lshl_add_u32 v211, v120, 4, v114\n"
  "v_mfma_f32_16x16x32_bf16 v[12:15], a[92:95], v[52:55], v[12:15]\n"
  "s_waitcnt lgkmcnt(0)\n"
  "v_exp_f32 v80, v80\n"
  "v_mfma_f32_16x16x32_bf16 v[0:3], a[96:99], v[56:59], v[0:3]\n"
  "s_add_u32 s52, s52, 18432\n"
  "ds_read_b128 v[32:35], v90 offset:28672\n"
  "v_or_b32 v213, s31, v212\n"
  "v_mfma_f32_16x16x32_bf16 v[0:3], a[112:115], v[60:63], v[0:3]\n"
  "s_cmp_eq_u32 s52, s54\n"
  "s_cselect_b32 s52, s53, s52\n"
  "v_cndmask_b32 v203, v121, v122, s[34:35]\n"
  "v_mfma_f32_16x16x32_bf16 v[0:3], a[128:131], v[64:67], v[0:3]\n"
  "ds_read_b128 v[36:39], v90 offset:29696\n"
  "v_mov_b32 v105, v203\n"
  "v_mfma_f32_16x16x32_bf16 v[0:3], a[144:147], v[68:71], v[0:3]\n"
  "v_exp_f32 v81, v81\n"
  "v_mfma_f32_16x16x32_bf16 v[0:3], a[160:163], v[72:75], v[0:3]\n"
  "v_mul_f32 v82, v18, v26\n"
  "v_mul_f32 v83, v19, v27\n"
  "v_mfma_f32_16x16x32_bf16 v[0:3], a[176:179], v[76:79], v[0:3]\n"
  "ds_read_b128 v[40:43], v90 offset:30720\n"
  "v_add_f32 v80, 1.0, v80\n"
  "v_mfma_f32_16x16x32_bf16 v[4:7], a[100:103], v[56:59], v[4:7]\n"
  "v_add_f32 v81, 1.0, v81\n"
  "v_mfma_f32_16x16x32_bf16 v[4:7], a[116:119], v[60:63], v[4:7]\n"
  "v_rcp_f32 v80, v80\n"
  "v_mfma_f32_16x16x32_bf16 v[4:7], a[132:135], v[64:67], v[4:7]\n"
  "ds_read_b128 v[44:47], v90 offset:31744\n"
  "v_mfma_f32_16x16x32_bf16 v[4:7], a[148:151], v[68:71], v[4:7]\n"
  "v_rcp_f32 v81, v81\n"
  "v_mfma_f32_16x16x32_bf16 v[4:7], a[164:167], v[72:75], v[4:7]\n"
  "v_mul_f32 v82, v82, v80\n"
  "v_mul_f32 v83, v83, v81\n"
  "v_mfma_f32_16x16x32_bf16 v[4:7], a[180:183], v[76:79], v[4:7]\n"
  "ds_read_b128 v[48:51], v91 offset:28672\n"
  "v_cvt_pk_bf16_f32 v85, v82, v83\n"
  "v_mfma_f32_16x16x32_bf16 v[8:11], a[104:107], v[56:59], v[8:11]\n"
  "v_mul_f32 v80, v20, v20\n"
  "v_mul_f32 v81, v21, v21\n"
  "v_mfma_f32_16x16x32_bf16 v[8:11], a[120:123], v[60:63], v[8:11]\n"
  "buffer_load_dword v214, v102, s[40:43], s60 offen offset:0\n"
  "v_fmaak_f32 v80, v88, v80, 0xc0135761\n"
  "v_mfma_f32_16x16x32_bf16 v[8:11], a[136:139], v[64:67], v[8:11]\n"
  "ds_read_b128 v[52:55], v91 offset:29696\n"
  "v_add_u32 v102, 8064, v102\n"
  "v_mfma_f32_16x16x32_bf16 v[8:11], a[152:155], v[68:71], v[8:11]\n"
  "v_fmaak_f32 v81, v88, v81, 0xc0135761\n"
  "v_mul_f32 v80, v20, v80\n"
  "v_mfma_f32_16x16x32_bf16 v[8:11], a[168:171], v[72:75], v[8:11]\n"
  "v_mul_f32 v81, v21, v81\n"
  "v_mfma_f32_16x16x32_bf16 v[8:11], a[184:187], v[76:79], v[8:11]\n"
  "v_exp_f32 v80, v80\n"
  "v_mfma_f32_16x16x32_bf16 v[12:15], a[108:111], v[56:59], v[12:15]\n"
  "v_exp_f32 v81, v81\n"
  "v_mfma_f32_16x16x32_bf16 v[12:15], a[124:127], v[60:63], v[12:15]\n"
  "v_mul_f32 v82, v20, v28\n"
  "v_mul_f32 v83, v21, v29\n"
  "v_mfma_f32_16x16x32_bf16 v[12:15], a[140:143], v[64:67], v[12:15]\n"
  "v_add_f32 v80, 1.0, v80\n"
  "v_add_f32 v81, 1.0, v81\n"
  "v_mfma_f32_16x16x32_bf16 v[12:15], a[156:159], v[68:71], v[12:15]\n"
  "v_rcp_f32 v80, v80\n"
  "v_mfma_f32_16x16x32_bf16 v[12:15], a[172:175], v[72:75], v[12:15]\n"
  "v_rcp_f32 v81, v81\n"
  "v_mfma_f32_16x16x32_bf16 v[12:15], a[188:191], v[76:79], v[12:15]\n"
  "s_waitcnt lgkmcnt(0)\n"
  "v_mul_f32 v82, v82, v80\n"
  "v_mul_f32 v83, v83, v81\n"
  "v_mfma_f32_16x16x32_bf16 v[0:3], a[192:195], v[32:35], v[0:3]\n"
  "ds_read_b128 v[56:59], v89 offset:0\n"
  "v_cvt_pk_bf16_f32 v86, v82, v83\n"
  "v_mfma_f32_16x16x32_bf16 v[0:3], a[208:211], v[36:39], v[0:3]\n"
  "v_mul_f32 v80, v22, v22\n"
  "v_mul_f32 v81, v23, v23\n"
  "v_mfma_f32_16x16x32_bf16 v[0:3], a[224:227], v[40:43], v[0:3]\n"
  "ds_read_b128 v[60:63], v89 offset:1024\n"
  "v_fmaak_f32 v80, v88, v80, 0xc0135761\n"
  "v_mfma_f32_16x16x32_bf16 v[0:3], a[240:243], v[44:47], v[0:3]\n"
  "v_fmaak_f32 v81, v88, v81, 0xc0135761\n"
  "v_mul_f32 v80, v22, v80\n"
  "v_mfma_f32_16x16x32_bf16 v[0:3], v[224:227], v[48:51], v[0:3]\n"
  "v_mul_f32 v81, v23, v81\n"
  "v_mfma_f32_16x16x32_bf16 v[0:3], v[240:243], v[52:55], v[0:3]\n"
  "ds_read_b128 v[64:67], v89 offset:2048\n"
  "v_mfma_f32_16x16x32_bf16 v[4:7], a[196:199], v[32:35], v[4:7]\n"
  "v_exp_f32 v80, v80\n"
  "v_mfma_f32_16x16x32_bf16 v[4:7], a[212:215], v[36:39], v[4:7]\n"
  "v_exp_f32 v81, v81\n"
  "v_mfma_f32_16x16x32_bf16 v[4:7], a[228:231], v[40:43], v[4:7]\n"
  "ds_read_b128 v[68:71], v89 offset:3072\n"
  "v_mul_f32 v82, v22, v30\n"
  "v_mfma_f32_16x16x32_bf16 v[4:7], a[244:247], v[44:47], v[4:7]\n"
  "v_mul_f32 v83, v23, v31\n"
  "v_add_f32 v80, 1.0, v80\n"
  "v_mfma_f32_16x16x32_bf16 v[4:7], v[228:231], v[48:51], v[4:7]\n"
  "v_add_f32 v81, 1.0, v81\n"
  "v_mfma_f32_16x16x32_bf16 v[4:7], v[244:247], v[52:55], v[4:7]\n"
  "ds_read_b128 v[72:75], v89 offset:4096\n"
  "v_mfma_f32_16x16x32_bf16 v[8:11], a[200:203], v[32:35], v[8:11]\n"
  "v_rcp_f32 v80, v80\n"
  "v_mfma_f32_16x16x32_bf16 v[8:11], a[216:219], v[36:39], v[8:11]\n"
  "v_rcp_f32 v81, v81\n"
  "v_mfma_f32_16x16x32_bf16 v[8:11], a[232:235], v[40:43], v[8:11]\n"
  "ds_read_b128 v[76:79], v89 offset:5120\n"
  "v_mul_f32 v82, v82, v80\n"
  "v_mfma_f32_16x16x32_bf16 v[8:11], a[248:251], v[44:47], v[8:11]\n"
  "v_mul_f32 v83, v83, v81\n"
  "v_cvt_pk_bf16_f32 v87, v82, v83\n"
  "v_mfma_f32_16x16x32_bf16 v[8:11], v[232:235], v[48:51], v[8:11]\n"
  "v_mfma_f32_16x16x32_bf16 v[8:11], v[248:251], v[52:55], v[8:11]\n"
  "v_mfma_f32_16x16x32_bf16 v[12:15], a[204:207], v[32:35], v[12:15]\n"
  "v_mfma_f32_16x16x32_bf16 v[12:15], a[220:223], v[36:39], v[12:15]\n"
  "v_mfma_f32_16x16x32_bf16 v[12:15], a[236:239], v[40:43], v[12:15]\n"
  "v_mfma_f32_16x16x32_bf16 v[12:15], a[252:255], v[44:47], v[12:15]\n"
  "buffer_store_dwordx4 v[84:87], v89, s[36:39], s55 offen sc1\n"
  "v_mfma_f32_16x16x32_bf16 v[12:15], v[236:239], v[48:51], v[12:15]\n"
  "s_add_u32 s55, s55, 147456\n"
  "v_mfma_f32_16x16x32_bf16 v[12:15], v[252:255], v[52:55], v[12:15]\n"
  "s_waitcnt lgkmcnt(0)\n"
  "v_mfma_f32_16x16x32_bf16 v[16:19], a[0:3], v[56:59], 0\n"
  "s_cmp_eq_u32 s55, s57\n"
  "s_cselect_b32 s55, s56, s55\n"
  "ds_read_b128 v[32:35], v89 offset:6144\n"
  "v_mfma_f32_16x16x32_bf16 v[16:19], a[16:19], v[60:63], v[16:19]\n"
  "v_mul_f32 v80, v0, v0\n"
  "v_mul_f32 v81, v1, v1\n"
  "v_mfma_f32_16x16x32_bf16 v[16:19], a[32:35], v[64:67], v[16:19]\n"
  "ds_read_b128 v[36:39], v89 offset:7168\n"
  "v_fmaak_f32 v80, v88, v80, 0xc0135761\n"
  "v_mfma_f32_16x16x32_bf16 v[16:19], a[48:51], v[68:71], v[16:19]\n"
  "v_fmaak_f32 v81, v88, v81, 0xc0135761\n"
  "v_mul_f32 v80, v0, v80\n"
  "v_mfma_f32_16x16x32_bf16 v[16:19], a[64:67], v[72:75], v[16:19]\n"
  "v_mul_f32 v81, v1, v81\n"
  "v_mfma_f32_16x16x32_bf16 v[16:19], a[80:83], v[76:79], v[16:19]\n"
  "ds_read_b128 v[40:43], v89 offset:8192\n"
  "v_mfma_f32_16x16x32_bf16 v[20:23], a[4:7], v[56:59], 0\n"
  "v_exp_f32 v80, v80\n"
  "v_mfma_f32_16x16x32_bf16 v[20:23], a[20:23], v[60:63], v[20:23]\n"
  "v_exp_f32 v81, v81\n"
  "v_mfma_f32_16x16x32_bf16 v[20:23], a[36:39], v[64:67], v[20:23]\n"
  "ds_read_b128 v[44:47], v89 offset:9216\n"
  "v_mul_f32 v82, v0, v8\n"
  "v_mfma_f32_16x16x32_bf16 v[20:23], a[52:55], v[68:71], v[20:23]\n"
  "v_mul_f32 v83, v1, v9\n"
  "v_add_f32 v80, 1.0, v80\n"
  "v_mfma_f32_16x16x32_bf16 v[20:23], a[68:71], v[72:75], v[20:23]\n"
  "v_add_f32 v81, 1.0, v81\n"
  "v_mfma_f32_16x16x32_bf16 v[20:23], a[84:87], v[76:79], v[20:23]\n"
  "ds_read_b128 v[48:51], v89 offset:10240\n"
  "v_mfma_f32_16x16x32_bf16 v[24:27], a[8:11], v[56:59], 0\n"
  "v_rcp_f32 v80, v80\n"
  "v_mfma_f32_16x16x32_bf16 v[24:27], a[24:27], v[60:63], v[24:27]\n"
  "v_rcp_f32 v81, v81\n"
  "v_mfma_f32_16x16x32_bf16 v[24:27], a[40:43], v[64:67], v[24:27]\n"
  "ds_read_b128 v[52:55], v89 offset:11264\n"
  "v_mul_f32 v82, v82, v80\n"
  "v_mfma_f32_16x16x32_bf16 v[24:27], a[56:59], v[68:71], v[24:27]\n"
  "v_mul_f32 v83, v83, v81\n"
  "v_cvt_pk_bf16_f32 v84, v82, v83\n"
  "v_mfma_f32_16x16x32_bf16 v[24:27], a[72:75], v[72:75], v[24:27]\n"
  "v_mul_f32 v80, v2, v2\n"
  "v_mul_f32 v81, v3, v3\n"
  "v_mfma_f32_16x16x32_bf16 v[24:27], a[88:91], v[76:79], v[24:27]\n"
  "v_fmaak_f32 v80, v88, v80, 0xc0135761\n"
  "v_fmaak_f32 v81, v88, v81, 0xc0135761\n"
  "v_mfma_f32_16x16x32_bf16 v[28:31], a[12:15], v[56:59], 0\n"
  "v_mul_f32 v80, v2, v80\n"
  "v_mul_f32 v81, v3, v81\n"
  "v_mfma_f32_16x16x32_bf16 v[28:31], a[28:31], v[60:63], v[28:31]\n"
  "v_exp_f32 v80, v80\n"
  "v_mfma_f32_16x16x32_bf16 v[28:31], a[44:47], v[64:67], v[28:31]\n"
  "v_exp_f32 v81, v81\n"
  "v_mfma_f32_16x16x32_bf16 v[28:31], a[60:63], v[68:71], v[28:31]\n"
  "v_mul_f32 v82, v2, v10\n"
  "v_mul_f32 v83, v3, v11\n"
  "v_mfma_f32_16x16x32_bf16 v[28:31], a[76:79], v[72:75], v[28:31]\n"
  "v_add_f32 v80, 1.0, v80\n"
  "v_add_f32 v81, 1.0, v81\n"
  "v_mfma_f32_16x16x32_bf16 v[28:31], a[92:95], v[76:79], v[28:31]\n"
  "s_waitcnt lgkmcnt(0)\n"
  "v_rcp_f32 v80, v80\n"
  "v_mfma_f32_16x16x32_bf16 v[16:19], a[96:99], v[32:35], v[16:19]\n"
  "ds_read_b128 v[56:59], v89 offset:12288\n"
  "v_mfma_f32_16x16x32_bf16 v[16:19], a[112:115], v[36:39], v[16:19]\n"
  "v_rcp_f32 v81, v81\n"
  "v_mfma_f32_16x16x32_bf16 v[16:19], a[128:131], v[40:43], v[16:19]\n"
  "ds_read_b128 v[60:63], v89 offset:13312\n"
  "v_mul_f32 v82, v82, v80\n"
  "v_mfma_f32_16x16x32_bf16 v[16:19], a[144:147], v[44:47], v[16:19]\n"
  "v_mul_f32 v83, v83, v81\n"
  "v_cvt_pk_bf16_f32 v85, v82, v83\n"
  "v_mfma_f32_16x16x32_bf16 v[16:19], a[160:163], v[48:51], v[16:19]\n"
  "v_mul_f32 v80, v4, v4\n"
  "v_mul_f32 v81, v5, v5\n"
  "v_mfma_f32_16x16x32_bf16 v[16:19], a[176:179], v[52:55], v[16:19]\n"
  "ds_read_b128 v[64:67], v89 offset:14336\n"
  "v_fmaak_f32 v80, v88, v80, 0xc0135761\n"
  "v_mfma_f32_16x16x32_bf16 v[20:23], a[100:103], v[32:35], v[20:23]\n"
  "v_fmaak_f32 v81, v88, v81, 0xc0135761\n"
  "v_mul_f32 v80, v4, v80\n"
  "v_mfma_f32_16x16x32_bf16 v[20:23], a[116:119], v[36:39], v[20:23]\n"
  "v_mul_f32 v81, v5, v81\n"
  "v_mfma_f32_16x16x32_bf16 v[20:23], a[132:135], v[40:43], v[20:23]\n"
  "ds_read_b128 v[68:71], v89 offset:15360\n"
  "v_mfma_f32_16x16x32_bf16 v[20:23], a[148:151], v[44:47], v[20:23]\n"
  "v_exp_f32 v80, v80\n"
  "v_mfma_f32_16x16x32_bf16 v[20:23], a[164:167], v[48:51], v[20:23]\n"
  "buffer_store_dwordx4 v[108:111], v107, s[36:39], s61 offen offset:0 sc1\n"
  "v_mfma_f32_16x16x32_bf16 v[20:23], a[180:183], v[52:55], v[20:23]\n"
  "ds_read_b128 v[72:75], v91 offset:26624\n"
  "v_mfma_f32_16x16x32_bf16 v[24:27], a[104:107], v[32:35], v[24:27]\n"
  "v_exp_f32 v81, v81\n"
  "v_mfma_f32_16x16x32_bf16 v[24:27], a[120:123], v[36:39], v[24:27]\n"
  "v_mul_f32 v82, v4, v12\n"
  "v_mul_f32 v83, v5, v13\n"
  "v_mfma_f32_16x16x32_bf16 v[24:27], a[136:139], v[40:43], v[24:27]\n"
  "ds_read_b128 v[76:79], v91 offset:27648\n"
  "v_add_f32 v80, 1.0, v80\n"
  "v_mfma_f32_16x16x32_bf16 v[24:27], a[152:155], v[44:47], v[24:27]\n"
  "v_add_f32 v81, 1.0, v81\n"
  "v_mfma_f32_16x16x32_bf16 v[24:27], a[168:171], v[48:51], v[24:27]\n"
  "v_rcp_f32 v80, v80\n"
  "v_mfma_f32_16x16x32_bf16 v[24:27], a[184:187], v[52:55], v[24:27]\n"
  "v_rcp_f32 v81, v81\n"
  "v_mfma_f32_16x16x32_bf16 v[28:31], a[108:111], v[32:35], v[28:31]\n"
  "v_mul_f32 v82, v82, v80\n"
  "v_mul_f32 v83, v83, v81\n"
  "v_mfma_f32_16x16x32_bf16 v[28:31], a[124:127], v[36:39], v[28:31]\n"
  "v_cvt_pk_bf16_f32 v86, v82, v83\n"
  "v_mul_f32 v80, v6, v6\n"
  "v_mfma_f32_16x16x32_bf16 v[28:31], a[140:143], v[40:43], v[28:31]\n"
  "v_mul_f32 v81, v7, v7\n"
  "v_fmaak_f32 v80, v88, v80, 0xc0135761\n"
  "v_mfma_f32_16x16x32_bf16 v[28:31], a[156:159], v[44:47], v[28:31]\n"
  "v_fmaak_f32 v81, v88, v81, 0xc0135761\n"
  "v_mul_f32 v80, v6, v80\n"
  "v_mfma_f32_16x16x32_bf16 v[28:31], a[172:175], v[48:51], v[28:31]\n"
  "v_mul_f32 v81, v7, v81\n"
  "v_mfma_f32_16x16x32_bf16 v[28:31], a[188:191], v[52:55], v[28:31]\n"
  "s_waitcnt vmcnt(25) lgkmcnt(0)\n"
  "s_barrier\n"
  "v_mfma_f32_16x16x32_bf16 v[16:19], a[192:195], v[56:59], v[16:19]\n"
  "s_add_u32 m0, s48, 65536\n"
  "ds_read_b128 v[32:35], v90 offset:32768\n"
  "v_mfma_f32_16x16x32_bf16 v[16:19], a[208:211], v[60:63], v[16:19]\n"
  "buffer_load_dwordx4 v92, s[36:39], s52 offen offset:0 sc1 lds\n"
  "v_mfma_f32_16x16x32_bf16 v[16:19], a[224:227], v[64:67], v[16:19]\n"
  "ds_read_b128 v[36:39], v90 offset:33792\n"
  "v_mfma_f32_16x16x32_bf16 v[16:19], a[240:243], v[68:71], v[16:19]\n"
  "v_exp_f32 v80, v80\n"
  "v_mfma_f32_16x16x32_bf16 v[16:19], v[224:227], v[72:75], v[16:19]\n"
  "v_exp_f32 v81, v81\n"
  "v_mfma_f32_16x16x32_bf16 v[16:19], v[240:243], v[76:79], v[16:19]\n"
  "ds_read_b128 v[40:43], v90 offset:34816\n"
  "v_mul_f32 v82, v6, v14\n"
  "v_mfma_f32_16x16x32_bf16 v[20:23], a[196:199], v[56:59], v[20:23]\n"
  "buffer_load_dwordx4 v92, s[36:39], s52 offen offset:1024 sc1 lds\n"
  "v_mul_f32 v83, v7, v15\n"
  "v_mfma_f32_16x16x32_bf16 v[20:23], a[212:215], v[60:63], v[20:23]\n"
  "v_add_f32 v80, 1.0, v80\n"
  "v_add_f32 v81, 1.0, v81\n"
  "v_mfma_f32_16x16x32_bf16 v[20:23], a[228:231], v[64:67], v[20:23]\n"
  "ds_read_b128 v[44:47], v90 offset:35840\n"
  "v_mfma_f32_16x16x32_bf16 v[20:23], a[244:247], v[68:71], v[20:23]\n"
  "v_rcp_f32 v80, v80\n"
  "v_mfma_f32_16x16x32_bf16 v[20:23], v[228:231], v[72:75], v[20:23]\n"
  "v_rcp_f32 v81, v81\n"
  "v_mfma_f32_16x16x32_bf16 v[20:23], v[244:247], v[76:79], v[20:23]\n"
  "buffer_load_dwordx4 v92, s[36:39], s52 offen offset:2048 sc1 lds\n"
  "ds_read_b128 v[48:51], v90 offset:36864\n"
  "v_mfma_f32_16x16x32_bf16 v[24:27], a[200:203], v[56:59], v[24:27]\n"
  "v_mul_f32 v82, v82, v80\n"
  "v_mul_f32 v83, v83, v81\n"
  "v_mfma_f32_16x16x32_bf16 v[24:27], a[216:219], v[60:63], v[24:27]\n"
  "v_cvt_pk_bf16_f32 v87, v82, v83\n"
  "v_mfma_f32_16x16x32_bf16 v[24:27], a[232:235], v[64:67], v[24:27]\n"
  "ds_read_b128 v[52:55], v90 offset:37888\n"
  "v_mfma_f32_16x16x32_bf16 v[24:27], a[248:251], v[68:71], v[24:27]\n"
  "v_mfma_f32_16x16x32_bf16 v[24:27], v[232:235], v[72:75], v[24:27]\n"
  "buffer_load_dwordx4 v92, s[36:39], s52 offen offset:3072 sc1 lds\n"
  "v_mfma_f32_16x16x32_bf16 v[24:27], v[248:251], v[76:79], v[24:27]\n"
  "v_mfma_f32_16x16x32_bf16 v[28:31], a[204:207], v[56:59], v[28:31]\n"
  "v_mfma_f32_16x16x32_bf16 v[28:31], a[220:223], v[60:63], v[28:31]\n"
  "v_mfma_f32_16x16x32_bf16 v[28:31], a[236:239], v[64:67], v[28:31]\n"
  "s_add_u32 m0, s49, 143360\n"
  "v_mfma_f32_16x16x32_bf16 v[28:31], a[252:255], v[68:71], v[28:31]\n"
  "buffer_load_dwordx4 v93, s[36:39], s52 offen sc1 lds\n"
  "v_mfma_f32_16x16x32_bf16 v[28:31], v[236:239], v[72:75], v[28:31]\n"
  "s_add_u32 s52, s52, 18432\n"
  "buffer_store_dwordx4 v[84:87], v89, s[36:39], s55 offen sc1\n"
  "v_mfma_f32_16x16x32_bf16 v[28:31], v[252:255], v[76:79], v[28:31]\n"
  "s_waitcnt lgkmcnt(0)\n"
  "v_mfma_f32_16x16x32_bf16 v[0:3], a[0:3], v[32:35], 0\n"
  "s_cmp_eq_u32 s52, s54\n"
  "s_cselect_b32 s52, s53, s52\n"
  "ds_read_b128 v[56:59], v90 offset:38912\n"
  "v_mfma_f32_16x16x32_bf16 v[0:3], a[16:19], v[36:39], v[0:3]\n"
  "s_add_u32 s55, s55, 147456\n"
  "v_mul_f32 v80, v16, v16\n"
  "v_mul_f32 v81, v17, v17\n"
  "v_mfma_f32_16x16x32_bf16 v[0:3], a[32:35], v[40:43], v[0:3]\n"
  "s_cmp_eq_u32 s55, s57\n"
  "s_cselect_b32 s55, s56, s55\n"
  "ds_read_b128 v[60:63], v90 offset:39936\n"
  "v_mfma_f32_16x16x32_bf16 v[0:3], a[48:51], v[44:47], v[0:3]\n"
  "v_fmaak_f32 v80, v88, v80, 0xc0135761\n"
  "v_fmaak_f32 v81, v88, v81, 0xc0135761\n"
  "v_mfma_f32_16x16x32_bf16 v[0:3], a[64:67], v[48:51], v[0:3]\n"
  "v_mul_f32 v80, v16, v80\n"
  "v_mul_f32 v81, v17, v81\n"
  "v_mfma_f32_16x16x32_bf16 v[0:3], a[80:83], v[52:55], v[0:3]\n"
  "ds_read_b128 v[64:67], v90 offset:40960\n"
  "v_mfma_f32_16x16x32_bf16 v[4:7], a[4:7], v[32:35], 0\n"
  "v_exp_f32 v80, v80\n"
  "v_mfma_f32_16x16x32_bf16 v[4:7], a[20:23], v[36:39], v[4:7]\n"
  "v_exp_f32 v81, v81\n"
  "v_mfma_f32_16x16x32_bf16 v[4:7], a[36:39], v[40:43], v[4:7]\n"
  "ds_read_b128 v[68:71], v90 offset:41984\n"
  "v_mul_f32 v82, v16, v24\n"
  "v_mfma_f32_16x16x32_bf16 v[4:7], a[52:55], v[44:47], v[4:7]\n"
  "v_mul_f32 v83, v17, v25\n"
  "v_add_f32 v80, 1.0, v80\n"
  "v_mfma_f32_16x16x32_bf16 v[4:7], a[68:71], v[48:51], v[4:7]\n"
  "v_add_f32 v81, 1.0, v81\n"
  "v_mfma_f32_16x16x32_bf16 v[4:7], a[84:87], v[52:55], v[4:7]\n"
  "ds_read_b128 v[72:75], v90 offset:43008\n"
  "v_mfma_f32_16x16x32_bf16 v[8:11], a[8:11], v[32:35], 0\n"
  "v_rcp_f32 v80, v80\n"
  "v_mfma_f32_16x16x32_bf16 v[8:11], a[24:27], v[36:39], v[8:11]\n"
  "v_rcp_f32 v81, v81\n"
  "v_mfma_f32_16x16x32_bf16 v[8:11], a[40:43], v[40:43], v[8:11]\n"
  "ds_read_b128 v[76:79], v90 offset:44032\n"
  "v_mul_f32 v82, v82, v80\n"
  "v_mfma_f32_16x16x32_bf16 v[8:11], a[56:59], v[44:47], v[8:11]\n"
  "v_mul_f32 v83, v83, v81\n"
  "v_cvt_pk_bf16_f32 v84, v82, v83\n"
  "v_mfma_f32_16x16x32_bf16 v[8:11], a[72:75], v[48:51], v[8:11]\n"
  "v_mul_f32 v80, v18, v18\n"
  "v_mul_f32 v81, v19, v19\n"
  "v_mfma_f32_16x16x32_bf16 v[8:11], a[88:91], v[52:55], v[8:11]\n"
  "v_fmaak_f32 v80, v88, v80, 0xc0135761\n"
  "v_fmaak_f32 v81, v88, v81, 0xc0135761\n"
  "v_mfma_f32_16x16x32_bf16 v[12:15], a[12:15], v[32:35], 0\n"
  "v_mul_f32 v80, v18, v80\n"
  "v_mul_f32 v81, v19, v81\n"
  "v_mfma_f32_16x16x32_bf16 v[12:15], a[28:31], v[36:39], v[12:15]\n"
  "v_exp_f32 v80, v80\n"
  "v_mfma_f32_16x16x32_bf16 v[12:15], a[44:47], v[40:43], v[12:15]\n"
  "v_exp_f32 v81, v81\n"
  "v_mfma_f32_16x16x32_bf16 v[12:15], a[60:63], v[44:47], v[12:15]\n"
  "v_mul_f32 v82, v18, v26\n"
  "v_mul_f32 v83, v19, v27\n"
  "v_mfma_f32_16x16x32_bf16 v[12:15], a[76:79], v[48:51], v[12:15]\n"
  "v_add_f32 v80, 1.0, v80\n"
  "v_add_f32 v81, 1.0, v81\n"
  "v_mfma_f32_16x16x32_bf16 v[12:15], a[92:95], v[52:55], v[12:15]\n"
  "s_waitcnt lgkmcnt(0)\n"
  "v_rcp_f32 v80, v80\n"
  "v_mfma_f32_16x16x32_bf16 v[0:3], a[96:99], v[56:59], v[0:3]\n"
  "ds_read_b128 v[32:35], v90 offset:45056\n"
  "v_mfma_f32_16x16x32_bf16 v[0:3], a[112:115], v[60:63], v[0:3]\n"
  "v_rcp_f32 v81, v81\n"
  "v_mfma_f32_16x16x32_bf16 v[0:3], a[128:131], v[64:67], v[0:3]\n"
  "ds_read_b128 v[36:39], v90 offset:46080\n"
  "v_mul_f32 v82, v82, v80\n"
  "v_mfma_f32_16x16x32_bf16 v[0:3], a[144:147], v[68:71], v[0:3]\n"
  "v_mul_f32 v83, v83, v81\n"
  "v_cvt_pk_bf16_f32 v85, v82, v83\n"
  "v_mfma_f32_16x16x32_bf16 v[0:3], a[160:163], v[72:75], v[0:3]\n"
  "v_mul_f32 v80, v20, v20\n"
  "v_mul_f32 v81, v21, v21\n"
  "v_mfma_f32_16x16x32_bf16 v[0:3], a[176:179], v[76:79], v[0:3]\n"
  "ds_read_b128 v[40:43], v90 offset:47104\n"
  "v_fmaak_f32 v80, v88, v80, 0xc0135761\n"
  "v_mfma_f32_16x16x32_bf16 v[4:7], a[100:103], v[56:59], v[4:7]\n"
  "v_fmaak_f32 v81, v88, v81, 0xc0135761\n"
  "v_mul_f32 v80, v20, v80\n"
  "v_mfma_f32_16x16x32_bf16 v[4:7], a[116:119], v[60:63], v[4:7]\n"
  "v_mul_f32 v81, v21, v81\n"
  "v_mfma_f32_16x16x32_bf16 v[4:7], a[132:135], v[64:67], v[4:7]\n"
  "ds_read_b128 v[44:47], v90 offset:48128\n"
  "v_mfma_f32_16x16x32_bf16 v[4:7], a[148:151], v[68:71], v[4:7]\n"
  "v_exp_f32 v80, v80\n"
  "v_mfma_f32_16x16x32_bf16 v[4:7], a[164:167], v[72:75], v[4:7]\n"
  "buffer_store_dwordx4 v[108:111], v107, s[36:39], s61 offen offset:1024 sc1\n"
  "v_mfma_f32_16x16x32_bf16 v[4:7], a[180:183], v[76:79], v[4:7]\n"
  "ds_read_b128 v[48:51], v91 offset:24576\n"
  "v_mfma_f32_16x16x32_bf16 v[8:11], a[104:107], v[56:59], v[8:11]\n"
  "v_exp_f32 v81, v81\n"
  "v_mfma_f32_16x16x32_bf16 v[8:11], a[120:123], v[60:63], v[8:11]\n"
  "buffer_load_dword v215, v102, s[40:43], s60 offen offset:0\n"
  "v_mul_f32 v82, v20, v28\n"
  "v_mfma_f32_16x16x32_bf16 v[8:11], a[136:139], v[64:67], v[8:11]\n"
  "ds_read_b128 v[52:55], v91 offset:25600\n"
  "v_add_u32 v102, 8064, v102\n"
  "v_mfma_f32_16x16x32_bf16 v[8:11], a[152:155], v[68:71], v[8:11]\n"
  "v_mul_f32 v83, v21, v29\n"
  "v_add_f32 v80, 1.0, v80\n"
  "v_mfma_f32_16x16x32_bf16 v[8:11], a[168:171], v[72:75], v[8:11]\n"
  "v_add_f32 v81, 1.0, v81\n"
  "v_mfma_f32_16x16x32_bf16 v[8:11], a[184:187], v[76:79], v[8:11]\n"
  "v_rcp_f32 v80, v80\n"
  "v_mfma_f32_16x16x32_bf16 v[12:15], a[108:111], v[56:59], v[12:15]\n"
  "v_rcp_f32 v81, v81\n"
  "v_mfma_f32_16x16x32_bf16 v[12:15], a[124:127], v[60:63], v[12:15]\n"
  "v_mul_f32 v82, v82, v80\n"
  "v_mul_f32 v83, v83, v81\n"
  "v_mfma_f32_16x16x32_bf16 v[12:15], a[140:143], v[64:67], v[12:15]\n"
  "v_cvt_pk_bf16_f32 v86, v82, v83\n"
  "v_mul_f32 v80, v22, v22\n"
  "v_mfma_f32_16x16x32_bf16 v[12:15], a[156:159], v[68:71], v[12:15]\n"
  "v_mul_f32 v81, v23, v23\n"
  "v_fmaak_f32 v80, v88, v80, 0xc0135761\n"
  "v_mfma_f32_16x16x32_bf16 v[12:15], a[172:175], v[72:75], v[12:15]\n"
  "s_nop 0\n"
  "s_cbranch_execz Ltramp%=\n"
  "v_fmaak_f32 v81, v88, v81, 0xc0135761\n"
  "v_mul_f32 v80, v22, v80\n"
  "v_mfma_f32_16x16x32_bf16 v[12:15], a[188:191], v[76:79], v[12:15]\n"
  "s_waitcnt lgkmcnt(0)\n"
  "v_mul_f32 v81, v23, v81\n"
  "v_mfma_f32_16x16x32_bf16 v[0:3], a[192:195], v[32:35], v[0:3]\n"
  "ds_read_b128 v[56:59], v90 offset:49152\n"
  "v_mfma_f32_16x16x32_bf16 v[0:3], a[208:211], v[36:39], v[0:3]\n"
  "v_exp_f32 v80, v80\n"
  "v_mfma_f32_16x16x32_bf16 v[0:3], a[224:227], v[40:43], v[0:3]\n"
  "ds_read_b128 v[60:63], v90 offset:50176\n"
  "v_mfma_f32_16x16x32_bf16 v[0:3], a[240:243], v[44:47], v[0:3]\n"
  "v_exp_f32 v81, v81\n"
  "v_mfma_f32_16x16x32_bf16 v[0:3], v[224:227], v[48:51], v[0:3]\n"
  "v_mul_f32 v82, v22, v30\n"
  "v_mul_f32 v83, v23, v31\n"
  "v_mfma_f32_16x16x32_bf16 v[0:3], v[240:243], v[52:55], v[0:3]\n"
  "ds_read_b128 v[64:67], v90 offset:51200\n"
  "v_add_f32 v80, 1.0, v80\n"
  "v_mfma_f32_16x16x32_bf16 v[4:7], a[196:199], v[32:35], v[4:7]\n"
  "v_add_f32 v81, 1.0, v81\n"
  "v_mfma_f32_16x16x32_bf16 v[4:7], a[212:215], v[36:39], v[4:7]\n"
  "v_rcp_f32 v80, v80\n"
  "v_mfma_f32_16x16x32_bf16 v[4:7], a[228:231], v[40:43], v[4:7]\n"
  "ds_read_b128 v[68:71], v90 offset:52224\n"
  "v_mfma_f32_16x16x32_bf16 v[4:7], a[244:247], v[44:47], v[4:7]\n"
  "v_rcp_f32 v81, v81\n"
  "v_mfma_f32_16x16x32_bf16 v[4:7], v[228:231], v[48:51], v[4:7]\n"
  "v_mul_f32 v82, v82, v80\n"
  "v_mul_f32 v83, v83, v81\n"
  "v_mfma_f32_16x16x32_bf16 v[4:7], v[244:247], v[52:55], v[4:7]\n"
  "ds_read_b128 v[72:75], v90 offset:53248\n"
  "v_cvt_pk_bf16_f32 v87, v82, v83\n"
  "v_mfma_f32_16x16x32_bf16 v[8:11], a[200:203], v[32:35], v[8:11]\n"
  "v_mfma_f32_16x16x32_bf16 v[8:11], a[216:219], v[36:39], v[8:11]\n"
  "v_mfma_f32_16x16x32_bf16 v[8:11], a[232:235], v[40:43], v[8:11]\n"
  "ds_read_b128 v[76:79], v90 offset:54272\n"
  "v_mfma_f32_16x16x32_bf16 v[8:11], a[248:251], v[44:47], v[8:11]\n"
  "v_mfma_f32_16x16x32_bf16 v[8:11], v[232:235], v[48:51], v[8:11]\n"
  "v_mfma_f32_16x16x32_bf16 v[8:11], v[248:251], v[52:55], v[8:11]\n"
  "v_mfma_f32_16x16x32_bf16 v[12:15], a[204:207], v[32:35], v[12:15]\n"
  "v_mfma_f32_16x16x32_bf16 v[12:15], a[220:223], v[36:39], v[12:15]\n"
  "v_mfma_f32_16x16x32_bf16 v[12:15], a[236:239], v[40:43], v[12:15]\n"
  "v_mfma_f32_16x16x32_bf16 v[12:15], a[252:255], v[44:47], v[12:15]\n"
  "buffer_store_dwordx4 v[84:87], v89, s[36:39], s55 offen sc1\n"
  "v_mfma_f32_16x16x32_bf16 v[12:15], v[236:239], v[48:51], v[12:15]\n"
  "s_add_u32 s55, s55, 147456\n"
  "v_mfma_f32_16x16x32_bf16 v[12:15], v[252:255], v[52:55], v[12:15]\n"
  "s_waitcnt lgkmcnt(0)\n"
  "v_mfma_f32_16x16x32_bf16 v[16:19], a[0:3], v[56:59], 0\n"
  "s_cmp_eq_u32 s55, s57\n"
  "s_cselect_b32 s55, s56, s55\n"
  "ds_read_b128 v[32:35], v90 offset:55296\n"
  "v_mfma_f32_16x16x32_bf16 v[16:19], a[16:19], v[60:63], v[16:19]\n"
  "v_mul_f32 v80, v0, v0\n"
  "v_mul_f32 v81, v1, v1\n"
  "v_mfma_f32_16x16x32_bf16 v[16:19], a[32:35], v[64:67], v[16:19]\n"
  "ds_read_b128 v[36:39], v90 offset:56320\n"
  "v_fmaak_f32 v80, v88, v80, 0xc0135761\n"
  "v_mfma_f32_16x16x32_bf16 v[16:19], a[48:51], v[68:71], v[16:19]\n"
  "s_waitcnt vmcnt(3)\n"
  "s_mov_b64 exec, 1\n"
  "buffer_atomic_add v104, off, s[24:27], s12 sc1\n"
  "s_mov_b64 exec, -1\n"
  "v_mfma_f32_16x16x32_bf16 v[16:19], a[64:67], v[72:75], v[16:19]\n"
  "v_fmaak_f32 v81, v88, v81, 0xc0135761\n"
  "v_mul_f32 v80, v0, v80\n"
  "v_mfma_f32_16x16x32_bf16 v[16:19], a[80:83], v[76:79], v[16:19]\n"
  "ds_read_b128 v[40:43], v90 offset:57344\n"
  "v_mul_f32 v81, v1, v81\n"
  "v_mfma_f32_16x16x32_bf16 v[20:23], a[4:7], v[56:59], 0\n"
  "v_exp_f32 v80, v80\n"
  "v_mfma_f32_16x16x32_bf16 v[20:23], a[20:23], v[60:63], v[20:23]\n"
  "v_exp_f32 v81, v81\n"
  "v_mfma_f32_16x16x32_bf16 v[20:23], a[36:39], v[64:67], v[20:23]\n"
  "ds_read_b128 v[44:47], v90 offset:58368\n"
  "v_mul_f32 v82, v0, v8\n"
  "v_mfma_f32_16x16x32_bf16 v[20:23], a[52:55], v[68:71], v[20:23]\n"
  "v_mul_f32 v83, v1, v9\n"
  "v_add_f32 v80, 1.0, v80\n"
  "v_mfma_f32_16x16x32_bf16 v[20:23], a[68:71], v[72:75], v[20:23]\n"
  "v_add_f32 v81, 1.0, v81\n"
  "v_mfma_f32_16x16x32_bf16 v[20:23], a[84:87], v[76:79], v[20:23]\n"
  "ds_read_b128 v[48:51], v90 offset:59392\n"
  "v_mfma_f32_16x16x32_bf16 v[24:27], a[8:11], v[56:59], 0\n"
  "v_rcp_f32 v80, v80\n"
  "v_mfma_f32_16x16x32_bf16 v[24:27], a[24:27], v[60:63], v[24:27]\n"
  "v_rcp_f32 v81, v81\n"
  "v_mfma_f32_16x16x32_bf16 v[24:27], a[40:43], v[64:67], v[24:27]\n"
  "ds_read_b128 v[52:55], v90 offset:60416\n"
  "v_mul_f32 v82, v82, v80\n"
  "v_mfma_f32_16x16x32_bf16 v[24:27], a[56:59], v[68:71], v[24:27]\n"
  "v_mul_f32 v83, v83, v81\n"
  "v_cvt_pk_bf16_f32 v84, v82, v83\n"
  "v_mfma_f32_16x16x32_bf16 v[24:27], a[72:75], v[72:75], v[24:27]\n"
  "v_mul_f32 v80, v2, v2\n"
  "v_mul_f32 v81, v3, v3\n"
  "v_mfma_f32_16x16x32_bf16 v[24:27], a[88:91], v[76:79], v[24:27]\n"
  "v_fmaak_f32 v80, v88, v80, 0xc0135761\n"
  "v_fmaak_f32 v81, v88, v81, 0xc0135761\n"
  "v_mfma_f32_16x16x32_bf16 v[28:31], a[12:15], v[56:59], 0\n"
  "v_mul_f32 v80, v2, v80\n"
  "v_mul_f32 v81, v3, v81\n"
  "v_mfma_f32_16x16x32_bf16 v[28:31], a[28:31], v[60:63], v[28:31]\n"
  "v_exp_f32 v80, v80\n"
  "v_mfma_f32_16x16x32_bf16 v[28:31], a[44:47], v[64:67], v[28:31]\n"
  "v_exp_f32 v81, v81\n"
  "v_mfma_f32_16x16x32_bf16 v[28:31], a[60:63], v[68:71], v[28:31]\n"
  "v_mul_f32 v82, v2, v10\n"
  "v_mul_f32 v83, v3, v11\n"
  "v_mfma_f32_16x16x32_bf16 v[28:31], a[76:79], v[72:75], v[28:31]\n"
  "v_add_f32 v80, 1.0, v80\n"
  "v_add_f32 v81, 1.0, v81\n"
  "v_mfma_f32_16x16x32_bf16 v[28:31], a[92:95], v[76:79], v[28:31]\n"
  "s_waitcnt lgkmcnt(0)\n"
  "v_rcp_f32 v80, v80\n"
  "v_mfma_f32_16x16x32_bf16 v[16:19], a[96:99], v[32:35], v[16:19]\n"
  "ds_read_b128 v[56:59], v90 offset:61440\n"
  "v_mfma_f32_16x16x32_bf16 v[16:19], a[112:115], v[36:39], v[16:19]\n"
  "v_rcp_f32 v81, v81\n"
  "v_mfma_f32_16x16x32_bf16 v[16:19], a[128:131], v[40:43], v[16:19]\n"
  "ds_read_b128 v[60:63], v90 offset:62464\n"
  "v_mul_f32 v82, v82, v80\n"
  "v_mfma_f32_16x16x32_bf16 v[16:19], a[144:147], v[44:47], v[16:19]\n"
  "v_mul_f32 v83, v83, v81\n"
  "v_cvt_pk_bf16_f32 v85, v82, v83\n"
  "v_mfma_f32_16x16x32_bf16 v[16:19], a[160:163], v[48:51], v[16:19]\n"
  "v_mul_f32 v80, v4, v4\n"
  "v_mul_f32 v81, v5, v5\n"
  "v_mfma_f32_16x16x32_bf16 v[16:19], a[176:179], v[52:55], v[16:19]\n"
  "ds_read_b128 v[64:67], v90 offset:63488\n"
  "v_fmaak_f32 v80, v88, v80, 0xc0135761\n"
  "v_mfma_f32_16x16x32_bf16 v[20:23], a[100:103], v[32:35], v[20:23]\n"
  "v_fmaak_f32 v81, v88, v81, 0xc0135761\n"
  "v_mul_f32 v80, v4, v80\n"
  "v_mfma_f32_16x16x32_bf16 v[20:23], a[116:119], v[36:39], v[20:23]\n"
  "v_mul_f32 v81, v5, v81\n"
  "v_mfma_f32_16x16x32_bf16 v[20:23], a[132:135], v[40:43], v[20:23]\n"
  "ds_read_b128 v[68:71], v90 offset:64512\n"
  "v_mfma_f32_16x16x32_bf16 v[20:23], a[148:151], v[44:47], v[20:23]\n"
  "v_exp_f32 v80, v80\n"
  "v_mfma_f32_16x16x32_bf16 v[20:23], a[164:167], v[48:51], v[20:23]\n"
  "buffer_store_dwordx4 v[108:111], v107, s[36:39], s61 offen offset:2048 sc1\n"
  "v_mfma_f32_16x16x32_bf16 v[20:23], a[180:183], v[52:55], v[20:23]\n"
  "ds_read_b128 v[72:75], v91 offset:22528\n"
  "v_mfma_f32_16x16x32_bf16 v[24:27], a[104:107], v[32:35], v[24:27]\n"
  "v_exp_f32 v81, v81\n"
  "v_mfma_f32_16x16x32_bf16 v[24:27], a[120:123], v[36:39], v[24:27]\n"
  "v_mul_f32 v82, v4, v12\n"
  "v_mul_f32 v83, v5, v13\n"
  "v_mfma_f32_16x16x32_bf16 v[24:27], a[136:139], v[40:43], v[24:27]\n"
  "ds_read_b128 v[76:79], v91 offset:23552\n"
  "v_add_f32 v80, 1.0, v80\n"
  "v_mfma_f32_16x16x32_bf16 v[24:27], a[152:155], v[44:47], v[24:27]\n"
  "v_add_f32 v81, 1.0, v81\n"
  "v_mfma_f32_16x16x32_bf16 v[24:27], a[168:171], v[48:51], v[24:27]\n"
  "v_rcp_f32 v80, v80\n"
  "v_mfma_f32_16x16x32_bf16 v[24:27], a[184:187], v[52:55], v[24:27]\n"
  "v_rcp_f32 v81, v81\n"
  "v_mfma_f32_16x16x32_bf16 v[28:31], a[108:111], v[32:35], v[28:31]\n"
  "v_mul_f32 v82, v82, v80\n"
  "v_mul_f32 v83, v83, v81\n"
  "v_mfma_f32_16x16x32_bf16 v[28:31], a[124:127], v[36:39], v[28:31]\n"
  "v_cvt_pk_bf16_f32 v86, v82, v83\n"
  "v_mul_f32 v80, v6, v6\n"
  "v_mfma_f32_16x16x32_bf16 v[28:31], a[140:143], v[40:43], v[28:31]\n"
  "v_mul_f32 v81, v7, v7\n"
  "v_fmaak_f32 v80, v88, v80, 0xc0135761\n"
  "v_mfma_f32_16x16x32_bf16 v[28:31], a[156:159], v[44:47], v[28:31]\n"
  "v_fmaak_f32 v81, v88, v81, 0xc0135761\n"
  "v_mul_f32 v80, v6, v80\n"
  "v_mfma_f32_16x16x32_bf16 v[28:31], a[172:175], v[48:51], v[28:31]\n"
  "v_mul_f32 v81, v7, v81\n"
  "v_mfma_f32_16x16x32_bf16 v[28:31], a[188:191], v[52:55], v[28:31]\n"
  "s_waitcnt vmcnt(25) lgkmcnt(0)\n"
  "s_barrier\n"
  "v_mfma_f32_16x16x32_bf16 v[16:19], a[192:195], v[56:59], v[16:19]\n"
  "ds_read_b128 v[32:35], v91 offset:0\n"
  "v_mfma_f32_16x16x32_bf16 v[16:19], a[208:211], v[60:63], v[16:19]\n"
  "v_exp_f32 v80, v80\n"
  "v_mfma_f32_16x16x32_bf16 v[16:19], a[224:227], v[64:67], v[16:19]\n"
  "ds_read_b128 v[36:39], v91 offset:1024\n"
  "v_mfma_f32_16x16x32_bf16 v[16:19], a[240:243], v[68:71], v[16:19]\n"
  "v_exp_f32 v81, v81\n"
  "v_mfma_f32_16x16x32_bf16 v[16:19], v[224:227], v[72:75], v[16:19]\n"
  "v_mul_f32 v82, v6, v14\n"
  "v_mul_f32 v83, v7, v15\n"
  "v_mfma_f32_16x16x32_bf16 v[16:19], v[240:243], v[76:79], v[16:19]\n"
  "ds_read_b128 v[40:43], v91 offset:2048\n"
  "v_add_f32 v80, 1.0, v80\n"
  "v_mfma_f32_16x16x32_bf16 v[20:23], a[196:199], v[56:59], v[20:23]\n"
  "v_add_f32 v81, 1.0, v81\n"
  "v_mfma_f32_16x16x32_bf16 v[20:23], a[212:215], v[60:63], v[20:23]\n"
  "v_rcp_f32 v80, v80\n"
  "v_mfma_f32_16x16x32_bf16 v[20:23], a[228:231], v[64:67], v[20:23]\n"
  "ds_read_b128 v[44:47], v91 offset:3072\n"
  "v_mfma_f32_16x16x32_bf16 v[20:23], a[244:247], v[68:71], v[20:23]\n"
  "v_rcp_f32 v81, v81\n"
  "v_mfma_f32_16x16x32_bf16 v[20:23], v[228:231], v[72:75], v[20:23]\n"
  "v_mul_f32 v82, v82, v80\n"
  "v_mul_f32 v83, v83, v81\n"
  "v_mfma_f32_16x16x32_bf16 v[20:23], v[244:247], v[76:79], v[20:23]\n"
  "ds_read_b128 v[48:51], v91 offset:4096\n"
  "v_cvt_pk_bf16_f32 v87, v82, v83\n"
  "v_mfma_f32_16x16x32_bf16 v[24:27], a[200:203], v[56:59], v[24:27]\n"
  "v_mfma_f32_16x16x32_bf16 v[24:27], a[216:219], v[60:63], v[24:27]\n"
  "v_mfma_f32_16x16x32_bf16 v[24:27], a[232:235], v[64:67], v[24:27]\n"
  "ds_read_b128 v[52:55], v91 offset:5120\n"
  "v_mfma_f32_16x16x32_bf16 v[24:27], a[248:251], v[68:71], v[24:27]\n"
  "v_mfma_f32_16x16x32_bf16 v[24:27], v[232:235], v[72:75], v[24:27]\n"
  "v_mfma_f32_16x16x32_bf16 v[24:27], v[248:251], v[76:79], v[24:27]\n"
  "v_mfma_f32_16x16x32_bf16 v[28:31], a[204:207], v[56:59], v[28:31]\n"
  "v_mfma_f32_16x16x32_bf16 v[28:31], a[220:223], v[60:63], v[28:31]\n"
  "v_mfma_f32_16x16x32_bf16 v[28:31], a[236:239], v[64:67], v[28:31]\n"
  "v_mfma_f32_16x16x32_bf16 v[28:31], a[252:255], v[68:71], v[28:31]\n"
  "buffer_store_dwordx4 v[84:87], v89, s[36:39], s55 offen sc1\n"
  "v_mfma_f32_16x16x32_bf16 v[28:31], v[236:239], v[72:75], v[28:31]\n"
  "s_add_u32 s55, s55, 147456\n"
  "v_mfma_f32_16x16x32_bf16 v[28:31], v[252:255], v[76:79], v[28:31]\n"
  "s_waitcnt lgkmcnt(0)\n"
  "v_mfma_f32_16x16x32_bf16 v[0:3], a[0:3], v[32:35], 0\n"
  "s_cmp_eq_u32 s55, s57\n"
  "s_cselect_b32 s55, s56, s55\n"
  "ds_read_b128 v[56:59], v91 offset:6144\n"
  "v_mfma_f32_16x16x32_bf16 v[0:3], a[16:19], v[36:39], v[0:3]\n"
  "v_mul_f32 v80, v16, v16\n"
  "v_mul_f32 v81, v17, v17\n"
  "v_mfma_f32_16x16x32_bf16 v[0:3], a[32:35], v[40:43], v[0:3]\n"
  "ds_read_b128 v[60:63], v91 offset:7168\n"
  "v_fmaak_f32 v80, v88, v80, 0xc0135761\n"
  "v_mfma_f32_16x16x32_bf16 v[0:3], a[48:51], v[44:47], v[0:3]\n"
  "v_fmaak_f32 v81, v88, v81, 0xc0135761\n"
  "v_mul_f32 v80, v16, v80\n"
  "v_mfma_f32_16x16x32_bf16 v[0:3], a[64:67], v[48:51], v[0:3]\n"
  "v_mul_f32 v81, v17, v81\n"
  "v_mfma_f32_16x16x32_bf16 v[0:3], a[80:83], v[52:55], v[0:3]\n"
  "ds_read_b128 v[64:67], v91 offset:8192\n"
  "v_mfma_f32_16x16x32_bf16 v[4:7], a[4:7], v[32:35], 0\n"
  "v_exp_f32 v80, v80\n"
  "v_mfma_f32_16x16x32_bf16 v[4:7], a[20:23], v[36:39], v[4:7]\n"
  "v_exp_f32 v81, v81\n"
  "v_mfma_f32_16x16x32_bf16 v[4:7], a[36:39], v[40:43], v[4:7]\n"
  "ds_read_b128 v[68:71], v91 offset:9216\n"
  "v_mul_f32 v82, v16, v24\n"
  "v_mfma_f32_16x16x32_bf16 v[4:7], a[52:55], v[44:47], v[4:7]\n"
  "v_mul_f32 v83, v17, v25\n"
  "v_add_f32 v80, 1.0, v80\n"
  "v_mfma_f32_16x16x32_bf16 v[4:7], a[68:71], v[48:51], v[4:7]\n"
  "v_add_f32 v81, 1.0, v81\n"
  "v_mfma_f32_16x16x32_bf16 v[4:7], a[84:87], v[52:55], v[4:7]\n"
  "ds_read_b128 v[72:75], v91 offset:10240\n"
  "v_mfma_f32_16x16x32_bf16 v[8:11], a[8:11], v[32:35], 0\n"
  "v_rcp_f32 v80, v80\n"
  "v_mfma_f32_16x16x32_bf16 v[8:11], a[24:27], v[36:39], v[8:11]\n"
  "v_rcp_f32 v81, v81\n"
  "v_mfma_f32_16x16x32_bf16 v[8:11], a[40:43], v[40:43], v[8:11]\n"
  "ds_read_b128 v[76:79], v91 offset:11264\n"
  "v_mul_f32 v82, v82, v80\n"
  "v_mfma_f32_16x16x32_bf16 v[8:11], a[56:59], v[44:47], v[8:11]\n"
  "v_mul_f32 v83, v83, v81\n"
  "v_cvt_pk_bf16_f32 v84, v82, v83\n"
  "v_mfma_f32_16x16x32_bf16 v[8:11], a[72:75], v[48:51], v[8:11]\n"
  "v_mul_f32 v80, v18, v18\n"
  "v_mul_f32 v81, v19, v19\n"
  "v_mfma_f32_16x16x32_bf16 v[8:11], a[88:91], v[52:55], v[8:11]\n"
  "v_fmaak_f32 v80, v88, v80, 0xc0135761\n"
  "v_fmaak_f32 v81, v88, v81, 0xc0135761\n"
  "v_mfma_f32_16x16x32_bf16 v[12:15], a[12:15], v[32:35], 0\n"
  "v_mul_f32 v80, v18, v80\n"
  "v_mul_f32 v81, v19, v81\n"
  "v_mfma_f32_16x16x32_bf16 v[12:15], a[28:31], v[36:39], v[12:15]\n"
  "v_exp_f32 v80, v80\n"
  "v_mfma_f32_16x16x32_bf16 v[12:15], a[44:47], v[40:43], v[12:15]\n"
  "v_exp_f32 v81, v81\n"
  "v_mfma_f32_16x16x32_bf16 v[12:15], a[60:63], v[44:47], v[12:15]\n"
  "v_mul_f32 v82, v18, v26\n"
  "v_mul_f32 v83, v19, v27\n"
  "v_mfma_f32_16x16x32_bf16 v[12:15], a[76:79], v[48:51], v[12:15]\n"
  "v_add_f32 v80, 1.0, v80\n"
  "v_add_f32 v81, 1.0, v81\n"
  "v_mfma_f32_16x16x32_bf16 v[12:15], a[92:95], v[52:55], v[12:15]\n"
  "s_waitcnt lgkmcnt(0)\n"
  "v_rcp_f32 v80, v80\n"
  "v_mfma_f32_16x16x32_bf16 v[0:3], a[96:99], v[56:59], v[0:3]\n"
  "ds_read_b128 v[32:35], v91 offset:12288\n"
  "v_mfma_f32_16x16x32_bf16 v[0:3], a[112:115], v[60:63], v[0:3]\n"
  "v_rcp_f32 v81, v81\n"
  "v_mfma_f32_16x16x32_bf16 v[0:3], a[128:131], v[64:67], v[0:3]\n"
  "ds_read_b128 v[36:39], v91 offset:13312\n"
  "v_mul_f32 v82, v82, v80\n"
  "v_mfma_f32_16x16x32_bf16 v[0:3], a[144:147], v[68:71], v[0:3]\n"
  "v_mul_f32 v83, v83, v81\n"
  "v_cvt_pk_bf16_f32 v85, v82, v83\n"
  "v_mfma_f32_16x16x32_bf16 v[0:3], a[160:163], v[72:75], v[0:3]\n"
  "v_mul_f32 v80, v20, v20\n"
  "v_mul_f32 v81, v21, v21\n"
  "v_mfma_f32_16x16x32_bf16 v[0:3], a[176:179], v[76:79], v[0:3]\n"
  "ds_read_b128 v[40:43], v91 offset:14336\n"
  "v_fmaak_f32 v80, v88, v80, 0xc0135761\n"
  "v_mfma_f32_16x16x32_bf16 v[4:7], a[100:103], v[56:59], v[4:7]\n"
  "v_fmaak_f32 v81, v88, v81, 0xc0135761\n"
  "v_mul_f32 v80, v20, v80\n"
  "v_mfma_f32_16x16x32_bf16 v[4:7], a[116:119], v[60:63], v[4:7]\n"
  "v_mul_f32 v81, v21, v81\n"
  "v_mfma_f32_16x16x32_bf16 v[4:7], a[132:135], v[64:67], v[4:7]\n"
  "ds_read_b128 v[44:47], v91 offset:15360\n"
  "v_mfma_f32_16x16x32_bf16 v[4:7], a[148:151], v[68:71], v[4:7]\n"
  "v_exp_f32 v80, v80\n"
  "v_mfma_f32_16x16x32_bf16 v[4:7], a[164:167], v[72:75], v[4:7]\n"
  "buffer_store_dwordx4 v[108:111], v107, s[36:39], s62 offen offset:0 sc1\n"
  "v_mfma_f32_16x16x32_bf16 v[4:7], a[180:183], v[76:79], v[4:7]\n"
  "ds_read_b128 v[48:51], v91 offset:20480\n"
  "v_mfma_f32_16x16x32_bf16 v[8:11], a[104:107], v[56:59], v[8:11]\n"
  "v_exp_f32 v81, v81\n"
  "v_mfma_f32_16x16x32_bf16 v[8:11], a[120:123], v[60:63], v[8:11]\n"
  "v_mul_f32 v82, v20, v28\n"
  "v_mul_f32 v83, v21, v29\n"
  "v_mfma_f32_16x16x32_bf16 v[8:11], a[136:139], v[64:67], v[8:11]\n"
  "ds_read_b128 v[52:55], v91 offset:21504\n"
  "v_add_f32 v80, 1.0, v80\n"
  "v_mfma_f32_16x16x32_bf16 v[8:11], a[152:155], v[68:71], v[8:11]\n"
  "v_add_f32 v81, 1.0, v81\n"
  "v_mfma_f32_16x16x32_bf16 v[8:11], a[168:171], v[72:75], v[8:11]\n"
  "v_rcp_f32 v80, v80\n"
  "v_mfma_f32_16x16x32_bf16 v[8:11], a[184:187], v[76:79], v[8:11]\n"
  "v_rcp_f32 v81, v81\n"
  "v_mfma_f32_16x16x32_bf16 v[12:15], a[108:111], v[56:59], v[12:15]\n"
  "v_mul_f32 v82, v82, v80\n"
  "v_mul_f32 v83, v83, v81\n"
  "v_mfma_f32_16x16x32_bf16 v[12:15], a[124:127], v[60:63], v[12:15]\n"
  "v_cvt_pk_bf16_f32 v86, v82, v83\n"
  "v_mul_f32 v80, v22, v22\n"
  "v_mfma_f32_16x16x32_bf16 v[12:15], a[140:143], v[64:67], v[12:15]\n"
  "v_mul_f32 v81, v23, v23\n"
  "v_fmaak_f32 v80, v88, v80, 0xc0135761\n"
  "v_mfma_f32_16x16x32_bf16 v[12:15], a[156:159], v[68:71], v[12:15]\n"
  "v_fmaak_f32 v81, v88, v81, 0xc0135761\n"
  "v_mul_f32 v80, v22, v80\n"
  "v_mfma_f32_16x16x32_bf16 v[12:15], a[172:175], v[72:75], v[12:15]\n"
  "v_mul_f32 v81, v23, v81\n"
  "v_mfma_f32_16x16x32_bf16 v[12:15], a[188:191], v[76:79], v[12:15]\n"
  "s_waitcnt lgkmcnt(0)\n"
  "v_exp_f32 v80, v80\n"
  "v_mfma_f32_16x16x32_bf16 v[0:3], a[192:195], v[32:35], v[0:3]\n"
  "ds_read_b128 v[56:59], v89 offset:32768\n"
  "v_mfma_f32_16x16x32_bf16 v[0:3], a[208:211], v[36:39], v[0:3]\n"
  "v_exp_f32 v81, v81\n"
  "v_mfma_f32_16x16x32_bf16 v[0:3], a[224:227], v[40:43], v[0:3]\n"
  "ds_read_b128 v[60:63], v89 offset:33792\n"
  "v_mul_f32 v82, v22, v30\n"
  "v_mfma_f32_16x16x32_bf16 v[0:3], a[240:243], v[44:47], v[0:3]\n"
  "v_mul_f32 v83, v23, v31\n"
  "v_add_f32 v80, 1.0, v80\n"
  "v_mfma_f32_16x16x32_bf16 v[0:3], v[224:227], v[48:51], v[0:3]\n"
  "v_add_f32 v81, 1.0, v81\n"
  "v_mfma_f32_16x16x32_bf16 v[0:3], v[240:243], v[52:55], v[0:3]\n"
  "ds_read_b128 v[64:67], v89 offset:34816\n"
  "v_mfma_f32_16x16x32_bf16 v[4:7], a[196:199], v[32:35], v[4:7]\n"
  "v_rcp_f32 v80, v80\n"
  "v_mfma_f32_16x16x32_bf16 v[4:7], a[212:215], v[36:39], v[4:7]\n"
  "v_rcp_f32 v81, v81\n"
  "v_mfma_f32_16x16x32_bf16 v[4:7], a[228:231], v[40:43], v[4:7]\n"
  "ds_read_b128 v[68:71], v89 offset:35840\n"
  "v_mul_f32 v82, v82, v80\n"
  "v_mfma_f32_16x16x32_bf16 v[4:7], a[244:247], v[44:47], v[4:7]\n"
  "v_mul_f32 v83, v83, v81\n"
  "v_cvt_pk_bf16_f32 v87, v82, v83\n"
  "v_mfma_f32_16x16x32_bf16 v[4:7], v[228:231], v[48:51], v[4:7]\n"
  "v_mfma_f32_16x16x32_bf16 v[4:7], v[244:247], v[52:55], v[4:7]\n"
  "ds_read_b128 v[72:75], v89 offset:36864\n"
  "v_mfma_f32_16x16x32_bf16 v[8:11], a[200:203], v[32:35], v[8:11]\n"
  "v_mfma_f32_16x16x32_bf16 v[8:11], a[216:219], v[36:39], v[8:11]\n"
  "v_mfma_f32_16x16x32_bf16 v[8:11], a[232:235], v[40:43], v[8:11]\n"
  "ds_read_b128 v[76:79], v89 offset:37888\n"
  "v_mfma_f32_16x16x32_bf16 v[8:11], a[248:251], v[44:47], v[8:11]\n"
  "v_mfma_f32_16x16x32_bf16 v[8:11], v[232:235], v[48:51], v[8:11]\n"
  "v_mfma_f32_16x16x32_bf16 v[8:11], v[248:251], v[52:55], v[8:11]\n"
  "v_mfma_f32_16x16x32_bf16 v[12:15], a[204:207], v[32:35], v[12:15]\n"
  "v_mfma_f32_16x16x32_bf16 v[12:15], a[220:223], v[36:39], v[12:15]\n"
  "v_mfma_f32_16x16x32_bf16 v[12:15], a[236:239], v[40:43], v[12:15]\n"
  "v_mfma_f32_16x16x32_bf16 v[12:15], a[252:255], v[44:47], v[12:15]\n"
  "buffer_store_dwordx4 v[84:87], v89, s[36:39], s55 offen sc1\n"
  "v_mfma_f32_16x16x32_bf16 v[12:15], v[236:239], v[48:51], v[12:15]\n"
  "s_add_u32 s55, s55, 147456\n"
  "v_mfma_f32_16x16x32_bf16 v[12:15], v[252:255], v[52:55], v[12:15]\n"
  "s_waitcnt lgkmcnt(0)\n"
  "v_mfma_f32_16x16x32_bf16 v[16:19], a[0:3], v[56:59], 0\n"
  "s_cmp_eq_u32 s55, s57\n"
  "s_cselect_b32 s55, s56, s55\n"
  "ds_read_b128 v[32:35], v89 offset:38912\n"
  "v_mfma_f32_16x16x32_bf16 v[16:19], a[16:19], v[60:63], v[16:19]\n"
  "v_mul_f32 v80, v0, v0\n"
  "v_mul_f32 v81, v1, v1\n"
  "v_mfma_f32_16x16x32_bf16 v[16:19], a[32:35], v[64:67], v[16:19]\n"
  "ds_read_b128 v[36:39], v89 offset:39936\n"
  "v_fmaak_f32 v80, v88, v80, 0xc0135761\n"
  "v_mfma_f32_16x16x32_bf16 v[16:19], a[48:51], v[68:71], v[16:19]\n"
  "v_fmaak_f32 v81, v88, v81, 0xc0135761\n"
  "v_mul_f32 v80, v0, v80\n"
  "v_mfma_f32_16x16x32_bf16 v[16:19], a[64:67], v[72:75], v[16:19]\n"
  "v_mul_f32 v81, v1, v81\n"
  "v_mfma_f32_16x16x32_bf16 v[16:19], a[80:83], v[76:79], v[16:19]\n"
  "ds_read_b128 v[40:43], v89 offset:40960\n"
  "v_mfma_f32_16x16x32_bf16 v[20:23], a[4:7], v[56:59], 0\n"
  "v_exp_f32 v80, v80\n"
  "v_mfma_f32_16x16x32_bf16 v[20:23], a[20:23], v[60:63], v[20:23]\n"
  "v_exp_f32 v81, v81\n"
  "v_mfma_f32_16x16x32_bf16 v[20:23], a[36:39], v[64:67], v[20:23]\n"
  "ds_read_b128 v[44:47], v89 offset:41984\n"
  "v_mul_f32 v82, v0, v8\n"
  "v_mfma_f32_16x16x32_bf16 v[20:23], a[52:55], v[68:71], v[20:23]\n"
  "v_mul_f32 v83, v1, v9\n"
  "v_add_f32 v80, 1.0, v80\n"
  "v_mfma_f32_16x16x32_bf16 v[20:23], a[68:71], v[72:75], v[20:23]\n"
  "v_add_f32 v81, 1.0, v81\n"
  "v_mfma_f32_16x16x32_bf16 v[20:23], a[84:87], v[76:79], v[20:23]\n"
  "ds_read_b128 v[48:51], v89 offset:43008\n"
  "v_mfma_f32_16x16x32_bf16 v[24:27], a[8:11], v[56:59], 0\n"
  "v_rcp_f32 v80, v80\n"
  "v_mfma_f32_16x16x32_bf16 v[24:27], a[24:27], v[60:63], v[24:27]\n"
  "v_rcp_f32 v81, v81\n"
  "v_mfma_f32_16x16x32_bf16 v[24:27], a[40:43], v[64:67], v[24:27]\n"
  "ds_read_b128 v[52:55], v89 offset:44032\n"
  "v_mul_f32 v82, v82, v80\n"
  "v_mfma_f32_16x16x32_bf16 v[24:27], a[56:59], v[68:71], v[24:27]\n"
  "v_mul_f32 v83, v83, v81\n"
  "v_cvt_pk_bf16_f32 v84, v82, v83\n"
  "v_mfma_f32_16x16x32_bf16 v[24:27], a[72:75], v[72:75], v[24:27]\n"
  "v_mul_f32 v80, v2, v2\n"
  "v_mul_f32 v81, v3, v3\n"
  "v_mfma_f32_16x16x32_bf16 v[24:27], a[88:91], v[76:79], v[24:27]\n"
  "v_fmaak_f32 v80, v88, v80, 0xc0135761\n"
  "v_fmaak_f32 v81, v88, v81, 0xc0135761\n"
  "v_mfma_f32_16x16x32_bf16 v[28:31], a[12:15], v[56:59], 0\n"
  "v_mul_f32 v80, v2, v80\n"
  "v_mul_f32 v81, v3, v81\n"
  "v_mfma_f32_16x16x32_bf16 v[28:31], a[28:31], v[60:63], v[28:31]\n"
  "v_exp_f32 v80, v80\n"
  "v_mfma_f32_16x16x32_bf16 v[28:31], a[44:47], v[64:67], v[28:31]\n"
  "v_exp_f32 v81, v81\n"
  "v_mfma_f32_16x16x32_bf16 v[28:31], a[60:63], v[68:71], v[28:31]\n"
  "v_mul_f32 v82, v2, v10\n"
  "v_mul_f32 v83, v3, v11\n"
  "v_mfma_f32_16x16x32_bf16 v[28:31], a[76:79], v[72:75], v[28:31]\n"
  "v_add_f32 v80, 1.0, v80\n"
  "v_add_f32 v81, 1.0, v81\n"
  "v_mfma_f32_16x16x32_bf16 v[28:31], a[92:95], v[76:79], v[28:31]\n"
  "s_waitcnt lgkmcnt(0)\n"
  "v_rcp_f32 v80, v80\n"
  "v_mfma_f32_16x16x32_bf16 v[16:19], a[96:99], v[32:35], v[16:19]\n"
  "ds_read_b128 v[56:59], v89 offset:45056\n"
  "v_mfma_f32_16x16x32_bf16 v[16:19], a[112:115], v[36:39], v[16:19]\n"
  "v_rcp_f32 v81, v81\n"
  "v_mfma_f32_16x16x32_bf16 v[16:19], a[128:131], v[40:43], v[16:19]\n"
  "ds_read_b128 v[60:63], v89 offset:46080\n"
  "v_mul_f32 v82, v82, v80\n"
  "v_mfma_f32_16x16x32_bf16 v[16:19], a[144:147], v[44:47], v[16:19]\n"
  "s_mov_b64 exec, 1\n"
  "buffer_load_dword v106, v105, s[24:27], 0 offen sc1\n"
  "s_mov_b64 exec, -1\n"
  "v_mfma_f32_16x16x32_bf16 v[16:19], a[160:163], v[48:51], v[16:19]\n"
  "v_mul_f32 v83, v83, v81\n"
  "v_cvt_pk_bf16_f32 v85, v82, v83\n"
  "v_mfma_f32_16x16x32_bf16 v[16:19], a[176:179], v[52:55], v[16:19]\n"
  "ds_read_b128 v[64:67], v89 offset:47104\n"
  "v_mul_f32 v80, v4, v4\n"
  "v_mfma_f32_16x16x32_bf16 v[20:23], a[100:103], v[32:35], v[20:23]\n"
  "v_mul_f32 v81, v5, v5\n"
  "v_fmaak_f32 v80, v88, v80, 0xc0135761\n"
  "v_mfma_f32_16x16x32_bf16 v[20:23], a[116:119], v[36:39], v[20:23]\n"
  "v_fmaak_f32 v81, v88, v81, 0xc0135761\n"
  "v_mul_f32 v80, v4, v80\n"
  "v_mfma_f32_16x16x32_bf16 v[20:23], a[132:135], v[40:43], v[20:23]\n"
  "ds_read_b128 v[68:71], v89 offset:48128\n"
  "v_mul_f32 v81, v5, v81\n"
  "v_mfma_f32_16x16x32_bf16 v[20:23], a[148:151], v[44:47], v[20:23]\n"
  "v_exp_f32 v80, v80\n"
  "v_mfma_f32_16x16x32_bf16 v[20:23], a[164:167], v[48:51], v[20:23]\n"
  "buffer_store_dwordx4 v[108:111], v107, s[36:39], s62 offen offset:1024 sc1\n"
  "v_mfma_f32_16x16x32_bf16 v[20:23], a[180:183], v[52:55], v[20:23]\n"
  "ds_read_b128 v[72:75], v91 offset:18432\n"
  "v_mfma_f32_16x16x32_bf16 v[24:27], a[104:107], v[32:35], v[24:27]\n"
  "v_exp_f32 v81, v81\n"
  "v_mfma_f32_16x16x32_bf16 v[24:27], a[120:123], v[36:39], v[24:27]\n"
  "v_mul_f32 v82, v4, v12\n"
  "v_mul_f32 v83, v5, v13\n"
  "v_mfma_f32_16x16x32_bf16 v[24:27], a[136:139], v[40:43], v[24:27]\n"
  "ds_read_b128 v[76:79], v91 offset:19456\n"
  "v_add_f32 v80, 1.0, v80\n"
  "v_mfma_f32_16x16x32_bf16 v[24:27], a[152:155], v[44:47], v[24:27]\n"
  "v_add_f32 v81, 1.0, v81\n"
  "v_mfma_f32_16x16x32_bf16 v[24:27], a[168:171], v[48:51], v[24:27]\n"
  "v_rcp_f32 v80, v80\n"
  "v_mfma_f32_16x16x32_bf16 v[24:27], a[184:187], v[52:55], v[24:27]\n"
  "v_rcp_f32 v81, v81\n"
  "v_mfma_f32_16x16x32_bf16 v[28:31], a[108:111], v[32:35], v[28:31]\n"
  "v_mul_f32 v82, v82, v80\n"
  "v_mul_f32 v83, v83, v81\n"
  "v_mfma_f32_16x16x32_bf16 v[28:31], a[124:127], v[36:39], v[28:31]\n"
  "v_cvt_pk_bf16_f32 v86, v82, v83\n"
  "v_mul_f32 v80, v6, v6\n"
  "v_mfma_f32_16x16x32_bf16 v[28:31], a[140:143], v[40:43], v[28:31]\n"
  "v_mul_f32 v81, v7, v7\n"
  "v_fmaak_f32 v80, v88, v80, 0xc0135761\n"
  "v_mfma_f32_16x16x32_bf16 v[28:31], a[156:159], v[44:47], v[28:31]\n"
  "v_fmaak_f32 v81, v88, v81, 0xc0135761\n"
  "v_mul_f32 v80, v6, v80\n"
  "v_mfma_f32_16x16x32_bf16 v[28:31], a[172:175], v[48:51], v[28:31]\n"
  "v_mul_f32 v81, v7, v81\n"
  "v_mfma_f32_16x16x32_bf16 v[28:31], a[188:191], v[52:55], v[28:31]\n"
  "s_waitcnt vmcnt(19) lgkmcnt(0)\n"
  "s_barrier\n"
  "v_mfma_f32_16x16x32_bf16 v[16:19], a[192:195], v[56:59], v[16:19]\n"
  "ds_read_b128 v[32:35], v89 offset:16384\n"
  "v_mfma_f32_16x16x32_bf16 v[16:19], a[208:211], v[60:63], v[16:19]\n"
  "v_exp_f32 v80, v80\n"
  "v_mfma_f32_16x16x32_bf16 v[16:19], a[224:227], v[64:67], v[16:19]\n"
  "ds_read_b128 v[36:39], v89 offset:17408\n"
  "v_mfma_f32_16x16x32_bf16 v[16:19], a[240:243], v[68:71], v[16:19]\n"
  "v_exp_f32 v81, v81\n"
  "v_mfma_f32_16x16x32_bf16 v[16:19], v[224:227], v[72:75], v[16:19]\n"
  "v_mul_f32 v82, v6, v14\n"
  "v_mul_f32 v83, v7, v15\n"
  "v_mfma_f32_16x16x32_bf16 v[16:19], v[240:243], v[76:79], v[16:19]\n"
  "ds_read_b128 v[40:43], v89 offset:18432\n"
  "v_add_f32 v80, 1.0, v80\n"
  "v_mfma_f32_16x16x32_bf16 v[20:23], a[196:199], v[56:59], v[20:23]\n"
  "v_add_f32 v81, 1.0, v81\n"
  "v_mfma_f32_16x16x32_bf16 v[20:23], a[212:215], v[60:63], v[20:23]\n"
  "v_rcp_f32 v80, v80\n"
  "v_mfma_f32_16x16x32_bf16 v[20:23], a[228:231], v[64:67], v[20:23]\n"
  "ds_read_b128 v[44:47], v89 offset:19456\n"
  "v_mfma_f32_16x16x32_bf16 v[20:23], a[244:247], v[68:71], v[20:23]\n"
  "v_rcp_f32 v81, v81\n"
  "v_mfma_f32_16x16x32_bf16 v[20:23], v[228:231], v[72:75], v[20:23]\n"
  "v_mul_f32 v82, v82, v80\n"
  "v_mul_f32 v83, v83, v81\n"
  "v_mfma_f32_16x16x32_bf16 v[20:23], v[244:247], v[76:79], v[20:23]\n"
  "ds_read_b128 v[48:51], v89 offset:20480\n"
  "v_cvt_pk_bf16_f32 v87, v82, v83\n"
  "v_mfma_f32_16x16x32_bf16 v[24:27], a[200:203], v[56:59], v[24:27]\n"
  "v_mfma_f32_16x16x32_bf16 v[24:27], a[216:219], v[60:63], v[24:27]\n"
  "v_mfma_f32_16x16x32_bf16 v[24:27], a[232:235], v[64:67], v[24:27]\n"
  "ds_read_b128 v[52:55], v89 offset:21504\n"
  "v_mfma_f32_16x16x32_bf16 v[24:27], a[248:251], v[68:71], v[24:27]\n"
  "v_mfma_f32_16x16x32_bf16 v[24:27], v[232:235], v[72:75], v[24:27]\n"
  "v_mfma_f32_16x16x32_bf16 v[24:27], v[248:251], v[76:79], v[24:27]\n"
  "v_mfma_f32_16x16x32_bf16 v[28:31], a[204:207], v[56:59], v[28:31]\n"
  "v_mfma_f32_16x16x32_bf16 v[28:31], a[220:223], v[60:63], v[28:31]\n"
  "v_mfma_f32_16x16x32_bf16 v[28:31], a[236:239], v[64:67], v[28:31]\n"
  "v_mfma_f32_16x16x32_bf16 v[28:31], a[252:255], v[68:71], v[28:31]\n"
  "buffer_store_dwordx4 v[84:87], v89, s[36:39], s55 offen sc1\n"
  "v_mfma_f32_16x16x32_bf16 v[28:31], v[236:239], v[72:75], v[28:31]\n"
  "s_add_u32 s55, s55, 147456\n"
  "v_mfma_f32_16x16x32_bf16 v[28:31], v[252:255], v[76:79], v[28:31]\n"
  "s_waitcnt lgkmcnt(0)\n"
  "v_mfma_f32_16x16x32_bf16 v[0:3], a[0:3], v[32:35], 0\n"
  "s_cmp_eq_u32 s55, s57\n"
  "s_cselect_b32 s55, s56, s55\n"
  "ds_read_b128 v[56:59], v89 offset:22528\n"
  "v_mfma_f32_16x16x32_bf16 v[0:3], a[16:19], v[36:39], v[0:3]\n"
  "v_mul_f32 v80, v16, v16\n"
  "v_mul_f32 v81, v17, v17\n"
  "v_mfma_f32_16x16x32_bf16 v[0:3], a[32:35], v[40:43], v[0:3]\n"
  "ds_read_b128 v[60:63], v89 offset:23552\n"
  "v_fmaak_f32 v80, v88, v80, 0xc0135761\n"
  "v_mfma_f32_16x16x32_bf16 v[0:3], a[48:51], v[44:47], v[0:3]\n"
  "v_fmaak_f32 v81, v88, v81, 0xc0135761\n"
  "v_mul_f32 v80, v16, v80\n"
  "v_mfma_f32_16x16x32_bf16 v[0:3], a[64:67], v[48:51], v[0:3]\n"
  "v_mul_f32 v81, v17, v81\n"
  "v_mfma_f32_16x16x32_bf16 v[0:3], a[80:83], v[52:55], v[0:3]\n"
  "ds_read_b128 v[64:67], v89 offset:24576\n"
  "v_mfma_f32_16x16x32_bf16 v[4:7], a[4:7], v[32:35], 0\n"
  "v_exp_f32 v80, v80\n"
  "v_mfma_f32_16x16x32_bf16 v[4:7], a[20:23], v[36:39], v[4:7]\n"
  "v_exp_f32 v81, v81\n"
  "v_mfma_f32_16x16x32_bf16 v[4:7], a[36:39], v[40:43], v[4:7]\n"
  "ds_read_b128 v[68:71], v89 offset:25600\n"
  "v_mul_f32 v82, v16, v24\n"
  "v_mfma_f32_16x16x32_bf16 v[4:7], a[52:55], v[44:47], v[4:7]\n"
  "v_mul_f32 v83, v17, v25\n"
  "v_add_f32 v80, 1.0, v80\n"
  "v_mfma_f32_16x16x32_bf16 v[4:7], a[68:71], v[48:51], v[4:7]\n"
  "v_add_f32 v81, 1.0, v81\n"
  "v_mfma_f32_16x16x32_bf16 v[4:7], a[84:87], v[52:55], v[4:7]\n"
  "ds_read_b128 v[72:75], v89 offset:26624\n"
  "v_mfma_f32_16x16x32_bf16 v[8:11], a[8:11], v[32:35], 0\n"
  "v_rcp_f32 v80, v80\n"
  "v_mfma_f32_16x16x32_bf16 v[8:11], a[24:27], v[36:39], v[8:11]\n"
  "v_rcp_f32 v81, v81\n"
  "v_mfma_f32_16x16x32_bf16 v[8:11], a[40:43], v[40:43], v[8:11]\n"
  "ds_read_b128 v[76:79], v89 offset:27648\n"
  "v_mul_f32 v82, v82, v80\n"
  "v_mfma_f32_16x16x32_bf16 v[8:11], a[56:59], v[44:47], v[8:11]\n"
  "v_mul_f32 v83, v83, v81\n"
  "v_cvt_pk_bf16_f32 v84, v82, v83\n"
  "v_mfma_f32_16x16x32_bf16 v[8:11], a[72:75], v[48:51], v[8:11]\n"
  "v_mul_f32 v80, v18, v18\n"
  "v_mul_f32 v81, v19, v19\n"
  "v_mfma_f32_16x16x32_bf16 v[8:11], a[88:91], v[52:55], v[8:11]\n"
  "v_fmaak_f32 v80, v88, v80, 0xc0135761\n"
  "v_fmaak_f32 v81, v88, v81, 0xc0135761\n"
  "v_mfma_f32_16x16x32_bf16 v[12:15], a[12:15], v[32:35], 0\n"
  "v_mul_f32 v80, v18, v80\n"
  "v_mul_f32 v81, v19, v81\n"
  "v_mfma_f32_16x16x32_bf16 v[12:15], a[28:31], v[36:39], v[12:15]\n"
  "v_exp_f32 v80, v80\n"
  "v_mfma_f32_16x16x32_bf16 v[12:15], a[44:47], v[40:43], v[12:15]\n"
  "v_exp_f32 v81, v81\n"
  "v_mfma_f32_16x16x32_bf16 v[12:15], a[60:63], v[44:47], v[12:15]\n"
  "v_mul_f32 v82, v18, v26\n"
  "v_mul_f32 v83, v19, v27\n"
  "v_mfma_f32_16x16x32_bf16 v[12:15], a[76:79], v[48:51], v[12:15]\n"
  "v_add_f32 v80, 1.0, v80\n"
  "v_add_f32 v81, 1.0, v81\n"
  "v_mfma_f32_16x16x32_bf16 v[12:15], a[92:95], v[52:55], v[12:15]\n"
  "s_waitcnt lgkmcnt(0)\n"
  "s_waitcnt vmcnt(2)\n"
  "v_cmp_gt_u32 vcc, 12, v106\n"
  "s_and_b64 vcc, vcc, 1\n"
  "s_cbranch_scc1 Lpollp0B%=\n"
  "Lpollrp0B%=:\n"
  "v_mfma_f32_16x16x32_bf16 v[0:3], a[96:99], v[56:59], v[0:3]\n"
  "s_add_u32 m0, s48, 0\n"
  "ds_read_b128 v[32:35], v89 offset:28672\n"
  "v_mfma_f32_16x16x32_bf16 v[0:3], a[112:115], v[60:63], v[0:3]\n"
  "buffer_load_dwordx4 v97, s[36:39], s59 offen offset:3072 sc1 lds\n"
  "v_mfma_f32_16x16x32_bf16 v[0:3], a[128:131], v[64:67], v[0:3]\n"
  "s_add_u32 s59, s59, 1024\n"
  "s_nop 0\n"
  "s_cbranch_execz Ltramp%=\n"
  "ds_read_b128 v[36:39], v89 offset:29696\n"
  "v_mfma_f32_16x16x32_bf16 v[0:3], a[144:147], v[68:71], v[0:3]\n"
  "v_rcp_f32 v80, v80\n"
  "v_mfma_f32_16x16x32_bf16 v[0:3], a[160:163], v[72:75], v[0:3]\n"
  "v_rcp_f32 v81, v81\n"
  "v_mfma_f32_16x16x32_bf16 v[0:3], a[176:179], v[76:79], v[0:3]\n"
  "ds_read_b128 v[40:43], v89 offset:30720\n"
  "v_mul_f32 v82, v82, v80\n"
  "v_mfma_f32_16x16x32_bf16 v[4:7], a[100:103], v[56:59], v[4:7]\n"
  "buffer_load_dwordx4 v94, s[40:43], s60 offen offset:0 lds\n"
  "v_mul_f32 v83, v83, v81\n"
  "v_mfma_f32_16x16x32_bf16 v[4:7], a[116:119], v[60:63], v[4:7]\n"
  "v_cvt_pk_bf16_f32 v85, v82, v83\n"
  "v_mul_f32 v80, v20, v20\n"
  "v_mfma_f32_16x16x32_bf16 v[4:7], a[132:135], v[64:67], v[4:7]\n"
  "ds_read_b128 v[44:47], v89 offset:31744\n"
  "v_mul_f32 v81, v21, v21\n"
  "v_mfma_f32_16x16x32_bf16 v[4:7], a[148:151], v[68:71], v[4:7]\n"
  "v_fmaak_f32 v80, v88, v80, 0xc0135761\n"
  "v_fmaak_f32 v81, v88, v81, 0xc0135761\n"
  "v_mfma_f32_16x16x32_bf16 v[4:7], a[164:167], v[72:75], v[4:7]\n"
  "buffer_store_dwordx4 v[108:111], v107, s[36:39], s62 offen offset:2048 sc1\n"
  "v_mul_f32 v80, v20, v80\n"
  "v_mfma_f32_16x16x32_bf16 v[4:7], a[180:183], v[76:79], v[4:7]\n"
  "buffer_load_dwordx4 v95, s[40:43], s60 offen offset:1024 lds\n"
  "ds_read_b128 v[48:51], v91 offset:16384\n"
  "v_mfma_f32_16x16x32_bf16 v[8:11], a[104:107], v[56:59], v[8:11]\n"
  "v_mul_f32 v81, v21, v81\n"
  "v_mfma_f32_16x16x32_bf16 v[8:11], a[120:123], v[60:63], v[8:11]\n"
  "v_exp_f32 v80, v80\n"
  "v_mfma_f32_16x16x32_bf16 v[8:11], a[136:139], v[64:67], v[8:11]\n"
  "ds_read_b128 v[52:55], v91 offset:17408\n"
  "v_mfma_f32_16x16x32_bf16 v[8:11], a[152:155], v[68:71], v[8:11]\n"
  "v_exp_f32 v81, v81\n"
  "v_mfma_f32_16x16x32_bf16 v[8:11], a[168:171], v[72:75], v[8:11]\n"
  "buffer_load_dwordx4 v96, s[40:43], s60 offen offset:2048 lds\n"
  "v_mul_f32 v82, v20, v28\n"
  "v_mfma_f32_16x16x32_bf16 v[8:11], a[184:187], v[76:79], v[8:11]\n"
  "s_add_u32 s60, s60, 36864\n"
  "v_mul_f32 v83, v21, v29\n"
  "v_add_f32 v80, 1.0, v80\n"
  "v_mfma_f32_16x16x32_bf16 v[12:15], a[108:111], v[56:59], v[12:15]\n"
  "v_add_f32 v81, 1.0, v81\n"
  "v_mfma_f32_16x16x32_bf16 v[12:15], a[124:127], v[60:63], v[12:15]\n"
  "v_rcp_f32 v80, v80\n"
  "v_mfma_f32_16x16x32_bf16 v[12:15], a[140:143], v[64:67], v[12:15]\n"
  "s_add_u32 m0, s48, 81920\n"
  "v_rcp_f32 v81, v81\n"
  "v_mfma_f32_16x16x32_bf16 v[12:15], a[156:159], v[68:71], v[12:15]\n"
  "buffer_load_dwordx4 v97, s[36:39], s59 offen offset:3072 sc1 lds\n"
  "v_mul_f32 v82, v82, v80\n"
  "v_mfma_f32_16x16x32_bf16 v[12:15], a[172:175], v[72:75], v[12:15]\n"
  "s_add_u32 s59, s59, 1024\n"
  "v_mul_f32 v83, v83, v81\n"
  "v_cvt_pk_bf16_f32 v86, v82, v83\n"
  "v_mfma_f32_16x16x32_bf16 v[12:15], a[188:191], v[76:79], v[12:15]\n"
  "s_waitcnt lgkmcnt(0)\n"
  "v_mul_f32 v80, v22, v22\n"
  "v_mul_f32 v81, v23, v23\n"
  "v_mfma_f32_16x16x32_bf16 v[0:3], a[192:195], v[32:35], v[0:3]\n"
  "ds_read_b128 v[56:59], v89 offset:49152\n"
  "v_fmaak_f32 v80, v88, v80, 0xc0135761\n"
  "v_mfma_f32_16x16x32_bf16 v[0:3], a[208:211], v[36:39], v[0:3]\n"
  "v_fmaak_f32 v81, v88, v81, 0xc0135761\n"
  "v_mul_f32 v80, v22, v80\n"
  "v_mfma_f32_16x16x32_bf16 v[0:3], a[224:227], v[40:43], v[0:3]\n"
  "buffer_load_dwordx4 v94, s[40:43], s60 offen offset:0 lds\n"
  "ds_read_b128 v[60:63], v89 offset:50176\n"
  "v_mfma_f32_16x16x32_bf16 v[0:3], a[240:243], v[44:47], v[0:3]\n"
  "v_mul_f32 v81, v23, v81\n"
  "v_mfma_f32_16x16x32_bf16 v[0:3], v[224:227], v[48:51], v[0:3]\n"
  "v_exp_f32 v80, v80\n"
  "v_mfma_f32_16x16x32_bf16 v[0:3], v[240:243], v[52:55], v[0:3]\n"
  "ds_read_b128 v[64:67], v89 offset:51200\n"
  "v_mfma_f32_16x16x32_bf16 v[4:7], a[196:199], v[32:35], v[4:7]\n"
  "v_exp_f32 v81, v81\n"
  "v_mfma_f32_16x16x32_bf16 v[4:7], a[212:215], v[36:39], v[4:7]\n"
  "buffer_load_dwordx4 v95, s[40:43], s60 offen offset:1024 lds\n"
  "v_mul_f32 v82, v22, v30\n"
  "v_mfma_f32_16x16x32_bf16 v[4:7], a[228:231], v[40:43], v[4:7]\n"
  "ds_read_b128 v[68:71], v89 offset:52224\n"
  "v_mul_f32 v83, v23, v31\n"
  "v_mfma_f32_16x16x32_bf16 v[4:7], a[244:247], v[44:47], v[4:7]\n"
  "v_add_f32 v80, 1.0, v80\n"
  "v_add_f32 v81, 1.0, v81\n"
  "v_mfma_f32_16x16x32_bf16 v[4:7], v[228:231], v[48:51], v[4:7]\n"
  "v_rcp_f32 v80, v80\n"
  "v_mfma_f32_16x16x32_bf16 v[4:7], v[244:247], v[52:55], v[4:7]\n"
  "ds_read_b128 v[72:75], v89 offset:53248\n"
  "v_mfma_f32_16x16x32_bf16 v[8:11], a[200:203], v[32:35], v[8:11]\n"
  "buffer_load_dwordx4 v96, s[40:43], s60 offen offset:2048 lds\n"
  "v_mfma_f32_16x16x32_bf16 v[8:11], a[216:219], v[36:39], v[8:11]\n"
  "s_add_u32 s60, s60, 36864\n"
  "v_rcp_f32 v81, v81\n"
  "v_mfma_f32_16x16x32_bf16 v[8:11], a[232:235], v[40:43], v[8:11]\n"
  "ds_read_b128 v[76:79], v89 offset:54272\n"
  "v_mul_f32 v82, v82, v80\n"
  "v_mfma_f32_16x16x32_bf16 v[8:11], a[248:251], v[44:47], v[8:11]\n"
  "v_mul_f32 v83, v83, v81\n"
  "v_cvt_pk_bf16_f32 v87, v82, v83\n"
  "v_mfma_f32_16x16x32_bf16 v[8:11], v[232:235], v[48:51], v[8:11]\n"
  "s_add_u32 m0, s48, 98304\n"
  "v_mfma_f32_16x16x32_bf16 v[8:11], v[248:251], v[52:55], v[8:11]\n"
  "buffer_load_dwordx4 v92, s[36:39], s52 offen offset:0 sc1 lds\n"
  "v_mfma_f32_16x16x32_bf16 v[12:15], a[204:207], v[32:35], v[12:15]\n"
  "v_mfma_f32_16x16x32_bf16 v[12:15], a[220:223], v[36:39], v[12:15]\n"
  "v_mfma_f32_16x16x32_bf16 v[12:15], a[236:239], v[40:43], v[12:15]\n"
  "v_mfma_f32_16x16x32_bf16 v[12:15], a[252:255], v[44:47], v[12:15]\n"
  "buffer_store_dwordx4 v[84:87], v89, s[36:39], s55 offen sc1\n"
  "v_mfma_f32_16x16x32_bf16 v[12:15], v[236:239], v[48:51], v[12:15]\n"
  "buffer_load_dwordx4 v92, s[36:39], s52 offen offset:1024 sc1 lds\n"
  "v_mfma_f32_16x16x32_bf16 v[12:15], v[252:255], v[52:55], v[12:15]\n"
  "s_waitcnt lgkmcnt(0)\n"
  "v_mfma_f32_16x16x32_bf16 v[16:19], a[0:3], v[56:59], 0\n"
  "s_add_u32 s55, s55, 147456\n"
  "ds_read_b128 v[32:35], v89 offset:55296\n"
  "v_mfma_f32_16x16x32_bf16 v[16:19], a[16:19], v[60:63], v[16:19]\n"
  "s_cmp_eq_u32 s55, s57\n"
  "s_cselect_b32 s55, s56, s55\n"
  "v_mul_f32 v80, v0, v0\n"
  "v_mfma_f32_16x16x32_bf16 v[16:19], a[32:35], v[64:67], v[16:19]\n"
  "ds_read_b128 v[36:39], v89 offset:56320\n"
  "v_mul_f32 v81, v1, v1\n"
  "v_mfma_f32_16x16x32_bf16 v[16:19], a[48:51], v[68:71], v[16:19]\n"
  "buffer_load_dwordx4 v92, s[36:39], s52 offen offset:2048 sc1 lds\n"
  "v_fmaak_f32 v80, v88, v80, 0xc0135761\n"
  "v_mfma_f32_16x16x32_bf16 v[16:19], a[64:67], v[72:75], v[16:19]\n"
  "v_fmaak_f32 v81, v88, v81, 0xc0135761\n"
  "v_mul_f32 v80, v0, v80\n"
  "v_mfma_f32_16x16x32_bf16 v[16:19], a[80:83], v[76:79], v[16:19]\n"
  "ds_read_b128 v[40:43], v89 offset:57344\n"
  "v_mul_f32 v81, v1, v81\n"
  "v_mfma_f32_16x16x32_bf16 v[20:23], a[4:7], v[56:59], 0\n"
  "v_exp_f32 v80, v80\n"
  "v_mfma_f32_16x16x32_bf16 v[20:23], a[20:23], v[60:63], v[20:23]\n"
  "v_exp_f32 v81, v81\n"
  "v_mfma_f32_16x16x32_bf16 v[20:23], a[36:39], v[64:67], v[20:23]\n"
  "buffer_load_dwordx4 v92, s[36:39], s52 offen offset:3072 sc1 lds\n"
  "ds_read_b128 v[44:47], v89 offset:58368\n"
  "v_mfma_f32_16x16x32_bf16 v[20:23], a[52:55], v[68:71], v[20:23]\n"
  "v_mul_f32 v82, v0, v8\n"
  "v_mul_f32 v83, v1, v9\n"
  "v_mfma_f32_16x16x32_bf16 v[20:23], a[68:71], v[72:75], v[20:23]\n"
  "v_add_f32 v80, 1.0, v80\n"
  "v_add_f32 v81, 1.0, v81\n"
  "v_mfma_f32_16x16x32_bf16 v[20:23], a[84:87], v[76:79], v[20:23]\n"
  "ds_read_b128 v[48:51], v89 offset:59392\n"
  "v_mfma_f32_16x16x32_bf16 v[24:27], a[8:11], v[56:59], 0\n"
  "s_add_u32 m0, s49, 141312\n"
  "v_rcp_f32 v80, v80\n"
  "v_mfma_f32_16x16x32_bf16 v[24:27], a[24:27], v[60:63], v[24:27]\n"
  "buffer_load_dwordx4 v93, s[36:39], s52 offen sc1 lds\n"
  "v_mfma_f32_16x16x32_bf16 v[24:27], a[40:43], v[64:67], v[24:27]\n"
  "s_add_u32 s52, s52, 18432\n"
  "ds_read_b128 v[52:55], v89 offset:60416\n"
  "v_mfma_f32_16x16x32_bf16 v[24:27], a[56:59], v[68:71], v[24:27]\n"
  "s_cmp_eq_u32 s52, s54\n"
  "s_cselect_b32 s52, s53, s52\n"
  "v_mfma_f32_16x16x32_bf16 v[24:27], a[72:75], v[72:75], v[24:27]\n"
  "v_rcp_f32 v81, v81\n"
  "v_mfma_f32_16x16x32_bf16 v[24:27], a[88:91], v[76:79], v[24:27]\n"
  "s_add_u32 m0, s48, 114688\n"
  "v_mul_f32 v82, v82, v80\n"
  "v_mul_f32 v83, v83, v81\n"
  "v_mfma_f32_16x16x32_bf16 v[28:31], a[12:15], v[56:59], 0\n"
  "buffer_load_dwordx4 v97, s[36:39], s59 offen offset:3072 sc1 lds\n"
  "v_cvt_pk_bf16_f32 v84, v82, v83\n"
  "v_mfma_f32_16x16x32_bf16 v[28:31], a[28:31], v[60:63], v[28:31]\n"
  "s_add_u32 s59, s59, 1024\n"
  "v_mul_f32 v80, v2, v2\n"
  "v_mul_f32 v81, v3, v3\n"
  "v_mfma_f32_16x16x32_bf16 v[28:31], a[44:47], v[64:67], v[28:31]\n"
  "v_fmaak_f32 v80, v88, v80, 0xc0135761\n"
  "v_fmaak_f32 v81, v88, v81, 0xc0135761\n"
  "v_mfma_f32_16x16x32_bf16 v[28:31], a[60:63], v[68:71], v[28:31]\n"
  "v_mul_f32 v80, v2, v80\n"
  "v_mul_f32 v81, v3, v81\n"
  "v_mfma_f32_16x16x32_bf16 v[28:31], a[76:79], v[72:75], v[28:31]\n"
  "v_exp_f32 v80, v80\n"
  "v_mfma_f32_16x16x32_bf16 v[28:31], a[92:95], v[76:79], v[28:31]\n"
  "s_waitcnt lgkmcnt(0)\n"
  "v_exp_f32 v81, v81\n"
  "v_mfma_f32_16x16x32_bf16 v[16:19], a[96:99], v[32:35], v[16:19]\n"
  "buffer_load_dwordx4 v94, s[40:43], s60 offen offset:0 lds\n"
  "ds_read_b128 v[56:59], v89 offset:61440\n"
  "v_mfma_f32_16x16x32_bf16 v[16:19], a[112:115], v[36:39], v[16:19]\n"
  "v_mul_f32 v82, v2, v10\n"
  "v_mul_f32 v83, v3, v11\n"
  "v_mfma_f32_16x16x32_bf16 v[16:19], a[128:131], v[40:43], v[16:19]\n"
  "ds_read_b128 v[60:63], v89 offset:62464\n"
  "v_add_f32 v80, 1.0, v80\n"
  "v_mfma_f32_16x16x32_bf16 v[16:19], a[144:147], v[44:47], v[16:19]\n"
  "v_add_f32 v81, 1.0, v81\n"
  "v_mfma_f32_16x16x32_bf16 v[16:19], a[160:163], v[48:51], v[16:19]\n"
  "buffer_load_dwordx4 v95, s[40:43], s60 offen offset:1024 lds\n"
  "v_mfma_f32_16x16x32_bf16 v[16:19], a[176:179], v[52:55], v[16:19]\n"
  "ds_read_b128 v[64:67], v89 offset:63488\n"
  "v_mfma_f32_16x16x32_bf16 v[20:23], a[100:103], v[32:35], v[20:23]\n"
  "v_rcp_f32 v80, v80\n"
  "v_mfma_f32_16x16x32_bf16 v[20:23], a[116:119], v[36:39], v[20:23]\n"
  "v_rcp_f32 v81, v81\n"
  "v_mfma_f32_16x16x32_bf16 v[20:23], a[132:135], v[40:43], v[20:23]\n"
  "ds_read_b128 v[68:71], v89 offset:64512\n"
  "v_mul_f32 v82, v82, v80\n"
  "v_mfma_f32_16x16x32_bf16 v[20:23], a[148:151], v[44:47], v[20:23]\n"
  "buffer_load_dwordx4 v96, s[40:43], s60 offen offset:2048 lds\n"
  "v_mul_f32 v83, v83, v81\n"
  "v_mfma_f32_16x16x32_bf16 v[20:23], a[164:167], v[48:51], v[20:23]\n"
  "s_add_u32 s60, s60, 36864\n"
  "v_cvt_pk_bf16_f32 v85, v82, v83\n"
  "v_mul_f32 v80, v4, v4\n"
  "v_mfma_f32_16x16x32_bf16 v[20:23], a[180:183], v[52:55], v[20:23]\n"
  "ds_read_b128 v[72:75], v91 offset:30720\n"
  "v_mul_f32 v81, v5, v5\n"
  "v_mfma_f32_16x16x32_bf16 v[24:27], a[104:107], v[32:35], v[24:27]\n"
  "v_fmaak_f32 v80, v88, v80, 0xc0135761\n"
  "v_fmaak_f32 v81, v88, v81, 0xc0135761\n"
  "v_mfma_f32_16x16x32_bf16 v[24:27], a[120:123], v[36:39], v[24:27]\n"
  "s_add_u32 m0, s48, 32768\n"
  "v_mul_f32 v80, v4, v80\n"
  "v_mul_f32 v81, v5, v81\n"
  "v_mfma_f32_16x16x32_bf16 v[24:27], a[136:139], v[40:43], v[24:27]\n"
  "buffer_load_dwordx4 v97, s[36:39], s59 offen offset:3072 sc1 lds\n"
  "ds_read_b128 v[76:79], v91 offset:31744\n"
  "v_mfma_f32_16x16x32_bf16 v[24:27], a[152:155], v[44:47], v[24:27]\n"
  "s_add_u32 s59, s59, 1024\n"
  "v_exp_f32 v80, v80\n"
  "v_mfma_f32_16x16x32_bf16 v[24:27], a[168:171], v[48:51], v[24:27]\n"
  "v_exp_f32 v81, v81\n"
  "v_mfma_f32_16x16x32_bf16 v[24:27], a[184:187], v[52:55], v[24:27]\n"
  "v_mul_f32 v82, v4, v12\n"
  "v_mul_f32 v83, v5, v13\n"
  "v_mfma_f32_16x16x32_bf16 v[28:31], a[108:111], v[32:35], v[28:31]\n"
  "v_add_f32 v80, 1.0, v80\n"
  "v_add_f32 v81, 1.0, v81\n"
  "v_mfma_f32_16x16x32_bf16 v[28:31], a[124:127], v[36:39], v[28:31]\n"
  "buffer_load_dwordx4 v94, s[40:43], s60 offen offset:0 lds\n"
  "v_mfma_f32_16x16x32_bf16 v[28:31], a[140:143], v[40:43], v[28:31]\n"
  "v_rcp_f32 v80, v80\n"
  "v_mfma_f32_16x16x32_bf16 v[28:31], a[156:159], v[44:47], v[28:31]\n"
  "v_rcp_f32 v81, v81\n"
  "v_mfma_f32_16x16x32_bf16 v[28:31], a[172:175], v[48:51], v[28:31]\n"
  "v_mul_f32 v82, v82, v80\n"
  "v_mul_f32 v83, v83, v81\n"
  "v_mfma_f32_16x16x32_bf16 v[28:31], a[188:191], v[52:55], v[28:31]\n"
  "s_waitcnt vmcnt(12) lgkmcnt(0)\n"
  "s_barrier\n"
  "v_mfma_f32_16x16x32_bf16 v[16:19], a[192:195], v[56:59], v[16:19]\n"
  "buffer_load_dwordx4 v95, s[40:43], s60 offen offset:1024 lds\n"
  "ds_read_b128 v[32:35], v90 offset:0\n"
  "v_mfma_f32_16x16x32_bf16 v[16:19], a[208:211], v[60:63], v[16:19]\n"
  "v_cvt_pk_bf16_f32 v86, v82, v83\n"
  "v_mul_f32 v80, v6, v6\n"
  "v_mfma_f32_16x16x32_bf16 v[16:19], a[224:227], v[64:67], v[16:19]\n"
  "ds_read_b128 v[36:39], v90 offset:1024\n"
  "v_mul_f32 v81, v7, v7\n"
  "v_mfma_f32_16x16x32_bf16 v[16:19], a[240:243], v[68:71], v[16:19]\n"
  "v_fmaak_f32 v80, v88, v80, 0xc0135761\n"
  "v_fmaak_f32 v81, v88, v81, 0xc0135761\n"
  "v_mfma_f32_16x16x32_bf16 v[16:19], v[224:227], v[72:75], v[16:19]\n"
  "v_mul_f32 v80, v6, v80\n"
  "v_mul_f32 v81, v7, v81\n"
  "v_mfma_f32_16x16x32_bf16 v[16:19], v[240:243], v[76:79], v[16:19]\n"
  "buffer_load_dwordx4 v96, s[40:43], s60 offen offset:2048 lds\n"
  "ds_read_b128 v[40:43], v90 offset:2048\n"
  "v_mfma_f32_16x16x32_bf16 v[20:23], a[196:199], v[56:59], v[20:23]\n"
  "s_add_u32 s60, s60, 36864\n"
  "v_exp_f32 v80, v80\n"
  "v_mfma_f32_16x16x32_bf16 v[20:23], a[212:215], v[60:63], v[20:23]\n"
  "v_exp_f32 v81, v81\n"
  "v_mfma_f32_16x16x32_bf16 v[20:23], a[228:231], v[64:67], v[20:23]\n"
  "ds_read_b128 v[44:47], v90 offset:3072\n"
  "v_mul_f32 v82, v6, v14\n"
  "v_mfma_f32_16x16x32_bf16 v[20:23], a[244:247], v[68:71], v[20:23]\n"
  "s_add_u32 m0, s48, 131072\n"
  "v_mul_f32 v83, v7, v15\n"
  "v_add_f32 v80, 1.0, v80\n"
  "v_mfma_f32_16x16x32_bf16 v[20:23], v[228:231], v[72:75], v[20:23]\n"
  "buffer_load_dwordx4 v92, s[36:39], s52 offen offset:0 sc1 lds\n"
  "v_add_f32 v81, 1.0, v81\n"
  "v_mfma_f32_16x16x32_bf16 v[20:23], v[244:247], v[76:79], v[20:23]\n"
  "ds_read_b128 v[48:51], v90 offset:4096\n"
  "v_mfma_f32_16x16x32_bf16 v[24:27], a[200:203], v[56:59], v[24:27]\n"
  "v_rcp_f32 v80, v80\n"
  "v_mfma_f32_16x16x32_bf16 v[24:27], a[216:219], v[60:63], v[24:27]\n"
  "v_rcp_f32 v81, v81\n"
  "v_mfma_f32_16x16x32_bf16 v[24:27], a[232:235], v[64:67], v[24:27]\n"
  "ds_read_b128 v[52:55], v90 offset:5120\n"
  "v_mul_f32 v82, v82, v80\n"
  "v_mfma_f32_16x16x32_bf16 v[24:27], a[248:251], v[68:71], v[24:27]\n"
  "buffer_load_dwordx4 v92, s[36:39], s52 offen offset:1024 sc1 lds\n"
  "v_mul_f32 v83, v83, v81\n"
  "v_mfma_f32_16x16x32_bf16 v[24:27], v[232:235], v[72:75], v[24:27]\n"
  "v_cvt_pk_bf16_f32 v87, v82, v83\n"
  "v_mfma_f32_16x16x32_bf16 v[24:27], v[248:251], v[76:79], v[24:27]\n"
  "v_mfma_f32_16x16x32_bf16 v[28:31], a[204:207], v[56:59], v[28:31]\n"
  "v_mfma_f32_16x16x32_bf16 v[28:31], a[220:223], v[60:63], v[28:31]\n"
  "v_mfma_f32_16x16x32_bf16 v[28:31], a[236:239], v[64:67], v[28:31]\n"
  "buffer_load_dwordx4 v92, s[36:39], s52 offen offset:2048 sc1 lds\n"
  "v_mfma_f32_16x16x32_bf16 v[28:31], a[252:255], v[68:71], v[28:31]\n"
  "buffer_store_dwordx4 v[84:87], v89, s[36:39], s55 offen sc1\n"
  "v_mfma_f32_16x16x32_bf16 v[28:31], v[236:239], v[72:75], v[28:31]\n"
  "s_add_u32 s55, s55, 147456\n"
  "v_mfma_f32_16x16x32_bf16 v[28:31], v[252:255], v[76:79], v[28:31]\n"
  "s_waitcnt lgkmcnt(0)\n"
  "v_mfma_f32_16x16x32_bf16 v[0:3], a[0:3], v[32:35], 0\n"
  "s_cmp_eq_u32 s55, s57\n"
  "s_cselect_b32 s55, s56, s55\n"
  "ds_read_b64_tr_b16 v[112:113], v99 offset:0\n"
  "v_mfma_f32_16x16x32_bf16 v[0:3], a[16:19], v[36:39], v[0:3]\n"
  "buffer_load_dwordx4 v92, s[36:39], s52 offen offset:3072 sc1 lds\n"
  "ds_read_b64_tr_b16 v[114:115], v99 offset:8192\n"
  "v_mfma_f32_16x16x32_bf16 v[0:3], a[32:35], v[40:43], v[0:3]\n"
  "v_mul_f32 v80, v16, v16\n"
  "v_mul_f32 v81, v17, v17\n"
  "v_mfma_f32_16x16x32_bf16 v[0:3], a[48:51], v[44:47], v[0:3]\n"
  "ds_read_b128 v[124:127], v89 offset:3072\n"
  "v_fmaak_f32 v80, v88, v80, 0xc0135761\n"
  "v_mfma_f32_16x16x32_bf16 v[0:3], a[64:67], v[48:51], v[0:3]\n"
  "s_waitcnt vmcnt(17)\n"
  "s_mov_b64 exec, 1\n"
  "buffer_atomic_add v104, off, s[24:27], s13 sc1\n"
  "s_mov_b64 exec, -1\n"
  "v_mfma_f32_16x16x32_bf16 v[0:3], a[80:83], v[52:55], v[0:3]\n"
  "s_add_u32 m0, s49, 139264\n"
  "ds_read_b128 v[128:131], v89 offset:7168\n"
  "v_fmaak_f32 v81, v88, v81, 0xc0135761\n"
  "v_mfma_f32_16x16x32_bf16 v[4:7], a[4:7], v[32:35], 0\n"
  "buffer_load_dwordx4 v93, s[36:39], s52 offen sc1 lds\n"
  "ds_read_b128 v[132:135], v89 offset:11264\n"
  "v_mfma_f32_16x16x32_bf16 v[4:7], a[20:23], v[36:39], v[4:7]\n"
  "s_add_u32 s52, s52, 18432\n"
  "v_mul_f32 v80, v16, v80\n"
  "v_mul_f32 v81, v17, v81\n"
  "v_mfma_f32_16x16x32_bf16 v[4:7], a[36:39], v[40:43], v[4:7]\n"
  "s_cmp_eq_u32 s52, s54\n"
  "s_cselect_b32 s52, s53, s52\n"
  "ds_read_b128 v[136:139], v89 offset:15360\n"
  "v_mfma_f32_16x16x32_bf16 v[4:7], a[52:55], v[44:47], v[4:7]\n"
  "v_exp_f32 v80, v80\n"
  "v_mfma_f32_16x16x32_bf16 v[4:7], a[68:71], v[48:51], v[4:7]\n"
  "s_add_u32 m0, s48, 16384\n"
  "ds_read_b64_tr_b16 v[116:117], v99 offset:1024\n"
  "v_mfma_f32_16x16x32_bf16 v[4:7], a[84:87], v[52:55], v[4:7]\n"
  "buffer_load_dwordx4 v97, s[36:39], s59 offen offset:3072 sc1 lds\n"
  "ds_read_b64_tr_b16 v[118:119], v99 offset:9216\n"
  "v_mfma_f32_16x16x32_bf16 v[8:11], a[8:11], v[32:35], 0\n"
  "s_add_u32 s59, s59, 1024\n"
  "v_exp_f32 v81, v81\n"
  "v_mfma_f32_16x16x32_bf16 v[8:11], a[24:27], v[36:39], v[8:11]\n"
  "ds_read_b64_tr_b16 v[120:121], v99 offset:2048\n"
  "v_mul_f32 v82, v16, v24\n"
  "v_mfma_f32_16x16x32_bf16 v[8:11], a[40:43], v[40:43], v[8:11]\n"
  "v_mul_f32 v83, v17, v25\n"
  "v_add_f32 v80, 1.0, v80\n"
  "v_mfma_f32_16x16x32_bf16 v[8:11], a[56:59], v[44:47], v[8:11]\n"
  "ds_read_b64_tr_b16 v[122:123], v99 offset:10240\n"
  "v_add_f32 v81, 1.0, v81\n"
  "v_mfma_f32_16x16x32_bf16 v[8:11], a[72:75], v[48:51], v[8:11]\n"
  "buffer_load_dwordx4 v94, s[40:43], s60 offen offset:0 lds\n"
  "v_mfma_f32_16x16x32_bf16 v[8:11], a[88:91], v[52:55], v[8:11]\n"
  "v_rcp_f32 v80, v80\n"
  "v_mfma_f32_16x16x32_bf16 v[12:15], a[12:15], v[32:35], 0\n"
  "v_rcp_f32 v81, v81\n"
  "v_mfma_f32_16x16x32_bf16 v[12:15], a[28:31], v[36:39], v[12:15]\n"
  "v_mul_f32 v82, v82, v80\n"
  "v_mul_f32 v83, v83, v81\n"
  "v_mfma_f32_16x16x32_bf16 v[12:15], a[44:47], v[40:43], v[12:15]\n"
  "v_cvt_pk_bf16_f32 v84, v82, v83\n"
  "v_mul_f32 v80, v18, v18\n"
  "v_mfma_f32_16x16x32_bf16 v[12:15], a[60:63], v[44:47], v[12:15]\n"
  "buffer_load_dwordx4 v95, s[40:43], s60 offen offset:1024 lds\n"
  "v_mul_f32 v81, v19, v19\n"
  "v_mfma_f32_16x16x32_bf16 v[12:15], a[76:79], v[48:51], v[12:15]\n"
  "v_fmaak_f32 v80, v88, v80, 0xc0135761\n"
  "v_fmaak_f32 v81, v88, v81, 0xc0135761\n"
  "v_mfma_f32_16x16x32_bf16 v[12:15], a[92:95], v[52:55], v[12:15]\n"
  "s_waitcnt lgkmcnt(0)\n"
  "v_mul_f32 v80, v18, v80\n"
  "v_mul_f32 v81, v19, v81\n"
  "v_mfma_f32_16x16x32_bf16 v[144:147], v[112:115], v[124:127], 0\n"
  "ds_read_b128 v[56:59], v90 offset:6144\n"
  "ds_read_b128 v[60:63], v90 offset:7168\n"
  "v_mfma_f32_16x16x32_bf16 v[148:151], v[112:115], v[128:131], 0\n"
  "ds_read_b128 v[64:67], v90 offset:8192\n"
  "v_mfma_f32_16x16x32_bf16 v[152:155], v[112:115], v[132:135], 0\n"
  "buffer_load_dwordx4 v96, s[40:43], s60 offen offset:2048 lds\n"
  "ds_read_b128 v[68:71], v90 offset:9216\n"
  "v_mfma_f32_16x16x32_bf16 v[156:159], v[112:115], v[136:139], 0\n"
  "s_add_u32 s60, s60, 36864\n"
  "ds_read_b128 v[72:75], v90 offset:10240\n"
  "v_mfma_f32_16x16x32_bf16 v[164:167], v[116:119], v[124:127], 0\n"
  "ds_read_b128 v[76:79], v90 offset:11264\n"
  "v_mfma_f32_16x16x32_bf16 v[168:171], v[116:119], v[128:131], 0\n"
  "v_exp_f32 v80, v80\n"
  "v_mfma_f32_16x16x32_bf16 v[172:175], v[116:119], v[132:135], 0\n"
  "s_add_u32 m0, s48, 49152\n"
  "v_exp_f32 v81, v81\n"
  "v_mfma_f32_16x16x32_bf16 v[176:179], v[116:119], v[136:139], 0\n"
  "buffer_load_dwordx4 v97, s[36:39], s59 offen offset:3072 sc1 lds\n"
  "v_mul_f32 v82, v18, v26\n"
  "v_mfma_f32_16x16x32_bf16 v[184:187], v[120:123], v[124:127], 0\n"
  "s_add_u32 s59, s59, 1024\n"
  "v_mul_f32 v83, v19, v27\n"
  "v_add_f32 v80, 1.0, v80\n"
  "v_mfma_f32_16x16x32_bf16 v[188:191], v[120:123], v[128:131], 0\n"
  "v_add_f32 v81, 1.0, v81\n"
  "v_mfma_f32_16x16x32_bf16 v[192:195], v[120:123], v[132:135], 0\n"
  "v_rcp_f32 v80, v80\n"
  "v_mfma_f32_16x16x32_bf16 v[196:199], v[120:123], v[136:139], 0\n"
  "s_waitcnt lgkmcnt(0)\n"
  "v_rcp_f32 v81, v81\n"
  "v_mfma_f32_16x16x32_bf16 v[0:3], a[96:99], v[56:59], v[0:3]\n"
  "buffer_load_dwordx4 v94, s[40:43], s60 offen offset:0 lds\n"
  "ds_read_b64_tr_b16 v[112:113], v100 offset:16384\n"
  "v_mfma_f32_16x16x32_bf16 v[0:3], a[112:115], v[60:63], v[0:3]\n"
  "ds_read_b64_tr_b16 v[114:115], v100 offset:24576\n"
  "v_mul_f32 v82, v82, v80\n"
  "v_mfma_f32_16x16x32_bf16 v[0:3], a[128:131], v[64:67], v[0:3]\n"
  "v_mul_f32 v83, v83, v81\n"
  "v_cvt_pk_bf16_f32 v85, v82, v83\n"
  "v_mfma_f32_16x16x32_bf16 v[0:3], a[144:147], v[68:71], v[0:3]\n"
  "ds_read_b128 v[124:127], v90 offset:19456\n"
  "v_mul_f32 v80, v20, v20\n"
  "v_mfma_f32_16x16x32_bf16 v[0:3], a[160:163], v[72:75], v[0:3]\n"
  "v_mul_f32 v81, v21, v21\n"
  "v_fmaak_f32 v80, v88, v80, 0xc0135761\n"
  "v_mfma_f32_16x16x32_bf16 v[0:3], a[176:179], v[76:79], v[0:3]\n"
  "buffer_load_dwordx4 v95, s[40:43], s60 offen offset:1024 lds\n"
  "ds_read_b128 v[128:131], v90 offset:23552\n"
  "v_mfma_f32_16x16x32_bf16 v[4:7], a[100:103], v[56:59], v[4:7]\n"
  "ds_read_b128 v[132:135], v90 offset:27648\n"
  "v_fmaak_f32 v81, v88, v81, 0xc0135761\n"
  "v_mfma_f32_16x16x32_bf16 v[4:7], a[116:119], v[60:63], v[4:7]\n"
  "v_mul_f32 v80, v20, v80\n"
  "v_mul_f32 v81, v21, v81\n"
  "v_mfma_f32_16x16x32_bf16 v[4:7], a[132:135], v[64:67], v[4:7]\n"
  "ds_read_b128 v[136:139], v90 offset:31744\n"
  "v_mfma_f32_16x16x32_bf16 v[4:7], a[148:151], v[68:71], v[4:7]\n"
  "v_exp_f32 v80, v80\n"
  "v_mfma_f32_16x16x32_bf16 v[4:7], a[164:167], v[72:75], v[4:7]\n"
  "buffer_load_dwordx4 v96, s[40:43], s60 offen offset:2048 lds\n"
  "ds_read_b64_tr_b16 v[116:117], v100 offset:17408\n"
  "v_mfma_f32_16x16x32_bf16 v[4:7], a[180:183], v[76:79], v[4:7]\n"
  "s_add_u32 s60, s60, 36864\n"
  "ds_read_b64_tr_b16 v[118:119], v100 offset:25600\n"
  "v_mfma_f32_16x16x32_bf16 v[8:11], a[104:107], v[56:59], v[8:11]\n"
  "v_exp_f32 v81, v81\n"
  "v_mfma_f32_16x16x32_bf16 v[8:11], a[120:123], v[60:63], v[8:11]\n"
  "ds_read_b64_tr_b16 v[120:121], v100 offset:18432\n"
  "v_mul_f32 v82, v20, v28\n"
  "v_mfma_f32_16x16x32_bf16 v[8:11], a[136:139], v[64:67], v[8:11]\n"
  "v_mul_f32 v83, v21, v29\n"
  "v_add_f32 v80, 1.0, v80\n"
  "v_mfma_f32_16x16x32_bf16 v[8:11], a[152:155], v[68:71], v[8:11]\n"
  "ds_read_b64_tr_b16 v[122:123], v100 offset:26624\n"
  "v_add_f32 v81, 1.0, v81\n"
  "v_mfma_f32_16x16x32_bf16 v[8:11], a[168:171], v[72:75], v[8:11]\n"
  "v_rcp_f32 v80, v80\n"
  "v_mfma_f32_16x16x32_bf16 v[8:11], a[184:187], v[76:79], v[8:11]\n"
  "v_rcp_f32 v81, v81\n"
  "v_mfma_f32_16x16x32_bf16 v[12:15], a[108:111], v[56:59], v[12:15]\n"
  "v_mul_f32 v82, v82, v80\n"
  "v_mul_f32 v83, v83, v81\n"
  "v_mfma_f32_16x16x32_bf16 v[12:15], a[124:127], v[60:63], v[12:15]\n"
  "v_cvt_pk_bf16_f32 v86, v82, v83\n"
  "v_mul_f32 v80, v22, v22\n"
  "v_mfma_f32_16x16x32_bf16 v[12:15], a[140:143], v[64:67], v[12:15]\n"
  "s_waitcnt vmcnt(35)\n"
  "s_mov_b64 exec, 1\n"
  "buffer_atomic_add v104, off, s[24:27], s58 sc1\n"
  "s_mov_b64 exec, -1\n"
  "v_mfma_f32_16x16x32_bf16 v[12:15], a[156:159], v[68:71], v[12:15]\n"
  "v_mul_f32 v81, v23, v23\n"
  "v_fmaak_f32 v80, v88, v80, 0xc0135761\n"
  "v_mfma_f32_16x16x32_bf16 v[12:15], a[172:175], v[72:75], v[12:15]\n"
  "v_fmaak_f32 v81, v88, v81, 0xc0135761\n"
  "v_mul_f32 v80, v22, v80\n"
  "v_mfma_f32_16x16x32_bf16 v[12:15], a[188:191], v[76:79], v[12:15]\n"
  "s_waitcnt lgkmcnt(0)\n"
  "v_mul_f32 v81, v23, v81\n"
  "v_mfma_f32_16x16x32_bf16 v[144:147], v[112:115], v[124:127], v[144:147]\n"
  "ds_read_b128 v[32:35], v90 offset:12288\n"
  "ds_read_b128 v[36:39], v90 offset:13312\n"
  "v_mfma_f32_16x16x32_bf16 v[148:151], v[112:115], v[128:131], v[148:151]\n"
  "ds_read_b128 v[40:43], v90 offset:14336\n"
  "v_mfma_f32_16x16x32_bf16 v[152:155], v[112:115], v[132:135], v[152:155]\n"
  "ds_read_b128 v[44:47], v90 offset:15360\n"
  "v_mfma_f32_16x16x32_bf16 v[156:159], v[112:115], v[136:139], v[156:159]\n"
  "ds_read_b128 v[48:51], v91 offset:28672\n"
  "v_mfma_f32_16x16x32_bf16 v[164:167], v[116:119], v[124:127], v[164:167]\n"
  "ds_read_b128 v[52:55], v91 offset:29696\n"
  "v_mfma_f32_16x16x32_bf16 v[168:171], v[116:119], v[128:131], v[168:171]\n"
  "v_exp_f32 v80, v80\n"
  "v_mfma_f32_16x16x32_bf16 v[172:175], v[116:119], v[132:135], v[172:175]\n"
  "v_exp_f32 v81, v81\n"
  "v_mfma_f32_16x16x32_bf16 v[176:179], v[116:119], v[136:139], v[176:179]\n"
  "v_mul_f32 v82, v22, v30\n"
  "v_mul_f32 v83, v23, v31\n"
  "v_mfma_f32_16x16x32_bf16 v[184:187], v[120:123], v[124:127], v[184:187]\n"
  "v_add_f32 v80, 1.0, v80\n"
  "v_add_f32 v81, 1.0, v81\n"
  "v_mfma_f32_16x16x32_bf16 v[188:191], v[120:123], v[128:131], v[188:191]\n"
  "v_rcp_f32 v80, v80\n"
  "v_mfma_f32_16x16x32_bf16 v[192:195], v[120:123], v[132:135], v[192:195]\n"
  "v_rcp_f32 v81, v81\n"
  "v_mfma_f32_16x16x32_bf16 v[196:199], v[120:123], v[136:139], v[196:199]\n"
  "s_waitcnt vmcnt(16) lgkmcnt(0)\n"
  "s_barrier\n"
  "v_mfma_f32_16x16x32_bf16 v[0:3], a[192:195], v[32:35], v[0:3]\n"
  "s_add_u32 m0, s48, 0\n"
  "ds_read_b128 v[56:59], v90 offset:32768\n"
  "v_mul_f32 v82, v82, v80\n"
  "v_mfma_f32_16x16x32_bf16 v[0:3], a[208:211], v[36:39], v[0:3]\n"
  "buffer_load_dwordx4 v92, s[36:39], s52 offen offset:0 sc1 lds\n"
  "v_mul_f32 v83, v83, v81\n"
  "v_mfma_f32_16x16x32_bf16 v[0:3], a[224:227], v[40:43], v[0:3]\n"
  "ds_read_b128 v[60:63], v90 offset:33792\n"
  "v_cvt_pk_bf16_f32 v87, v82, v83\n"
  "v_mfma_f32_16x16x32_bf16 v[0:3], a[240:243], v[44:47], v[0:3]\n"
  "v_mfma_f32_16x16x32_bf16 v[0:3], v[224:227], v[48:51], v[0:3]\n"
  "v_mfma_f32_16x16x32_bf16 v[0:3], v[240:243], v[52:55], v[0:3]\n"
  "ds_read_b128 v[64:67], v90 offset:34816\n"
  "v_mfma_f32_16x16x32_bf16 v[4:7], a[196:199], v[32:35], v[4:7]\n"
  "buffer_load_dwordx4 v92, s[36:39], s52 offen offset:1024 sc1 lds\n"
  "v_mfma_f32_16x16x32_bf16 v[4:7], a[212:215], v[36:39], v[4:7]\n"
  "v_mfma_f32_16x16x32_bf16 v[4:7], a[228:231], v[40:43], v[4:7]\n"
  "ds_read_b128 v[68:71], v90 offset:35840\n"
  "v_mfma_f32_16x16x32_bf16 v[4:7], a[244:247], v[44:47], v[4:7]\n"
  "v_mfma_f32_16x16x32_bf16 v[4:7], v[228:231], v[48:51], v[4:7]\n"
  "v_mfma_f32_16x16x32_bf16 v[4:7], v[244:247], v[52:55], v[4:7]\n"
  "buffer_load_dwordx4 v92, s[36:39], s52 offen offset:2048 sc1 lds\n"
  "ds_read_b128 v[72:75], v90 offset:36864\n"
  "v_mfma_f32_16x16x32_bf16 v[8:11], a[200:203], v[32:35], v[8:11]\n"
  "v_mfma_f32_16x16x32_bf16 v[8:11], a[216:219], v[36:39], v[8:11]\n"
  "v_mfma_f32_16x16x32_bf16 v[8:11], a[232:235], v[40:43], v[8:11]\n"
  "ds_read_b128 v[76:79], v90 offset:37888\n"
  "v_mfma_f32_16x16x32_bf16 v[8:11], a[248:251], v[44:47], v[8:11]\n"
  "v_mfma_f32_16x16x32_bf16 v[8:11], v[232:235], v[48:51], v[8:11]\n"
  "buffer_load_dwordx4 v92, s[36:39], s52 offen offset:3072 sc1 lds\n"
  "v_mfma_f32_16x16x32_bf16 v[8:11], v[248:251], v[52:55], v[8:11]\n"
  "v_mfma_f32_16x16x32_bf16 v[12:15], a[204:207], v[32:35], v[12:15]\n"
  "v_mfma_f32_16x16x32_bf16 v[12:15], a[220:223], v[36:39], v[12:15]\n"
  "v_mfma_f32_16x16x32_bf16 v[12:15], a[236:239], v[40:43], v[12:15]\n"
  "s_add_u32 m0, s49, 137216\n"
  "v_mfma_f32_16x16x32_bf16 v[12:15], a[252:255], v[44:47], v[12:15]\n"
  "buffer_load_dwordx4 v93, s[36:39], s52 offen sc1 lds\n"
  "v_mfma_f32_16x16x32_bf16 v[12:15], v[236:239], v[48:51], v[12:15]\n"
  "s_add_u32 s52, s52, 18432\n"
  "buffer_store_dwordx4 v[84:87], v89, s[36:39], s55 offen sc1\n"
  "v_mfma_f32_16x16x32_bf16 v[12:15], v[252:255], v[52:55], v[12:15]\n"
  "s_waitcnt lgkmcnt(0)\n"
  "s_add_u32 s55, s55, 147456\n"
  "v_mfma_f32_16x16x32_bf16 v[16:19], a[0:3], v[56:59], 0\n"
  "s_cmp_eq_u32 s52, s54\n"
  "s_cselect_b32 s52, s53, s52\n"
  "ds_read_b64_tr_b16 v[112:113], v100 offset:49152\n"
  "v_mfma_f32_16x16x32_bf16 v[16:19], a[16:19], v[60:63], v[16:19]\n"
  "s_add_u32 m0, s48, 65536\n"
  "s_cmp_eq_u32 s55, s57\n"
  "s_cselect_b32 s55, s56, s55\n"
  "ds_read_b64_tr_b16 v[114:115], v100 offset:57344\n"
  "v_mfma_f32_16x16x32_bf16 v[16:19], a[32:35], v[64:67], v[16:19]\n"
  "buffer_load_dwordx4 v97, s[36:39], s59 offen offset:3072 sc1 lds\n"
  "v_mul_f32 v80, v0, v0\n"
  "v_mfma_f32_16x16x32_bf16 v[16:19], a[48:51], v[68:71], v[16:19]\n"
  "s_add_u32 s59, s59, 1024\n"
  "ds_read_b128 v[124:127], v90 offset:52224\n"
  "v_mul_f32 v81, v1, v1\n"
  "v_mfma_f32_16x16x32_bf16 v[16:19], a[64:67], v[72:75], v[16:19]\n"
  "v_fmaak_f32 v80, v88, v80, 0xc0135761\n"
  "v_fmaak_f32 v81, v88, v81, 0xc0135761\n"
  "v_mfma_f32_16x16x32_bf16 v[16:19], a[80:83], v[76:79], v[16:19]\n"
  "ds_read_b128 v[128:131], v90 offset:56320\n"
  "v_mul_f32 v80, v0, v80\n"
  "v_mfma_f32_16x16x32_bf16 v[20:23], a[4:7], v[56:59], 0\n"
  "ds_read_b128 v[132:135], v90 offset:60416\n"
  "v_mul_f32 v81, v1, v81\n"
  "v_mfma_f32_16x16x32_bf16 v[20:23], a[20:23], v[60:63], v[20:23]\n"
  "buffer_load_dwordx4 v94, s[40:43], s60 offen offset:0 lds\n"
  "v_mfma_f32_16x16x32_bf16 v[20:23], a[36:39], v[64:67], v[20:23]\n"
  "ds_read_b128 v[136:139], v90 offset:64512\n"
  "v_mfma_f32_16x16x32_bf16 v[20:23], a[52:55], v[68:71], v[20:23]\n"
  "v_exp_f32 v80, v80\n"
  "v_mfma_f32_16x16x32_bf16 v[20:23], a[68:71], v[72:75], v[20:23]\n"
  "ds_read_b64_tr_b16 v[116:117], v100 offset:50176\n"
  "v_mfma_f32_16x16x32_bf16 v[20:23], a[84:87], v[76:79], v[20:23]\n"
  "ds_read_b64_tr_b16 v[118:119], v100 offset:58368\n"
  "v_mfma_f32_16x16x32_bf16 v[24:27], a[8:11], v[56:59], 0\n"
  "buffer_load_dwordx4 v95, s[40:43], s60 offen offset:1024 lds\n"
  "v_mfma_f32_16x16x32_bf16 v[24:27], a[24:27], v[60:63], v[24:27]\n"
  "ds_read_b64_tr_b16 v[120:121], v100 offset:51200\n"
  "v_mfma_f32_16x16x32_bf16 v[24:27], a[40:43], v[64:67], v[24:27]\n"
  "v_exp_f32 v81, v81\n"
  "v_mfma_f32_16x16x32_bf16 v[24:27], a[56:59], v[68:71], v[24:27]\n"
  "ds_read_b64_tr_b16 v[122:123], v100 offset:59392\n"
  "v_mul_f32 v82, v0, v8\n"
  "v_mfma_f32_16x16x32_bf16 v[24:27], a[72:75], v[72:75], v[24:27]\n"
  "v_mul_f32 v83, v1, v9\n"
  "v_add_f32 v80, 1.0, v80\n"
  "v_mfma_f32_16x16x32_bf16 v[24:27], a[88:91], v[76:79], v[24:27]\n"
  "buffer_load_dwordx4 v96, s[40:43], s60 offen offset:2048 lds\n"
  "v_add_f32 v81, 1.0, v81\n"
  "v_mfma_f32_16x16x32_bf16 v[28:31], a[12:15], v[56:59], 0\n"
  "s_cbranch_execz Ltramp%=\n"
  "s_add_u32 s60, s60, 36864\n"
  "v_rcp_f32 v80, v80\n"
  "v_mfma_f32_16x16x32_bf16 v[28:31], a[28:31], v[60:63], v[28:31]\n"
  "v_rcp_f32 v81, v81\n"
  "v_mfma_f32_16x16x32_bf16 v[28:31], a[44:47], v[64:67], v[28:31]\n"
  "v_mul_f32 v82, v82, v80\n"
  "v_mul_f32 v83, v83, v81\n"
  "v_mfma_f32_16x16x32_bf16 v[28:31], a[60:63], v[68:71], v[28:31]\n"
  "s_add_u32 m0, s48, 81920\n"
  "v_cvt_pk_bf16_f32 v84, v82, v83\n"
  "v_mul_f32 v80, v2, v2\n"
  "v_mfma_f32_16x16x32_bf16 v[28:31], a[76:79], v[72:75], v[28:31]\n"
  "buffer_load_dwordx4 v97, s[36:39], s59 offen offset:3072 sc1 lds\n"
  "v_mul_f32 v81, v3, v3\n"
  "v_mfma_f32_16x16x32_bf16 v[28:31], a[92:95], v[76:79], v[28:31]\n"
  "s_waitcnt lgkmcnt(0)\n"
  "v_fmaak_f32 v80, v88, v80, 0xc0135761\n"
  "v_fmaak_f32 v81, v88, v81, 0xc0135761\n"
  "v_mfma_f32_16x16x32_bf16 v[144:147], v[112:115], v[124:127], v[144:147]\n"
  "s_add_u32 s59, s59, 1024\n"
  "ds_read_b128 v[32:35], v90 offset:38912\n"
  "ds_read_b128 v[36:39], v90 offset:39936\n"
  "v_mfma_f32_16x16x32_bf16 v[148:151], v[112:115], v[128:131], v[148:151]\n"
  "ds_read_b128 v[40:43], v90 offset:40960\n"
  "v_mul_f32 v80, v2, v80\n"
  "v_mfma_f32_16x16x32_bf16 v[152:155], v[112:115], v[132:135], v[152:155]\n"
  "ds_read_b128 v[44:47], v90 offset:41984\n"
  "v_mul_f32 v81, v3, v81\n"
  "v_mfma_f32_16x16x32_bf16 v[156:159], v[112:115], v[136:139], v[156:159]\n"
  "buffer_load_dwordx4 v94, s[40:43], s60 offen offset:0 lds\n"
  "ds_read_b128 v[48:51], v90 offset:43008\n"
  "v_mfma_f32_16x16x32_bf16 v[164:167], v[116:119], v[124:127], v[164:167]\n"
  "ds_read_b128 v[52:55], v90 offset:44032\n"
  "v_mfma_f32_16x16x32_bf16 v[168:171], v[116:119], v[128:131], v[168:171]\n"
  "v_exp_f32 v80, v80\n"
  "v_mfma_f32_16x16x32_bf16 v[172:175], v[116:119], v[132:135], v[172:175]\n"
  "v_exp_f32 v81, v81\n"
  "v_mfma_f32_16x16x32_bf16 v[176:179], v[116:119], v[136:139], v[176:179]\n"
  "v_mul_f32 v82, v2, v10\n"
  "v_mul_f32 v83, v3, v11\n"
  "v_mfma_f32_16x16x32_bf16 v[184:187], v[120:123], v[124:127], v[184:187]\n"
  "buffer_load_dwordx4 v95, s[40:43], s60 offen offset:1024 lds\n"
  "v_add_f32 v80, 1.0, v80\n"
  "v_mfma_f32_16x16x32_bf16 v[188:191], v[120:123], v[128:131], v[188:191]\n"
  "v_add_f32 v81, 1.0, v81\n"
  "v_mfma_f32_16x16x32_bf16 v[192:195], v[120:123], v[132:135], v[192:195]\n"
  "v_rcp_f32 v80, v80\n"
  "v_mfma_f32_16x16x32_bf16 v[196:199], v[120:123], v[136:139], v[196:199]\n"
  "s_waitcnt lgkmcnt(0)\n"
  "v_rcp_f32 v81, v81\n"
  "v_mfma_f32_16x16x32_bf16 v[16:19], a[96:99], v[32:35], v[16:19]\n"
  "ds_read_b64_tr_b16 v[112:113], v99 offset:32768\n"
  "v_mul_f32 v82, v82, v80\n"
  "v_mfma_f32_16x16x32_bf16 v[16:19], a[112:115], v[36:39], v[16:19]\n"
  "buffer_load_dwordx4 v96, s[40:43], s60 offen offset:2048 lds\n"
  "ds_read_b64_tr_b16 v[114:115], v99 offset:40960\n"
  "v_mfma_f32_16x16x32_bf16 v[16:19], a[128:131], v[40:43], v[16:19]\n"
  "s_add_u32 s60, s60, 36864\n"
  "v_mul_f32 v83, v83, v81\n"
  "v_cvt_pk_bf16_f32 v85, v82, v83\n"
  "v_mfma_f32_16x16x32_bf16 v[16:19], a[144:147], v[44:47], v[16:19]\n"
  "ds_read_b128 v[124:127], v89 offset:35840\n"
  "v_mul_f32 v80, v4, v4\n"
  "v_mfma_f32_16x16x32_bf16 v[16:19], a[160:163], v[48:51], v[16:19]\n"
  "v_mul_f32 v81, v5, v5\n"
  "v_fmaak_f32 v80, v88, v80, 0xc0135761\n"
  "v_mfma_f32_16x16x32_bf16 v[16:19], a[176:179], v[52:55], v[16:19]\n"
  "ds_read_b128 v[128:131], v89 offset:39936\n"
  "v_fmaak_f32 v81, v88, v81, 0xc0135761\n"
  "v_mfma_f32_16x16x32_bf16 v[20:23], a[100:103], v[32:35], v[20:23]\n"
  "ds_read_b128 v[132:135], v89 offset:44032\n"
  "v_mul_f32 v80, v4, v80\n"
  "v_mfma_f32_16x16x32_bf16 v[20:23], a[116:119], v[36:39], v[20:23]\n"
  "v_mul_f32 v81, v5, v81\n"
  "v_mfma_f32_16x16x32_bf16 v[20:23], a[132:135], v[40:43], v[20:23]\n"
  "ds_read_b128 v[136:139], v89 offset:48128\n"
  "v_mfma_f32_16x16x32_bf16 v[20:23], a[148:151], v[44:47], v[20:23]\n"
  "v_exp_f32 v80, v80\n"
  "v_mfma_f32_16x16x32_bf16 v[20:23], a[164:167], v[48:51], v[20:23]\n"
  "ds_read_b64_tr_b16 v[116:117], v99 offset:33792\n"
  "v_mfma_f32_16x16x32_bf16 v[20:23], a[180:183], v[52:55], v[20:23]\n"
  "ds_read_b64_tr_b16 v[118:119], v99 offset:41984\n"
  "v_mfma_f32_16x16x32_bf16 v[24:27], a[104:107], v[32:35], v[24:27]\n"
  "v_exp_f32 v81, v81\n"
  "v_mfma_f32_16x16x32_bf16 v[24:27], a[120:123], v[36:39], v[24:27]\n"
  "ds_read_b64_tr_b16 v[120:121], v99 offset:34816\n"
  "v_mul_f32 v82, v4, v12\n"
  "v_mfma_f32_16x16x32_bf16 v[24:27], a[136:139], v[40:43], v[24:27]\n"
  "v_mul_f32 v83, v5, v13\n"
  "v_add_f32 v80, 1.0, v80\n"
  "v_mfma_f32_16x16x32_bf16 v[24:27], a[152:155], v[44:47], v[24:27]\n"
  "ds_read_b64_tr_b16 v[122:123], v99 offset:43008\n"
  "v_add_f32 v81, 1.0, v81\n"
  "v_mfma_f32_16x16x32_bf16 v[24:27], a[168:171], v[48:51], v[24:27]\n"
  "v_rcp_f32 v80, v80\n"
  "v_mfma_f32_16x16x32_bf16 v[24:27], a[184:187], v[52:55], v[24:27]\n"
  "v_rcp_f32 v81, v81\n"
  "v_mfma_f32_16x16x32_bf16 v[28:31], a[108:111], v[32:35], v[28:31]\n"
  "v_mul_f32 v82, v82, v80\n"
  "v_mul_f32 v83, v83, v81\n"
  "v_mfma_f32_16x16x32_bf16 v[28:31], a[124:127], v[36:39], v[28:31]\n"
  "v_cvt_pk_bf16_f32 v86, v82, v83\n"
  "v_mul_f32 v80, v6, v6\n"
  "v_mfma_f32_16x16x32_bf16 v[28:31], a[140:143], v[40:43], v[28:31]\n"
  "v_mul_f32 v81, v7, v7\n"
  "v_fmaak_f32 v80, v88, v80, 0xc0135761\n"
  "v_mfma_f32_16x16x32_bf16 v[28:31], a[156:159], v[44:47], v[28:31]\n"
  "v_fmaak_f32 v81, v88, v81, 0xc0135761\n"
  "v_mul_f32 v80, v6, v80\n"
  "v_mfma_f32_16x16x32_bf16 v[28:31], a[172:175], v[48:51], v[28:31]\n"
  "v_mul_f32 v81, v7, v81\n"
  "v_mfma_f32_16x16x32_bf16 v[28:31], a[188:191], v[52:55], v[28:31]\n"
  "s_waitcnt lgkmcnt(0)\n"
  "v_exp_f32 v80, v80\n"
  "v_mfma_f32_16x16x32_bf16 v[144:147], v[112:115], v[124:127], v[144:147]\n"
  "ds_read_b128 v[56:59], v90 offset:45056\n"
  "ds_read_b128 v[60:63], v90 offset:46080\n"
  "v_mfma_f32_16x16x32_bf16 v[148:151], v[112:115], v[128:131], v[148:151]\n"
  "ds_read_b128 v[64:67], v90 offset:47104\n"
  "v_mfma_f32_16x16x32_bf16 v[152:155], v[112:115], v[132:135], v[152:155]\n"
  "ds_read_b128 v[68:71], v90 offset:48128\n"
  "v_mfma_f32_16x16x32_bf16 v[156:159], v[112:115], v[136:139], v[156:159]\n"
  "ds_read_b128 v[72:75], v91 offset:26624\n"
  "v_mfma_f32_16x16x32_bf16 v[164:167], v[116:119], v[124:127], v[164:167]\n"
  "ds_read_b128 v[76:79], v91 offset:27648\n"
  "v_mfma_f32_16x16x32_bf16 v[168:171], v[116:119], v[128:131], v[168:171]\n"
  "v_exp_f32 v81, v81\n"
  "v_mfma_f32_16x16x32_bf16 v[172:175], v[116:119], v[132:135], v[172:175]\n"
  "v_mul_f32 v82, v6, v14\n"
  "v_mul_f32 v83, v7, v15\n"
  "v_mfma_f32_16x16x32_bf16 v[176:179], v[116:119], v[136:139], v[176:179]\n"
  "v_add_f32 v80, 1.0, v80\n"
  "v_add_f32 v81, 1.0, v81\n"
  "v_mfma_f32_16x16x32_bf16 v[184:187], v[120:123], v[124:127], v[184:187]\n"
  "v_rcp_f32 v80, v80\n"
  "v_mfma_f32_16x16x32_bf16 v[188:191], v[120:123], v[128:131], v[188:191]\n"
  "v_rcp_f32 v81, v81\n"
  "v_mfma_f32_16x16x32_bf16 v[192:195], v[120:123], v[132:135], v[192:195]\n"
  "v_mul_f32 v82, v82, v80\n"
  "v_mul_f32 v83, v83, v81\n"
  "v_mfma_f32_16x16x32_bf16 v[196:199], v[120:123], v[136:139], v[196:199]\n"
  "s_waitcnt vmcnt(15) lgkmcnt(0)\n"
  "s_barrier\n"
  "v_mfma_f32_16x16x32_bf16 v[16:19], a[192:195], v[56:59], v[16:19]\n"
  "s_add_u32 m0, s48, 32768\n"
  "ds_read_b128 v[32:35], v91 offset:0\n"
  "v_cvt_pk_bf16_f32 v87, v82, v83\n"
  "v_mfma_f32_16x16x32_bf16 v[16:19], a[208:211], v[60:63], v[16:19]\n"
  "buffer_load_dwordx4 v92, s[36:39], s52 offen offset:0 sc1 lds\n"
  "v_mfma_f32_16x16x32_bf16 v[16:19], a[224:227], v[64:67], v[16:19]\n"
  "ds_read_b128 v[36:39], v91 offset:1024\n"
  "v_mfma_f32_16x16x32_bf16 v[16:19], a[240:243], v[68:71], v[16:19]\n"
  "v_mfma_f32_16x16x32_bf16 v[16:19], v[224:227], v[72:75], v[16:19]\n"
  "v_mfma_f32_16x16x32_bf16 v[16:19], v[240:243], v[76:79], v[16:19]\n"
  "ds_read_b128 v[40:43], v91 offset:2048\n"
  "v_mfma_f32_16x16x32_bf16 v[20:23], a[196:199], v[56:59], v[20:23]\n"
  "buffer_load_dwordx4 v92, s[36:39], s52 offen offset:1024 sc1 lds\n"
  "v_mfma_f32_16x16x32_bf16 v[20:23], a[212:215], v[60:63], v[20:23]\n"
  "v_mfma_f32_16x16x32_bf16 v[20:23], a[228:231], v[64:67], v[20:23]\n"
  "ds_read_b128 v[44:47], v91 offset:3072\n"
  "v_mfma_f32_16x16x32_bf16 v[20:23], a[244:247], v[68:71], v[20:23]\n"
  "v_mfma_f32_16x16x32_bf16 v[20:23], v[228:231], v[72:75], v[20:23]\n"
  "v_mfma_f32_16x16x32_bf16 v[20:23], v[244:247], v[76:79], v[20:23]\n"
  "buffer_load_dwordx4 v92, s[36:39], s52 offen offset:2048 sc1 lds\n"
  "ds_read_b128 v[48:51], v91 offset:4096\n"
  "v_mfma_f32_16x16x32_bf16 v[24:27], a[200:203], v[56:59], v[24:27]\n"
  "v_mfma_f32_16x16x32_bf16 v[24:27], a[216:219], v[60:63], v[24:27]\n"
  "v_mfma_f32_16x16x32_bf16 v[24:27], a[232:235], v[64:67], v[24:27]\n"
  "ds_read_b128 v[52:55], v91 offset:5120\n"
  "v_mfma_f32_16x16x32_bf16 v[24:27], a[248:251], v[68:71], v[24:27]\n"
  "v_mfma_f32_16x16x32_bf16 v[24:27], v[232:235], v[72:75], v[24:27]\n"
  "buffer_load_dwordx4 v92, s[36:39], s52 offen offset:3072 sc1 lds\n"
  "v_mfma_f32_16x16x32_bf16 v[24:27], v[248:251], v[76:79], v[24:27]\n"
  "v_mfma_f32_16x16x32_bf16 v[28:31], a[204:207], v[56:59], v[28:31]\n"
  "v_mfma_f32_16x16x32_bf16 v[28:31], a[220:223], v[60:63], v[28:31]\n"
  "v_mfma_f32_16x16x32_bf16 v[28:31], a[236:239], v[64:67], v[28:31]\n"
  "s_add_u32 m0, s49, 135168\n"
  "v_mfma_f32_16x16x32_bf16 v[28:31], a[252:255], v[68:71], v[28:31]\n"
  "buffer_load_dwordx4 v93, s[36:39], s52 offen sc1 lds\n"
  "v_mfma_f32_16x16x32_bf16 v[28:31], v[236:239], v[72:75], v[28:31]\n"
  "s_add_u32 s52, s52, 18432\n"
  "buffer_store_dwordx4 v[84:87], v89, s[36:39], s55 offen sc1\n"
  "v_mfma_f32_16x16x32_bf16 v[28:31], v[252:255], v[76:79], v[28:31]\n"
  "s_waitcnt lgkmcnt(0)\n"
  "s_add_u32 s55, s55, 147456\n"
  "v_mfma_f32_16x16x32_bf16 v[0:3], a[0:3], v[32:35], 0\n"
  "s_cmp_eq_u32 s52, s54\n"
  "s_cselect_b32 s52, s53, s52\n"
  "ds_read_b64_tr_b16 v[112:113], v99 offset:16384\n"
  "v_mfma_f32_16x16x32_bf16 v[0:3], a[16:19], v[36:39], v[0:3]\n"
  "s_add_u32 m0, s48, 98304\n"
  "s_cmp_eq_u32 s55, s57\n"
  "s_cselect_b32 s55, s56, s55\n"
  "ds_read_b64_tr_b16 v[114:115], v99 offset:24576\n"
  "v_mfma_f32_16x16x32_bf16 v[0:3], a[32:35], v[40:43], v[0:3]\n"
  "buffer_load_dwordx4 v97, s[36:39], s59 offen offset:3072 sc1 lds\n"
  "v_mul_f32 v80, v16, v16\n"
  "v_mfma_f32_16x16x32_bf16 v[0:3], a[48:51], v[44:47], v[0:3]\n"
  "s_add_u32 s59, s59, 1024\n"
  "ds_read_b128 v[124:127], v89 offset:19456\n"
  "v_mul_f32 v81, v17, v17\n"
  "v_mfma_f32_16x16x32_bf16 v[0:3], a[64:67], v[48:51], v[0:3]\n"
  "v_fmaak_f32 v80, v88, v80, 0xc0135761\n"
  "v_fmaak_f32 v81, v88, v81, 0xc0135761\n"
  "v_mfma_f32_16x16x32_bf16 v[0:3], a[80:83], v[52:55], v[0:3]\n"
  "ds_read_b128 v[128:131], v89 offset:23552\n"
  "v_mul_f32 v80, v16, v80\n"
  "v_mfma_f32_16x16x32_bf16 v[4:7], a[4:7], v[32:35], 0\n"
  "ds_read_b128 v[132:135], v89 offset:27648\n"
  "v_mul_f32 v81, v17, v81\n"
  "v_mfma_f32_16x16x32_bf16 v[4:7], a[20:23], v[36:39], v[4:7]\n"
  "buffer_load_dwordx4 v94, s[40:43], s60 offen offset:0 lds\n"
  "v_mfma_f32_16x16x32_bf16 v[4:7], a[36:39], v[40:43], v[4:7]\n"
  "ds_read_b128 v[136:139], v89 offset:31744\n"
  "v_mfma_f32_16x16x32_bf16 v[4:7], a[52:55], v[44:47], v[4:7]\n"
  "v_exp_f32 v80, v80\n"
  "v_mfma_f32_16x16x32_bf16 v[4:7], a[68:71], v[48:51], v[4:7]\n"
  "ds_read_b64_tr_b16 v[116:117], v99 offset:17408\n"
  "v_mfma_f32_16x16x32_bf16 v[4:7], a[84:87], v[52:55], v[4:7]\n"
  "ds_read_b64_tr_b16 v[118:119], v99 offset:25600\n"
  "v_mfma_f32_16x16x32_bf16 v[8:11], a[8:11], v[32:35], 0\n"
  "buffer_load_dwordx4 v95, s[40:43], s60 offen offset:1024 lds\n"
  "v_mfma_f32_16x16x32_bf16 v[8:11], a[24:27], v[36:39], v[8:11]\n"
  "ds_read_b64_tr_b16 v[120:121], v99 offset:18432\n"
  "v_mfma_f32_16x16x32_bf16 v[8:11], a[40:43], v[40:43], v[8:11]\n"
  "v_exp_f32 v81, v81\n"
  "v_mfma_f32_16x16x32_bf16 v[8:11], a[56:59], v[44:47], v[8:11]\n"
  "ds_read_b64_tr_b16 v[122:123], v99 offset:26624\n"
  "v_mul_f32 v82, v16, v24\n"
  "v_mfma_f32_16x16x32_bf16 v[8:11], a[72:75], v[48:51], v[8:11]\n"
  "v_mul_f32 v83, v17, v25\n"
  "v_add_f32 v80, 1.0, v80\n"
  "v_mfma_f32_16x16x32_bf16 v[8:11], a[88:91], v[52:55], v[8:11]\n"
  "buffer_load_dwordx4 v96, s[40:43], s60 offen offset:2048 lds\n"
  "v_add_f32 v81, 1.0, v81\n"
  "v_mfma_f32_16x16x32_bf16 v[12:15], a[12:15], v[32:35], 0\n"
  "s_add_u32 s60, s60, 36864\n"
  "v_rcp_f32 v80, v80\n"
  "v_mfma_f32_16x16x32_bf16 v[12:15], a[28:31], v[36:39], v[12:15]\n"
  "v_rcp_f32 v81, v81\n"
  "v_mfma_f32_16x16x32_bf16 v[12:15], a[44:47], v[40:43], v[12:15]\n"
  "v_mul_f32 v82, v82, v80\n"
  "v_mul_f32 v83, v83, v81\n"
  "v_mfma_f32_16x16x32_bf16 v[12:15], a[60:63], v[44:47], v[12:15]\n"
  "s_add_u32 m0, s48, 114688\n"
  "v_cvt_pk_bf16_f32 v84, v82, v83\n"
  "v_mul_f32 v80, v18, v18\n"
  "v_mfma_f32_16x16x32_bf16 v[12:15], a[76:79], v[48:51], v[12:15]\n"
  "buffer_load_dwordx4 v97, s[36:39], s59 offen offset:3072 sc1 lds\n"
  "v_mul_f32 v81, v19, v19\n"
  "v_mfma_f32_16x16x32_bf16 v[12:15], a[92:95], v[52:55], v[12:15]\n"
  "s_waitcnt lgkmcnt(0)\n"
  "v_fmaak_f32 v80, v88, v80, 0xc0135761\n"
  "v_fmaak_f32 v81, v88, v81, 0xc0135761\n"
  "v_mfma_f32_16x16x32_bf16 v[144:147], v[112:115], v[124:127], v[144:147]\n"
  "s_add_u32 s59, s59, 1024\n"
  "ds_read_b128 v[56:59], v91 offset:6144\n"
  "ds_read_b128 v[60:63], v91 offset:7168\n"
  "v_mfma_f32_16x16x32_bf16 v[148:151], v[112:115], v[128:131], v[148:151]\n"
  "ds_read_b128 v[64:67], v91 offset:8192\n"
  "v_mul_f32 v80, v18, v80\n"
  "v_mfma_f32_16x16x32_bf16 v[152:155], v[112:115], v[132:135], v[152:155]\n"
  "ds_read_b128 v[68:71], v91 offset:9216\n"
  "v_mul_f32 v81, v19, v81\n"
  "v_mfma_f32_16x16x32_bf16 v[156:159], v[112:115], v[136:139], v[156:159]\n"
  "buffer_load_dwordx4 v94, s[40:43], s60 offen offset:0 lds\n"
  "ds_read_b128 v[72:75], v91 offset:10240\n"
  "v_mfma_f32_16x16x32_bf16 v[164:167], v[116:119], v[124:127], v[164:167]\n"
  "ds_read_b128 v[76:79], v91 offset:11264\n"
  "v_mfma_f32_16x16x32_bf16 v[168:171], v[116:119], v[128:131], v[168:171]\n"
  "v_exp_f32 v80, v80\n"
  "v_mfma_f32_16x16x32_bf16 v[172:175], v[116:119], v[132:135], v[172:175]\n"
  "v_exp_f32 v81, v81\n"
  "v_mfma_f32_16x16x32_bf16 v[176:179], v[116:119], v[136:139], v[176:179]\n"
  "v_mul_f32 v82, v18, v26\n"
  "v_mul_f32 v83, v19, v27\n"
  "v_mfma_f32_16x16x32_bf16 v[184:187], v[120:123], v[124:127], v[184:187]\n"
  "buffer_load_dwordx4 v95, s[40:43], s60 offen offset:1024 lds\n"
  "v_add_f32 v80, 1.0, v80\n"
  "v_mfma_f32_16x16x32_bf16 v[188:191], v[120:123], v[128:131], v[188:191]\n"
  "v_add_f32 v81, 1.0, v81\n"
  "v_mfma_f32_16x16x32_bf16 v[192:195], v[120:123], v[132:135], v[192:195]\n"
  "v_rcp_f32 v80, v80\n"
  "v_mfma_f32_16x16x32_bf16 v[196:199], v[120:123], v[136:139], v[196:199]\n"
  "s_waitcnt lgkmcnt(0)\n"
  "v_rcp_f32 v81, v81\n"
  "v_mfma_f32_16x16x32_bf16 v[0:3], a[96:99], v[56:59], v[0:3]\n"
  "ds_read_b64_tr_b16 v[112:113], v99 offset:49152\n"
  "v_mul_f32 v82, v82, v80\n"
  "v_mfma_f32_16x16x32_bf16 v[0:3], a[112:115], v[60:63], v[0:3]\n"
  "buffer_load_dwordx4 v96, s[40:43], s60 offen offset:2048 lds\n"
  "ds_read_b64_tr_b16 v[114:115], v99 offset:57344\n"
  "v_mfma_f32_16x16x32_bf16 v[0:3], a[128:131], v[64:67], v[0:3]\n"
  "s_add_u32 s60, s60, 36864\n"
  "v_mul_f32 v83, v83, v81\n"
  "v_cvt_pk_bf16_f32 v85, v82, v83\n"
  "v_mfma_f32_16x16x32_bf16 v[0:3], a[144:147], v[68:71], v[0:3]\n"
  "ds_read_b128 v[124:127], v89 offset:52224\n"
  "v_mul_f32 v80, v20, v20\n"
  "v_mfma_f32_16x16x32_bf16 v[0:3], a[160:163], v[72:75], v[0:3]\n"
  "v_mul_f32 v81, v21, v21\n"
  "v_fmaak_f32 v80, v88, v80, 0xc0135761\n"
  "v_mfma_f32_16x16x32_bf16 v[0:3], a[176:179], v[76:79], v[0:3]\n"
  "ds_read_b128 v[128:131], v89 offset:56320\n"
  "v_fmaak_f32 v81, v88, v81, 0xc0135761\n"
  "v_mfma_f32_16x16x32_bf16 v[4:7], a[100:103], v[56:59], v[4:7]\n"
  "ds_read_b128 v[132:135], v89 offset:60416\n"
  "v_mul_f32 v80, v20, v80\n"
  "v_mfma_f32_16x16x32_bf16 v[4:7], a[116:119], v[60:63], v[4:7]\n"
  "v_mul_f32 v81, v21, v81\n"
  "v_mfma_f32_16x16x32_bf16 v[4:7], a[132:135], v[64:67], v[4:7]\n"
  "ds_read_b128 v[136:139], v89 offset:64512\n"
  "v_mfma_f32_16x16x32_bf16 v[4:7], a[148:151], v[68:71], v[4:7]\n"
  "v_exp_f32 v80, v80\n"
  "v_mfma_f32_16x16x32_bf16 v[4:7], a[164:167], v[72:75], v[4:7]\n"
  "ds_read_b64_tr_b16 v[116:117], v99 offset:50176\n"
  "v_mfma_f32_16x16x32_bf16 v[4:7], a[180:183], v[76:79], v[4:7]\n"
  "ds_read_b64_tr_b16 v[118:119], v99 offset:58368\n"
  "v_mfma_f32_16x16x32_bf16 v[8:11], a[104:107], v[56:59], v[8:11]\n"
  "v_exp_f32 v81, v81\n"
  "v_mfma_f32_16x16x32_bf16 v[8:11], a[120:123], v[60:63], v[8:11]\n"
  "ds_read_b64_tr_b16 v[120:121], v99 offset:51200\n"
  "v_mul_f32 v82, v20, v28\n"
  "v_mfma_f32_16x16x32_bf16 v[8:11], a[136:139], v[64:67], v[8:11]\n"
  "v_mul_f32 v83, v21, v29\n"
  "v_add_f32 v80, 1.0, v80\n"
  "v_mfma_f32_16x16x32_bf16 v[8:11], a[152:155], v[68:71], v[8:11]\n"
  "ds_read_b64_tr_b16 v[122:123], v99 offset:59392\n"
  "v_add_f32 v81, 1.0, v81\n"
  "v_mfma_f32_16x16x32_bf16 v[8:11], a[168:171], v[72:75], v[8:11]\n"
  "v_rcp_f32 v80, v80\n"
  "v_mfma_f32_16x16x32_bf16 v[8:11], a[184:187], v[76:79], v[8:11]\n"
  "v_rcp_f32 v81, v81\n"
  "v_mfma_f32_16x16x32_bf16 v[12:15], a[108:111], v[56:59], v[12:15]\n"
  "v_mul_f32 v82, v82, v80\n"
  "v_mul_f32 v83, v83, v81\n"
  "v_mfma_f32_16x16x32_bf16 v[12:15], a[124:127], v[60:63], v[12:15]\n"
  "v_cvt_pk_bf16_f32 v86, v82, v83\n"
  "v_mul_f32 v80, v22, v22\n"
  "v_mfma_f32_16x16x32_bf16 v[12:15], a[140:143], v[64:67], v[12:15]\n"
  "v_mul_f32 v81, v23, v23\n"
  "v_fmaak_f32 v80, v88, v80, 0xc0135761\n"
  "v_mfma_f32_16x16x32_bf16 v[12:15], a[156:159], v[68:71], v[12:15]\n"
  "v_fmaak_f32 v81, v88, v81, 0xc0135761\n"
  "v_mul_f32 v80, v22, v80\n"
  "v_mfma_f32_16x16x32_bf16 v[12:15], a[172:175], v[72:75], v[12:15]\n"
  "v_mul_f32 v81, v23, v81\n"
  "v_mfma_f32_16x16x32_bf16 v[12:15], a[188:191], v[76:79], v[12:15]\n"
  "s_waitcnt lgkmcnt(0)\n"
  "v_exp_f32 v80, v80\n"
  "v_mfma_f32_16x16x32_bf16 v[144:147], v[112:115], v[124:127], v[144:147]\n"
  "ds_read_b128 v[32:35], v91 offset:12288\n"
  "ds_read_b128 v[36:39], v91 offset:13312\n"
  "v_mfma_f32_16x16x32_bf16 v[148:151], v[112:115], v[128:131], v[148:151]\n"
  "ds_read_b128 v[40:43], v91 offset:14336\n"
  "v_mfma_f32_16x16x32_bf16 v[152:155], v[112:115], v[132:135], v[152:155]\n"
  "ds_read_b128 v[44:47], v91 offset:15360\n"
  "v_mfma_f32_16x16x32_bf16 v[156:159], v[112:115], v[136:139], v[156:159]\n"
  "ds_read_b128 v[48:51], v91 offset:24576\n"
  "v_mfma_f32_16x16x32_bf16 v[164:167], v[116:119], v[124:127], v[164:167]\n"
  "ds_read_b128 v[52:55], v91 offset:25600\n"
  "v_mfma_f32_16x16x32_bf16 v[168:171], v[116:119], v[128:131], v[168:171]\n"
  "v_exp_f32 v81, v81\n"
  "v_mfma_f32_16x16x32_bf16 v[172:175], v[116:119], v[132:135], v[172:175]\n"
  "v_mul_f32 v82, v22, v30\n"
  "v_mul_f32 v83, v23, v31\n"
  "v_mfma_f32_16x16x32_bf16 v[176:179], v[116:119], v[136:139], v[176:179]\n"
  "v_add_f32 v80, 1.0, v80\n"
  "v_add_f32 v81, 1.0, v81\n"
  "v_mfma_f32_16x16x32_bf16 v[184:187], v[120:123], v[124:127], v[184:187]\n"
  "v_rcp_f32 v80, v80\n"
  "v_mfma_f32_16x16x32_bf16 v[188:191], v[120:123], v[128:131], v[188:191]\n"
  "v_rcp_f32 v81, v81\n"
  "v_mfma_f32_16x16x32_bf16 v[192:195], v[120:123], v[132:135], v[192:195]\n"
  "v_mul_f32 v82, v82, v80\n"
  "v_mul_f32 v83, v83, v81\n"
  "v_mfma_f32_16x16x32_bf16 v[196:199], v[120:123], v[136:139], v[196:199]\n"
  "s_waitcnt vmcnt(14) lgkmcnt(0)\n"
  "s_barrier\n"
  "v_mfma_f32_16x16x32_bf16 v[0:3], a[192:195], v[32:35], v[0:3]\n"
  "s_add_u32 m0, s48, 16384\n"
  "ds_read_b128 v[56:59], v89 offset:0\n"
  "v_cvt_pk_bf16_f32 v87, v82, v83\n"
  "v_mfma_f32_16x16x32_bf16 v[0:3], a[208:211], v[36:39], v[0:3]\n"
  "buffer_load_dwordx4 v92, s[36:39], s52 offen offset:0 sc1 lds\n"
  "v_mfma_f32_16x16x32_bf16 v[0:3], a[224:227], v[40:43], v[0:3]\n"
  "ds_read_b128 v[60:63], v89 offset:1024\n"
  "v_mfma_f32_16x16x32_bf16 v[0:3], a[240:243], v[44:47], v[0:3]\n"
  "v_mfma_f32_16x16x32_bf16 v[0:3], v[224:227], v[48:51], v[0:3]\n"
  "v_mfma_f32_16x16x32_bf16 v[0:3], v[240:243], v[52:55], v[0:3]\n"
  "ds_read_b128 v[64:67], v89 offset:2048\n"
  "v_mfma_f32_16x16x32_bf16 v[4:7], a[196:199], v[32:35], v[4:7]\n"
  "buffer_load_dwordx4 v92, s[36:39], s52 offen offset:1024 sc1 lds\n"
  "v_mfma_f32_16x16x32_bf16 v[4:7], a[212:215], v[36:39], v[4:7]\n"
  "v_mfma_f32_16x16x32_bf16 v[4:7], a[228:231], v[40:43], v[4:7]\n"
  "ds_read_b128 v[68:71], v89 offset:3072\n"
  "v_mfma_f32_16x16x32_bf16 v[4:7], a[244:247], v[44:47], v[4:7]\n"
  "v_mfma_f32_16x16x32_bf16 v[4:7], v[228:231], v[48:51], v[4:7]\n"
  "v_mfma_f32_16x16x32_bf16 v[4:7], v[244:247], v[52:55], v[4:7]\n"
  "buffer_load_dwordx4 v92, s[36:39], s52 offen offset:2048 sc1 lds\n"
  "ds_read_b128 v[72:75], v89 offset:4096\n"
  "v_mfma_f32_16x16x32_bf16 v[8:11], a[200:203], v[32:35], v[8:11]\n"
  "v_mfma_f32_16x16x32_bf16 v[8:11], a[216:219], v[36:39], v[8:11]\n"
  "v_mfma_f32_16x16x32_bf16 v[8:11], a[232:235], v[40:43], v[8:11]\n"
  "ds_read_b128 v[76:79], v89 offset:5120\n"
  "v_mfma_f32_16x16x32_bf16 v[8:11], a[248:251], v[44:47], v[8:11]\n"
  "v_mfma_f32_16x16x32_bf16 v[8:11], v[232:235], v[48:51], v[8:11]\n"
  "buffer_load_dwordx4 v92, s[36:39], s52 offen offset:3072 sc1 lds\n"
  "v_mfma_f32_16x16x32_bf16 v[8:11], v[248:251], v[52:55], v[8:11]\n"
  "v_mfma_f32_16x16x32_bf16 v[12:15], a[204:207], v[32:35], v[12:15]\n"
  "v_mfma_f32_16x16x32_bf16 v[12:15], a[220:223], v[36:39], v[12:15]\n"
  "v_mfma_f32_16x16x32_bf16 v[12:15], a[236:239], v[40:43], v[12:15]\n"
  "s_add_u32 m0, s49, 133120\n"
  "v_mfma_f32_16x16x32_bf16 v[12:15], a[252:255], v[44:47], v[12:15]\n"
  "buffer_load_dwordx4 v93, s[36:39], s52 offen sc1 lds\n"
  "v_mfma_f32_16x16x32_bf16 v[12:15], v[236:239], v[48:51], v[12:15]\n"
  "s_add_u32 s52, s52, 18432\n"
  "buffer_store_dwordx4 v[84:87], v89, s[36:39], s55 offen sc1\n"
  "v_mfma_f32_16x16x32_bf16 v[12:15], v[252:255], v[52:55], v[12:15]\n"
  "s_waitcnt lgkmcnt(0)\n"
  "s_add_u32 s55, s55, 147456\n"
  "v_mfma_f32_16x16x32_bf16 v[16:19], a[0:3], v[56:59], 0\n"
  "s_cmp_eq_u32 s52, s54\n"
  "s_cselect_b32 s52, s53, s52\n"
  "ds_read_b64_tr_b16 v[112:113], v100 offset:0\n"
  "v_mfma_f32_16x16x32_bf16 v[16:19], a[16:19], v[60:63], v[16:19]\n"
  "s_add_u32 m0, s48, 49152\n"
  "s_cmp_eq_u32 s55, s57\n"
  "s_cselect_b32 s55, s56, s55\n"
  "ds_read_b64_tr_b16 v[114:115], v100 offset:8192\n"
  "v_mfma_f32_16x16x32_bf16 v[16:19], a[32:35], v[64:67], v[16:19]\n"
  "buffer_load_dwordx4 v97, s[36:39], s59 offen offset:3072 sc1 lds\n"
  "v_mul_f32 v80, v0, v0\n"
  "v_mfma_f32_16x16x32_bf16 v[16:19], a[48:51], v[68:71], v[16:19]\n"
  "s_add_u32 s59, s59, 1024\n"
  "ds_read_b128 v[124:127], v90 offset:3072\n"
  "v_mul_f32 v81, v1, v1\n"
  "v_mfma_f32_16x16x32_bf16 v[16:19], a[64:67], v[72:75], v[16:19]\n"
  "v_fmaak_f32 v80, v88, v80, 0xc0135761\n"
  "v_fmaak_f32 v81, v88, v81, 0xc0135761\n"
  "v_mfma_f32_16x16x32_bf16 v[16:19], a[80:83], v[76:79], v[16:19]\n"
  "ds_read_b128 v[128:131], v90 offset:7168\n"
  "v_mul_f32 v80, v0, v80\n"
  "v_mfma_f32_16x16x32_bf16 v[20:23], a[4:7], v[56:59], 0\n"
  "ds_read_b128 v[132:135], v90 offset:11264\n"
  "v_mul_f32 v81, v1, v81\n"
  "v_mfma_f32_16x16x32_bf16 v[20:23], a[20:23], v[60:63], v[20:23]\n"
  "buffer_load_dwordx4 v94, s[40:43], s60 offen offset:0 lds\n"
  "v_mfma_f32_16x16x32_bf16 v[20:23], a[36:39], v[64:67], v[20:23]\n"
  "ds_read_b128 v[136:139], v90 offset:15360\n"
  "v_mfma_f32_16x16x32_bf16 v[20:23], a[52:55], v[68:71], v[20:23]\n"
  "v_exp_f32 v80, v80\n"
  "v_mfma_f32_16x16x32_bf16 v[20:23], a[68:71], v[72:75], v[20:23]\n"
  "ds_read_b64_tr_b16 v[116:117], v100 offset:1024\n"
  "v_mfma_f32_16x16x32_bf16 v[20:23], a[84:87], v[76:79], v[20:23]\n"
  "ds_read_b64_tr_b16 v[118:119], v100 offset:9216\n"
  "v_mfma_f32_16x16x32_bf16 v[24:27], a[8:11], v[56:59], 0\n"
  "buffer_load_dwordx4 v95, s[40:43], s60 offen offset:1024 lds\n"
  "v_mfma_f32_16x16x32_bf16 v[24:27], a[24:27], v[60:63], v[24:27]\n"
  "ds_read_b64_tr_b16 v[120:121], v100 offset:2048\n"
  "v_mfma_f32_16x16x32_bf16 v[24:27], a[40:43], v[64:67], v[24:27]\n"
  "v_exp_f32 v81, v81\n"
  "v_mfma_f32_16x16x32_bf16 v[24:27], a[56:59], v[68:71], v[24:27]\n"
  "ds_read_b64_tr_b16 v[122:123], v100 offset:10240\n"
  "v_mul_f32 v82, v0, v8\n"
  "v_mfma_f32_16x16x32_bf16 v[24:27], a[72:75], v[72:75], v[24:27]\n"
  "v_mul_f32 v83, v1, v9\n"
  "v_add_f32 v80, 1.0, v80\n"
  "v_mfma_f32_16x16x32_bf16 v[24:27], a[88:91], v[76:79], v[24:27]\n"
  "buffer_load_dwordx4 v96, s[40:43], s60 offen offset:2048 lds\n"
  "v_add_f32 v81, 1.0, v81\n"
  "v_mfma_f32_16x16x32_bf16 v[28:31], a[12:15], v[56:59], 0\n"
  "s_add_u32 s60, s60, 36864\n"
  "v_rcp_f32 v80, v80\n"
  "v_mfma_f32_16x16x32_bf16 v[28:31], a[28:31], v[60:63], v[28:31]\n"
  "v_rcp_f32 v81, v81\n"
  "v_mfma_f32_16x16x32_bf16 v[28:31], a[44:47], v[64:67], v[28:31]\n"
  "v_mul_f32 v82, v82, v80\n"
  "v_mul_f32 v83, v83, v81\n"
  "v_mfma_f32_16x16x32_bf16 v[28:31], a[60:63], v[68:71], v[28:31]\n"
  "s_add_u32 m0, s48, 131072\n"
  "v_cvt_pk_bf16_f32 v84, v82, v83\n"
  "v_mul_f32 v80, v2, v2\n"
  "v_mfma_f32_16x16x32_bf16 v[28:31], a[76:79], v[72:75], v[28:31]\n"
  "buffer_load_dwordx4 v97, s[36:39], s59 offen offset:3072 sc1 lds\n"
  "v_mul_f32 v81, v3, v3\n"
  "v_mfma_f32_16x16x32_bf16 v[28:31], a[92:95], v[76:79], v[28:31]\n"
  "s_waitcnt lgkmcnt(0)\n"
  "v_fmaak_f32 v80, v88, v80, 0xc0135761\n"
  "v_fmaak_f32 v81, v88, v81, 0xc0135761\n"
  "v_mfma_f32_16x16x32_bf16 v[144:147], v[112:115], v[124:127], v[144:147]\n"
  "s_add_u32 s59, s59, 25600\n"
  "ds_read_b128 v[32:35], v89 offset:6144\n"
  "ds_read_b128 v[36:39], v89 offset:7168\n"
  "v_mfma_f32_16x16x32_bf16 v[148:151], v[112:115], v[128:131], v[148:151]\n"
  "ds_read_b128 v[40:43], v89 offset:8192\n"
  "v_mul_f32 v80, v2, v80\n"
  "v_mfma_f32_16x16x32_bf16 v[152:155], v[112:115], v[132:135], v[152:155]\n"
  "ds_read_b128 v[44:47], v89 offset:9216\n"
  "v_mul_f32 v81, v3, v81\n"
  "v_mfma_f32_16x16x32_bf16 v[156:159], v[112:115], v[136:139], v[156:159]\n"
  "buffer_load_dwordx4 v94, s[40:43], s60 offen offset:0 lds\n"
  "ds_read_b128 v[48:51], v89 offset:10240\n"
  "v_mfma_f32_16x16x32_bf16 v[164:167], v[116:119], v[124:127], v[164:167]\n"
  "ds_read_b128 v[52:55], v89 offset:11264\n"
  "v_mfma_f32_16x16x32_bf16 v[168:171], v[116:119], v[128:131], v[168:171]\n"
  "s_mov_b64 exec, 3\n"
  "buffer_load_dword v106, v105, s[24:27], 0 offen sc1\n"
  "s_mov_b64 exec, -1\n"
  "v_mfma_f32_16x16x32_bf16 v[172:175], v[116:119], v[132:135], v[172:175]\n"
  "v_exp_f32 v80, v80\n"
  "v_mfma_f32_16x16x32_bf16 v[176:179], v[116:119], v[136:139], v[176:179]\n"
  "v_exp_f32 v81, v81\n"
  "v_mfma_f32_16x16x32_bf16 v[184:187], v[120:123], v[124:127], v[184:187]\n"
  "buffer_load_dwordx4 v95, s[40:43], s60 offen offset:1024 lds\n"
  "v_mul_f32 v82, v2, v10\n"
  "v_mfma_f32_16x16x32_bf16 v[188:191], v[120:123], v[128:131], v[188:191]\n"
  "v_mul_f32 v83, v3, v11\n"
  "v_add_f32 v80, 1.0, v80\n"
  "v_mfma_f32_16x16x32_bf16 v[192:195], v[120:123], v[132:135], v[192:195]\n"
  "v_add_f32 v81, 1.0, v81\n"
  "v_mfma_f32_16x16x32_bf16 v[196:199], v[120:123], v[136:139], v[196:199]\n"
  "s_waitcnt lgkmcnt(0)\n"
  "v_rcp_f32 v80, v80\n"
  "v_mfma_f32_16x16x32_bf16 v[16:19], a[96:99], v[32:35], v[16:19]\n"
  "ds_read_b64_tr_b16 v[112:113], v100 offset:16384\n"
  "v_mfma_f32_16x16x32_bf16 v[16:19], a[112:115], v[36:39], v[16:19]\n"
  "buffer_load_dwordx4 v96, s[40:43], s60 offen offset:2048 lds\n"
  "ds_read_b64_tr_b16 v[114:115], v100 offset:24576\n"
  "v_mfma_f32_16x16x32_bf16 v[16:19], a[128:131], v[40:43], v[16:19]\n"
  "s_add_u32 s60, s60, 921600\n"
  "v_rcp_f32 v81, v81\n"
  "v_mfma_f32_16x16x32_bf16 v[16:19], a[144:147], v[44:47], v[16:19]\n"
  "s_cmp_ge_u32 s60, 5308416\n"
  "s_cselect_b32 s64, 5308416, 0\n"
  "s_cselect_b32 s65, 147456, 0\n"
  "s_sub_u32 s60, s60, s64\n"
  "s_sub_u32 s59, s59, s65\n"
  "ds_read_b128 v[124:127], v90 offset:19456\n"
  "v_mfma_f32_16x16x32_bf16 v[16:19], a[160:163], v[48:51], v[16:19]\n"
  "v_mul_f32 v82, v82, v80\n"
  "v_mul_f32 v83, v83, v81\n"
  "v_mfma_f32_16x16x32_bf16 v[16:19], a[176:179], v[52:55], v[16:19]\n"
  "ds_read_b128 v[128:131], v90 offset:23552\n"
  "v_cvt_pk_bf16_f32 v85, v82, v83\n"
  "v_mfma_f32_16x16x32_bf16 v[20:23], a[100:103], v[32:35], v[20:23]\n"
  "ds_read_b128 v[132:135], v90 offset:27648\n"
  "v_mul_f32 v80, v4, v4\n"
  "v_mfma_f32_16x16x32_bf16 v[20:23], a[116:119], v[36:39], v[20:23]\n"
  "v_mul_f32 v81, v5, v5\n"
  "v_fmaak_f32 v80, v88, v80, 0xc0135761\n"
  "v_mfma_f32_16x16x32_bf16 v[20:23], a[132:135], v[40:43], v[20:23]\n"
  "ds_read_b128 v[136:139], v90 offset:31744\n"
  "v_fmaak_f32 v81, v88, v81, 0xc0135761\n"
  "v_mfma_f32_16x16x32_bf16 v[20:23], a[148:151], v[44:47], v[20:23]\n"
  "v_mul_f32 v80, v4, v80\n"
  "v_mul_f32 v81, v5, v81\n"
  "v_mfma_f32_16x16x32_bf16 v[20:23], a[164:167], v[48:51], v[20:23]\n"
  "ds_read_b64_tr_b16 v[116:117], v100 offset:17408\n"
  "v_mfma_f32_16x16x32_bf16 v[20:23], a[180:183], v[52:55], v[20:23]\n"
  "ds_read_b64_tr_b16 v[118:119], v100 offset:25600\n"
  "v_mfma_f32_16x16x32_bf16 v[24:27], a[104:107], v[32:35], v[24:27]\n"
  "v_exp_f32 v80, v80\n"
  "v_mfma_f32_16x16x32_bf16 v[24:27], a[120:123], v[36:39], v[24:27]\n"
  "ds_read_b64_tr_b16 v[120:121], v100 offset:18432\n"
  "v_mfma_f32_16x16x32_bf16 v[24:27], a[136:139], v[40:43], v[24:27]\n"
  "v_exp_f32 v81, v81\n"
  "v_mfma_f32_16x16x32_bf16 v[24:27], a[152:155], v[44:47], v[24:27]\n"
  "ds_read_b64_tr_b16 v[122:123], v100 offset:26624\n"
  "v_mul_f32 v82, v4, v12\n"
  "v_mfma_f32_16x16x32_bf16 v[24:27], a[168:171], v[48:51], v[24:27]\n"
  "v_mul_f32 v83, v5, v13\n"
  "v_add_f32 v80, 1.0, v80\n"
  "v_mfma_f32_16x16x32_bf16 v[24:27], a[184:187], v[52:55], v[24:27]\n"
  "v_add_f32 v81, 1.0, v81\n"
  "v_mfma_f32_16x16x32_bf16 v[28:31], a[108:111], v[32:35], v[28:31]\n"
  "v_rcp_f32 v80, v80\n"
  "v_mfma_f32_16x16x32_bf16 v[28:31], a[124:127], v[36:39], v[28:31]\n"
  "v_rcp_f32 v81, v81\n"
  "v_mfma_f32_16x16x32_bf16 v[28:31], a[140:143], v[40:43], v[28:31]\n"
  "v_mul_f32 v82, v82, v80\n"
  "v_mul_f32 v83, v83, v81\n"
  "v_mfma_f32_16x16x32_bf16 v[28:31], a[156:159], v[44:47], v[28:31]\n"
  "v_cvt_pk_bf16_f32 v86, v82, v83\n"
  "v_mul_f32 v80, v6, v6\n"
  "v_mfma_f32_16x16x32_bf16 v[28:31], a[172:175], v[48:51], v[28:31]\n"
  "v_mul_f32 v81, v7, v7\n"
  "v_fmaak_f32 v80, v88, v80, 0xc0135761\n"
  "v_mfma_f32_16x16x32_bf16 v[28:31], a[188:191], v[52:55], v[28:31]\n"
  "s_waitcnt lgkmcnt(0)\n"
  "v_fmaak_f32 v81, v88, v81, 0xc0135761\n"
  "v_mul_f32 v80, v6, v80\n"
  "v_mfma_f32_16x16x32_bf16 v[144:147], v[112:115], v[124:127], v[144:147]\n"
  "ds_read_b128 v[56:59], v89 offset:12288\n"
  "ds_read_b128 v[60:63], v89 offset:13312\n"
  "v_mfma_f32_16x16x32_bf16 v[148:151], v[112:115], v[128:131], v[148:151]\n"
  "ds_read_b128 v[64:67], v89 offset:14336\n"
  "v_mul_f32 v81, v7, v81\n"
  "v_mfma_f32_16x16x32_bf16 v[152:155], v[112:115], v[132:135], v[152:155]\n"
  "s_cbranch_execz Ltramp%=\n"
  "ds_read_b128 v[68:71], v89 offset:15360\n"
  "v_mfma_f32_16x16x32_bf16 v[156:159], v[112:115], v[136:139], v[156:159]\n"
  "ds_read_b128 v[72:75], v91 offset:22528\n"
  "v_mfma_f32_16x16x32_bf16 v[164:167], v[116:119], v[124:127], v[164:167]\n"
  "ds_read_b128 v[76:79], v91 offset:23552\n"
  "v_mfma_f32_16x16x32_bf16 v[168:171], v[116:119], v[128:131], v[168:171]\n"
  "v_exp_f32 v80, v80\n"
  "v_mfma_f32_16x16x32_bf16 v[172:175], v[116:119], v[132:135], v[172:175]\n"
  "v_exp_f32 v81, v81\n"
  "v_mfma_f32_16x16x32_bf16 v[176:179], v[116:119], v[136:139], v[176:179]\n"
  "v_mul_f32 v82, v6, v14\n"
  "v_mul_f32 v83, v7, v15\n"
  "v_mfma_f32_16x16x32_bf16 v[184:187], v[120:123], v[124:127], v[184:187]\n"
  "v_add_f32 v80, 1.0, v80\n"
  "v_add_f32 v81, 1.0, v81\n"
  "v_mfma_f32_16x16x32_bf16 v[188:191], v[120:123], v[128:131], v[188:191]\n"
  "v_rcp_f32 v80, v80\n"
  "v_mfma_f32_16x16x32_bf16 v[192:195], v[120:123], v[132:135], v[192:195]\n"
  "v_rcp_f32 v81, v81\n"
  "v_mfma_f32_16x16x32_bf16 v[196:199], v[120:123], v[136:139], v[196:199]\n"
  "s_waitcnt vmcnt(15) lgkmcnt(0)\n"
  "s_barrier\n"
  "v_mfma_f32_16x16x32_bf16 v[16:19], a[192:195], v[56:59], v[16:19]\n"
  "s_add_u32 m0, s48, 0\n"
  "ds_read_b128 v[32:35], v89 offset:32768\n"
  "v_mul_f32 v82, v82, v80\n"
  "v_mfma_f32_16x16x32_bf16 v[16:19], a[208:211], v[60:63], v[16:19]\n"
  "buffer_load_dwordx4 v92, s[36:39], s52 offen offset:0 sc1 lds\n"
  "v_mul_f32 v83, v83, v81\n"
  "v_mfma_f32_16x16x32_bf16 v[16:19], a[224:227], v[64:67], v[16:19]\n"
  "ds_read_b128 v[36:39], v89 offset:33792\n"
  "v_cvt_pk_bf16_f32 v87, v82, v83\n"
  "v_mfma_f32_16x16x32_bf16 v[16:19], a[240:243], v[68:71], v[16:19]\n"
  "v_mfma_f32_16x16x32_bf16 v[16:19], v[224:227], v[72:75], v[16:19]\n"
  "v_mfma_f32_16x16x32_bf16 v[16:19], v[240:243], v[76:79], v[16:19]\n"
  "ds_read_b128 v[40:43], v89 offset:34816\n"
  "v_mfma_f32_16x16x32_bf16 v[20:23], a[196:199], v[56:59], v[20:23]\n"
  "buffer_load_dwordx4 v92, s[36:39], s52 offen offset:1024 sc1 lds\n"
  "v_mfma_f32_16x16x32_bf16 v[20:23], a[212:215], v[60:63], v[20:23]\n"
  "v_mfma_f32_16x16x32_bf16 v[20:23], a[228:231], v[64:67], v[20:23]\n"
  "ds_read_b128 v[44:47], v89 offset:35840\n"
  "v_mfma_f32_16x16x32_bf16 v[20:23], a[244:247], v[68:71], v[20:23]\n"
  "v_mfma_f32_16x16x32_bf16 v[20:23], v[228:231], v[72:75], v[20:23]\n"
  "v_mfma_f32_16x16x32_bf16 v[20:23], v[244:247], v[76:79], v[20:23]\n"
  "buffer_load_dwordx4 v92, s[36:39], s52 offen offset:2048 sc1 lds\n"
  "ds_read_b128 v[48:51], v89 offset:36864\n"
  "v_mfma_f32_16x16x32_bf16 v[24:27], a[200:203], v[56:59], v[24:27]\n"
  "v_mfma_f32_16x16x32_bf16 v[24:27], a[216:219], v[60:63], v[24:27]\n"
  "v_mfma_f32_16x16x32_bf16 v[24:27], a[232:235], v[64:67], v[24:27]\n"
  "ds_read_b128 v[52:55], v89 offset:37888\n"
  "v_mfma_f32_16x16x32_bf16 v[24:27], a[248:251], v[68:71], v[24:27]\n"
  "v_mfma_f32_16x16x32_bf16 v[24:27], v[232:235], v[72:75], v[24:27]\n"
  "buffer_load_dwordx4 v92, s[36:39], s52 offen offset:3072 sc1 lds\n"
  "v_mfma_f32_16x16x32_bf16 v[24:27], v[248:251], v[76:79], v[24:27]\n"
  "v_mfma_f32_16x16x32_bf16 v[28:31], a[204:207], v[56:59], v[28:31]\n"
  "v_mfma_f32_16x16x32_bf16 v[28:31], a[220:223], v[60:63], v[28:31]\n"
  "v_mfma_f32_16x16x32_bf16 v[28:31], a[236:239], v[64:67], v[28:31]\n"
  "s_add_u32 m0, s49, 145408\n"
  "v_mfma_f32_16x16x32_bf16 v[28:31], a[252:255], v[68:71], v[28:31]\n"
  "buffer_load_dwordx4 v93, s[36:39], s52 offen sc1 lds\n"
  "v_mfma_f32_16x16x32_bf16 v[28:31], v[236:239], v[72:75], v[28:31]\n"
  "s_add_u32 s52, s52, 18432\n"
  "buffer_store_dwordx4 v[84:87], v89, s[36:39], s55 offen sc1\n"
  "v_mfma_f32_16x16x32_bf16 v[28:31], v[252:255], v[76:79], v[28:31]\n"
  "s_waitcnt lgkmcnt(0)\n"
  "s_add_u32 s55, s55, 147456\n"
  "v_mfma_f32_16x16x32_bf16 v[0:3], a[0:3], v[32:35], 0\n"
  "s_cmp_eq_u32 s52, s54\n"
  "s_cselect_b32 s52, s53, s52\n"
  "ds_read_b64_tr_b16 v[112:113], v100 offset:32768\n"
  "s_waitcnt vmcnt(8)\n"
  "v_cmp_gt_u32 vcc, 12, v106\n"
  "s_and_b64 vcc, vcc, 3\n"
  "s_cbranch_scc1 Lpollp12B%=\n"
  "Lpollrp12B%=:\n"
  "v_mfma_f32_16x16x32_bf16 v[0:3], a[16:19], v[36:39], v[0:3]\n"
  "s_add_u32 m0, s48, 65536\n"
  "s_cmp_eq_u32 s55, s57\n"
  "s_cselect_b32 s55, s56, s55\n"
  "ds_read_b64_tr_b16 v[114:115], v100 offset:40960\n"
  "v_mfma_f32_16x16x32_bf16 v[0:3], a[32:35], v[40:43], v[0:3]\n"
  "buffer_load_dwordx4 v97, s[36:39], s59 offen offset:3072 sc1 lds\n"
  "v_mul_f32 v80, v16, v16\n"
  "v_mfma_f32_16x16x32_bf16 v[0:3], a[48:51], v[44:47], v[0:3]\n"
  "s_add_u32 s59, s59, 1024\n"
  "ds_read_b128 v[124:127], v90 offset:35840\n"
  "v_mul_f32 v81, v17, v17\n"
  "v_mfma_f32_16x16x32_bf16 v[0:3], a[64:67], v[48:51], v[0:3]\n"
  "v_fmaak_f32 v80, v88, v80, 0xc0135761\n"
  "v_fmaak_f32 v81, v88, v81, 0xc0135761\n"
  "v_mfma_f32_16x16x32_bf16 v[0:3], a[80:83], v[52:55], v[0:3]\n"
  "ds_read_b128 v[128:131], v90 offset:39936\n"
  "v_mul_f32 v80, v16, v80\n"
  "v_mfma_f32_16x16x32_bf16 v[4:7], a[4:7], v[32:35], 0\n"
  "ds_read_b128 v[132:135], v90 offset:44032\n"
  "v_mul_f32 v81, v17, v81\n"
  "v_mfma_f32_16x16x32_bf16 v[4:7], a[20:23], v[36:39], v[4:7]\n"
  "buffer_load_dwordx4 v94, s[40:43], s60 offen offset:0 lds\n"
  "v_mfma_f32_16x16x32_bf16 v[4:7], a[36:39], v[40:43], v[4:7]\n"
  "ds_read_b128 v[136:139], v90 offset:48128\n"
  "v_mfma_f32_16x16x32_bf16 v[4:7], a[52:55], v[44:47], v[4:7]\n"
  "v_exp_f32 v80, v80\n"
  "v_mfma_f32_16x16x32_bf16 v[4:7], a[68:71], v[48:51], v[4:7]\n"
  "ds_read_b64_tr_b16 v[116:117], v100 offset:33792\n"
  "v_mfma_f32_16x16x32_bf16 v[4:7], a[84:87], v[52:55], v[4:7]\n"
  "ds_read_b64_tr_b16 v[118:119], v100 offset:41984\n"
  "v_mfma_f32_16x16x32_bf16 v[8:11], a[8:11], v[32:35], 0\n"
  "buffer_load_dwordx4 v95, s[40:43], s60 offen offset:1024 lds\n"
  "v_mfma_f32_16x16x32_bf16 v[8:11], a[24:27], v[36:39], v[8:11]\n"
  "ds_read_b64_tr_b16 v[120:121], v100 offset:34816\n"
  "v_mfma_f32_16x16x32_bf16 v[8:11], a[40:43], v[40:43], v[8:11]\n"
  "v_exp_f32 v81, v81\n"
  "v_mfma_f32_16x16x32_bf16 v[8:11], a[56:59], v[44:47], v[8:11]\n"
  "ds_read_b64_tr_b16 v[122:123], v100 offset:43008\n"
  "v_mul_f32 v82, v16, v24\n"
  "v_mfma_f32_16x16x32_bf16 v[8:11], a[72:75], v[48:51], v[8:11]\n"
  "v_mul_f32 v83, v17, v25\n"
  "v_add_f32 v80, 1.0, v80\n"
  "v_mfma_f32_16x16x32_bf16 v[8:11], a[88:91], v[52:55], v[8:11]\n"
  "buffer_load_dwordx4 v96, s[40:43], s60 offen offset:2048 lds\n"
  "v_add_f32 v81, 1.0, v81\n"
  "v_mfma_f32_16x16x32_bf16 v[12:15], a[12:15], v[32:35], 0\n"
  "s_add_u32 s60, s60, 36864\n"
  "v_rcp_f32 v80, v80\n"
  "v_mfma_f32_16x16x32_bf16 v[12:15], a[28:31], v[36:39], v[12:15]\n"
  "v_rcp_f32 v81, v81\n"
  "v_mfma_f32_16x16x32_bf16 v[12:15], a[44:47], v[40:43], v[12:15]\n"
  "v_mul_f32 v82, v82, v80\n"
  "v_mul_f32 v83, v83, v81\n"
  "v_mfma_f32_16x16x32_bf16 v[12:15], a[60:63], v[44:47], v[12:15]\n"
  "s_add_u32 m0, s48, 81920\n"
  "v_cvt_pk_bf16_f32 v84, v82, v83\n"
  "v_mul_f32 v80, v18, v18\n"
  "v_mfma_f32_16x16x32_bf16 v[12:15], a[76:79], v[48:51], v[12:15]\n"
  "buffer_load_dwordx4 v97, s[36:39], s59 offen offset:3072 sc1 lds\n"
  "v_mul_f32 v81, v19, v19\n"
  "v_mfma_f32_16x16x32_bf16 v[12:15], a[92:95], v[52:55], v[12:15]\n"
  "s_waitcnt lgkmcnt(0)\n"
  "v_fmaak_f32 v80, v88, v80, 0xc0135761\n"
  "v_fmaak_f32 v81, v88, v81, 0xc0135761\n"
  "v_mfma_f32_16x16x32_bf16 v[144:147], v[112:115], v[124:127], v[144:147]\n"
  "s_add_u32 s59, s59, 1024\n"
  "ds_read_b128 v[56:59], v89 offset:38912\n"
  "ds_read_b128 v[60:63], v89 offset:39936\n"
  "v_mfma_f32_16x16x32_bf16 v[148:151], v[112:115], v[128:131], v[148:151]\n"
  "ds_read_b128 v[64:67], v89 offset:40960\n"
  "v_mul_f32 v80, v18, v80\n"
  "v_mfma_f32_16x16x32_bf16 v[152:155], v[112:115], v[132:135], v[152:155]\n"
  "ds_read_b128 v[68:71], v89 offset:41984\n"
  "v_mul_f32 v81, v19, v81\n"
  "v_mfma_f32_16x16x32_bf16 v[156:159], v[112:115], v[136:139], v[156:159]\n"
  "buffer_load_dwordx4 v94, s[40:43], s60 offen offset:0 lds\n"
  "ds_read_b128 v[72:75], v89 offset:43008\n"
  "v_mfma_f32_16x16x32_bf16 v[164:167], v[116:119], v[124:127], v[164:167]\n"
  "ds_read_b128 v[76:79], v89 offset:44032\n"
  "v_mfma_f32_16x16x32_bf16 v[168:171], v[116:119], v[128:131], v[168:171]\n"
  "v_exp_f32 v80, v80\n"
  "v_mfma_f32_16x16x32_bf16 v[172:175], v[116:119], v[132:135], v[172:175]\n"
  "v_exp_f32 v81, v81\n"
  "v_mfma_f32_16x16x32_bf16 v[176:179], v[116:119], v[136:139], v[176:179]\n"
  "v_mul_f32 v82, v18, v26\n"
  "v_mul_f32 v83, v19, v27\n"
  "v_mfma_f32_16x16x32_bf16 v[184:187], v[120:123], v[124:127], v[184:187]\n"
  "buffer_load_dwordx4 v95, s[40:43], s60 offen offset:1024 lds\n"
  "v_add_f32 v80, 1.0, v80\n"
  "v_mfma_f32_16x16x32_bf16 v[188:191], v[120:123], v[128:131], v[188:191]\n"
  "v_add_f32 v81, 1.0, v81\n"
  "v_mfma_f32_16x16x32_bf16 v[192:195], v[120:123], v[132:135], v[192:195]\n"
  "v_rcp_f32 v80, v80\n"
  "v_mfma_f32_16x16x32_bf16 v[196:199], v[120:123], v[136:139], v[196:199]\n"
  "s_waitcnt lgkmcnt(0)\n"
  "v_rcp_f32 v81, v81\n"
  "v_mfma_f32_16x16x32_bf16 v[0:3], a[96:99], v[56:59], v[0:3]\n"
  "ds_read_b64_tr_b16 v[112:113], v100 offset:49152\n"
  "v_mul_f32 v82, v82, v80\n"
  "v_mfma_f32_16x16x32_bf16 v[0:3], a[112:115], v[60:63], v[0:3]\n"
  "buffer_load_dwordx4 v96, s[40:43], s60 offen offset:2048 lds\n"
  "ds_read_b64_tr_b16 v[114:115], v100 offset:57344\n"
  "v_mfma_f32_16x16x32_bf16 v[0:3], a[128:131], v[64:67], v[0:3]\n"
  "s_add_u32 s60, s60, 36864\n"
  "v_mul_f32 v83, v83, v81\n"
  "v_cvt_pk_bf16_f32 v85, v82, v83\n"
  "v_mfma_f32_16x16x32_bf16 v[0:3], a[144:147], v[68:71], v[0:3]\n"
  "ds_read_b128 v[124:127], v90 offset:52224\n"
  "v_mul_f32 v80, v20, v20\n"
  "v_mfma_f32_16x16x32_bf16 v[0:3], a[160:163], v[72:75], v[0:3]\n"
  "v_mul_f32 v81, v21, v21\n"
  "v_fmaak_f32 v80, v88, v80, 0xc0135761\n"
  "v_mfma_f32_16x16x32_bf16 v[0:3], a[176:179], v[76:79], v[0:3]\n"
  "ds_read_b128 v[128:131], v90 offset:56320\n"
  "v_fmaak_f32 v81, v88, v81, 0xc0135761\n"
  "v_mfma_f32_16x16x32_bf16 v[4:7], a[100:103], v[56:59], v[4:7]\n"
  "ds_read_b128 v[132:135], v90 offset:60416\n"
  "v_mul_f32 v80, v20, v80\n"
  "v_mfma_f32_16x16x32_bf16 v[4:7], a[116:119], v[60:63], v[4:7]\n"
  "v_mul_f32 v81, v21, v81\n"
  "v_mfma_f32_16x16x32_bf16 v[4:7], a[132:135], v[64:67], v[4:7]\n"
  "ds_read_b128 v[136:139], v90 offset:64512\n"
  "v_mfma_f32_16x16x32_bf16 v[4:7], a[148:151], v[68:71], v[4:7]\n"
  "v_exp_f32 v80, v80\n"
  "v_mfma_f32_16x16x32_bf16 v[4:7], a[164:167], v[72:75], v[4:7]\n"
  "ds_read_b64_tr_b16 v[116:117], v100 offset:50176\n"
  "v_mfma_f32_16x16x32_bf16 v[4:7], a[180:183], v[76:79], v[4:7]\n"
  "ds_read_b64_tr_b16 v[118:119], v100 offset:58368\n"
  "v_mfma_f32_16x16x32_bf16 v[8:11], a[104:107], v[56:59], v[8:11]\n"
  "v_exp_f32 v81, v81\n"
  "v_mfma_f32_16x16x32_bf16 v[8:11], a[120:123], v[60:63], v[8:11]\n"
  "ds_read_b64_tr_b16 v[120:121], v100 offset:51200\n"
  "v_mul_f32 v82, v20, v28\n"
  "v_mfma_f32_16x16x32_bf16 v[8:11], a[136:139], v[64:67], v[8:11]\n"
  "v_mul_f32 v83, v21, v29\n"
  "v_add_f32 v80, 1.0, v80\n"
  "v_mfma_f32_16x16x32_bf16 v[8:11], a[152:155], v[68:71], v[8:11]\n"
  "ds_read_b64_tr_b16 v[122:123], v100 offset:59392\n"
  "v_add_f32 v81, 1.0, v81\n"
  "v_mfma_f32_16x16x32_bf16 v[8:11], a[168:171], v[72:75], v[8:11]\n"
  "v_rcp_f32 v80, v80\n"
  "v_mfma_f32_16x16x32_bf16 v[8:11], a[184:187], v[76:79], v[8:11]\n"
  "v_rcp_f32 v81, v81\n"
  "v_mfma_f32_16x16x32_bf16 v[12:15], a[108:111], v[56:59], v[12:15]\n"
  "v_mul_f32 v82, v82, v80\n"
  "v_mul_f32 v83, v83, v81\n"
  "v_mfma_f32_16x16x32_bf16 v[12:15], a[124:127], v[60:63], v[12:15]\n"
  "v_cvt_pk_bf16_f32 v86, v82, v83\n"
  "v_mul_f32 v80, v22, v22\n"
  "v_mfma_f32_16x16x32_bf16 v[12:15], a[140:143], v[64:67], v[12:15]\n"
  "v_mul_f32 v81, v23, v23\n"
  "v_fmaak_f32 v80, v88, v80, 0xc0135761\n"
  "v_mfma_f32_16x16x32_bf16 v[12:15], a[156:159], v[68:71], v[12:15]\n"
  "v_fmaak_f32 v81, v88, v81, 0xc0135761\n"
  "v_mul_f32 v80, v22, v80\n"
  "v_mfma_f32_16x16x32_bf16 v[12:15], a[172:175], v[72:75], v[12:15]\n"
  "v_mul_f32 v81, v23, v81\n"
  "v_mfma_f32_16x16x32_bf16 v[12:15], a[188:191], v[76:79], v[12:15]\n"
  "s_waitcnt lgkmcnt(0)\n"
  "v_exp_f32 v80, v80\n"
  "v_mfma_f32_16x16x32_bf16 v[144:147], v[112:115], v[124:127], v[144:147]\n"
  "ds_read_b128 v[32:35], v89 offset:45056\n"
  "ds_read_b128 v[36:39], v89 offset:46080\n"
  "v_mfma_f32_16x16x32_bf16 v[148:151], v[112:115], v[128:131], v[148:151]\n"
  "ds_read_b128 v[40:43], v89 offset:47104\n"
  "v_mfma_f32_16x16x32_bf16 v[152:155], v[112:115], v[132:135], v[152:155]\n"
  "ds_read_b128 v[44:47], v89 offset:48128\n"
  "v_mfma_f32_16x16x32_bf16 v[156:159], v[112:115], v[136:139], v[156:159]\n"
  "ds_read_b128 v[48:51], v91 offset:20480\n"
  "v_mfma_f32_16x16x32_bf16 v[164:167], v[116:119], v[124:127], v[164:167]\n"
  "ds_read_b128 v[52:55], v91 offset:21504\n"
  "v_mfma_f32_16x16x32_bf16 v[168:171], v[116:119], v[128:131], v[168:171]\n"
  "v_exp_f32 v81, v81\n"
  "v_mfma_f32_16x16x32_bf16 v[172:175], v[116:119], v[132:135], v[172:175]\n"
  "v_mul_f32 v82, v22, v30\n"
  "v_mul_f32 v83, v23, v31\n"
  "v_mfma_f32_16x16x32_bf16 v[176:179], v[116:119], v[136:139], v[176:179]\n"
  "v_add_f32 v80, 1.0, v80\n"
  "v_add_f32 v81, 1.0, v81\n"
  "v_mfma_f32_16x16x32_bf16 v[184:187], v[120:123], v[124:127], v[184:187]\n"
  "v_rcp_f32 v80, v80\n"
  "v_mfma_f32_16x16x32_bf16 v[188:191], v[120:123], v[128:131], v[188:191]\n"
  "v_rcp_f32 v81, v81\n"
  "v_mfma_f32_16x16x32_bf16 v[192:195], v[120:123], v[132:135], v[192:195]\n"
  "v_mul_f32 v82, v82, v80\n"
  "v_mul_f32 v83, v83, v81\n"
  "v_mfma_f32_16x16x32_bf16 v[196:199], v[120:123], v[136:139], v[196:199]\n"
  "s_waitcnt vmcnt(14) lgkmcnt(0)\n"
  "s_barrier\n"
  "v_mfma_f32_16x16x32_bf16 v[0:3], a[192:195], v[32:35], v[0:3]\n"
  "s_add_u32 m0, s48, 32768\n"
  "ds_read_b128 v[56:59], v89 offset:16384\n"
  "v_cvt_pk_bf16_f32 v87, v82, v83\n"
  "v_mfma_f32_16x16x32_bf16 v[0:3], a[208:211], v[36:39], v[0:3]\n"
  "buffer_load_dwordx4 v97, s[36:39], s59 offen offset:3072 sc1 lds\n"
  "v_mfma_f32_16x16x32_bf16 v[0:3], a[224:227], v[40:43], v[0:3]\n"
  "s_add_u32 s59, s59, 1024\n"
  "ds_read_b128 v[60:63], v89 offset:17408\n"
  "v_mfma_f32_16x16x32_bf16 v[0:3], a[240:243], v[44:47], v[0:3]\n"
  "s_waitcnt vmcnt(9)\n"
  "s_mov_b64 exec, 1\n"
  "buffer_atomic_add v104, off, s[24:27], s14 sc1\n"
  "s_mov_b64 exec, -1\n"
  "v_mfma_f32_16x16x32_bf16 v[0:3], v[224:227], v[48:51], v[0:3]\n"
  "v_mfma_f32_16x16x32_bf16 v[0:3], v[240:243], v[52:55], v[0:3]\n"
  "ds_read_b128 v[64:67], v89 offset:18432\n"
  "v_mfma_f32_16x16x32_bf16 v[4:7], a[196:199], v[32:35], v[4:7]\n"
  "buffer_load_dwordx4 v94, s[40:43], s60 offen offset:0 lds\n"
  "v_mfma_f32_16x16x32_bf16 v[4:7], a[212:215], v[36:39], v[4:7]\n"
  "v_mfma_f32_16x16x32_bf16 v[4:7], a[228:231], v[40:43], v[4:7]\n"
  "ds_read_b128 v[68:71], v89 offset:19456\n"
  "v_mfma_f32_16x16x32_bf16 v[4:7], a[244:247], v[44:47], v[4:7]\n"
  "v_mfma_f32_16x16x32_bf16 v[4:7], v[228:231], v[48:51], v[4:7]\n"
  "v_mfma_f32_16x16x32_bf16 v[4:7], v[244:247], v[52:55], v[4:7]\n"
  "buffer_load_dwordx4 v95, s[40:43], s60 offen offset:1024 lds\n"
  "ds_read_b128 v[72:75], v89 offset:20480\n"
  "v_mfma_f32_16x16x32_bf16 v[8:11], a[200:203], v[32:35], v[8:11]\n"
  "v_mfma_f32_16x16x32_bf16 v[8:11], a[216:219], v[36:39], v[8:11]\n"
  "v_mfma_f32_16x16x32_bf16 v[8:11], a[232:235], v[40:43], v[8:11]\n"
  "ds_read_b128 v[76:79], v89 offset:21504\n"
  "v_mfma_f32_16x16x32_bf16 v[8:11], a[248:251], v[44:47], v[8:11]\n"
  "v_mfma_f32_16x16x32_bf16 v[8:11], v[232:235], v[48:51], v[8:11]\n"
  "buffer_load_dwordx4 v96, s[40:43], s60 offen offset:2048 lds\n"
  "v_mfma_f32_16x16x32_bf16 v[8:11], v[248:251], v[52:55], v[8:11]\n"
  "s_add_u32 s60, s60, 36864\n"
  "v_mfma_f32_16x16x32_bf16 v[12:15], a[204:207], v[32:35], v[12:15]\n"
  "v_mfma_f32_16x16x32_bf16 v[12:15], a[220:223], v[36:39], v[12:15]\n"
  "v_mfma_f32_16x16x32_bf16 v[12:15], a[236:239], v[40:43], v[12:15]\n"
  "s_add_u32 m0, s48, 98304\n"
  "v_mfma_f32_16x16x32_bf16 v[12:15], a[252:255], v[44:47], v[12:15]\n"
  "buffer_load_dwordx4 v97, s[36:39], s59 offen offset:3072 sc1 lds\n"
  "v_mfma_f32_16x16x32_bf16 v[12:15], v[236:239], v[48:51], v[12:15]\n"
  "s_add_u32 s59, s59, 1024\n"
  "buffer_store_dwordx4 v[84:87], v89, s[36:39], s55 offen sc1\n"
  "v_mfma_f32_16x16x32_bf16 v[12:15], v[252:255], v[52:55], v[12:15]\n"
  "s_waitcnt lgkmcnt(0)\n"
  "v_mfma_f32_16x16x32_bf16 v[16:19], a[0:3], v[56:59], 0\n"
  "s_add_u32 s55, s55, 147456\n"
  "ds_read_b64_tr_b16 v[112:113], v99 offset:49152\n"
  "v_mfma_f32_16x16x32_bf16 v[16:19], a[16:19], v[60:63], v[16:19]\n"
  "s_cmp_eq_u32 s55, s57\n"
  "s_cselect_b32 s55, s56, s55\n"
  "ds_read_b64_tr_b16 v[114:115], v99 offset:57344\n"
  "v_mfma_f32_16x16x32_bf16 v[16:19], a[32:35], v[64:67], v[16:19]\n"
  "buffer_load_dwordx4 v94, s[40:43], s60 offen offset:0 lds\n"
  "v_mul_f32 v80, v0, v0\n"
  "v_mfma_f32_16x16x32_bf16 v[16:19], a[48:51], v[68:71], v[16:19]\n"
  "ds_read_b128 v[124:127], v89 offset:52224\n"
  "v_mul_f32 v81, v1, v1\n"
  "v_mfma_f32_16x16x32_bf16 v[16:19], a[64:67], v[72:75], v[16:19]\n"
  "v_fmaak_f32 v80, v88, v80, 0xc0135761\n"
  "v_fmaak_f32 v81, v88, v81, 0xc0135761\n"
  "v_mfma_f32_16x16x32_bf16 v[16:19], a[80:83], v[76:79], v[16:19]\n"
  "ds_read_b128 v[128:131], v89 offset:56320\n"
  "v_mul_f32 v80, v0, v80\n"
  "v_mfma_f32_16x16x32_bf16 v[20:23], a[4:7], v[56:59], 0\n"
  "ds_read_b128 v[132:135], v89 offset:60416\n"
  "v_mul_f32 v81, v1, v81\n"
  "v_mfma_f32_16x16x32_bf16 v[20:23], a[20:23], v[60:63], v[20:23]\n"
  "buffer_load_dwordx4 v95, s[40:43], s60 offen offset:1024 lds\n"
  "v_mfma_f32_16x16x32_bf16 v[20:23], a[36:39], v[64:67], v[20:23]\n"
  "ds_read_b128 v[136:139], v89 offset:64512\n"
  "v_mfma_f32_16x16x32_bf16 v[20:23], a[52:55], v[68:71], v[20:23]\n"
  "v_exp_f32 v80, v80\n"
  "v_mfma_f32_16x16x32_bf16 v[20:23], a[68:71], v[72:75], v[20:23]\n"
  "ds_read_b64_tr_b16 v[116:117], v99 offset:50176\n"
  "v_mfma_f32_16x16x32_bf16 v[20:23], a[84:87], v[76:79], v[20:23]\n"
  "ds_read_b64_tr_b16 v[118:119], v99 offset:58368\n"
  "v_mfma_f32_16x16x32_bf16 v[24:27], a[8:11], v[56:59], 0\n"
  "buffer_load_dwordx4 v96, s[40:43], s60 offen offset:2048 lds\n"
  "v_mfma_f32_16x16x32_bf16 v[24:27], a[24:27], v[60:63], v[24:27]\n"
  "s_add_u32 s60, s60, 36864\n"
  "ds_read_b64_tr_b16 v[120:121], v99 offset:51200\n"
  "v_mfma_f32_16x16x32_bf16 v[24:27], a[40:43], v[64:67], v[24:27]\n"
  "v_exp_f32 v81, v81\n"
  "v_mfma_f32_16x16x32_bf16 v[24:27], a[56:59], v[68:71], v[24:27]\n"
  "ds_read_b64_tr_b16 v[122:123], v99 offset:59392\n"
  "v_mul_f32 v82, v0, v8\n"
  "v_mfma_f32_16x16x32_bf16 v[24:27], a[72:75], v[72:75], v[24:27]\n"
  "s_add_u32 m0, s48, 114688\n"
  "v_mul_f32 v83, v1, v9\n"
  "v_add_f32 v80, 1.0, v80\n"
  "v_mfma_f32_16x16x32_bf16 v[24:27], a[88:91], v[76:79], v[24:27]\n"
  "buffer_load_dwordx4 v97, s[36:39], s59 offen offset:3072 sc1 lds\n"
  "v_add_f32 v81, 1.0, v81\n"
  "v_mfma_f32_16x16x32_bf16 v[28:31], a[12:15], v[56:59], 0\n"
  "s_add_u32 s59, s59, 1024\n"
  "v_rcp_f32 v80, v80\n"
  "v_mfma_f32_16x16x32_bf16 v[28:31], a[28:31], v[60:63], v[28:31]\n"
  "v_rcp_f32 v81, v81\n"
  "v_mfma_f32_16x16x32_bf16 v[28:31], a[44:47], v[64:67], v[28:31]\n"
  "v_mul_f32 v82, v82, v80\n"
  "v_mul_f32 v83, v83, v81\n"
  "v_mfma_f32_16x16x32_bf16 v[28:31], a[60:63], v[68:71], v[28:31]\n"
  "v_cvt_pk_bf16_f32 v84, v82, v83\n"
  "v_mul_f32 v80, v2, v2\n"
  "v_mfma_f32_16x16x32_bf16 v[28:31], a[76:79], v[72:75], v[28:31]\n"
  "buffer_load_dwordx4 v94, s[40:43], s60 offen offset:0 lds\n"
  "v_mul_f32 v81, v3, v3\n"
  "v_mfma_f32_16x16x32_bf16 v[28:31], a[92:95], v[76:79], v[28:31]\n"
  "s_waitcnt lgkmcnt(0)\n"
  "v_fmaak_f32 v80, v88, v80, 0xc0135761\n"
  "v_fmaak_f32 v81, v88, v81, 0xc0135761\n"
  "v_mfma_f32_16x16x32_bf16 v[144:147], v[112:115], v[124:127], v[144:147]\n"
  "ds_read_b128 v[32:35], v89 offset:22528\n"
  "ds_read_b128 v[36:39], v89 offset:23552\n"
  "v_mfma_f32_16x16x32_bf16 v[148:151], v[112:115], v[128:131], v[148:151]\n"
  "ds_read_b128 v[40:43], v89 offset:24576\n"
  "v_mul_f32 v80, v2, v80\n"
  "v_mfma_f32_16x16x32_bf16 v[152:155], v[112:115], v[132:135], v[152:155]\n"
  "ds_read_b128 v[44:47], v89 offset:25600\n"
  "v_mul_f32 v81, v3, v81\n"
  "v_mfma_f32_16x16x32_bf16 v[156:159], v[112:115], v[136:139], v[156:159]\n"
  "buffer_load_dwordx4 v95, s[40:43], s60 offen offset:1024 lds\n"
  "ds_read_b128 v[48:51], v89 offset:26624\n"
  "v_mfma_f32_16x16x32_bf16 v[164:167], v[116:119], v[124:127], v[164:167]\n"
  "ds_read_b128 v[52:55], v89 offset:27648\n"
  "v_mfma_f32_16x16x32_bf16 v[168:171], v[116:119], v[128:131], v[168:171]\n"
  "v_exp_f32 v80, v80\n"
  "v_mfma_f32_16x16x32_bf16 v[172:175], v[116:119], v[132:135], v[172:175]\n"
  "v_exp_f32 v81, v81\n"
  "v_mfma_f32_16x16x32_bf16 v[176:179], v[116:119], v[136:139], v[176:179]\n"
  "v_mul_f32 v82, v2, v10\n"
  "v_mul_f32 v83, v3, v11\n"
  "v_mfma_f32_16x16x32_bf16 v[184:187], v[120:123], v[124:127], v[184:187]\n"
  "buffer_load_dwordx4 v96, s[40:43], s60 offen offset:2048 lds\n"
  "v_add_f32 v80, 1.0, v80\n"
  "v_mfma_f32_16x16x32_bf16 v[188:191], v[120:123], v[128:131], v[188:191]\n"
  "s_add_u32 s60, s60, 36864\n"
  "v_add_f32 v81, 1.0, v81\n"
  "v_mfma_f32_16x16x32_bf16 v[192:195], v[120:123], v[132:135], v[192:195]\n"
  "v_rcp_f32 v80, v80\n"
  "v_mfma_f32_16x16x32_bf16 v[196:199], v[120:123], v[136:139], v[196:199]\n"
  "s_waitcnt lgkmcnt(0)\n"
  "v_rcp_f32 v81, v81\n"
  "v_mfma_f32_16x16x32_bf16 v[16:19], a[96:99], v[32:35], v[16:19]\n"
  "ds_read_b64_tr_b16 v[112:113], v101 offset:0\n"
  "v_mul_f32 v82, v82, v80\n"
  "v_mfma_f32_16x16x32_bf16 v[16:19], a[112:115], v[36:39], v[16:19]\n"
  "ds_read_b64_tr_b16 v[114:115], v101 offset:8192\n"
  "v_mul_f32 v83, v83, v81\n"
  "v_mfma_f32_16x16x32_bf16 v[16:19], a[128:131], v[40:43], v[16:19]\n"
  "v_cvt_pk_bf16_f32 v85, v82, v83\n"
  "v_mul_f32 v80, v4, v4\n"
  "v_mfma_f32_16x16x32_bf16 v[16:19], a[144:147], v[44:47], v[16:19]\n"
  "ds_read_b128 v[124:127], v91 offset:3072\n"
  "v_mul_f32 v81, v5, v5\n"
  "v_mfma_f32_16x16x32_bf16 v[16:19], a[160:163], v[48:51], v[16:19]\n"
  "v_fmaak_f32 v80, v88, v80, 0xc0135761\n"
  "v_fmaak_f32 v81, v88, v81, 0xc0135761\n"
  "v_mfma_f32_16x16x32_bf16 v[16:19], a[176:179], v[52:55], v[16:19]\n"
  "ds_read_b128 v[128:131], v91 offset:7168\n"
  "v_mul_f32 v80, v4, v80\n"
  "v_mfma_f32_16x16x32_bf16 v[20:23], a[100:103], v[32:35], v[20:23]\n"
  "ds_read_b128 v[132:135], v91 offset:11264\n"
  "v_mul_f32 v81, v5, v81\n"
  "v_mfma_f32_16x16x32_bf16 v[20:23], a[116:119], v[36:39], v[20:23]\n"
  "v_exp_f32 v80, v80\n"
  "v_mfma_f32_16x16x32_bf16 v[20:23], a[132:135], v[40:43], v[20:23]\n"
  "ds_read_b128 v[136:139], v91 offset:15360\n"
  "v_mfma_f32_16x16x32_bf16 v[20:23], a[148:151], v[44:47], v[20:23]\n"
  "v_exp_f32 v81, v81\n"
  "v_mfma_f32_16x16x32_bf16 v[20:23], a[164:167], v[48:51], v[20:23]\n"
  "ds_read_b64_tr_b16 v[116:117], v101 offset:1024\n"
  "v_mul_f32 v82, v4, v12\n"
  "v_mfma_f32_16x16x32_bf16 v[20:23], a[180:183], v[52:55], v[20:23]\n"
  "ds_read_b64_tr_b16 v[118:119], v101 offset:9216\n"
  "v_mul_f32 v83, v5, v13\n"
  "v_mfma_f32_16x16x32_bf16 v[24:27], a[104:107], v[32:35], v[24:27]\n"
  "v_add_f32 v80, 1.0, v80\n"
  "v_add_f32 v81, 1.0, v81\n"
  "v_mfma_f32_16x16x32_bf16 v[24:27], a[120:123], v[36:39], v[24:27]\n"
  "ds_read_b64_tr_b16 v[120:121], v101 offset:2048\n"
  "v_mfma_f32_16x16x32_bf16 v[24:27], a[136:139], v[40:43], v[24:27]\n"
  "v_rcp_f32 v80, v80\n"
  "v_mfma_f32_16x16x32_bf16 v[24:27], a[152:155], v[44:47], v[24:27]\n"
  "ds_read_b64_tr_b16 v[122:123], v101 offset:10240\n"
  "v_mfma_f32_16x16x32_bf16 v[24:27], a[168:171], v[48:51], v[24:27]\n"
  "v_rcp_f32 v81, v81\n"
  "v_mfma_f32_16x16x32_bf16 v[24:27], a[184:187], v[52:55], v[24:27]\n"
  "v_mul_f32 v82, v82, v80\n"
  "v_mul_f32 v83, v83, v81\n"
  "v_mfma_f32_16x16x32_bf16 v[28:31], a[108:111], v[32:35], v[28:31]\n"
  "v_cvt_pk_bf16_f32 v86, v82, v83\n"
  "v_mul_f32 v80, v6, v6\n"
  "v_mfma_f32_16x16x32_bf16 v[28:31], a[124:127], v[36:39], v[28:31]\n"
  "v_mul_f32 v81, v7, v7\n"
  "v_fmaak_f32 v80, v88, v80, 0xc0135761\n"
  "v_mfma_f32_16x16x32_bf16 v[28:31], a[140:143], v[40:43], v[28:31]\n"
  "v_fmaak_f32 v81, v88, v81, 0xc0135761\n"
  "v_mul_f32 v80, v6, v80\n"
  "v_mfma_f32_16x16x32_bf16 v[28:31], a[156:159], v[44:47], v[28:31]\n"
  "v_mul_f32 v81, v7, v81\n"
  "v_mfma_f32_16x16x32_bf16 v[28:31], a[172:175], v[48:51], v[28:31]\n"
  "v_exp_f32 v80, v80\n"
  "v_mfma_f32_16x16x32_bf16 v[28:31], a[188:191], v[52:55], v[28:31]\n"
  "s_waitcnt lgkmcnt(0)\n"
  "v_exp_f32 v81, v81\n"
  "v_mfma_f32_16x16x32_bf16 v[144:147], v[112:115], v[124:127], v[144:147]\n"
  "ds_read_b128 v[56:59], v89 offset:28672\n"
  "ds_read_b128 v[60:63], v89 offset:29696\n"
  "v_mfma_f32_16x16x32_bf16 v[148:151], v[112:115], v[128:131], v[148:151]\n"
  "ds_read_b128 v[64:67], v89 offset:30720\n"
  "v_mul_f32 v82, v6, v14\n"
  "v_mfma_f32_16x16x32_bf16 v[152:155], v[112:115], v[132:135], v[152:155]\n"
  "ds_read_b128 v[68:71], v89 offset:31744\n"
  "v_mul_f32 v83, v7, v15\n"
  "v_mfma_f32_16x16x32_bf16 v[156:159], v[112:115], v[136:139], v[156:159]\n"
  "ds_read_b128 v[72:75], v91 offset:18432\n"
  "v_add_f32 v80, 1.0, v80\n"
  "v_mfma_f32_16x16x32_bf16 v[164:167], v[116:119], v[124:127], v[164:167]\n"
  "ds_read_b128 v[76:79], v91 offset:19456\n"
  "v_add_f32 v81, 1.0, v81\n"
  "v_mfma_f32_16x16x32_bf16 v[168:171], v[116:119], v[128:131], v[168:171]\n"
  "v_rcp_f32 v80, v80\n"
  "v_mfma_f32_16x16x32_bf16 v[172:175], v[116:119], v[132:135], v[172:175]\n"
  "v_rcp_f32 v81, v81\n"
  "v_mfma_f32_16x16x32_bf16 v[176:179], v[116:119], v[136:139], v[176:179]\n"
  "v_mul_f32 v82, v82, v80\n"
  "v_mul_f32 v83, v83, v81\n"
  "v_mfma_f32_16x16x32_bf16 v[184:187], v[120:123], v[124:127], v[184:187]\n"
  "v_cvt_pk_bf16_f32 v87, v82, v83\n"
  "v_mfma_f32_16x16x32_bf16 v[188:191], v[120:123], v[128:131], v[188:191]\n"
  "v_mfma_f32_16x16x32_bf16 v[192:195], v[120:123], v[132:135], v[192:195]\n"
  "v_mfma_f32_16x16x32_bf16 v[196:199], v[120:123], v[136:139], v[196:199]\n"
  "s_waitcnt vmcnt(14) lgkmcnt(0)\n"
  "s_barrier\n"
  "v_mfma_f32_16x16x32_bf16 v[16:19], a[192:195], v[56:59], v[16:19]\n"
  "s_add_u32 m0, s48, 16384\n"
  "ds_read_b128 v[32:35], v89 offset:0\n"
  "v_mfma_f32_16x16x32_bf16 v[16:19], a[208:211], v[60:63], v[16:19]\n"
  "buffer_load_dwordx4 v97, s[36:39], s59 offen offset:3072 sc1 lds\n"
  "v_mfma_f32_16x16x32_bf16 v[16:19], a[224:227], v[64:67], v[16:19]\n"
  "s_add_u32 s59, s59, 1024\n"
  "ds_read_b128 v[36:39], v89 offset:1024\n"
  "v_mfma_f32_16x16x32_bf16 v[16:19], a[240:243], v[68:71], v[16:19]\n"
  "v_mfma_f32_16x16x32_bf16 v[16:19], v[224:227], v[72:75], v[16:19]\n"
  "v_mfma_f32_16x16x32_bf16 v[16:19], v[240:243], v[76:79], v[16:19]\n"
  "ds_read_b128 v[40:43], v89 offset:2048\n"
  "v_mfma_f32_16x16x32_bf16 v[20:23], a[196:199], v[56:59], v[20:23]\n"
  "buffer_load_dwordx4 v94, s[40:43], s60 offen offset:0 lds\n"
  "v_mfma_f32_16x16x32_bf16 v[20:23], a[212:215], v[60:63], v[20:23]\n"
  "v_mfma_f32_16x16x32_bf16 v[20:23], a[228:231], v[64:67], v[20:23]\n"
  "ds_read_b128 v[44:47], v89 offset:3072\n"
  "v_mfma_f32_16x16x32_bf16 v[20:23], a[244:247], v[68:71], v[20:23]\n"
  "v_mfma_f32_16x16x32_bf16 v[20:23], v[228:231], v[72:75], v[20:23]\n"
  "v_mfma_f32_16x16x32_bf16 v[20:23], v[244:247], v[76:79], v[20:23]\n"
  "buffer_load_dwordx4 v95, s[40:43], s60 offen offset:1024 lds\n"
  "ds_read_b128 v[48:51], v89 offset:4096\n"
  "v_mfma_f32_16x16x32_bf16 v[24:27], a[200:203], v[56:59], v[24:27]\n"
  "v_mfma_f32_16x16x32_bf16 v[24:27], a[216:219], v[60:63], v[24:27]\n"
  "v_mfma_f32_16x16x32_bf16 v[24:27], a[232:235], v[64:67], v[24:27]\n"
  "ds_read_b128 v[52:55], v89 offset:5120\n"
  "v_mfma_f32_16x16x32_bf16 v[24:27], a[248:251], v[68:71], v[24:27]\n"
  "v_mfma_f32_16x16x32_bf16 v[24:27], v[232:235], v[72:75], v[24:27]\n"
  "buffer_load_dwordx4 v96, s[40:43], s60 offen offset:2048 lds\n"
  "v_mfma_f32_16x16x32_bf16 v[24:27], v[248:251], v[76:79], v[24:27]\n"
  "s_add_u32 s60, s60, 36864\n"
  "v_mfma_f32_16x16x32_bf16 v[28:31], a[204:207], v[56:59], v[28:31]\n"
  "v_mfma_f32_16x16x32_bf16 v[28:31], a[220:223], v[60:63], v[28:31]\n"
  "v_mfma_f32_16x16x32_bf16 v[28:31], a[236:239], v[64:67], v[28:31]\n"
  "s_add_u32 m0, s48, 49152\n"
  "v_mfma_f32_16x16x32_bf16 v[28:31], a[252:255], v[68:71], v[28:31]\n"
  "buffer_load_dwordx4 v97, s[36:39], s59 offen offset:3072 sc1 lds\n"
  "v_mfma_f32_16x16x32_bf16 v[28:31], v[236:239], v[72:75], v[28:31]\n"
  "s_add_u32 s59, s59, 1024\n"
  "buffer_store_dwordx4 v[84:87], v89, s[36:39], s55 offen sc1\n"
  "v_mfma_f32_16x16x32_bf16 v[28:31], v[252:255], v[76:79], v[28:31]\n"
  "s_waitcnt lgkmcnt(0)\n"
  "v_mfma_f32_16x16x32_bf16 v[0:3], a[0:3], v[32:35], 0\n"
  "s_add_u32 s55, s55, 147456\n"
  "ds_read_b64_tr_b16 v[112:113], v100 offset:0\n"
  "v_mfma_f32_16x16x32_bf16 v[0:3], a[16:19], v[36:39], v[0:3]\n"
  "s_cmp_eq_u32 s55, s57\n"
  "s_cselect_b32 s55, s56, s55\n"
  "ds_read_b64_tr_b16 v[114:115], v100 offset:8192\n"
  "v_mfma_f32_16x16x32_bf16 v[0:3], a[32:35], v[40:43], v[0:3]\n"
  "buffer_load_dwordx4 v94, s[40:43], s60 offen offset:0 lds\n"
  "v_mul_f32 v80, v16, v16\n"
  "v_mfma_f32_16x16x32_bf16 v[0:3], a[48:51], v[44:47], v[0:3]\n"
  "ds_read_b128 v[124:127], v90 offset:3072\n"
  "v_mul_f32 v81, v17, v17\n"
  "v_mfma_f32_16x16x32_bf16 v[0:3], a[64:67], v[48:51], v[0:3]\n"
  "v_fmaak_f32 v80, v88, v80, 0xc0135761\n"
  "v_fmaak_f32 v81, v88, v81, 0xc0135761\n"
  "v_mfma_f32_16x16x32_bf16 v[0:3], a[80:83], v[52:55], v[0:3]\n"
  "ds_read_b128 v[128:131], v90 offset:7168\n"
  "v_mul_f32 v80, v16, v80\n"
  "v_mfma_f32_16x16x32_bf16 v[4:7], a[4:7], v[32:35], 0\n"
  "ds_read_b128 v[132:135], v90 offset:11264\n"
  "v_mul_f32 v81, v17, v81\n"
  "v_mfma_f32_16x16x32_bf16 v[4:7], a[20:23], v[36:39], v[4:7]\n"
  "buffer_load_dwordx4 v95, s[40:43], s60 offen offset:1024 lds\n"
  "v_mfma_f32_16x16x32_bf16 v[4:7], a[36:39], v[40:43], v[4:7]\n"
  "ds_read_b128 v[136:139], v90 offset:15360\n"
  "v_mfma_f32_16x16x32_bf16 v[4:7], a[52:55], v[44:47], v[4:7]\n"
  "v_exp_f32 v80, v80\n"
  "v_mfma_f32_16x16x32_bf16 v[4:7], a[68:71], v[48:51], v[4:7]\n"
  "ds_read_b64_tr_b16 v[116:117], v100 offset:1024\n"
  "v_mfma_f32_16x16x32_bf16 v[4:7], a[84:87], v[52:55], v[4:7]\n"
  "ds_read_b64_tr_b16 v[118:119], v100 offset:9216\n"
  "v_mfma_f32_16x16x32_bf16 v[8:11], a[8:11], v[32:35], 0\n"
  "buffer_load_dwordx4 v96, s[40:43], s60 offen offset:2048 lds\n"
  "v_mfma_f32_16x16x32_bf16 v[8:11], a[24:27], v[36:39], v[8:11]\n"
  "s_add_u32 s60, s60, 36864\n"
  "ds_read_b64_tr_b16 v[120:121], v100 offset:2048\n"
  "v_mfma_f32_16x16x32_bf16 v[8:11], a[40:43], v[40:43], v[8:11]\n"
  "v_exp_f32 v81, v81\n"
  "v_mfma_f32_16x16x32_bf16 v[8:11], a[56:59], v[44:47], v[8:11]\n"
  "ds_read_b64_tr_b16 v[122:123], v100 offset:10240\n"
  "v_mul_f32 v82, v16, v24\n"
  "v_mfma_f32_16x16x32_bf16 v[8:11], a[72:75], v[48:51], v[8:11]\n"
  "s_add_u32 m0, s48, 131072\n"
  "v_mul_f32 v83, v17, v25\n"
  "v_add_f32 v80, 1.0, v80\n"
  "v_mfma_f32_16x16x32_bf16 v[8:11], a[88:91], v[52:55], v[8:11]\n"
  "buffer_load_dwordx4 v97, s[36:39], s59 offen offset:3072 sc1 lds\n"
  "v_add_f32 v81, 1.0, v81\n"
  "s_cbranch_execz Ltramp%=\n"
  "v_mfma_f32_16x16x32_bf16 v[12:15], a[12:15], v[32:35], 0\n"
  "s_add_u32 s59, s59, 1024\n"
  "v_rcp_f32 v80, v80\n"
  "v_mfma_f32_16x16x32_bf16 v[12:15], a[28:31], v[36:39], v[12:15]\n"
  "v_rcp_f32 v81, v81\n"
  "v_mfma_f32_16x16x32_bf16 v[12:15], a[44:47], v[40:43], v[12:15]\n"
  "v_mul_f32 v82, v82, v80\n"
  "v_mul_f32 v83, v83, v81\n"
  "v_mfma_f32_16x16x32_bf16 v[12:15], a[60:63], v[44:47], v[12:15]\n"
  "v_cvt_pk_bf16_f32 v84, v82, v83\n"
  "v_mul_f32 v80, v18, v18\n"
  "v_mfma_f32_16x16x32_bf16 v[12:15], a[76:79], v[48:51], v[12:15]\n"
  "buffer_load_dwordx4 v94, s[40:43], s60 offen offset:0 lds\n"
  "v_mul_f32 v81, v19, v19\n"
  "v_mfma_f32_16x16x32_bf16 v[12:15], a[92:95], v[52:55], v[12:15]\n"
  "s_waitcnt lgkmcnt(0)\n"
  "v_fmaak_f32 v80, v88, v80, 0xc0135761\n"
  "v_fmaak_f32 v81, v88, v81, 0xc0135761\n"
  "v_mfma_f32_16x16x32_bf16 v[144:147], v[112:115], v[124:127], v[144:147]\n"
  "ds_read_b128 v[56:59], v89 offset:6144\n"
  "ds_read_b128 v[60:63], v89 offset:7168\n"
  "v_mfma_f32_16x16x32_bf16 v[148:151], v[112:115], v[128:131], v[148:151]\n"
  "ds_read_b128 v[64:67], v89 offset:8192\n"
  "v_mul_f32 v80, v18, v80\n"
  "v_mfma_f32_16x16x32_bf16 v[152:155], v[112:115], v[132:135], v[152:155]\n"
  "ds_read_b128 v[68:71], v89 offset:9216\n"
  "v_mul_f32 v81, v19, v81\n"
  "v_mfma_f32_16x16x32_bf16 v[156:159], v[112:115], v[136:139], v[156:159]\n"
  "buffer_load_dwordx4 v95, s[40:43], s60 offen offset:1024 lds\n"
  "ds_read_b128 v[72:75], v89 offset:10240\n"
  "v_mfma_f32_16x16x32_bf16 v[164:167], v[116:119], v[124:127], v[164:167]\n"
  "ds_read_b128 v[76:79], v89 offset:11264\n"
  "v_mfma_f32_16x16x32_bf16 v[168:171], v[116:119], v[128:131], v[168:171]\n"
  "v_exp_f32 v80, v80\n"
  "v_mfma_f32_16x16x32_bf16 v[172:175], v[116:119], v[132:135], v[172:175]\n"
  "v_exp_f32 v81, v81\n"
  "v_mfma_f32_16x16x32_bf16 v[176:179], v[116:119], v[136:139], v[176:179]\n"
  "v_mul_f32 v82, v18, v26\n"
  "v_mul_f32 v83, v19, v27\n"
  "v_mfma_f32_16x16x32_bf16 v[184:187], v[120:123], v[124:127], v[184:187]\n"
  "buffer_load_dwordx4 v96, s[40:43], s60 offen offset:2048 lds\n"
  "v_add_f32 v80, 1.0, v80\n"
  "v_mfma_f32_16x16x32_bf16 v[188:191], v[120:123], v[128:131], v[188:191]\n"
  "s_add_u32 s60, s60, 36864\n"
  "v_add_f32 v81, 1.0, v81\n"
  "v_mfma_f32_16x16x32_bf16 v[192:195], v[120:123], v[132:135], v[192:195]\n"
  "v_rcp_f32 v80, v80\n"
  "v_mfma_f32_16x16x32_bf16 v[196:199], v[120:123], v[136:139], v[196:199]\n"
  "s_waitcnt lgkmcnt(0)\n"
  "v_rcp_f32 v81, v81\n"
  "v_mfma_f32_16x16x32_bf16 v[0:3], a[96:99], v[56:59], v[0:3]\n"
  "ds_read_b64_tr_b16 v[112:113], v100 offset:16384\n"
  "v_mul_f32 v82, v82, v80\n"
  "v_mfma_f32_16x16x32_bf16 v[0:3], a[112:115], v[60:63], v[0:3]\n"
  "ds_read_b64_tr_b16 v[114:115], v100 offset:24576\n"
  "v_mul_f32 v83, v83, v81\n"
  "v_mfma_f32_16x16x32_bf16 v[0:3], a[128:131], v[64:67], v[0:3]\n"
  "v_cvt_pk_bf16_f32 v85, v82, v83\n"
  "v_mul_f32 v80, v20, v20\n"
  "v_mfma_f32_16x16x32_bf16 v[0:3], a[144:147], v[68:71], v[0:3]\n"
  "ds_read_b128 v[124:127], v90 offset:19456\n"
  "v_mul_f32 v81, v21, v21\n"
  "v_mfma_f32_16x16x32_bf16 v[0:3], a[160:163], v[72:75], v[0:3]\n"
  "v_fmaak_f32 v80, v88, v80, 0xc0135761\n"
  "v_fmaak_f32 v81, v88, v81, 0xc0135761\n"
  "v_mfma_f32_16x16x32_bf16 v[0:3], a[176:179], v[76:79], v[0:3]\n"
  "ds_read_b128 v[128:131], v90 offset:23552\n"
  "v_mul_f32 v80, v20, v80\n"
  "v_mfma_f32_16x16x32_bf16 v[4:7], a[100:103], v[56:59], v[4:7]\n"
  "ds_read_b128 v[132:135], v90 offset:27648\n"
  "v_mul_f32 v81, v21, v81\n"
  "v_mfma_f32_16x16x32_bf16 v[4:7], a[116:119], v[60:63], v[4:7]\n"
  "v_exp_f32 v80, v80\n"
  "v_mfma_f32_16x16x32_bf16 v[4:7], a[132:135], v[64:67], v[4:7]\n"
  "ds_read_b128 v[136:139], v90 offset:31744\n"
  "v_mfma_f32_16x16x32_bf16 v[4:7], a[148:151], v[68:71], v[4:7]\n"
  "v_exp_f32 v81, v81\n"
  "v_mfma_f32_16x16x32_bf16 v[4:7], a[164:167], v[72:75], v[4:7]\n"
  "ds_read_b64_tr_b16 v[116:117], v100 offset:17408\n"
  "v_mul_f32 v82, v20, v28\n"
  "v_mfma_f32_16x16x32_bf16 v[4:7], a[180:183], v[76:79], v[4:7]\n"
  "ds_read_b64_tr_b16 v[118:119], v100 offset:25600\n"
  "v_mul_f32 v83, v21, v29\n"
  "v_mfma_f32_16x16x32_bf16 v[8:11], a[104:107], v[56:59], v[8:11]\n"
  "v_add_f32 v80, 1.0, v80\n"
  "v_add_f32 v81, 1.0, v81\n"
  "v_mfma_f32_16x16x32_bf16 v[8:11], a[120:123], v[60:63], v[8:11]\n"
  "ds_read_b64_tr_b16 v[120:121], v100 offset:18432\n"
  "v_mfma_f32_16x16x32_bf16 v[8:11], a[136:139], v[64:67], v[8:11]\n"
  "v_rcp_f32 v80, v80\n"
  "v_mfma_f32_16x16x32_bf16 v[8:11], a[152:155], v[68:71], v[8:11]\n"
  "ds_read_b64_tr_b16 v[122:123], v100 offset:26624\n"
  "v_mfma_f32_16x16x32_bf16 v[8:11], a[168:171], v[72:75], v[8:11]\n"
  "v_rcp_f32 v81, v81\n"
  "v_mfma_f32_16x16x32_bf16 v[8:11], a[184:187], v[76:79], v[8:11]\n"
  "v_mul_f32 v82, v82, v80\n"
  "v_mul_f32 v83, v83, v81\n"
  "v_mfma_f32_16x16x32_bf16 v[12:15], a[108:111], v[56:59], v[12:15]\n"
  "v_cvt_pk_bf16_f32 v86, v82, v83\n"
  "v_mul_f32 v80, v22, v22\n"
  "v_mfma_f32_16x16x32_bf16 v[12:15], a[124:127], v[60:63], v[12:15]\n"
  "v_mul_f32 v81, v23, v23\n"
  "v_fmaak_f32 v80, v88, v80, 0xc0135761\n"
  "v_mfma_f32_16x16x32_bf16 v[12:15], a[140:143], v[64:67], v[12:15]\n"
  "v_fmaak_f32 v81, v88, v81, 0xc0135761\n"
  "v_mul_f32 v80, v22, v80\n"
  "v_mfma_f32_16x16x32_bf16 v[12:15], a[156:159], v[68:71], v[12:15]\n"
  "v_mul_f32 v81, v23, v81\n"
  "v_mfma_f32_16x16x32_bf16 v[12:15], a[172:175], v[72:75], v[12:15]\n"
  "v_exp_f32 v80, v80\n"
  "v_mfma_f32_16x16x32_bf16 v[12:15], a[188:191], v[76:79], v[12:15]\n"
  "s_waitcnt lgkmcnt(0)\n"
  "v_exp_f32 v81, v81\n"
  "v_mfma_f32_16x16x32_bf16 v[144:147], v[112:115], v[124:127], v[144:147]\n"
  "ds_read_b128 v[32:35], v89 offset:12288\n"
  "ds_read_b128 v[36:39], v89 offset:13312\n"
  "v_mfma_f32_16x16x32_bf16 v[148:151], v[112:115], v[128:131], v[148:151]\n"
  "ds_read_b128 v[40:43], v89 offset:14336\n"
  "v_mul_f32 v82, v22, v30\n"
  "v_mfma_f32_16x16x32_bf16 v[152:155], v[112:115], v[132:135], v[152:155]\n"
  "ds_read_b128 v[44:47], v89 offset:15360\n"
  "v_mul_f32 v83, v23, v31\n"
  "v_mfma_f32_16x16x32_bf16 v[156:159], v[112:115], v[136:139], v[156:159]\n"
  "ds_read_b128 v[48:51], v91 offset:30720\n"
  "v_add_f32 v80, 1.0, v80\n"
  "v_mfma_f32_16x16x32_bf16 v[164:167], v[116:119], v[124:127], v[164:167]\n"
  "ds_read_b128 v[52:55], v91 offset:31744\n"
  "v_add_f32 v81, 1.0, v81\n"
  "v_mfma_f32_16x16x32_bf16 v[168:171], v[116:119], v[128:131], v[168:171]\n"
  "v_rcp_f32 v80, v80\n"
  "v_mfma_f32_16x16x32_bf16 v[172:175], v[116:119], v[132:135], v[172:175]\n"
  "v_rcp_f32 v81, v81\n"
  "v_mfma_f32_16x16x32_bf16 v[176:179], v[116:119], v[136:139], v[176:179]\n"
  "v_mul_f32 v82, v82, v80\n"
  "v_mul_f32 v83, v83, v81\n"
  "v_mfma_f32_16x16x32_bf16 v[184:187], v[120:123], v[124:127], v[184:187]\n"
  "v_cvt_pk_bf16_f32 v87, v82, v83\n"
  "v_mfma_f32_16x16x32_bf16 v[188:191], v[120:123], v[128:131], v[188:191]\n"
  "v_mfma_f32_16x16x32_bf16 v[192:195], v[120:123], v[132:135], v[192:195]\n"
  "v_mfma_f32_16x16x32_bf16 v[196:199], v[120:123], v[136:139], v[196:199]\n"
  "s_waitcnt vmcnt(22) lgkmcnt(0)\n"
  "s_barrier\n"
  "v_mfma_f32_16x16x32_bf16 v[0:3], a[192:195], v[32:35], v[0:3]\n"
  "s_add_u32 m0, s48, 0\n"
  "ds_read_b64_tr_b16 v[112:113], v99 offset:32768\n"
  "v_mfma_f32_16x16x32_bf16 v[0:3], a[208:211], v[36:39], v[0:3]\n"
  "buffer_load_dwordx4 v97, s[36:39], s59 offen offset:3072 sc1 lds\n"
  "ds_read_b64_tr_b16 v[114:115], v99 offset:40960\n"
  "v_mfma_f32_16x16x32_bf16 v[0:3], a[224:227], v[40:43], v[0:3]\n"
  "s_add_u32 s59, s59, 1024\n"
  "v_mfma_f32_16x16x32_bf16 v[0:3], a[240:243], v[44:47], v[0:3]\n"
  "ds_read_b128 v[124:127], v89 offset:35840\n"
  "v_mfma_f32_16x16x32_bf16 v[0:3], v[224:227], v[48:51], v[0:3]\n"
  "v_mfma_f32_16x16x32_bf16 v[0:3], v[240:243], v[52:55], v[0:3]\n"
  "ds_read_b128 v[128:131], v89 offset:39936\n"
  "v_mfma_f32_16x16x32_bf16 v[4:7], a[196:199], v[32:35], v[4:7]\n"
  "buffer_load_dwordx4 v94, s[40:43], s60 offen offset:0 lds\n"
  "ds_read_b128 v[132:135], v89 offset:44032\n"
  "v_mfma_f32_16x16x32_bf16 v[4:7], a[212:215], v[36:39], v[4:7]\n"
  "v_mfma_f32_16x16x32_bf16 v[4:7], a[228:231], v[40:43], v[4:7]\n"
  "ds_read_b128 v[136:139], v89 offset:48128\n"
  "v_mfma_f32_16x16x32_bf16 v[4:7], a[244:247], v[44:47], v[4:7]\n"
  "v_mfma_f32_16x16x32_bf16 v[4:7], v[228:231], v[48:51], v[4:7]\n"
  "ds_read_b64_tr_b16 v[116:117], v99 offset:33792\n"
  "v_mfma_f32_16x16x32_bf16 v[4:7], v[244:247], v[52:55], v[4:7]\n"
  "buffer_load_dwordx4 v95, s[40:43], s60 offen offset:1024 lds\n"
  "ds_read_b64_tr_b16 v[118:119], v99 offset:41984\n"
  "v_mfma_f32_16x16x32_bf16 v[8:11], a[200:203], v[32:35], v[8:11]\n"
  "v_mfma_f32_16x16x32_bf16 v[8:11], a[216:219], v[36:39], v[8:11]\n"
  "ds_read_b64_tr_b16 v[120:121], v99 offset:34816\n"
  "v_mfma_f32_16x16x32_bf16 v[8:11], a[232:235], v[40:43], v[8:11]\n"
  "v_mfma_f32_16x16x32_bf16 v[8:11], a[248:251], v[44:47], v[8:11]\n"
  "ds_read_b64_tr_b16 v[122:123], v99 offset:43008\n"
  "v_mfma_f32_16x16x32_bf16 v[8:11], v[232:235], v[48:51], v[8:11]\n"
  "buffer_load_dwordx4 v96, s[40:43], s60 offen offset:2048 lds\n"
  "v_mfma_f32_16x16x32_bf16 v[8:11], v[248:251], v[52:55], v[8:11]\n"
  "s_add_u32 s60, s60, 36864\n"
  "v_mfma_f32_16x16x32_bf16 v[12:15], a[204:207], v[32:35], v[12:15]\n"
  "v_mfma_f32_16x16x32_bf16 v[12:15], a[220:223], v[36:39], v[12:15]\n"
  "v_mfma_f32_16x16x32_bf16 v[12:15], a[236:239], v[40:43], v[12:15]\n"
  "s_add_u32 m0, s48, 65536\n"
  "v_mfma_f32_16x16x32_bf16 v[12:15], a[252:255], v[44:47], v[12:15]\n"
  "buffer_load_dwordx4 v97, s[36:39], s59 offen offset:3072 sc1 lds\n"
  "v_mfma_f32_16x16x32_bf16 v[12:15], v[236:239], v[48:51], v[12:15]\n"
  "s_add_u32 s59, s59, 1024\n"
  "buffer_store_dwordx4 v[84:87], v89, s[36:39], s55 offen sc1\n"
  "v_mfma_f32_16x16x32_bf16 v[12:15], v[252:255], v[52:55], v[12:15]\n"
  "s_waitcnt vmcnt(23) lgkmcnt(0)\n"
  "s_barrier\n"
  "v_mfma_f32_16x16x32_bf16 v[144:147], v[112:115], v[124:127], v[144:147]\n"
  "s_add_u32 s55, s55, 147456\n"
  "ds_read_b64_tr_b16 v[32:33], v100 offset:32768\n"
  "ds_read_b64_tr_b16 v[34:35], v100 offset:40960\n"
  "v_mfma_f32_16x16x32_bf16 v[148:151], v[112:115], v[128:131], v[148:151]\n"
  "s_cmp_eq_u32 s55, s57\n"
  "s_cselect_b32 s55, s56, s55\n"
  "ds_read_b128 v[44:47], v90 offset:35840\n"
  "v_mul_f32 v80, v0, v0\n"
  "v_mul_f32 v81, v1, v1\n"
  "v_mfma_f32_16x16x32_bf16 v[152:155], v[112:115], v[132:135], v[152:155]\n"
  "ds_read_b128 v[48:51], v90 offset:39936\n"
  "ds_read_b128 v[52:55], v90 offset:44032\n"
  "v_fmaak_f32 v80, v88, v80, 0xc0135761\n"
  "v_fmaak_f32 v81, v88, v81, 0xc0135761\n"
  "v_mfma_f32_16x16x32_bf16 v[156:159], v[112:115], v[136:139], v[156:159]\n"
  "buffer_load_dwordx4 v94, s[40:43], s60 offen offset:0 lds\n"
  "ds_read_b128 v[56:59], v90 offset:48128\n"
  "v_mul_f32 v80, v0, v80\n"
  "v_mul_f32 v81, v1, v81\n"
  "v_mfma_f32_16x16x32_bf16 v[164:167], v[116:119], v[124:127], v[164:167]\n"
  "ds_read_b64_tr_b16 v[36:37], v100 offset:33792\n"
  "ds_read_b64_tr_b16 v[38:39], v100 offset:41984\n"
  "v_exp_f32 v80, v80\n"
  "v_mfma_f32_16x16x32_bf16 v[168:171], v[116:119], v[128:131], v[168:171]\n"
  "ds_read_b64_tr_b16 v[40:41], v100 offset:34816\n"
  "v_exp_f32 v81, v81\n"
  "v_mul_f32 v82, v0, v8\n"
  "v_mfma_f32_16x16x32_bf16 v[172:175], v[116:119], v[132:135], v[172:175]\n"
  "ds_read_b64_tr_b16 v[42:43], v100 offset:43008\n"
  "v_mul_f32 v83, v1, v9\n"
  "v_add_f32 v80, 1.0, v80\n"
  "v_add_f32 v81, 1.0, v81\n"
  "v_mfma_f32_16x16x32_bf16 v[176:179], v[116:119], v[136:139], v[176:179]\n"
  "buffer_load_dwordx4 v95, s[40:43], s60 offen offset:1024 lds\n"
  "v_rcp_f32 v80, v80\n"
  "v_mfma_f32_16x16x32_bf16 v[184:187], v[120:123], v[124:127], v[184:187]\n"
  "v_rcp_f32 v81, v81\n"
  "v_mul_f32 v82, v82, v80\n"
  "v_mul_f32 v83, v83, v81\n"
  "v_mfma_f32_16x16x32_bf16 v[188:191], v[120:123], v[128:131], v[188:191]\n"
  "v_cvt_pk_bf16_f32 v84, v82, v83\n"
  "v_mul_f32 v80, v2, v2\n"
  "v_mul_f32 v81, v3, v3\n"
  "v_fmaak_f32 v80, v88, v80, 0xc0135761\n"
  "v_mfma_f32_16x16x32_bf16 v[192:195], v[120:123], v[132:135], v[192:195]\n"
  "v_fmaak_f32 v81, v88, v81, 0xc0135761\n"
  "v_mul_f32 v80, v2, v80\n"
  "v_mul_f32 v81, v3, v81\n"
  "v_mfma_f32_16x16x32_bf16 v[196:199], v[120:123], v[136:139], v[196:199]\n"
  "s_waitcnt vmcnt(21) lgkmcnt(0)\n"
  "s_barrier\n"
  "v_mfma_f32_16x16x32_bf16 v[144:147], v[32:35], v[44:47], v[144:147]\n"
  "ds_read_b64_tr_b16 v[112:113], v100 offset:49152\n"
  "ds_read_b64_tr_b16 v[114:115], v100 offset:57344\n"
  "v_exp_f32 v80, v80\n"
  "v_mfma_f32_16x16x32_bf16 v[148:151], v[32:35], v[48:51], v[148:151]\n"
  "buffer_load_dwordx4 v96, s[40:43], s60 offen offset:2048 lds\n"
  "ds_read_b128 v[124:127], v90 offset:52224\n"
  "v_exp_f32 v81, v81\n"
  "v_mfma_f32_16x16x32_bf16 v[152:155], v[32:35], v[52:55], v[152:155]\n"
  "s_add_u32 s60, s60, 36864\n"
  "ds_read_b128 v[128:131], v90 offset:56320\n"
  "ds_read_b128 v[132:135], v90 offset:60416\n"
  "v_mul_f32 v82, v2, v10\n"
  "v_mul_f32 v83, v3, v11\n"
  "v_mfma_f32_16x16x32_bf16 v[156:159], v[32:35], v[56:59], v[156:159]\n"
  "ds_read_b128 v[136:139], v90 offset:64512\n"
  "s_mov_b64 exec, 7\n"
  "buffer_load_dword v106, v105, s[24:27], 0 offen sc1\n"
  "s_mov_b64 exec, -1\n"
  "v_mfma_f32_16x16x32_bf16 v[164:167], v[36:39], v[44:47], v[164:167]\n"
  "s_add_u32 m0, s48, 81920\n"
  "ds_read_b64_tr_b16 v[116:117], v100 offset:50176\n"
  "ds_read_b64_tr_b16 v[118:119], v100 offset:58368\n"
  "v_add_f32 v80, 1.0, v80\n"
  "v_add_f32 v81, 1.0, v81\n"
  "v_mfma_f32_16x16x32_bf16 v[168:171], v[36:39], v[48:51], v[168:171]\n"
  "buffer_load_dwordx4 v97, s[36:39], s59 offen offset:3072 sc1 lds\n"
  "ds_read_b64_tr_b16 v[120:121], v100 offset:51200\n"
  "v_rcp_f32 v80, v80\n"
  "v_mfma_f32_16x16x32_bf16 v[172:175], v[36:39], v[52:55], v[172:175]\n"
  "s_add_u32 s59, s59, 1024\n"
  "ds_read_b64_tr_b16 v[122:123], v100 offset:59392\n"
  "v_rcp_f32 v81, v81\n"
  "v_mul_f32 v82, v82, v80\n"
  "v_mfma_f32_16x16x32_bf16 v[176:179], v[36:39], v[56:59], v[176:179]\n"
  "v_mul_f32 v83, v83, v81\n"
  "v_cvt_pk_bf16_f32 v85, v82, v83\n"
  "v_mul_f32 v80, v4, v4\n"
  "v_mul_f32 v81, v5, v5\n"
  "v_mfma_f32_16x16x32_bf16 v[184:187], v[40:43], v[44:47], v[184:187]\n"
  "v_fmaak_f32 v80, v88, v80, 0xc0135761\n"
  "v_fmaak_f32 v81, v88, v81, 0xc0135761\n"
  "v_mul_f32 v80, v4, v80\n"
  "v_mul_f32 v81, v5, v81\n"
  "v_mfma_f32_16x16x32_bf16 v[188:191], v[40:43], v[48:51], v[188:191]\n"
  "v_exp_f32 v80, v80\n"
  "v_exp_f32 v81, v81\n"
  "v_mfma_f32_16x16x32_bf16 v[192:195], v[40:43], v[52:55], v[192:195]\n"
  "buffer_load_dwordx4 v94, s[40:43], s60 offen offset:0 lds\n"
  "v_mul_f32 v82, v4, v12\n"
  "v_mul_f32 v83, v5, v13\n"
  "v_add_f32 v80, 1.0, v80\n"
  "v_mfma_f32_16x16x32_bf16 v[196:199], v[40:43], v[56:59], v[196:199]\n"
  "s_waitcnt vmcnt(21) lgkmcnt(0)\n"
  "s_barrier\n"
  "v_mfma_f32_16x16x32_bf16 v[144:147], v[112:115], v[124:127], v[144:147]\n"
  "ds_read_b64_tr_b16 v[32:33], v99 offset:16384\n"
  "ds_read_b64_tr_b16 v[34:35], v99 offset:24576\n"
  "v_add_f32 v81, 1.0, v81\n"
  "v_mfma_f32_16x16x32_bf16 v[148:151], v[112:115], v[128:131], v[148:151]\n"
  "ds_read_b128 v[44:47], v89 offset:19456\n"
  "v_rcp_f32 v80, v80\n"
  "v_mfma_f32_16x16x32_bf16 v[152:155], v[112:115], v[132:135], v[152:155]\n"
  "ds_read_b128 v[48:51], v89 offset:23552\n"
  "ds_read_b128 v[52:55], v89 offset:27648\n"
  "v_rcp_f32 v81, v81\n"
  "v_mfma_f32_16x16x32_bf16 v[156:159], v[112:115], v[136:139], v[156:159]\n"
  "buffer_load_dwordx4 v95, s[40:43], s60 offen offset:1024 lds\n"
  "ds_read_b128 v[56:59], v89 offset:31744\n"
  "v_mul_f32 v82, v82, v80\n"
  "v_mul_f32 v83, v83, v81\n"
  "v_mfma_f32_16x16x32_bf16 v[164:167], v[116:119], v[124:127], v[164:167]\n"
  "ds_read_b64_tr_b16 v[36:37], v99 offset:17408\n"
  "ds_read_b64_tr_b16 v[38:39], v99 offset:25600\n"
  "v_cvt_pk_bf16_f32 v86, v82, v83\n"
  "v_mul_f32 v80, v6, v6\n"
  "v_mfma_f32_16x16x32_bf16 v[168:171], v[116:119], v[128:131], v[168:171]\n"
  "ds_read_b64_tr_b16 v[40:41], v99 offset:18432\n"
  "v_mul_f32 v81, v7, v7\n"
  "v_fmaak_f32 v80, v88, v80, 0xc0135761\n"
  "v_fmaak_f32 v81, v88, v81, 0xc0135761\n"
  "v_mfma_f32_16x16x32_bf16 v[172:175], v[116:119], v[132:135], v[172:175]\n"
  "ds_read_b64_tr_b16 v[42:43], v99 offset:26624\n"
  "v_mul_f32 v80, v6, v80\n"
  "v_mul_f32 v81, v7, v81\n"
  "v_mfma_f32_16x16x32_bf16 v[176:179], v[116:119], v[136:139], v[176:179]\n"
  "v_exp_f32 v80, v80\n"
  "v_exp_f32 v81, v81\n"
  "v_mfma_f32_16x16x32_bf16 v[184:187], v[120:123], v[124:127], v[184:187]\n"
  "buffer_load_dwordx4 v96, s[40:43], s60 offen offset:2048 lds\n"
  "v_mul_f32 v82, v6, v14\n"
  "v_mul_f32 v83, v7, v15\n"
  "v_add_f32 v80, 1.0, v80\n"
  "v_mfma_f32_16x16x32_bf16 v[188:191], v[120:123], v[128:131], v[188:191]\n"
  "s_add_u32 s60, s60, 36864\n"
  "v_add_f32 v81, 1.0, v81\n"
  "v_rcp_f32 v80, v80\n"
  "v_mfma_f32_16x16x32_bf16 v[192:195], v[120:123], v[132:135], v[192:195]\n"
  "v_rcp_f32 v81, v81\n"
  "v_mul_f32 v82, v82, v80\n"
  "v_mul_f32 v83, v83, v81\n"
  "v_mfma_f32_16x16x32_bf16 v[196:199], v[120:123], v[136:139], v[196:199]\n"
  "s_waitcnt vmcnt(18) lgkmcnt(0)\n"
  "s_barrier\n"
  "v_mfma_f32_16x16x32_bf16 v[144:147], v[32:35], v[44:47], v[144:147]\n"
  "s_add_u32 m0, s48, 147456\n"
  "ds_read_b64_tr_b16 v[112:113], v99 offset:49152\n"
  "ds_read_b64_tr_b16 v[114:115], v99 offset:57344\n"
  "v_cvt_pk_bf16_f32 v87, v82, v83\n"
  "v_mfma_f32_16x16x32_bf16 v[148:151], v[32:35], v[48:51], v[148:151]\n"
  "buffer_load_dwordx4 v97, s[36:39], s59 offen offset:3072 sc1 lds\n"
  "ds_read_b128 v[124:127], v89 offset:52224\n"
  "v_mfma_f32_16x16x32_bf16 v[152:155], v[32:35], v[52:55], v[152:155]\n"
  "s_add_u32 s59, s59, 25600\n"
  "ds_read_b128 v[128:131], v89 offset:56320\n"
  "ds_read_b128 v[132:135], v89 offset:60416\n"
  "v_mfma_f32_16x16x32_bf16 v[156:159], v[32:35], v[56:59], v[156:159]\n"
  "ds_read_b128 v[136:139], v89 offset:64512\n"
  "v_mfma_f32_16x16x32_bf16 v[164:167], v[36:39], v[44:47], v[164:167]\n"
  "ds_read_b64_tr_b16 v[116:117], v99 offset:50176\n"
  "ds_read_b64_tr_b16 v[118:119], v99 offset:58368\n"
  "v_mfma_f32_16x16x32_bf16 v[168:171], v[36:39], v[48:51], v[168:171]\n"
  "ds_read_b64_tr_b16 v[120:121], v99 offset:51200\n"
  "v_mfma_f32_16x16x32_bf16 v[172:175], v[36:39], v[52:55], v[172:175]\n"
  "buffer_load_dwordx4 v94, s[40:43], s60 offen offset:0 lds\n"
  "ds_read_b64_tr_b16 v[122:123], v99 offset:59392\n"
  "v_mfma_f32_16x16x32_bf16 v[176:179], v[36:39], v[56:59], v[176:179]\n"
  "v_mfma_f32_16x16x32_bf16 v[184:187], v[40:43], v[44:47], v[184:187]\n"
  "v_mfma_f32_16x16x32_bf16 v[188:191], v[40:43], v[48:51], v[188:191]\n"
  "buffer_store_dwordx4 v[84:87], v89, s[36:39], s55 offen sc1\n"
  "v_mfma_f32_16x16x32_bf16 v[192:195], v[40:43], v[52:55], v[192:195]\n"
  "v_mfma_f32_16x16x32_bf16 v[196:199], v[40:43], v[56:59], v[196:199]\n"
  "s_waitcnt vmcnt(17) lgkmcnt(0)\n"
  "s_barrier\n"
  "v_mfma_f32_16x16x32_bf16 v[144:147], v[112:115], v[124:127], v[144:147]\n"
  "ds_read_b64_tr_b16 v[32:33], v101 offset:0\n"
  "ds_read_b64_tr_b16 v[34:35], v101 offset:8192\n"
  "v_mfma_f32_16x16x32_bf16 v[148:151], v[112:115], v[128:131], v[148:151]\n"
  "buffer_load_dwordx4 v95, s[40:43], s60 offen offset:1024 lds\n"
  "ds_read_b128 v[44:47], v91 offset:3072\n"
  "v_mfma_f32_16x16x32_bf16 v[152:155], v[112:115], v[132:135], v[152:155]\n"
  "ds_read_b128 v[48:51], v91 offset:7168\n"
  "ds_read_b128 v[52:55], v91 offset:11264\n"
  "v_mfma_f32_16x16x32_bf16 v[156:159], v[112:115], v[136:139], v[156:159]\n"
  "buffer_load_dwordx4 v96, s[40:43], s60 offen offset:2048 lds\n"
  "ds_read_b128 v[56:59], v91 offset:15360\n"
  "v_mfma_f32_16x16x32_bf16 v[164:167], v[116:119], v[124:127], v[164:167]\n"
  "s_add_u32 s60, s60, 921600\n"
  "ds_read_b64_tr_b16 v[36:37], v101 offset:1024\n"
  "ds_read_b64_tr_b16 v[38:39], v101 offset:9216\n"
  "v_mfma_f32_16x16x32_bf16 v[168:171], v[116:119], v[128:131], v[168:171]\n"
  "s_cmp_ge_u32 s60, 5308416\n"
  "s_cselect_b32 s64, 5308416, 0\n"
  "s_cselect_b32 s65, 147456, 0\n"
  "s_sub_u32 s60, s60, s64\n"
  "s_sub_u32 s59, s59, s65\n"
  "ds_read_b64_tr_b16 v[40:41], v101 offset:2048\n"
  "s_waitcnt vmcnt(9)\n"
  "v_cmp_gt_u32 vcc, 12, v106\n"
  "s_and_b64 vcc, vcc, 7\n"
  "s_cbranch_scc1 Lpollp24B%=\n"
  "Lpollrp24B%=:\n"
  "v_mfma_f32_16x16x32_bf16 v[172:175], v[116:119], v[132:135], v[172:175]\n"
  "s_add_u32 m0, s48, 32768\n"
  "ds_read_b64_tr_b16 v[42:43], v101 offset:10240\n"
  "v_mfma_f32_16x16x32_bf16 v[176:179], v[116:119], v[136:139], v[176:179]\n"
  "buffer_load_dwordx4 v97, s[36:39], s59 offen offset:3072 sc1 lds\n"
  "v_mfma_f32_16x16x32_bf16 v[184:187], v[120:123], v[124:127], v[184:187]\n"
  "s_add_u32 s59, s59, 1024\n"
  "v_mfma_f32_16x16x32_bf16 v[188:191], v[120:123], v[128:131], v[188:191]\n"
  "buffer_load_dwordx4 v94, s[40:43], s60 offen offset:0 lds\n"
  "v_mfma_f32_16x16x32_bf16 v[192:195], v[120:123], v[132:135], v[192:195]\n"
  "v_mfma_f32_16x16x32_bf16 v[196:199], v[120:123], v[136:139], v[196:199]\n"
  "s_waitcnt vmcnt(17) lgkmcnt(0)\n"
  "s_barrier\n"
  "v_mfma_f32_16x16x32_bf16 v[144:147], v[32:35], v[44:47], v[144:147]\n"
  "ds_read_b64_tr_b16 v[112:113], v99 offset:0\n"
  "ds_read_b64_tr_b16 v[114:115], v99 offset:8192\n"
  "v_mfma_f32_16x16x32_bf16 v[148:151], v[32:35], v[48:51], v[148:151]\n"
  "buffer_load_dwordx4 v95, s[40:43], s60 offen offset:1024 lds\n"
  "ds_read_b128 v[124:127], v89 offset:3072\n"
  "v_mfma_f32_16x16x32_bf16 v[152:155], v[32:35], v[52:55], v[152:155]\n"
  "ds_read_b128 v[128:131], v89 offset:7168\n"
  "ds_read_b128 v[132:135], v89 offset:11264\n"
  "v_mfma_f32_16x16x32_bf16 v[156:159], v[32:35], v[56:59], v[156:159]\n"
  "buffer_load_dwordx4 v96, s[40:43], s60 offen offset:2048 lds\n"
  "ds_read_b128 v[136:139], v89 offset:15360\n"
  "v_mfma_f32_16x16x32_bf16 v[164:167], v[36:39], v[44:47], v[164:167]\n"
  "s_add_u32 s60, s60, 36864\n"
  "ds_read_b64_tr_b16 v[116:117], v99 offset:1024\n"
  "ds_read_b64_tr_b16 v[118:119], v99 offset:9216\n"
  "v_mfma_f32_16x16x32_bf16 v[168:171], v[36:39], v[48:51], v[168:171]\n"
  "s_add_u32 m0, s48, 98304\n"
  "ds_read_b64_tr_b16 v[120:121], v99 offset:2048\n"
  "v_mfma_f32_16x16x32_bf16 v[172:175], v[36:39], v[52:55], v[172:175]\n"
  "buffer_load_dwordx4 v97, s[36:39], s59 offen offset:3072 sc1 lds\n"
  "ds_read_b64_tr_b16 v[122:123], v99 offset:10240\n"
  "v_mfma_f32_16x16x32_bf16 v[176:179], v[36:39], v[56:59], v[176:179]\n"
  "s_add_u32 s59, s59, 1024\n"
  "v_mfma_f32_16x16x32_bf16 v[184:187], v[40:43], v[44:47], v[184:187]\n"
  "buffer_load_dwordx4 v94, s[40:43], s60 offen offset:0 lds\n"
  "v_mfma_f32_16x16x32_bf16 v[188:191], v[40:43], v[48:51], v[188:191]\n"
  "v_mfma_f32_16x16x32_bf16 v[192:195], v[40:43], v[52:55], v[192:195]\n"
  "v_mfma_f32_16x16x32_bf16 v[196:199], v[40:43], v[56:59], v[196:199]\n"
  "s_waitcnt vmcnt(16) lgkmcnt(0)\n"
  "s_barrier\n"
  "v_mfma_f32_16x16x32_bf16 v[144:147], v[112:115], v[124:127], v[144:147]\n"
  "ds_read_b64_tr_b16 v[32:33], v100 offset:0\n"
  "ds_read_b64_tr_b16 v[34:35], v100 offset:8192\n"
  "v_mfma_f32_16x16x32_bf16 v[148:151], v[112:115], v[128:131], v[148:151]\n"
  "buffer_load_dwordx4 v95, s[40:43], s60 offen offset:1024 lds\n"
  "ds_read_b128 v[44:47], v90 offset:3072\n"
  "v_mfma_f32_16x16x32_bf16 v[152:155], v[112:115], v[132:135], v[152:155]\n"
  "ds_read_b128 v[48:51], v90 offset:7168\n"
  "ds_read_b128 v[52:55], v90 offset:11264\n"
  "v_mfma_f32_16x16x32_bf16 v[156:159], v[112:115], v[136:139], v[156:159]\n"
  "buffer_load_dwordx4 v96, s[40:43], s60 offen offset:2048 lds\n"
  "ds_read_b128 v[56:59], v90 offset:15360\n"
  "v_mfma_f32_16x16x32_bf16 v[164:167], v[116:119], v[124:127], v[164:167]\n"
  "s_add_u32 s60, s60, 36864\n"
  "ds_read_b64_tr_b16 v[36:37], v100 offset:1024\n"
  "ds_read_b64_tr_b16 v[38:39], v100 offset:9216\n"
  "v_mfma_f32_16x16x32_bf16 v[168:171], v[116:119], v[128:131], v[168:171]\n"
  "s_add_u32 m0, s48, 114688\n"
  "ds_read_b64_tr_b16 v[40:41], v100 offset:2048\n"
  "v_mfma_f32_16x16x32_bf16 v[172:175], v[116:119], v[132:135], v[172:175]\n"
  "buffer_load_dwordx4 v97, s[36:39], s59 offen offset:3072 sc1 lds\n"
  "ds_read_b64_tr_b16 v[42:43], v100 offset:10240\n"
  "v_mfma_f32_16x16x32_bf16 v[176:179], v[116:119], v[136:139], v[176:179]\n"
  "s_add_u32 s59, s59, 1024\n"
  "v_mfma_f32_16x16x32_bf16 v[184:187], v[120:123], v[124:127], v[184:187]\n"
  "buffer_load_dwordx4 v94, s[40:43], s60 offen offset:0 lds\n"
  "v_mfma_f32_16x16x32_bf16 v[188:191], v[120:123], v[128:131], v[188:191]\n"
  "v_mfma_f32_16x16x32_bf16 v[192:195], v[120:123], v[132:135], v[192:195]\n"
  "v_mfma_f32_16x16x32_bf16 v[196:199], v[120:123], v[136:139], v[196:199]\n"
  "s_waitcnt vmcnt(15) lgkmcnt(0)\n"
  "s_barrier\n"
  "v_mfma_f32_16x16x32_bf16 v[144:147], v[32:35], v[44:47], v[144:147]\n"
  "ds_read_b64_tr_b16 v[112:113], v100 offset:16384\n"
  "ds_read_b64_tr_b16 v[114:115], v100 offset:24576\n"
  "v_mfma_f32_16x16x32_bf16 v[148:151], v[32:35], v[48:51], v[148:151]\n"
  "buffer_load_dwordx4 v95, s[40:43], s60 offen offset:1024 lds\n"
  "ds_read_b128 v[124:127], v90 offset:19456\n"
  "v_mfma_f32_16x16x32_bf16 v[152:155], v[32:35], v[52:55], v[152:155]\n"
  "ds_read_b128 v[128:131], v90 offset:23552\n"
  "ds_read_b128 v[132:135], v90 offset:27648\n"
  "v_mfma_f32_16x16x32_bf16 v[156:159], v[32:35], v[56:59], v[156:159]\n"
  "buffer_load_dwordx4 v96, s[40:43], s60 offen offset:2048 lds\n"
  "ds_read_b128 v[136:139], v90 offset:31744\n"
  "v_mfma_f32_16x16x32_bf16 v[164:167], v[36:39], v[44:47], v[164:167]\n"
  "s_add_u32 s60, s60, 36864\n"
  "ds_read_b64_tr_b16 v[116:117], v100 offset:17408\n"
  "ds_read_b64_tr_b16 v[118:119], v100 offset:25600\n"
  "v_mfma_f32_16x16x32_bf16 v[168:171], v[36:39], v[48:51], v[168:171]\n"
  "s_add_u32 m0, s48, 16384\n"
  "ds_read_b64_tr_b16 v[120:121], v100 offset:18432\n"
  "v_mfma_f32_16x16x32_bf16 v[172:175], v[36:39], v[52:55], v[172:175]\n"
  "buffer_load_dwordx4 v97, s[36:39], s59 offen offset:3072 sc1 lds\n"
  "ds_read_b64_tr_b16 v[122:123], v100 offset:26624\n"
  "v_mfma_f32_16x16x32_bf16 v[176:179], v[36:39], v[56:59], v[176:179]\n"
  "s_add_u32 s59, s59, 1024\n"
  "v_mfma_f32_16x16x32_bf16 v[184:187], v[40:43], v[44:47], v[184:187]\n"
  "buffer_load_dwordx4 v94, s[40:43], s60 offen offset:0 lds\n"
  "v_mfma_f32_16x16x32_bf16 v[188:191], v[40:43], v[48:51], v[188:191]\n"
  "s_waitcnt vmcnt(16)\n"
  "s_mov_b64 exec, 1\n"
  "buffer_atomic_add v104, off, s[24:27], s15 sc1\n"
  "s_mov_b64 exec, -1\n"
  "v_mfma_f32_16x16x32_bf16 v[192:195], v[40:43], v[52:55], v[192:195]\n"
  "v_mfma_f32_16x16x32_bf16 v[196:199], v[40:43], v[56:59], v[196:199]\n"
  "s_waitcnt vmcnt(15) lgkmcnt(0)\n"
  "s_barrier\n"
  "v_mfma_f32_16x16x32_bf16 v[144:147], v[112:115], v[124:127], v[144:147]\n"
  "ds_read_b64_tr_b16 v[32:33], v101 offset:16384\n"
  "ds_read_b64_tr_b16 v[34:35], v101 offset:24576\n"
  "v_mfma_f32_16x16x32_bf16 v[148:151], v[112:115], v[128:131], v[148:151]\n"
  "buffer_load_dwordx4 v95, s[40:43], s60 offen offset:1024 lds\n"
  "ds_read_b128 v[44:47], v91 offset:19456\n"
  "v_mfma_f32_16x16x32_bf16 v[152:155], v[112:115], v[132:135], v[152:155]\n"
  "ds_read_b128 v[48:51], v91 offset:23552\n"
  "ds_read_b128 v[52:55], v91 offset:27648\n"
  "v_mfma_f32_16x16x32_bf16 v[156:159], v[112:115], v[136:139], v[156:159]\n"
  "buffer_load_dwordx4 v96, s[40:43], s60 offen offset:2048 lds\n"
  "ds_read_b128 v[56:59], v91 offset:31744\n"
  "v_mfma_f32_16x16x32_bf16 v[164:167], v[116:119], v[124:127], v[164:167]\n"
  "s_add_u32 s60, s60, 36864\n"
  "ds_read_b64_tr_b16 v[36:37], v101 offset:17408\n"
  "ds_read_b64_tr_b16 v[38:39], v101 offset:25600\n"
  "v_mfma_f32_16x16x32_bf16 v[168:171], v[116:119], v[128:131], v[168:171]\n"
  "s_add_u32 m0, s48, 49152\n"
  "buffer_load_dwordx4 v[224:227], v207, s[36:39], s63 offen sc1\n"
  "ds_read_b64_tr_b16 v[40:41], v101 offset:18432\n"
  "v_mfma_f32_16x16x32_bf16 v[172:175], v[116:119], v[132:135], v[172:175]\n"
  "buffer_load_dwordx4 v97, s[36:39], s59 offen offset:3072 sc1 lds\n"
  "ds_read_b64_tr_b16 v[42:43], v101 offset:26624\n"
  "v_mfma_f32_16x16x32_bf16 v[176:179], v[116:119], v[136:139], v[176:179]\n"
  "s_add_u32 s59, s59, 1024\n"
  "v_mfma_f32_16x16x32_bf16 v[184:187], v[120:123], v[124:127], v[184:187]\n"
  "buffer_load_dwordx4 v94, s[40:43], s60 offen offset:0 lds\n"
  "v_mfma_f32_16x16x32_bf16 v[188:191], v[120:123], v[128:131], v[188:191]\n"
  "buffer_load_dwordx4 v95, s[40:43], s60 offen offset:1024 lds\n"
  "v_mfma_f32_16x16x32_bf16 v[192:195], v[120:123], v[132:135], v[192:195]\n"
  "v_mfma_f32_16x16x32_bf16 v[196:199], v[120:123], v[136:139], v[196:199]\n"
  "s_waitcnt vmcnt(17) lgkmcnt(0)\n"
  "s_barrier\n"
  "v_mfma_f32_16x16x32_bf16 v[144:147], v[32:35], v[44:47], v[144:147]\n"
  "ds_read_b64_tr_b16 v[112:113], v99 offset:32768\n"
  "ds_read_b64_tr_b16 v[114:115], v99 offset:40960\n"
  "v_mfma_f32_16x16x32_bf16 v[148:151], v[32:35], v[48:51], v[148:151]\n"
  "buffer_load_dwordx4 v96, s[40:43], s60 offen offset:2048 lds\n"
  "ds_read_b128 v[124:127], v89 offset:35840\n"
  "v_mfma_f32_16x16x32_bf16 v[152:155], v[32:35], v[52:55], v[152:155]\n"
  "s_add_u32 s60, s60, 36864\n"
  "ds_read_b128 v[128:131], v89 offset:39936\n"
  "ds_read_b128 v[132:135], v89 offset:44032\n"
  "v_mfma_f32_16x16x32_bf16 v[156:159], v[32:35], v[56:59], v[156:159]\n"
  "s_add_u32 m0, s48, 131072\n"
  "ds_read_b128 v[136:139], v89 offset:48128\n"
  "v_mfma_f32_16x16x32_bf16 v[164:167], v[36:39], v[44:47], v[164:167]\n"
  "ds_read_b64_tr_b16 v[116:117], v99 offset:33792\n"
  "ds_read_b64_tr_b16 v[118:119], v99 offset:41984\n"
  "v_mfma_f32_16x16x32_bf16 v[168:171], v[36:39], v[48:51], v[168:171]\n"
  "buffer_load_dwordx4 v97, s[36:39], s59 offen offset:3072 sc1 lds\n"
  "ds_read_b64_tr_b16 v[120:121], v99 offset:34816\n"
  "v_mfma_f32_16x16x32_bf16 v[172:175], v[36:39], v[52:55], v[172:175]\n"
  "s_add_u32 s59, s59, 1024\n"
  "ds_read_b64_tr_b16 v[122:123], v99 offset:43008\n"
  "v_mfma_f32_16x16x32_bf16 v[176:179], v[36:39], v[56:59], v[176:179]\n"
  "buffer_load_dwordx4 v94, s[40:43], s60 offen offset:0 lds\n"
  "v_mfma_f32_16x16x32_bf16 v[184:187], v[40:43], v[44:47], v[184:187]\n"
  "buffer_load_dwordx4 v95, s[40:43], s60 offen offset:1024 lds\n"
  "v_mfma_f32_16x16x32_bf16 v[188:191], v[40:43], v[48:51], v[188:191]\n"
  "buffer_load_dwordx4 v96, s[40:43], s60 offen offset:2048 lds\n"
  "v_mfma_f32_16x16x32_bf16 v[192:195], v[40:43], v[52:55], v[192:195]\n"
  "s_add_u32 s60, s60, 36864\n"
  "v_mfma_f32_16x16x32_bf16 v[196:199], v[40:43], v[56:59], v[196:199]\n"
  "s_waitcnt vmcnt(18) lgkmcnt(0)\n"
  "s_barrier\n"
  "v_mfma_f32_16x16x32_bf16 v[144:147], v[112:115], v[124:127], v[144:147]\n"
  "s_add_u32 m0, s48, 0\n"
  "buffer_load_dwordx4 v[228:231], v208, s[36:39], s63 offen sc1\n"
  "ds_read_b64_tr_b16 v[32:33], v100 offset:32768\n"
  "ds_read_b64_tr_b16 v[34:35], v100 offset:40960\n"
  "s_nop 0\n"
  "s_cbranch_execz Ltramp%=\n"
  "v_mfma_f32_16x16x32_bf16 v[148:151], v[112:115], v[128:131], v[148:151]\n"
  "buffer_load_dwordx4 v97, s[36:39], s59 offen offset:3072 sc1 lds\n"
  "ds_read_b128 v[44:47], v90 offset:35840\n"
  "v_mfma_f32_16x16x32_bf16 v[152:155], v[112:115], v[132:135], v[152:155]\n"
  "s_add_u32 s59, s59, 1024\n"
  "ds_read_b128 v[48:51], v90 offset:39936\n"
  "ds_read_b128 v[52:55], v90 offset:44032\n"
  "v_mfma_f32_16x16x32_bf16 v[156:159], v[112:115], v[136:139], v[156:159]\n"
  "buffer_load_dwordx4 v94, s[40:43], s60 offen offset:0 lds\n"
  "ds_read_b128 v[56:59], v90 offset:48128\n"
  "v_mfma_f32_16x16x32_bf16 v[164:167], v[116:119], v[124:127], v[164:167]\n"
  "ds_read_b64_tr_b16 v[36:37], v100 offset:33792\n"
  "ds_read_b64_tr_b16 v[38:39], v100 offset:41984\n"
  "v_mfma_f32_16x16x32_bf16 v[168:171], v[116:119], v[128:131], v[168:171]\n"
  "buffer_load_dwordx4 v95, s[40:43], s60 offen offset:1024 lds\n"
  "ds_read_b64_tr_b16 v[40:41], v100 offset:34816\n"
  "v_mfma_f32_16x16x32_bf16 v[172:175], v[116:119], v[132:135], v[172:175]\n"
  "buffer_load_dwordx4 v96, s[40:43], s60 offen offset:2048 lds\n"
  "ds_read_b64_tr_b16 v[42:43], v100 offset:43008\n"
  "v_mfma_f32_16x16x32_bf16 v[176:179], v[116:119], v[136:139], v[176:179]\n"
  "s_add_u32 s60, s60, 36864\n"
  "v_mfma_f32_16x16x32_bf16 v[184:187], v[120:123], v[124:127], v[184:187]\n"
  "s_add_u32 m0, s48, 65536\n"
  "v_mfma_f32_16x16x32_bf16 v[188:191], v[120:123], v[128:131], v[188:191]\n"
  "buffer_load_dwordx4 v97, s[36:39], s59 offen offset:3072 sc1 lds\n"
  "v_mfma_f32_16x16x32_bf16 v[192:195], v[120:123], v[132:135], v[192:195]\n"
  "s_add_u32 s59, s59, 1024\n"
  "v_mfma_f32_16x16x32_bf16 v[196:199], v[120:123], v[136:139], v[196:199]\n"
  "s_waitcnt vmcnt(20) lgkmcnt(0)\n"
  "s_barrier\n"
  "v_mfma_f32_16x16x32_bf16 v[144:147], v[32:35], v[44:47], v[144:147]\n"
  "buffer_load_dwordx4 v94, s[40:43], s60 offen offset:0 lds\n"
  "ds_read_b64_tr_b16 v[112:113], v100 offset:49152\n"
  "ds_read_b64_tr_b16 v[114:115], v100 offset:57344\n"
  "v_mfma_f32_16x16x32_bf16 v[148:151], v[32:35], v[48:51], v[148:151]\n"
  "buffer_load_dwordx4 v95, s[40:43], s60 offen offset:1024 lds\n"
  "ds_read_b128 v[124:127], v90 offset:52224\n"
  "v_mfma_f32_16x16x32_bf16 v[152:155], v[32:35], v[52:55], v[152:155]\n"
  "ds_read_b128 v[128:131], v90 offset:56320\n"
  "ds_read_b128 v[132:135], v90 offset:60416\n"
  "v_mfma_f32_16x16x32_bf16 v[156:159], v[32:35], v[56:59], v[156:159]\n"
  "buffer_load_dwordx4 v96, s[40:43], s60 offen offset:2048 lds\n"
  "ds_read_b128 v[136:139], v90 offset:64512\n"
  "v_mfma_f32_16x16x32_bf16 v[164:167], v[36:39], v[44:47], v[164:167]\n"
  "s_add_u32 s60, s60, 36864\n"
  "ds_read_b64_tr_b16 v[116:117], v100 offset:50176\n"
  "ds_read_b64_tr_b16 v[118:119], v100 offset:58368\n"
  "v_mfma_f32_16x16x32_bf16 v[168:171], v[36:39], v[48:51], v[168:171]\n"
  "s_add_u32 m0, s48, 81920\n"
  "s_add_u32 s51, s59, 3072\n"
  "ds_read_b64_tr_b16 v[120:121], v100 offset:51200\n"
  "v_mfma_f32_16x16x32_bf16 v[172:175], v[36:39], v[52:55], v[172:175]\n"
  "buffer_load_dwordx4 v97, s[36:39], s51 offen offset:0 sc1 lds\n"
  "ds_read_b64_tr_b16 v[122:123], v100 offset:59392\n"
  "v_mfma_f32_16x16x32_bf16 v[176:179], v[36:39], v[56:59], v[176:179]\n"
  "buffer_load_dwordx4 v97, s[36:39], s51 offen offset:1024 sc1 lds\n"
  "v_mfma_f32_16x16x32_bf16 v[184:187], v[40:43], v[44:47], v[184:187]\n"
  "s_add_u32 s53, s60, 1032192\n"
  "s_add_u32 s52, s59, 28672\n"
  "v_mfma_f32_16x16x32_bf16 v[188:191], v[40:43], v[48:51], v[188:191]\n"
  "s_cmp_ge_u32 s53, 5308416\n"
  "s_cselect_b32 s64, 5308416, 0\n"
  "s_cselect_b32 s65, 147456, 0\n"
  "s_sub_u32 s53, s53, s64\n"
  "s_sub_u32 s52, s52, s65\n"
  "v_mfma_f32_16x16x32_bf16 v[192:195], v[40:43], v[52:55], v[192:195]\n"
  "buffer_load_dwordx4 v97, s[36:39], s51 offen offset:2048 sc1 lds\n"
  "v_mfma_f32_16x16x32_bf16 v[196:199], v[40:43], v[56:59], v[196:199]\n"
  "s_waitcnt vmcnt(21) lgkmcnt(0)\n"
  "s_barrier\n"
  "v_mfma_f32_16x16x32_bf16 v[144:147], v[112:115], v[124:127], v[144:147]\n"
  "ds_read_b64_tr_b16 v[32:33], v99 offset:16384\n"
  "ds_read_b64_tr_b16 v[34:35], v99 offset:24576\n"
  "v_mfma_f32_16x16x32_bf16 v[148:151], v[112:115], v[128:131], v[148:151]\n"
  "buffer_load_dwordx4 v97, s[36:39], s51 offen offset:3072 sc1 lds\n"
  "ds_read_b128 v[44:47], v89 offset:19456\n"
  "v_mfma_f32_16x16x32_bf16 v[152:155], v[112:115], v[132:135], v[152:155]\n"
  "s_add_u32 m0, s48, 147456\n"
  "s_add_u32 s51, s60, 0\n"
  "ds_read_b128 v[48:51], v89 offset:23552\n"
  "ds_read_b128 v[52:55], v89 offset:27648\n"
  "v_mfma_f32_16x16x32_bf16 v[156:159], v[112:115], v[136:139], v[156:159]\n"
  "buffer_load_dwordx4 v94, s[40:43], s51 offen offset:0 lds\n"
  "ds_read_b128 v[56:59], v89 offset:31744\n"
  "v_mfma_f32_16x16x32_bf16 v[164:167], v[116:119], v[124:127], v[164:167]\n"
  "s_add_u32 s51, s60, 35840\n"
  "ds_read_b64_tr_b16 v[36:37], v99 offset:17408\n"
  "ds_read_b64_tr_b16 v[38:39], v99 offset:25600\n"
  "v_mfma_f32_16x16x32_bf16 v[168:171], v[116:119], v[128:131], v[168:171]\n"
  "buffer_load_dwordx4 v94, s[40:43], s51 offen offset:1024 lds\n"
  "ds_read_b64_tr_b16 v[40:41], v99 offset:18432\n"
  "v_mfma_f32_16x16x32_bf16 v[172:175], v[116:119], v[132:135], v[172:175]\n"
  "s_add_u32 s51, s60, 71680\n"
  "ds_read_b64_tr_b16 v[42:43], v99 offset:26624\n"
  "v_mfma_f32_16x16x32_bf16 v[176:179], v[116:119], v[136:139], v[176:179]\n"
  "buffer_load_dwordx4 v94, s[40:43], s51 offen offset:2048 lds\n"
  "v_mfma_f32_16x16x32_bf16 v[184:187], v[120:123], v[124:127], v[184:187]\n"
  "s_add_u32 s51, s60, 107520\n"
  "v_mfma_f32_16x16x32_bf16 v[188:191], v[120:123], v[128:131], v[188:191]\n"
  "buffer_load_dwordx4 v94, s[40:43], s51 offen offset:3072 lds\n"
  "s_mov_b64 exec, 31\n"
  "buffer_load_dword v106, v105, s[24:27], 0 offen sc1\n"
  "s_mov_b64 exec, -1\n"
  "v_mfma_f32_16x16x32_bf16 v[192:195], v[120:123], v[132:135], v[192:195]\n"
  "s_add_u32 m0, s48, 32768\n"
  "s_add_u32 s51, s60, 1024\n"
  "v_mfma_f32_16x16x32_bf16 v[196:199], v[120:123], v[136:139], v[196:199]\n"
  "s_waitcnt vmcnt(22) lgkmcnt(0)\n"
  "s_barrier\n"
  "v_mfma_f32_16x16x32_bf16 v[144:147], v[32:35], v[44:47], v[144:147]\n"
  "ds_read_b64_tr_b16 v[112:113], v99 offset:49152\n"
  "ds_read_b64_tr_b16 v[114:115], v99 offset:57344\n"
  "v_mfma_f32_16x16x32_bf16 v[148:151], v[32:35], v[48:51], v[148:151]\n"
  "buffer_load_dwordx4 v95, s[40:43], s51 offen offset:0 lds\n"
  "ds_read_b128 v[124:127], v89 offset:52224\n"
  "v_mfma_f32_16x16x32_bf16 v[152:155], v[32:35], v[52:55], v[152:155]\n"
  "s_add_u32 s51, s60, 36864\n"
  "ds_read_b128 v[128:131], v89 offset:56320\n"
  "ds_read_b128 v[132:135], v89 offset:60416\n"
  "v_mfma_f32_16x16x32_bf16 v[156:159], v[32:35], v[56:59], v[156:159]\n"
  "buffer_load_dwordx4 v95, s[40:43], s51 offen offset:1024 lds\n"
  "ds_read_b128 v[136:139], v89 offset:64512\n"
  "v_mfma_f32_16x16x32_bf16 v[164:167], v[36:39], v[44:47], v[164:167]\n"
  "s_add_u32 s51, s60, 72704\n"
  "ds_read_b64_tr_b16 v[116:117], v99 offset:50176\n"
  "ds_read_b64_tr_b16 v[118:119], v99 offset:58368\n"
  "v_mfma_f32_16x16x32_bf16 v[168:171], v[36:39], v[48:51], v[168:171]\n"
  "buffer_load_dwordx4 v95, s[40:43], s51 offen offset:2048 lds\n"
  "ds_read_b64_tr_b16 v[120:121], v99 offset:51200\n"
  "v_mfma_f32_16x16x32_bf16 v[172:175], v[36:39], v[52:55], v[172:175]\n"
  "s_add_u32 s51, s60, 108544\n"
  "ds_read_b64_tr_b16 v[122:123], v99 offset:59392\n"
  "v_mfma_f32_16x16x32_bf16 v[176:179], v[36:39], v[56:59], v[176:179]\n"
  "buffer_load_dwordx4 v95, s[40:43], s51 offen offset:3072 lds\n"
  "v_mfma_f32_16x16x32_bf16 v[184:187], v[40:43], v[44:47], v[184:187]\n"
  "s_add_u32 m0, s48, 98304\n"
  "s_add_u32 s51, s53, 0\n"
  "v_mfma_f32_16x16x32_bf16 v[188:191], v[40:43], v[48:51], v[188:191]\n"
  "buffer_load_dwordx4 v94, s[40:43], s51 offen offset:0 lds\n"
  "v_mfma_f32_16x16x32_bf16 v[192:195], v[40:43], v[52:55], v[192:195]\n"
  "s_add_u32 s51, s53, 35840\n"
  "v_mfma_f32_16x16x32_bf16 v[196:199], v[40:43], v[56:59], v[196:199]\n"
  "s_waitcnt vmcnt(23) lgkmcnt(0)\n"
  "s_barrier\n"
  "v_mfma_f32_16x16x32_bf16 v[144:147], v[112:115], v[124:127], v[144:147]\n"
  "ds_read_b64_tr_b16 v[32:33], v101 offset:0\n"
  "ds_read_b64_tr_b16 v[34:35], v101 offset:8192\n"
  "v_mfma_f32_16x16x32_bf16 v[148:151], v[112:115], v[128:131], v[148:151]\n"
  "buffer_load_dwordx4 v94, s[40:43], s51 offen offset:1024 lds\n"
  "ds_read_b128 v[44:47], v91 offset:3072\n"
  "v_mfma_f32_16x16x32_bf16 v[152:155], v[112:115], v[132:135], v[152:155]\n"
  "s_add_u32 s51, s53, 71680\n"
  "ds_read_b128 v[48:51], v91 offset:7168\n"
  "ds_read_b128 v[52:55], v91 offset:11264\n"
  "v_mfma_f32_16x16x32_bf16 v[156:159], v[112:115], v[136:139], v[156:159]\n"
  "buffer_load_dwordx4 v94, s[40:43], s51 offen offset:2048 lds\n"
  "ds_read_b128 v[56:59], v91 offset:15360\n"
  "v_mfma_f32_16x16x32_bf16 v[164:167], v[116:119], v[124:127], v[164:167]\n"
  "s_add_u32 s51, s53, 107520\n"
  "ds_read_b64_tr_b16 v[36:37], v101 offset:1024\n"
  "ds_read_b64_tr_b16 v[38:39], v101 offset:9216\n"
  "v_mfma_f32_16x16x32_bf16 v[168:171], v[116:119], v[128:131], v[168:171]\n"
  "buffer_load_dwordx4 v94, s[40:43], s51 offen offset:3072 lds\n"
  "ds_read_b64_tr_b16 v[40:41], v101 offset:2048\n"
  "v_mfma_f32_16x16x32_bf16 v[172:175], v[116:119], v[132:135], v[172:175]\n"
  "s_add_u32 m0, s48, 114688\n"
  "s_add_u32 s51, s53, 147456\n"
  "ds_read_b64_tr_b16 v[42:43], v101 offset:10240\n"
  "v_mfma_f32_16x16x32_bf16 v[176:179], v[116:119], v[136:139], v[176:179]\n"
  "buffer_load_dwordx4 v94, s[40:43], s51 offen offset:0 lds\n"
  "v_mfma_f32_16x16x32_bf16 v[184:187], v[120:123], v[124:127], v[184:187]\n"
  "s_add_u32 s51, s53, 183296\n"
  "v_mfma_f32_16x16x32_bf16 v[188:191], v[120:123], v[128:131], v[188:191]\n"
  "buffer_load_dwordx4 v94, s[40:43], s51 offen offset:1024 lds\n"
  "v_mfma_f32_16x16x32_bf16 v[192:195], v[120:123], v[132:135], v[192:195]\n"
  "s_add_u32 s51, s53, 219136\n"
  "v_mfma_f32_16x16x32_bf16 v[196:199], v[120:123], v[136:139], v[196:199]\n"
  "s_waitcnt vmcnt(23) lgkmcnt(0)\n"
  "s_barrier\n"
  "v_mfma_f32_16x16x32_bf16 v[144:147], v[32:35], v[44:47], v[144:147]\n"
  "ds_read_b64_tr_b16 v[112:113], v99 offset:0\n"
  "ds_read_b64_tr_b16 v[114:115], v99 offset:8192\n"
  "v_mfma_f32_16x16x32_bf16 v[148:151], v[32:35], v[48:51], v[148:151]\n"
  "buffer_load_dwordx4 v94, s[40:43], s51 offen offset:2048 lds\n"
  "ds_read_b128 v[124:127], v89 offset:3072\n"
  "v_mfma_f32_16x16x32_bf16 v[152:155], v[32:35], v[52:55], v[152:155]\n"
  "s_add_u32 s51, s53, 254976\n"
  "ds_read_b128 v[128:131], v89 offset:7168\n"
  "ds_read_b128 v[132:135], v89 offset:11264\n"
  "v_mfma_f32_16x16x32_bf16 v[156:159], v[32:35], v[56:59], v[156:159]\n"
  "buffer_load_dwordx4 v94, s[40:43], s51 offen offset:3072 lds\n"
  "ds_read_b128 v[136:139], v89 offset:15360\n"
  "v_mfma_f32_16x16x32_bf16 v[164:167], v[36:39], v[44:47], v[164:167]\n"
  "s_add_u32 m0, s48, 16384\n"
  "s_add_u32 s51, s53, 294912\n"
  "ds_read_b64_tr_b16 v[116:117], v99 offset:1024\n"
  "ds_read_b64_tr_b16 v[118:119], v99 offset:9216\n"
  "v_mfma_f32_16x16x32_bf16 v[168:171], v[36:39], v[48:51], v[168:171]\n"
  "buffer_load_dwordx4 v94, s[40:43], s51 offen offset:0 lds\n"
  "ds_read_b64_tr_b16 v[120:121], v99 offset:2048\n"
  "v_mfma_f32_16x16x32_bf16 v[172:175], v[36:39], v[52:55], v[172:175]\n"
  "s_add_u32 s51, s53, 330752\n"
  "ds_read_b64_tr_b16 v[122:123], v99 offset:10240\n"
  "v_mfma_f32_16x16x32_bf16 v[176:179], v[36:39], v[56:59], v[176:179]\n"
  "buffer_load_dwordx4 v94, s[40:43], s51 offen offset:1024 lds\n"
  "v_mfma_f32_16x16x32_bf16 v[184:187], v[40:43], v[44:47], v[184:187]\n"
  "s_add_u32 s51, s53, 366592\n"
  "v_mfma_f32_16x16x32_bf16 v[188:191], v[40:43], v[48:51], v[188:191]\n"
  "buffer_load_dwordx4 v94, s[40:43], s51 offen offset:2048 lds\n"
  "v_mfma_f32_16x16x32_bf16 v[192:195], v[40:43], v[52:55], v[192:195]\n"
  "s_add_u32 s51, s53, 402432\n"
  "v_mfma_f32_16x16x32_bf16 v[196:199], v[40:43], v[56:59], v[196:199]\n"
  "s_waitcnt vmcnt(24) lgkmcnt(0)\n"
  "s_barrier\n"
  "v_mfma_f32_16x16x32_bf16 v[144:147], v[112:115], v[124:127], v[144:147]\n"
  "ds_read_b64_tr_b16 v[32:33], v100 offset:0\n"
  "ds_read_b64_tr_b16 v[34:35], v100 offset:8192\n"
  "v_mfma_f32_16x16x32_bf16 v[148:151], v[112:115], v[128:131], v[148:151]\n"
  "buffer_load_dwordx4 v94, s[40:43], s51 offen offset:3072 lds\n"
  "ds_read_b128 v[44:47], v90 offset:3072\n"
  "s_waitcnt vmcnt(16)\n"
  "v_cmp_gt_u32 vcc, 12, v106\n"
  "s_and_b64 vcc, vcc, 31\n"
  "s_cbranch_scc1 Lpollp127B%=\n"
  "Lpollrp127B%=:\n"
  "v_mfma_f32_16x16x32_bf16 v[152:155], v[112:115], v[132:135], v[152:155]\n"
  "s_add_u32 m0, s48, 49152\n"
  "s_add_u32 s51, s52, 3072\n"
  "ds_read_b128 v[48:51], v90 offset:7168\n"
  "ds_read_b128 v[52:55], v90 offset:11264\n"
  "v_mfma_f32_16x16x32_bf16 v[156:159], v[112:115], v[136:139], v[156:159]\n"
  "buffer_load_dwordx4 v97, s[36:39], s51 offen offset:0 sc1 lds\n"
  "ds_read_b128 v[56:59], v90 offset:15360\n"
  "v_mfma_f32_16x16x32_bf16 v[164:167], v[116:119], v[124:127], v[164:167]\n"
  "ds_read_b64_tr_b16 v[36:37], v100 offset:1024\n"
  "ds_read_b64_tr_b16 v[38:39], v100 offset:9216\n"
  "v_mfma_f32_16x16x32_bf16 v[168:171], v[116:119], v[128:131], v[168:171]\n"
  "buffer_load_dwordx4 v97, s[36:39], s51 offen offset:1024 sc1 lds\n"
  "ds_read_b64_tr_b16 v[40:41], v100 offset:2048\n"
  "v_mfma_f32_16x16x32_bf16 v[172:175], v[116:119], v[132:135], v[172:175]\n"
  "buffer_load_dwordx4 v97, s[36:39], s51 offen offset:2048 sc1 lds\n"
  "ds_read_b64_tr_b16 v[42:43], v100 offset:10240\n"
  "v_mfma_f32_16x16x32_bf16 v[176:179], v[116:119], v[136:139], v[176:179]\n"
  "buffer_load_dwordx4 v97, s[36:39], s51 offen offset:3072 sc1 lds\n"
  "v_mfma_f32_16x16x32_bf16 v[184:187], v[120:123], v[124:127], v[184:187]\n"
  "s_add_u32 m0, s48, 131072\n"
  "s_add_u32 s51, s52, 7168\n"
  "v_add_u32 v210, 0, v210\n"
  "v_mfma_f32_16x16x32_bf16 v[188:191], v[120:123], v[128:131], v[188:191]\n"
  "buffer_load_dwordx4 v97, s[36:39], s51 offen offset:0 sc1 lds\n"
  "v_add_u32 v211, 0, v211\n"
  "v_mfma_f32_16x16x32_bf16 v[192:195], v[120:123], v[132:135], v[192:195]\n"
  "buffer_load_dwordx4 v97, s[36:39], s51 offen offset:1024 sc1 lds\n"
  "v_mfma_f32_16x16x32_bf16 v[196:199], v[120:123], v[136:139], v[196:199]\n"
  "s_waitcnt vmcnt(23) lgkmcnt(0)\n"
  "s_barrier\n"
  "v_mfma_f32_16x16x32_bf16 v[144:147], v[32:35], v[44:47], v[144:147]\n"
  "ds_read_b64_tr_b16 v[112:113], v101 offset:16384\n"
  "ds_read_b64_tr_b16 v[114:115], v101 offset:24576\n"
  "ds_read_b64_tr_b16 v[116:117], v101 offset:17408\n"
  "v_mfma_f32_16x16x32_bf16 v[148:151], v[32:35], v[48:51], v[148:151]\n"
  "buffer_load_dwordx4 v97, s[36:39], s51 offen offset:2048 sc1 lds\n"
  "ds_read_b64_tr_b16 v[118:119], v101 offset:25600\n"
  "ds_read_b64_tr_b16 v[120:121], v101 offset:18432\n"
  "v_mfma_f32_16x16x32_bf16 v[152:155], v[32:35], v[52:55], v[152:155]\n"
  "ds_read_b64_tr_b16 v[122:123], v101 offset:26624\n"
  "ds_read_b64_tr_b16 v[124:125], v101 offset:19456\n"
  "ds_read_b64_tr_b16 v[126:127], v101 offset:27648\n"
  "v_mfma_f32_16x16x32_bf16 v[156:159], v[32:35], v[56:59], v[156:159]\n"
  "buffer_load_dwordx4 v97, s[36:39], s51 offen offset:3072 sc1 lds\n"
  "ds_read_b128 a[160:163], v90 offset:16384\n"
  "ds_read_b128 a[180:183], v90 offset:17408\n"
  "v_mfma_f32_16x16x32_bf16 v[164:167], v[36:39], v[44:47], v[164:167]\n"
  "s_add_u32 m0, s48, 0\n"
  "s_add_u32 s51, s52, 11264\n"
  "ds_read_b128 a[200:203], v90 offset:18432\n"
  "ds_read_b128 a[220:223], v90 offset:19456\n"
  "v_mfma_f32_16x16x32_bf16 v[168:171], v[36:39], v[48:51], v[168:171]\n"
  "ds_read_b128 a[164:167], v90 offset:20480\n"
  "ds_read_b128 a[184:187], v90 offset:21504\n"
  "ds_read_b128 a[204:207], v90 offset:22528\n"
  "v_mfma_f32_16x16x32_bf16 v[172:175], v[36:39], v[52:55], v[172:175]\n"
  "buffer_load_dwordx4 v97, s[36:39], s51 offen offset:0 sc1 lds\n"
  "ds_read_b128 a[224:227], v90 offset:23552\n"
  "ds_read_b128 a[168:171], v90 offset:24576\n"
  "v_mfma_f32_16x16x32_bf16 v[176:179], v[36:39], v[56:59], v[176:179]\n"
  "ds_read_b128 a[188:191], v90 offset:25600\n"
  "ds_read_b128 a[208:211], v90 offset:26624\n"
  "ds_read_b128 a[228:231], v90 offset:27648\n"
  "v_mfma_f32_16x16x32_bf16 v[184:187], v[40:43], v[44:47], v[184:187]\n"
  "buffer_load_dwordx4 v97, s[36:39], s51 offen offset:1024 sc1 lds\n"
  "ds_read_b128 a[172:175], v90 offset:28672\n"
  "ds_read_b128 a[192:195], v90 offset:29696\n"
  "v_mfma_f32_16x16x32_bf16 v[188:191], v[40:43], v[48:51], v[188:191]\n"
  "ds_read_b128 a[212:215], v90 offset:30720\n"
  "ds_read_b128 a[232:235], v90 offset:31744\n"
  "v_mfma_f32_16x16x32_bf16 v[192:195], v[40:43], v[52:55], v[192:195]\n"
  "buffer_load_dwordx4 v97, s[36:39], s51 offen offset:2048 sc1 lds\n"
  "v_mfma_f32_16x16x32_bf16 v[196:199], v[40:43], v[56:59], v[196:199]\n"
  "s_waitcnt vmcnt(23) lgkmcnt(0)\n"
  "s_barrier\n"
  "v_mfma_f32_16x16x32_bf16 v[144:147], v[112:115], a[160:163], v[144:147]\n"
  "buffer_load_dwordx4 v97, s[36:39], s51 offen offset:3072 sc1 lds\n"
  "ds_read_b64_tr_b16 v[32:33], v99 offset:32768\n"
  "v_mfma_f32_16x16x32_bf16 v[144:147], v[116:119], a[180:183], v[144:147]\n"
  "s_add_u32 m0, s48, 65536\n"
  "s_add_u32 s51, s53, 1024\n"
  "ds_read_b64_tr_b16 v[34:35], v99 offset:40960\n"
  "v_mfma_f32_16x16x32_bf16 v[144:147], v[120:123], a[200:203], v[144:147]\n"
  "buffer_load_dwordx4 v95, s[40:43], s51 offen offset:0 lds\n"
  "ds_read_b64_tr_b16 v[36:37], v99 offset:33792\n"
  "v_mfma_f32_16x16x32_bf16 v[144:147], v[124:127], a[220:223], v[144:147]\n"
  "s_add_u32 s51, s53, 36864\n"
  "ds_read_b64_tr_b16 v[38:39], v99 offset:41984\n"
  "v_mfma_f32_16x16x32_bf16 v[148:151], v[112:115], a[164:167], v[148:151]\n"
  "buffer_load_dwordx4 v95, s[40:43], s51 offen offset:1024 lds\n"
  "ds_read_b64_tr_b16 v[40:41], v99 offset:34816\n"
  "v_mfma_f32_16x16x32_bf16 v[148:151], v[116:119], a[184:187], v[148:151]\n"
  "s_add_u32 s51, s53, 72704\n"
  "ds_read_b64_tr_b16 v[42:43], v99 offset:43008\n"
  "v_mfma_f32_16x16x32_bf16 v[148:151], v[120:123], a[204:207], v[148:151]\n"
  "buffer_load_dwordx4 v95, s[40:43], s51 offen offset:2048 lds\n"
  "ds_read_b64_tr_b16 v[44:45], v99 offset:35840\n"
  "v_mfma_f32_16x16x32_bf16 v[148:151], v[124:127], a[224:227], v[148:151]\n"
  "s_add_u32 s51, s53, 108544\n"
  "ds_read_b64_tr_b16 v[46:47], v99 offset:44032\n"
  "v_mfma_f32_16x16x32_bf16 v[152:155], v[112:115], a[168:171], v[152:155]\n"
  "buffer_load_dwordx4 v95, s[40:43], s51 offen offset:3072 lds\n"
  "v_mfma_f32_16x16x32_bf16 v[152:155], v[116:119], a[188:191], v[152:155]\n"
  "s_add_u32 m0, s48, 147456\n"
  "s_add_u32 s51, s53, 148480\n"
  "v_mfma_f32_16x16x32_bf16 v[152:155], v[120:123], a[208:211], v[152:155]\n"
  "buffer_load_dwordx4 v95, s[40:43], s51 offen offset:0 lds\n"
  "v_mfma_f32_16x16x32_bf16 v[152:155], v[124:127], a[228:231], v[152:155]\n"
  "s_add_u32 s51, s53, 184320\n"
  "v_mfma_f32_16x16x32_bf16 v[156:159], v[112:115], a[172:175], v[156:159]\n"
  "buffer_load_dwordx4 v95, s[40:43], s51 offen offset:1024 lds\n"
  "v_mfma_f32_16x16x32_bf16 v[156:159], v[116:119], a[192:195], v[156:159]\n"
  "v_mfma_f32_16x16x32_bf16 v[156:159], v[120:123], a[212:215], v[156:159]\n"
  "v_mfma_f32_16x16x32_bf16 v[156:159], v[124:127], a[232:235], v[156:159]\n"
  "s_waitcnt vmcnt(14) lgkmcnt(0)\n"
  "s_barrier\n"
  "v_mfma_f32_16x16x32_bf16 v[164:167], v[32:35], a[160:163], v[164:167]\n"
  "s_add_u32 s51, s53, 220160\n"
  "ds_read_b64_tr_b16 v[112:113], v100 offset:32768\n"
  "ds_read_b64_tr_b16 v[114:115], v100 offset:40960\n"
  "v_mfma_f32_16x16x32_bf16 v[164:167], v[36:39], a[180:183], v[164:167]\n"
  "buffer_load_dwordx4 v95, s[40:43], s51 offen offset:2048 lds\n"
  "ds_read_b64_tr_b16 v[116:117], v100 offset:33792\n"
  "ds_read_b64_tr_b16 v[118:119], v100 offset:41984\n"
  "v_mfma_f32_16x16x32_bf16 v[164:167], v[40:43], a[200:203], v[164:167]\n"
  "ds_read_b64_tr_b16 v[120:121], v100 offset:34816\n"
  "ds_read_b64_tr_b16 v[122:123], v100 offset:43008\n"
  "v_mfma_f32_16x16x32_bf16 v[164:167], v[44:47], a[220:223], v[164:167]\n"
  "ds_read_b64_tr_b16 v[124:125], v100 offset:35840\n"
  "ds_read_b64_tr_b16 v[126:127], v100 offset:44032\n"
  "v_mfma_f32_16x16x32_bf16 v[168:171], v[32:35], a[164:167], v[168:171]\n"
  "ds_read_b128 a[0:3], v89 offset:49152\n"
  "ds_read_b128 a[20:23], v89 offset:50176\n"
  "v_mfma_f32_16x16x32_bf16 v[168:171], v[36:39], a[184:187], v[168:171]\n"
  "s_add_u32 s51, s53, 256000\n"
  "ds_read_b128 a[40:43], v89 offset:51200\n"
  "ds_read_b128 a[60:63], v89 offset:52224\n"
  "v_mfma_f32_16x16x32_bf16 v[168:171], v[40:43], a[204:207], v[168:171]\n"
  "buffer_load_dwordx4 v95, s[40:43], s51 offen offset:3072 lds\n"
  "ds_read_b128 a[4:7], v89 offset:53248\n"
  "ds_read_b128 a[24:27], v89 offset:54272\n"
  "v_mfma_f32_16x16x32_bf16 v[168:171], v[44:47], a[224:227], v[168:171]\n"
  "ds_read_b128 a[44:47], v89 offset:55296\n"
  "ds_read_b128 a[64:67], v89 offset:56320\n"
  "v_mfma_f32_16x16x32_bf16 v[172:175], v[32:35], a[168:171], v[172:175]\n"
  "s_add_u32 m0, s48, 32768\n"
  "s_add_u32 s51, s53, 295936\n"
  "ds_read_b128 a[8:11], v89 offset:57344\n"
  "ds_read_b128 a[28:31], v89 offset:58368\n"
  "v_mfma_f32_16x16x32_bf16 v[172:175], v[36:39], a[188:191], v[172:175]\n"
  "buffer_load_dwordx4 v95, s[40:43], s51 offen offset:0 lds\n"
  "ds_read_b128 a[48:51], v89 offset:59392\n"
  "ds_read_b128 a[68:71], v89 offset:60416\n"
  "v_mfma_f32_16x16x32_bf16 v[172:175], v[40:43], a[208:211], v[172:175]\n"
  "ds_read_b128 a[12:15], v89 offset:61440\n"
  "ds_read_b128 a[32:35], v89 offset:62464\n"
  "v_mfma_f32_16x16x32_bf16 v[172:175], v[44:47], a[228:231], v[172:175]\n"
  "s_add_u32 s51, s53, 331776\n"
  "ds_read_b128 a[52:55], v89 offset:63488\n"
  "ds_read_b128 a[72:75], v89 offset:64512\n"
  "v_mfma_f32_16x16x32_bf16 v[176:179], v[32:35], a[172:175], v[176:179]\n"
  "buffer_load_dwordx4 v95, s[40:43], s51 offen offset:1024 lds\n"
  "v_mfma_f32_16x16x32_bf16 v[176:179], v[36:39], a[192:195], v[176:179]\n"
  "v_mfma_f32_16x16x32_bf16 v[176:179], v[40:43], a[212:215], v[176:179]\n"
  "v_mfma_f32_16x16x32_bf16 v[176:179], v[44:47], a[232:235], v[176:179]\n"
  "s_waitcnt vmcnt(14) lgkmcnt(0)\n"
  "s_barrier\n"
  "v_mfma_f32_16x16x32_bf16 v[144:147], v[112:115], a[0:3], v[144:147]\n"
  "s_add_u32 s51, s53, 367616\n"
  "ds_read_b64_tr_b16 v[32:33], v100 offset:49152\n"
  "ds_read_b64_tr_b16 v[34:35], v100 offset:57344\n"
  "v_mfma_f32_16x16x32_bf16 v[144:147], v[116:119], a[20:23], v[144:147]\n"
  "buffer_load_dwordx4 v95, s[40:43], s51 offen offset:2048 lds\n"
  "ds_read_b64_tr_b16 v[36:37], v100 offset:50176\n"
  "ds_read_b64_tr_b16 v[38:39], v100 offset:58368\n"
  "v_mfma_f32_16x16x32_bf16 v[144:147], v[120:123], a[40:43], v[144:147]\n"
  "ds_read_b64_tr_b16 v[40:41], v100 offset:51200\n"
  "ds_read_b64_tr_b16 v[42:43], v100 offset:59392\n"
  "v_mfma_f32_16x16x32_bf16 v[144:147], v[124:127], a[60:63], v[144:147]\n"
  "ds_read_b64_tr_b16 v[44:45], v100 offset:52224\n"
  "ds_read_b64_tr_b16 v[46:47], v100 offset:60416\n"
  "v_mfma_f32_16x16x32_bf16 v[148:151], v[112:115], a[4:7], v[148:151]\n"
  "ds_read_b128 a[80:83], v91 offset:0\n"
  "ds_read_b128 a[100:103], v91 offset:1024\n"
  "v_mfma_f32_16x16x32_bf16 v[148:151], v[116:119], a[24:27], v[148:151]\n"
  "s_add_u32 s51, s53, 403456\n"
  "ds_read_b128 a[120:123], v91 offset:2048\n"
  "ds_read_b128 a[140:143], v91 offset:3072\n"
  "v_mfma_f32_16x16x32_bf16 v[148:151], v[120:123], a[44:47], v[148:151]\n"
  "buffer_load_dwordx4 v95, s[40:43], s51 offen offset:3072 lds\n"
  "ds_read_b128 a[84:87], v91 offset:4096\n"
  "ds_read_b128 a[104:107], v91 offset:5120\n"
  "v_mfma_f32_16x16x32_bf16 v[148:151], v[124:127], a[64:67], v[148:151]\n"
  "ds_read_b128 a[124:127], v91 offset:6144\n"
  "ds_read_b128 a[144:147], v91 offset:7168\n"
  "v_mfma_f32_16x16x32_bf16 v[152:155], v[112:115], a[8:11], v[152:155]\n"
  "s_add_u32 m0, s48, 49152\n"
  "s_add_u32 s51, s53, 2048\n"
  "ds_read_b128 a[88:91], v91 offset:8192\n"
  "ds_read_b128 a[108:111], v91 offset:9216\n"
  "v_mfma_f32_16x16x32_bf16 v[152:155], v[116:119], a[28:31], v[152:155]\n"
  "buffer_load_dwordx4 v96, s[40:43], s51 offen offset:0 lds\n"
  "ds_read_b128 a[128:131], v91 offset:10240\n"
  "ds_read_b128 a[148:151], v91 offset:11264\n"
  "v_mfma_f32_16x16x32_bf16 v[152:155], v[120:123], a[48:51], v[152:155]\n"
  "ds_read_b128 a[92:95], v91 offset:12288\n"
  "ds_read_b128 a[112:115], v91 offset:13312\n"
  "v_mfma_f32_16x16x32_bf16 v[152:155], v[124:127], a[68:71], v[152:155]\n"
  "s_add_u32 s51, s53, 37888\n"
  "ds_read_b128 a[132:135], v91 offset:14336\n"
  "ds_read_b128 a[152:155], v91 offset:15360\n"
  "v_mfma_f32_16x16x32_bf16 v[156:159], v[112:115], a[12:15], v[156:159]\n"
  "buffer_load_dwordx4 v96, s[40:43], s51 offen offset:1024 lds\n"
  "v_mfma_f32_16x16x32_bf16 v[156:159], v[116:119], a[32:35], v[156:159]\n"
  "v_mfma_f32_16x16x32_bf16 v[156:159], v[120:123], a[52:55], v[156:159]\n"
  "v_mfma_f32_16x16x32_bf16 v[156:159], v[124:127], a[72:75], v[156:159]\n"
  "s_waitcnt vmcnt(14) lgkmcnt(0)\n"
  "s_barrier\n"
  "v_mfma_f32_16x16x32_bf16 v[144:147], v[32:35], a[80:83], v[144:147]\n"
  "s_add_u32 s51, s53, 73728\n"
  "ds_read_b64_tr_b16 v[112:113], v99 offset:16384\n"
  "ds_read_b64_tr_b16 v[114:115], v99 offset:24576\n"
  "v_mfma_f32_16x16x32_bf16 v[144:147], v[36:39], a[100:103], v[144:147]\n"
  "buffer_load_dwordx4 v96, s[40:43], s51 offen offset:2048 lds\n"
  "ds_read_b64_tr_b16 v[116:117], v99 offset:17408\n"
  "ds_read_b64_tr_b16 v[118:119], v99 offset:25600\n"
  "v_mfma_f32_16x16x32_bf16 v[144:147], v[40:43], a[120:123], v[144:147]\n"
  "ds_read_b64_tr_b16 v[120:121], v99 offset:18432\n"
  "ds_read_b64_tr_b16 v[122:123], v99 offset:26624\n"
  "v_mfma_f32_16x16x32_bf16 v[144:147], v[44:47], a[140:143], v[144:147]\n"
  "ds_read_b64_tr_b16 v[124:125], v99 offset:19456\n"
  "ds_read_b64_tr_b16 v[126:127], v99 offset:27648\n"
  "v_mfma_f32_16x16x32_bf16 v[148:151], v[32:35], a[84:87], v[148:151]\n"
  "ds_read_b128 a[160:163], v89 offset:0\n"
  "ds_read_b128 a[180:183], v89 offset:1024\n"
  "v_mfma_f32_16x16x32_bf16 v[148:151], v[36:39], a[104:107], v[148:151]\n"
  "s_add_u32 s51, s53, 109568\n"
  "ds_read_b128 a[200:203], v89 offset:2048\n"
  "ds_read_b128 a[220:223], v89 offset:3072\n"
  "v_mfma_f32_16x16x32_bf16 v[148:151], v[40:43], a[124:127], v[148:151]\n"
  "buffer_load_dwordx4 v96, s[40:43], s51 offen offset:3072 lds\n"
  "ds_read_b128 a[164:167], v89 offset:4096\n"
  "ds_read_b128 a[184:187], v89 offset:5120\n"
  "v_mfma_f32_16x16x32_bf16 v[148:151], v[44:47], a[144:147], v[148:151]\n"
  "ds_read_b128 a[204:207], v89 offset:6144\n"
  "ds_read_b128 a[224:227], v89 offset:7168\n"
  "v_mfma_f32_16x16x32_bf16 v[152:155], v[32:35], a[88:91], v[152:155]\n"
  "s_add_u32 m0, s48, 98304\n"
  "s_add_u32 s51, s53, 149504\n"
  "ds_read_b128 a[168:171], v89 offset:8192\n"
  "ds_read_b128 a[188:191], v89 offset:9216\n"
  "v_mfma_f32_16x16x32_bf16 v[152:155], v[36:39], a[108:111], v[152:155]\n"
  "buffer_load_dwordx4 v96, s[40:43], s51 offen offset:0 lds\n"
  "ds_read_b128 a[208:211], v89 offset:10240\n"
  "ds_read_b128 a[228:231], v89 offset:11264\n"
  "v_mfma_f32_16x16x32_bf16 v[152:155], v[40:43], a[128:131], v[152:155]\n"
  "ds_read_b128 a[172:175], v89 offset:12288\n"
  "ds_read_b128 a[192:195], v89 offset:13312\n"
  "v_mfma_f32_16x16x32_bf16 v[152:155], v[44:47], a[148:151], v[152:155]\n"
  "s_add_u32 s51, s53, 185344\n"
  "ds_read_b128 a[212:215], v89 offset:14336\n"
  "ds_read_b128 a[232:235], v89 offset:15360\n"
  "v_mfma_f32_16x16x32_bf16 v[156:159], v[32:35], a[92:95], v[156:159]\n"
  "buffer_load_dwordx4 v96, s[40:43], s51 offen offset:1024 lds\n"
  "v_mfma_f32_16x16x32_bf16 v[156:159], v[36:39], a[112:115], v[156:159]\n"
  "v_mfma_f32_16x16x32_bf16 v[156:159], v[40:43], a[132:135], v[156:159]\n"
  "v_mfma_f32_16x16x32_bf16 v[156:159], v[44:47], a[152:155], v[156:159]\n"
  "s_waitcnt vmcnt(14) lgkmcnt(0)\n"
  "s_barrier\n"
  "v_mfma_f32_16x16x32_bf16 v[144:147], v[112:115], a[160:163], v[144:147]\n"
  "s_add_u32 s51, s53, 221184\n"
  "ds_read_b64_tr_b16 v[32:33], v100 offset:0\n"
  "v_mfma_f32_16x16x32_bf16 v[144:147], v[116:119], a[180:183], v[144:147]\n"
  "buffer_load_dwordx4 v96, s[40:43], s51 offen offset:2048 lds\n"
  "ds_read_b64_tr_b16 v[34:35], v100 offset:8192\n"
  "v_mfma_f32_16x16x32_bf16 v[144:147], v[120:123], a[200:203], v[144:147]\n"
  "ds_read_b64_tr_b16 v[36:37], v100 offset:1024\n"
  "v_mfma_f32_16x16x32_bf16 v[144:147], v[124:127], a[220:223], v[144:147]\n"
  "ds_read_b64_tr_b16 v[38:39], v100 offset:9216\n"
  "v_mfma_f32_16x16x32_bf16 v[148:151], v[112:115], a[164:167], v[148:151]\n"
  "ds_read_b64_tr_b16 v[40:41], v100 offset:2048\n"
  "v_mfma_f32_16x16x32_bf16 v[148:151], v[116:119], a[184:187], v[148:151]\n"
  "s_add_u32 s51, s53, 257024\n"
  "ds_read_b64_tr_b16 v[42:43], v100 offset:10240\n"
  "v_mfma_f32_16x16x32_bf16 v[148:151], v[120:123], a[204:207], v[148:151]\n"
  "buffer_load_dwordx4 v96, s[40:43], s51 offen offset:3072 lds\n"
  "ds_read_b64_tr_b16 v[44:45], v100 offset:3072\n"
  "v_mfma_f32_16x16x32_bf16 v[148:151], v[124:127], a[224:227], v[148:151]\n"
  "ds_read_b64_tr_b16 v[46:47], v100 offset:11264\n"
  "v_mfma_f32_16x16x32_bf16 v[152:155], v[112:115], a[168:171], v[152:155]\n"
  "s_add_u32 m0, s48, 114688\n"
  "s_add_u32 s51, s53, 296960\n"
  "v_mfma_f32_16x16x32_bf16 v[152:155], v[116:119], a[188:191], v[152:155]\n"
  "buffer_load_dwordx4 v96, s[40:43], s51 offen offset:0 lds\n"
  "v_mfma_f32_16x16x32_bf16 v[152:155], v[120:123], a[208:211], v[152:155]\n"
  "v_mfma_f32_16x16x32_bf16 v[152:155], v[124:127], a[228:231], v[152:155]\n"
  "s_add_u32 s51, s53, 332800\n"
  "v_mfma_f32_16x16x32_bf16 v[156:159], v[112:115], a[172:175], v[156:159]\n"
  "buffer_load_dwordx4 v96, s[40:43], s51 offen offset:1024 lds\n"
  "v_cvt_pk_bf16_f32 v0, v144, v145\n"
  "v_mfma_f32_16x16x32_bf16 v[156:159], v[116:119], a[192:195], v[156:159]\n"
  "v_cvt_pk_bf16_f32 v1, v146, v147\n"
  "v_cvt_pk_bf16_f32 v2, v148, v149\n"
  "v_mfma_f32_16x16x32_bf16 v[156:159], v[120:123], a[212:215], v[156:159]\n"
  "v_cvt_pk_bf16_f32 v3, v150, v151\n"
  "s_cbranch_execz Ltramp%=\n"
  "v_mfma_f32_16x16x32_bf16 v[156:159], v[124:127], a[232:235], v[156:159]\n"
  "s_waitcnt vmcnt(14) lgkmcnt(0)\n"
  "s_barrier\n"
  "v_mfma_f32_16x16x32_bf16 v[164:167], v[32:35], a[0:3], v[164:167]\n"
  "s_add_u32 s51, s53, 368640\n"
  "ds_read_b64_tr_b16 v[112:113], v101 offset:16384\n"
  "v_mfma_f32_16x16x32_bf16 v[164:167], v[36:39], a[20:23], v[164:167]\n"
  "buffer_load_dwordx4 v96, s[40:43], s51 offen offset:2048 lds\n"
  "ds_read_b64_tr_b16 v[114:115], v101 offset:24576\n"
  "v_mfma_f32_16x16x32_bf16 v[164:167], v[40:43], a[40:43], v[164:167]\n"
  "ds_read_b64_tr_b16 v[116:117], v101 offset:17408\n"
  "v_mfma_f32_16x16x32_bf16 v[164:167], v[44:47], a[60:63], v[164:167]\n"
  "ds_read_b64_tr_b16 v[118:119], v101 offset:25600\n"
  "v_mfma_f32_16x16x32_bf16 v[168:171], v[32:35], a[4:7], v[168:171]\n"
  "ds_read_b64_tr_b16 v[120:121], v101 offset:18432\n"
  "v_cvt_pk_bf16_f32 v4, v152, v153\n"
  "v_mfma_f32_16x16x32_bf16 v[168:171], v[36:39], a[24:27], v[168:171]\n"
  "s_add_u32 s51, s53, 404480\n"
  "ds_read_b64_tr_b16 v[122:123], v101 offset:26624\n"
  "v_cvt_pk_bf16_f32 v5, v154, v155\n"
  "v_mfma_f32_16x16x32_bf16 v[168:171], v[40:43], a[44:47], v[168:171]\n"
  "buffer_load_dwordx4 v96, s[40:43], s51 offen offset:3072 lds\n"
  "ds_read_b64_tr_b16 v[124:125], v101 offset:19456\n"
  "v_mfma_f32_16x16x32_bf16 v[168:171], v[44:47], a[64:67], v[168:171]\n"
  "ds_read_b64_tr_b16 v[126:127], v101 offset:27648\n"
  "v_cvt_pk_bf16_f32 v6, v156, v157\n"
  "v_mfma_f32_16x16x32_bf16 v[172:175], v[32:35], a[8:11], v[172:175]\n"
  "s_add_u32 m0, s48, 131072\n"
  "s_add_u32 s51, s60, 2048\n"
  "v_cvt_pk_bf16_f32 v7, v158, v159\n"
  "v_mfma_f32_16x16x32_bf16 v[172:175], v[36:39], a[28:31], v[172:175]\n"
  "buffer_load_dwordx4 v96, s[40:43], s51 offen offset:0 lds\n"
  "v_mfma_f32_16x16x32_bf16 v[172:175], v[40:43], a[48:51], v[172:175]\n"
  "v_permlane16_swap_b32 v0, v2\n"
  "v_mfma_f32_16x16x32_bf16 v[172:175], v[44:47], a[68:71], v[172:175]\n"
  "s_add_u32 s51, s60, 37888\n"
  "v_permlane16_swap_b32 v1, v3\n"
  "v_mfma_f32_16x16x32_bf16 v[176:179], v[32:35], a[12:15], v[176:179]\n"
  "buffer_load_dwordx4 v96, s[40:43], s51 offen offset:1024 lds\n"
  "buffer_store_dwordx4 v[0:3], v89, s[36:39], s16 offen offset:0 sc1\n"
  "v_mfma_f32_16x16x32_bf16 v[176:179], v[36:39], a[32:35], v[176:179]\n"
  "v_permlane16_swap_b32 v4, v6\n"
  "v_mfma_f32_16x16x32_bf16 v[176:179], v[40:43], a[52:55], v[176:179]\n"
  "v_permlane16_swap_b32 v5, v7\n"
  "v_mfma_f32_16x16x32_bf16 v[176:179], v[44:47], a[72:75], v[176:179]\n"
  "s_waitcnt vmcnt(15) lgkmcnt(0)\n"
  "s_barrier\n"
  "v_mfma_f32_16x16x32_bf16 v[164:167], v[112:115], a[80:83], v[164:167]\n"
  "s_add_u32 s51, s60, 73728\n"
  "ds_read_b64_tr_b16 v[32:33], v99 offset:32768\n"
  "buffer_store_dwordx4 v[4:7], v89, s[36:39], s16 offen offset:1024 sc1\n"
  "v_mfma_f32_16x16x32_bf16 v[164:167], v[116:119], a[100:103], v[164:167]\n"
  "buffer_load_dwordx4 v96, s[40:43], s51 offen offset:2048 lds\n"
  "ds_read_b64_tr_b16 v[34:35], v99 offset:40960\n"
  "v_mfma_f32_16x16x32_bf16 v[164:167], v[120:123], a[120:123], v[164:167]\n"
  "ds_read_b64_tr_b16 v[36:37], v99 offset:33792\n"
  "v_mfma_f32_16x16x32_bf16 v[164:167], v[124:127], a[140:143], v[164:167]\n"
  "ds_read_b64_tr_b16 v[38:39], v99 offset:41984\n"
  "v_mfma_f32_16x16x32_bf16 v[168:171], v[112:115], a[84:87], v[168:171]\n"
  "ds_read_b64_tr_b16 v[40:41], v99 offset:34816\n"
  "v_mfma_f32_16x16x32_bf16 v[168:171], v[116:119], a[104:107], v[168:171]\n"
  "s_add_u32 s51, s60, 109568\n"
  "ds_read_b64_tr_b16 v[42:43], v99 offset:43008\n"
  "v_mfma_f32_16x16x32_bf16 v[168:171], v[120:123], a[124:127], v[168:171]\n"
  "buffer_load_dwordx4 v96, s[40:43], s51 offen offset:3072 lds\n"
  "ds_read_b64_tr_b16 v[44:45], v99 offset:35840\n"
  "v_mfma_f32_16x16x32_bf16 v[168:171], v[124:127], a[144:147], v[168:171]\n"
  "ds_read_b64_tr_b16 v[46:47], v99 offset:44032\n"
  "v_mfma_f32_16x16x32_bf16 v[172:175], v[112:115], a[88:91], v[172:175]\n"
  "v_mfma_f32_16x16x32_bf16 v[172:175], v[116:119], a[108:111], v[172:175]\n"
  "v_mfma_f32_16x16x32_bf16 v[172:175], v[120:123], a[128:131], v[172:175]\n"
  "v_mfma_f32_16x16x32_bf16 v[172:175], v[124:127], a[148:151], v[172:175]\n"
  "v_mfma_f32_16x16x32_bf16 v[176:179], v[112:115], a[92:95], v[176:179]\n"
  "v_mfma_f32_16x16x32_bf16 v[176:179], v[116:119], a[112:115], v[176:179]\n"
  "v_mfma_f32_16x16x32_bf16 v[176:179], v[120:123], a[132:135], v[176:179]\n"
  "v_mfma_f32_16x16x32_bf16 v[176:179], v[124:127], a[152:155], v[176:179]\n"
  "s_waitcnt vmcnt(14) lgkmcnt(0)\n"
  "s_barrier\n"
  "v_mfma_f32_16x16x32_bf16 v[164:167], v[32:35], a[160:163], v[164:167]\n"
  "ds_read_b64_tr_b16 v[112:113], v99 offset:49152\n"
  "v_mfma_f32_16x16x32_bf16 v[164:167], v[36:39], a[180:183], v[164:167]\n"
  "ds_read_b64_tr_b16 v[114:115], v99 offset:57344\n"
  "v_mfma_f32_16x16x32_bf16 v[164:167], v[40:43], a[200:203], v[164:167]\n"
  "ds_read_b64_tr_b16 v[116:117], v99 offset:50176\n"
  "v_mfma_f32_16x16x32_bf16 v[164:167], v[44:47], a[220:223], v[164:167]\n"
  "ds_read_b64_tr_b16 v[118:119], v99 offset:58368\n"
  "v_mfma_f32_16x16x32_bf16 v[168:171], v[32:35], a[164:167], v[168:171]\n"
  "ds_read_b64_tr_b16 v[120:121], v99 offset:51200\n"
  "v_mfma_f32_16x16x32_bf16 v[168:171], v[36:39], a[184:187], v[168:171]\n"
  "ds_read_b64_tr_b16 v[122:123], v99 offset:59392\n"
  "v_mfma_f32_16x16x32_bf16 v[168:171], v[40:43], a[204:207], v[168:171]\n"
  "ds_read_b64_tr_b16 v[124:125], v99 offset:52224\n"
  "v_mfma_f32_16x16x32_bf16 v[168:171], v[44:47], a[224:227], v[168:171]\n"
  "ds_read_b64_tr_b16 v[126:127], v99 offset:60416\n"
  "v_mfma_f32_16x16x32_bf16 v[172:175], v[32:35], a[168:171], v[172:175]\n"
  "v_mfma_f32_16x16x32_bf16 v[172:175], v[36:39], a[188:191], v[172:175]\n"
  "v_mfma_f32_16x16x32_bf16 v[172:175], v[40:43], a[208:211], v[172:175]\n"
  "v_mfma_f32_16x16x32_bf16 v[172:175], v[44:47], a[228:231], v[172:175]\n"
  "v_mfma_f32_16x16x32_bf16 v[176:179], v[32:35], a[172:175], v[176:179]\n"
  "v_cvt_pk_bf16_f32 v12, v164, v165\n"
  "v_cvt_pk_bf16_f32 v13, v166, v167\n"
  "v_mfma_f32_16x16x32_bf16 v[176:179], v[36:39], a[192:195], v[176:179]\n"
  "v_cvt_pk_bf16_f32 v14, v168, v169\n"
  "v_cvt_pk_bf16_f32 v15, v170, v171\n"
  "v_mfma_f32_16x16x32_bf16 v[176:179], v[40:43], a[212:215], v[176:179]\n"
  "v_permlane16_swap_b32 v12, v14\n"
  "v_mfma_f32_16x16x32_bf16 v[176:179], v[44:47], a[232:235], v[176:179]\n"
  "s_waitcnt vmcnt(10) lgkmcnt(0)\n"
  "s_barrier\n"
  "v_mfma_f32_16x16x32_bf16 v[184:187], v[112:115], a[0:3], v[184:187]\n"
  "ds_read_b64_tr_b16 v[32:33], v100 offset:32768\n"
  "v_mfma_f32_16x16x32_bf16 v[184:187], v[116:119], a[20:23], v[184:187]\n"
  "ds_read_b64_tr_b16 v[34:35], v100 offset:40960\n"
  "v_mfma_f32_16x16x32_bf16 v[184:187], v[120:123], a[40:43], v[184:187]\n"
  "ds_read_b64_tr_b16 v[36:37], v100 offset:33792\n"
  "v_mfma_f32_16x16x32_bf16 v[184:187], v[124:127], a[60:63], v[184:187]\n"
  "ds_read_b64_tr_b16 v[38:39], v100 offset:41984\n"
  "v_mfma_f32_16x16x32_bf16 v[188:191], v[112:115], a[4:7], v[188:191]\n"
  "ds_read_b64_tr_b16 v[40:41], v100 offset:34816\n"
  "v_cvt_pk_bf16_f32 v16, v172, v173\n"
  "v_mfma_f32_16x16x32_bf16 v[188:191], v[116:119], a[24:27], v[188:191]\n"
  "ds_read_b64_tr_b16 v[42:43], v100 offset:43008\n"
  "v_cvt_pk_bf16_f32 v17, v174, v175\n"
  "v_mfma_f32_16x16x32_bf16 v[188:191], v[120:123], a[44:47], v[188:191]\n"
  "ds_read_b64_tr_b16 v[44:45], v100 offset:35840\n"
  "v_cvt_pk_bf16_f32 v18, v176, v177\n"
  "v_mfma_f32_16x16x32_bf16 v[188:191], v[124:127], a[64:67], v[188:191]\n"
  "ds_read_b64_tr_b16 v[46:47], v100 offset:44032\n"
  "v_cvt_pk_bf16_f32 v19, v178, v179\n"
  "v_mfma_f32_16x16x32_bf16 v[192:195], v[112:115], a[8:11], v[192:195]\n"
  "v_permlane16_swap_b32 v13, v15\n"
  "v_mfma_f32_16x16x32_bf16 v[192:195], v[116:119], a[28:31], v[192:195]\n"
  "buffer_store_dwordx4 v[12:15], v89, s[36:39], s17 offen offset:0 sc1\n"
  "v_mfma_f32_16x16x32_bf16 v[192:195], v[120:123], a[48:51], v[192:195]\n"
  "v_permlane16_swap_b32 v16, v18\n"
  "v_mfma_f32_16x16x32_bf16 v[192:195], v[124:127], a[68:71], v[192:195]\n"
  "v_permlane16_swap_b32 v17, v19\n"
  "v_mfma_f32_16x16x32_bf16 v[196:199], v[112:115], a[12:15], v[196:199]\n"
  "buffer_store_dwordx4 v[16:19], v89, s[36:39], s17 offen offset:1024 sc1\n"
  "v_mfma_f32_16x16x32_bf16 v[196:199], v[116:119], a[32:35], v[196:199]\n"
  "v_mfma_f32_16x16x32_bf16 v[196:199], v[120:123], a[52:55], v[196:199]\n"
  "v_mfma_f32_16x16x32_bf16 v[196:199], v[124:127], a[72:75], v[196:199]\n"
  "s_waitcnt vmcnt(8) lgkmcnt(0)\n"
  "s_barrier\n"
  "v_mfma_f32_16x16x32_bf16 v[184:187], v[32:35], a[80:83], v[184:187]\n"
  "ds_read_b64_tr_b16 v[112:113], v100 offset:49152\n"
  "v_mfma_f32_16x16x32_bf16 v[184:187], v[36:39], a[100:103], v[184:187]\n"
  "ds_read_b64_tr_b16 v[114:115], v100 offset:57344\n"
  "v_mfma_f32_16x16x32_bf16 v[184:187], v[40:43], a[120:123], v[184:187]\n"
  "ds_read_b64_tr_b16 v[116:117], v100 offset:50176\n"
  "v_mfma_f32_16x16x32_bf16 v[184:187], v[44:47], a[140:143], v[184:187]\n"
  "ds_read_b64_tr_b16 v[118:119], v100 offset:58368\n"
  "v_mfma_f32_16x16x32_bf16 v[188:191], v[32:35], a[84:87], v[188:191]\n"
  "ds_read_b64_tr_b16 v[120:121], v100 offset:51200\n"
  "v_mfma_f32_16x16x32_bf16 v[188:191], v[36:39], a[104:107], v[188:191]\n"
  "ds_read_b64_tr_b16 v[122:123], v100 offset:59392\n"
  "v_mfma_f32_16x16x32_bf16 v[188:191], v[40:43], a[124:127], v[188:191]\n"
  "ds_read_b64_tr_b16 v[124:125], v100 offset:52224\n"
  "v_mfma_f32_16x16x32_bf16 v[188:191], v[44:47], a[144:147], v[188:191]\n"
  "ds_read_b64_tr_b16 v[126:127], v100 offset:60416\n"
  "v_mfma_f32_16x16x32_bf16 v[192:195], v[32:35], a[88:91], v[192:195]\n"
  "v_mfma_f32_16x16x32_bf16 v[192:195], v[36:39], a[108:111], v[192:195]\n"
  "v_mfma_f32_16x16x32_bf16 v[192:195], v[40:43], a[128:131], v[192:195]\n"
  "v_mfma_f32_16x16x32_bf16 v[192:195], v[44:47], a[148:151], v[192:195]\n"
  "v_mfma_f32_16x16x32_bf16 v[196:199], v[32:35], a[92:95], v[196:199]\n"
  "v_mfma_f32_16x16x32_bf16 v[196:199], v[36:39], a[112:115], v[196:199]\n"
  "v_mfma_f32_16x16x32_bf16 v[196:199], v[40:43], a[132:135], v[196:199]\n"
  "v_mfma_f32_16x16x32_bf16 v[196:199], v[44:47], a[152:155], v[196:199]\n"
  "s_waitcnt vmcnt(2) lgkmcnt(0)\n"
  "s_barrier\n"
  "v_mfma_f32_16x16x32_bf16 v[184:187], v[112:115], a[160:163], v[184:187]\n"
  "ds_read_b64_tr_b16 v[32:33], v101 offset:0\n"
  "ds_read_b64_tr_b16 v[34:35], v101 offset:8192\n"
  "v_mfma_f32_16x16x32_bf16 v[184:187], v[116:119], a[180:183], v[184:187]\n"
  "ds_read_b64_tr_b16 v[36:37], v101 offset:1024\n"
  "ds_read_b64_tr_b16 v[38:39], v101 offset:9216\n"
  "v_mfma_f32_16x16x32_bf16 v[184:187], v[120:123], a[200:203], v[184:187]\n"
  "ds_read_b64_tr_b16 v[40:41], v101 offset:2048\n"
  "ds_read_b64_tr_b16 v[42:43], v101 offset:10240\n"
  "v_mfma_f32_16x16x32_bf16 v[184:187], v[124:127], a[220:223], v[184:187]\n"
  "ds_read_b64_tr_b16 v[44:45], v101 offset:3072\n"
  "ds_read_b64_tr_b16 v[46:47], v101 offset:11264\n"
  "v_mfma_f32_16x16x32_bf16 v[188:191], v[112:115], a[164:167], v[188:191]\n"
  "ds_read_b128 a[0:3], v90 offset:16384\n"
  "ds_read_b128 a[20:23], v90 offset:17408\n"
  "v_mfma_f32_16x16x32_bf16 v[188:191], v[116:119], a[184:187], v[188:191]\n"
  "ds_read_b128 a[40:43], v90 offset:18432\n"
  "ds_read_b128 a[60:63], v90 offset:19456\n"
  "v_mfma_f32_16x16x32_bf16 v[188:191], v[120:123], a[204:207], v[188:191]\n"
  "ds_read_b128 a[4:7], v90 offset:20480\n"
  "ds_read_b128 a[24:27], v90 offset:21504\n"
  "v_mfma_f32_16x16x32_bf16 v[188:191], v[124:127], a[224:227], v[188:191]\n"
  "ds_read_b128 a[44:47], v90 offset:22528\n"
  "ds_read_b128 a[64:67], v90 offset:23552\n"
  "v_mfma_f32_16x16x32_bf16 v[192:195], v[112:115], a[168:171], v[192:195]\n"
  "ds_read_b128 a[8:11], v90 offset:24576\n"
  "ds_read_b128 a[28:31], v90 offset:25600\n"
  "v_mfma_f32_16x16x32_bf16 v[192:195], v[116:119], a[188:191], v[192:195]\n"
  "ds_read_b128 a[48:51], v90 offset:26624\n"
  "ds_read_b128 a[68:71], v90 offset:27648\n"
  "v_mfma_f32_16x16x32_bf16 v[192:195], v[120:123], a[208:211], v[192:195]\n"
  "ds_read_b128 a[12:15], v90 offset:28672\n"
  "ds_read_b128 a[32:35], v90 offset:29696\n"
  "v_mfma_f32_16x16x32_bf16 v[192:195], v[124:127], a[228:231], v[192:195]\n"
  "ds_read_b128 a[52:55], v90 offset:30720\n"
  "ds_read_b128 a[72:75], v90 offset:31744\n"
  "v_mfma_f32_16x16x32_bf16 v[196:199], v[112:115], a[172:175], v[196:199]\n"
  "v_mfma_f32_16x16x32_bf16 v[196:199], v[116:119], a[192:195], v[196:199]\n"
  "v_mfma_f32_16x16x32_bf16 v[196:199], v[120:123], a[212:215], v[196:199]\n"
  "v_mfma_f32_16x16x32_bf16 v[196:199], v[124:127], a[232:235], v[196:199]\n"
  "s_waitcnt lgkmcnt(0)\n"
  "v_mfma_f32_16x16x32_bf16 v[184:187], v[32:35], a[0:3], v[184:187]\n"
  "buffer_load_dwordx4 v[64:67], v212, s[36:39], s61 offen offset:0 sc1\n"
  "v_mfma_f32_16x16x32_bf16 v[184:187], v[36:39], a[20:23], v[184:187]\n"
  "v_mfma_f32_16x16x32_bf16 v[184:187], v[40:43], a[40:43], v[184:187]\n"
  "v_mfma_f32_16x16x32_bf16 v[184:187], v[44:47], a[60:63], v[184:187]\n"
  "buffer_load_dwordx4 v[68:71], v212, s[36:39], s61 offen offset:1024 sc1\n"
  "v_mfma_f32_16x16x32_bf16 v[188:191], v[32:35], a[4:7], v[188:191]\n"
  "v_mfma_f32_16x16x32_bf16 v[188:191], v[36:39], a[24:27], v[188:191]\n"
  "v_mfma_f32_16x16x32_bf16 v[188:191], v[40:43], a[44:47], v[188:191]\n"
  "v_mfma_f32_16x16x32_bf16 v[188:191], v[44:47], a[64:67], v[188:191]\n"
  "v_mfma_f32_16x16x32_bf16 v[192:195], v[32:35], a[8:11], v[192:195]\n"
  "v_mfma_f32_16x16x32_bf16 v[192:195], v[36:39], a[28:31], v[192:195]\n"
  "v_mfma_f32_16x16x32_bf16 v[192:195], v[40:43], a[48:51], v[192:195]\n"
  "v_mfma_f32_16x16x32_bf16 v[192:195], v[44:47], a[68:71], v[192:195]\n"
  "ds_write_b128 v210, v[184:187] offset:0\n"
  "v_mfma_f32_16x16x32_bf16 v[196:199], v[32:35], a[12:15], v[196:199]\n"
  "v_mfma_f32_16x16x32_bf16 v[196:199], v[36:39], a[32:35], v[196:199]\n"
  "v_mfma_f32_16x16x32_bf16 v[196:199], v[40:43], a[52:55], v[196:199]\n"
  "v_mfma_f32_16x16x32_bf16 v[196:199], v[44:47], a[72:75], v[196:199]\n"
  "ds_write_b128 v210, v[188:191] offset:4096\n"
  "s_nop 1\n"
  "ds_write_b128 v210, v[192:195] offset:8192\n"
  "s_nop 3\n"
  "ds_write_b128 v210, v[196:199] offset:12288\n"
  "s_waitcnt lgkmcnt(0)\n"
  "s_barrier\n"
  "ds_read_b128 v[0:3], v211 offset:0\n"
  "ds_read_b128 v[4:7], v211 offset:256\n"
  "ds_read_b128 v[8:11], v211 offset:8192\n"
  "ds_read_b128 v[12:15], v211 offset:8448\n"
  "v_mov_b32 v24, 0x3f80\n"
  "v_mov_b32 v25, 0x3f800000\n"
  "s_waitcnt vmcnt(2)\n"
  "buffer_load_dwordx4 v[76:79], v212, s[36:39], s62 offen offset:0 sc1\n"
  "buffer_load_dwordx4 v[80:83], v212, s[36:39], s62 offen offset:1024 sc1\n"
  "s_waitcnt lgkmcnt(3)\n"
  "v_dot2c_f32_bf16 v0, v24, v224\n"
  "v_dot2c_f32_bf16 v1, v25, v224\n"
  "v_dot2c_f32_bf16 v2, v24, v225\n"
  "v_dot2c_f32_bf16 v3, v25, v225\n"
  "s_waitcnt lgkmcnt(2)\n"
  "v_dot2c_f32_bf16 v4, v24, v226\n"
  "v_dot2c_f32_bf16 v5, v25, v226\n"
  "v_dot2c_f32_bf16 v6, v24, v227\n"
  "v_dot2c_f32_bf16 v7, v25, v227\n"
  "s_waitcnt lgkmcnt(1)\n"
  "v_dot2c_f32_bf16 v8, v24, v228\n"
  "v_dot2c_f32_bf16 v9, v25, v228\n"
  "v_dot2c_f32_bf16 v10, v24, v229\n"
  "v_dot2c_f32_bf16 v11, v25, v229\n"
  "s_waitcnt lgkmcnt(0)\n"
  "v_dot2c_f32_bf16 v12, v24, v230\n"
  "v_dot2c_f32_bf16 v13, v25, v230\n"
  "v_dot2c_f32_bf16 v14, v24, v231\n"
  "v_dot2c_f32_bf16 v15, v25, v231\n"
  "s_waitcnt vmcnt(3)\n"
  "v_max_u32 v26, v64, v65\n"
  "v_max3_u32 v26, v66, v67, v26\n"
  "s_waitcnt vmcnt(2)\n"
  "v_max3_u32 v26, v68, v69, v26\n"
  "v_max3_u32 v26, v70, v71, v26\n"
  "v_cmp_eq_u32 vcc, -1, v26\n"
  "s_cmp_eq_u64 vcc, 0\n"
  "s_cbranch_scc0 Lx0B%=\n"
  "Lxok0B%=:\n"
  "v_dot2c_f32_bf16 v0, v24, v64\n"
  "v_dot2c_f32_bf16 v1, v25, v64\n"
  "v_dot2c_f32_bf16 v2, v24, v65\n"
  "v_dot2c_f32_bf16 v3, v25, v65\n"
  "v_dot2c_f32_bf16 v4, v24, v66\n"
  "v_dot2c_f32_bf16 v5, v25, v66\n"
  "v_dot2c_f32_bf16 v6, v24, v67\n"
  "v_dot2c_f32_bf16 v7, v25, v67\n"
  "v_dot2c_f32_bf16 v8, v24, v68\n"
  "v_dot2c_f32_bf16 v9, v25, v68\n"
  "v_dot2c_f32_bf16 v10, v24, v69\n"
  "v_dot2c_f32_bf16 v11, v25, v69\n"
  "v_dot2c_f32_bf16 v12, v24, v70\n"
  "v_dot2c_f32_bf16 v13, v25, v70\n"
  "v_dot2c_f32_bf16 v14, v24, v71\n"
  "v_dot2c_f32_bf16 v15, v25, v71\n"
  "s_waitcnt vmcnt(1)\n"
  "v_max_u32 v26, v76, v77\n"
  "v_max3_u32 v26, v78, v79, v26\n"
  "s_waitcnt vmcnt(0)\n"
  "v_max3_u32 v26, v80, v81, v26\n"
  "v_max3_u32 v26, v82, v83, v26\n"
  "v_cmp_eq_u32 vcc, -1, v26\n"
  "s_cmp_eq_u64 vcc, 0\n"
  "s_cbranch_scc0 Lx1B%=\n"
  "Lxok1B%=:\n"
  "v_dot2c_f32_bf16 v1, v25, v76\n"
  "v_dot2c_f32_bf16 v0, v24, v76\n"
  "v_dot2c_f32_bf16 v2, v24, v77\n"
  "v_dot2c_f32_bf16 v3, v25, v77\n"
  "v_dot2c_f32_bf16 v4, v24, v78\n"
  "v_dot2c_f32_bf16 v6, v24, v79\n"
  "v_dot2c_f32_bf16 v5, v25, v78\n"
  "v_dot2c_f32_bf16 v7, v25, v79\n"
  "v_cvt_pk_bf16_f32 v237, v2, v3\n"
  "v_cvt_pk_bf16_f32 v236, v0, v1\n"
  "v_cvt_pk_bf16_f32 v238, v4, v5\n"
  "v_cvt_pk_bf16_f32 v239, v6, v7\n"
  "buffer_store_dwordx4 v[236:239], v204, s[44:47], 0 offen sc1\n"
  "v_dot2c_f32_bf16 v8, v24, v80\n"
  "v_dot2c_f32_bf16 v10, v24, v81\n"
  "v_dot2c_f32_bf16 v9, v25, v80\n"
  "v_dot2c_f32_bf16 v11, v25, v81\n"
  "v_dot2c_f32_bf16 v12, v24, v82\n"
  "v_dot2c_f32_bf16 v13, v25, v82\n"
  "v_dot2c_f32_bf16 v14, v24, v83\n"
  "v_dot2c_f32_bf16 v15, v25, v83\n"
  "v_cvt_pk_bf16_f32 v241, v10, v11\n"
  "v_cvt_pk_bf16_f32 v240, v8, v9\n"
  "v_cvt_pk_bf16_f32 v242, v12, v13\n"
  "v_cvt_pk_bf16_f32 v243, v14, v15\n"
  "buffer_store_dwordx4 v[240:243], v205, s[44:47], 0 offen sc1\n"
  "Lend%=:\n"
  : "+v"(tid) : "s"(__builtin_amdgcn_kernarg_segment_ptr()), "s"(blockIdx.x) : "v1", "v2", "v3", "v4", "v5", "v6", "v7", "v8", "v9", "v10", "v11", "v12", "v13", "v14", "v15", "v16", "v17", "v18", "v19", "v20", "v21", "v22", "v23", "v24", "v25", "v26", "v27", "v28", "v29", "v30", "v31", "v32", "v33", "v34", "v35", "v36", "v37", "v38", "v39", "v40", "v41", "v42", "v43", "v44", "v45", "v46", "v47", "v48", "v49", "v50", "v51", "v52", "v53", "v54", "v55", "v56", "v57", "v58", "v59", "v60", "v61", "v62", "v63", "v64", "v65", "v66", "v67", "v68", "v69", "v70", "v71", "v72", "v73", "v74", "v75", "v76", "v77", "v78", "v79", "v80", "v81", "v82", "v83", "v84", "v85", "v86", "v87", "v88", "v89", "v90", "v91", "v92", "v93", "v94", "v95", "v96", "v97", "v98", "v99", "v100", "v101", "v102", "v103", "v104", "v105", "v106", "v107", "v108", "v109", "v110", "v111", "v112", "v113", "v114", "v115", "v116", "v117", "v118", "v119", "v120", "v121", "v122", "v123", "v124", "v125", "v126", "v127", "v128", "v129", "v130", "v131", "v132", "v133", "v134", "v135", "v136", "v137", "v138", "v139", "v140", "v141", "v142", "v143", "v144", "v145", "v146", "v147", "v148", "v149", "v150", "v151", "v152", "v153", "v154", "v155", "v156", "v157", "v158", "v159", "v160", "v161", "v162", "v163", "v164", "v165", "v166", "v167", "v168", "v169", "v170", "v171", "v172", "v173", "v174", "v175", "v176", "v177", "v178", "v179", "v180", "v181", "v182", "v183", "v184", "v185", "v186", "v187", "v188", "v189", "v190", "v191", "v192", "v193", "v194", "v195", "v196", "v197", "v198", "v199", "v200", "v201", "v202", "v203", "v204", "v205", "v206", "v207", "v208", "v209", "v210", "v211", "v212", "v213", "v214", "v215", "v216", "v217", "v218", "v219", "v220", "v221", "v222", "v223", "v224", "v225", "v226", "v227", "v228", "v229", "v230", "v231", "v232", "v233", "v234", "v235", "v236", "v237", "v238", "v239", "v240", "v241", "v242", "v243", "v244", "v245", "v246", "v247", "v248", "v249", "v250", "v251", "v252", "v253", "v254", "v255", "a0", "a1", "a2", "a3", "a4", "a5", "a6", "a7", "a8", "a9", "a10", "a11", "a12", "a13", "a14", "a15", "a16", "a17", "a18", "a19", "a20", "a21", "a22", "a23", "a24", "a25", "a26", "a27", "a28", "a29", "a30", "a31", "a32", "a33", "a34", "a35", "a36", "a37", "a38", "a39", "a40", "a41", "a42", "a43", "a44", "a45", "a46", "a47", "a48", "a49", "a50", "a51", "a52", "a53", "a54", "a55", "a56", "a57", "a58", "a59", "a60", "a61", "a62", "a63", "a64", "a65", "a66", "a67", "a68", "a69", "a70", "a71", "a72", "a73", "a74", "a75", "a76", "a77", "a78", "a79", "a80", "a81", "a82", "a83", "a84", "a85", "a86", "a87", "a88", "a89", "a90", "a91", "a92", "a93", "a94", "a95", "a96", "a97", "a98", "a99", "a100", "a101", "a102", "a103", "a104", "a105", "a106", "a107", "a108", "a109", "a110", "a111", "a112", "a113", "a114", "a115", "a116", "a117", "a118", "a119", "a120", "a121", "a122", "a123", "a124", "a125", "a126", "a127", "a128", "a129", "a130", "a131", "a132", "a133", "a134", "a135", "a136", "a137", "a138", "a139", "a140", "a141", "a142", "a143", "a144", "a145", "a146", "a147", "a148", "a149", "a150", "a151", "a152", "a153", "a154", "a155", "a156", "a157", "a158", "a159", "a160", "a161", "a162", "a163", "a164", "a165", "a166", "a167", "a168", "a169", "a170", "a171", "a172", "a173", "a174", "a175", "a176", "a177", "a178", "a179", "a180", "a181", "a182", "a183", "a184", "a185", "a186", "a187", "a188", "a189", "a190", "a191", "a192", "a193", "a194", "a195", "a196", "a197", "a198", "a199", "a200", "a201", "a202", "a203", "a204", "a205", "a206", "a207", "a208", "a209", "a210", "a211", "a212", "a213", "a214", "a215", "a216", "a217", "a218", "a219", "a220", "a221", "a222", "a223", "a224", "a225", "a226", "a227", "a228", "a229", "a230", "a231", "a232", "a233", "a234", "a235", "a236", "a237", "a238", "a239", "a240", "a241", "a242", "a243", "a244", "a245", "a246", "a247", "a248", "a249", "a250", "a251", "a252", "a253", "a254", "a255", "s6", "s7", "s8", "s9", "s10", "s11", "s12", "s13", "s14", "s15", "s16", "s17", "s18", "s19", "s20", "s21", "s22", "s23", "s24", "s25", "s26", "s27", "s28", "s29", "s30", "s31", "s34", "s35", "s36", "s37", "s38", "s39", "s40", "s41", "s42", "s43", "s44", "s45", "s46", "s47", "s48", "s49", "s50", "s51", "s52", "s53", "s54", "s55", "s56", "s57", "s58", "s59", "s60", "s61", "s62", "s63", "s64", "s65", "s66", "s67", "s68", "s69", "s70", "s71", "s72", "s73", "s74", "s75", "s76", "s77", "s78", "s79", "s80", "s81", "s82", "s83", "s84", "s85", "s86", "s87", "s88", "s89", "s90", "s91", "s92", "s93", "s94", "s95", "s96", "s97", "s98", "s99", "vcc", "scc", "memory");
}

extern "C" void kernel_launch(void* const* d_in, const int* in_sizes, int n_in, void* d_out, int out_size, void* d_ws, size_t ws_size, hipStream_t stream) {
  char* ws = (char*)d_ws;

  k_fused<<<256, 256, 0, stream>>>(d_in[0], d_in[4], d_in[5], d_in[6], d_out, ws, d_in[1], d_in[2]);
}
```
